```python
import torch

def _softmax(x, axis=-1):
    return torch.softmax(x, dim=axis)
def _gelu(x):
    return torch.nn.functional.gelu(x, approximate="tanh")
def _silu(x):
    return torch.nn.functional.silu(x)
def _sigmoid(x):
    return torch.sigmoid(x)
def _layernorm(x, axis=-1, eps=1e-5):
    m = x.mean(dim=axis, keepdim=True)
    v = x.var(dim=axis, keepdim=True, unbiased=False)
    return (x - m) / torch.sqrt(v + eps)
def _scatter_add(tgt, idx, src, axis=0):
    return tgt.clone().index_add_(axis, idx, src)
def _segment_sum(src, seg, n):
    out = torch.zeros((n,) + src.shape[1:], dtype=src.dtype)
    return out.index_add_(0, seg, src)
def _pad(x, pw):
    flat = tuple(v for pair in reversed(pw) for v in pair)
    return torch.nn.functional.pad(x, flat)

kernel_name = 'parallel_sum__sliding_block_cached__glu_gated_geglu__decode'

def setup_inputs():

    _w = (lambda a: (lambda t: t.to(torch.bfloat16) if t.is_floating_point() else t)(torch.as_tensor(a)))
    def _A_setup():
        B, Sq, D = 2, 1, 448
        Hq, Hkv, Sk, Dh = 16, 8, 32768, 256
        assert Hq % Hkv == 0, (Hq, Hkv)
        return {
            "x": _w((lambda *s: torch.randn(*s, dtype=torch.bfloat16) * 0.05)(B, Sq, D)),
            "k_cache": _w((lambda *s: torch.randn(*s, dtype=torch.bfloat16) * 1.6)(B, Hkv, Sk, Dh)),
            "v_cache": _w((lambda *s: torch.randn(*s, dtype=torch.bfloat16) * 0.8)(B, Hkv, Sk, Dh)),
            "wq": _w((lambda *s: torch.randn(*s, dtype=torch.bfloat16) * 1.6)(D, Hq * Dh) * (D ** -0.5)),
            "wo": _w((lambda *s: torch.randn(*s, dtype=torch.bfloat16) * 0.8)(Hq * Dh, D) * ((Hq * Dh) ** -0.5)),
        }
    _A_in = _A_setup()

    def _B_setup():
        B, S, Dm, Df = 2, 1, 448, 1792
        return {
            "x": _w((lambda *s: torch.randn(*s, dtype=torch.bfloat16) * 0.05)(B, S, Dm)),
            "w_gate": _w((lambda *s: torch.randn(*s, dtype=torch.bfloat16) * 0.4)(Dm, Df) * (Dm ** -0.5)),
            "w_up": _w((lambda *s: torch.randn(*s, dtype=torch.bfloat16) * 0.4)(Dm, Df) * (Dm ** -0.5)),
            "w_down": _w((lambda *s: torch.randn(*s, dtype=torch.bfloat16) * 0.4)(Df, Dm) * (Df ** -0.5)),
        }
    _B_in = _B_setup()

    return {
        "A_x": _A_in["x"],
        "A_k_cache": _A_in["k_cache"],
        "A_v_cache": _A_in["v_cache"],
        "A_wq": _A_in["wq"],
        "A_wo": _A_in["wo"],
        "B_x": _B_in["x"],
        "B_w_gate": _B_in["w_gate"],
        "B_w_up": _B_in["w_up"],
        "B_w_down": _B_in["w_down"],
    }

def reference(A_x, A_k_cache, A_v_cache, A_wq, A_wo, B_x, B_w_gate, B_w_up, B_w_down):
    def _A_ref(x, k_cache, v_cache, wq, wo):
        B, Sq, D = x.shape
        Hq, Dh = 16, 256
        Hkv, Sk = k_cache.shape[1], k_cache.shape[2]
        W = min(4096, max(256, Sk // 4))
        rep = Hq // Hkv
        q = (lambda x, *d: x.permute(*d))((x @ wq).reshape(B, Sq, Hq, Dh), 0, 2, 1, 3)
        k = (lambda x, n, axis: torch.repeat_interleave(x, n, dim=axis))(k_cache[:, :, Sk - W:, :], rep, axis=1)
        v = (lambda x, n, axis: torch.repeat_interleave(x, n, dim=axis))(v_cache[:, :, Sk - W:, :], rep, axis=1)
        scores = torch.einsum("bhqd,bhkd->bhqk", q, k) * (Dh ** -0.5)
        p = _softmax(scores, axis=-1)
        o = torch.einsum("bhqk,bhkd->bhqd", p, v)
        return (lambda x, *d: x.permute(*d))(o, 0, 2, 1, 3).reshape(B, Sq, Hq * Dh) @ wo

    def _B_ref(x, w_gate, w_up, w_down):
        g = torch.einsum("bsd,df->bsf", x, w_gate)
        u = torch.einsum("bsd,df->bsf", x, w_up)
        h = _gelu(g) * u
        return torch.einsum("bsf,fd->bsd", h, w_down)

    x = A_x
    xn = _layernorm(x, axis=-1)
    return x + _A_ref(xn, A_k_cache, A_v_cache, A_wq, A_wo) + _B_ref(xn, B_w_gate, B_w_up, B_w_down)
```

```cpp
#include <hip/hip_runtime.h>
#include <hip/hip_bf16.h>
#include <stdint.h>

typedef unsigned int u32;
typedef unsigned short u16;
typedef u32 u32x4 __attribute__((ext_vector_type(4)));
typedef __bf16 bf16x2_t __attribute__((ext_vector_type(2)));

constexpr int B = 2, D = 448, HQ = 16, HKV = 8, SK = 32768, DH = 256, W = 4096, DF = 1792;
constexpr int NSPLIT = 16, KPS = W / NSPLIT;

__device__ __forceinline__ float bf2f(u16 v) { return __uint_as_float((u32)v << 16); }
__device__ __forceinline__ float lo(u32 v) { return __uint_as_float(v << 16); }
__device__ __forceinline__ float hi(u32 v) { return __uint_as_float(v & 0xffff0000u); }
__device__ __forceinline__ float rbf(float f) { return __bfloat162float(__float2bfloat16(f)); }
__device__ __forceinline__ float dot2(u32 a, u32 b, float c) {
  return __builtin_amdgcn_fdot2_f32_bf16(__builtin_bit_cast(bf16x2_t, a), __builtin_bit_cast(bf16x2_t, b), c, false);
}
__device__ __forceinline__ float wsum(float v) { for (int o = 32; o; o >>= 1) v += __shfl_xor(v, o); return v; }
__device__ __forceinline__ float wmax(float v) { for (int o = 32; o; o >>= 1) v = fmaxf(v, __shfl_xor(v, o)); return v; }

__device__ void layernorm(const u16* x, float* xn  ) {
  int w = threadIdx.x >> 6, lane = threadIdx.x & 63;
  if (w < B) {
    float v[7]; float s = 0.f;
    for (int i = 0; i < 7; ++i) { v[i] = bf2f(x[w * D + lane + 64 * i]); s += v[i]; }
    float mean = wsum(s) * (1.f / D);
    float q = 0.f;
    for (int i = 0; i < 7; ++i) { float d = v[i] - mean; q += d * d; }
    float var = rbf(wsum(q) * (1.f / D));
    float mb = rbf(mean);
    float den = rbf(sqrtf(rbf(var + 1e-5f)));
    for (int i = 0; i < 7; ++i) xn[w * D + lane + 64 * i] = rbf(rbf(v[i] - mb) / den);
  }
  __syncthreads();
}

__global__ void __launch_bounds__(256) k_proj(const u16* x, const u16* wq, const u16* wg, const u16* wu, float* q, float* h) {
  __shared__ float xn[B * D];
  __shared__ float red[2][32][2][64];
  layernorm(x, xn);
  int t = threadIdx.x, c8 = t & 7, r = t >> 3;
  bool isq = blockIdx.x < 64;
  int nmat = isq ? 1 : 2;
  for (int m = 0; m < nmat; ++m) {
    const u16* wp = isq ? wq + blockIdx.x * 64 : (m ? wu : wg) + (blockIdx.x - 64) * 64;
    int ld = isq ? HQ * DH : DF;
    float a0[8] = {}, a1[8] = {};
    for (int k = r; k < D; k += 32) {
      u32x4 wv = *(const u32x4*)(wp + (size_t)k * ld + c8 * 8);
      float x0 = xn[k], x1 = xn[D + k];
      for (int j = 0; j < 4; ++j) {
        a0[2 * j] += x0 * lo(wv[j]); a0[2 * j + 1] += x0 * hi(wv[j]);
        a1[2 * j] += x1 * lo(wv[j]); a1[2 * j + 1] += x1 * hi(wv[j]);
      }
    }
    for (int j = 0; j < 8; ++j) { red[m][r][0][c8 * 8 + j] = a0[j]; red[m][r][1][c8 * 8 + j] = a1[j]; }
  }
  __syncthreads();
  if (t < 128) {
    int b = t >> 6, c = t & 63;
    float s0 = 0.f, s1 = 0.f;
    for (int i = 0; i < 32; ++i) { s0 += red[0][i][b][c]; if (!isq) s1 += red[1][i][b][c]; }
    if (isq) q[b * HQ * DH + blockIdx.x * 64 + c] = rbf(s0);
    else {
      float g = rbf(s0), u = rbf(s1);
      float ge = rbf(0.5f * g * (1.f + tanhf(0.7978845608028654f * (g + 0.044715f * g * g * g))));
      h[b * DF + (blockIdx.x - 64) * 64 + c] = rbf(ge * u);
    }
  }
}

__global__ void __launch_bounds__(512) k_attn(const u16* kc, const u16* vc, const float* q, float* po, float* pml) {
  __shared__ float sp[8][2][32];
  __shared__ float so[8][2][256];
  __shared__ float sml[8][2][2];
  int wg = blockIdx.x, kvh = wg & 7, b = (wg >> 3) & 1, sp_i = wg >> 4;
  int w = threadIdx.x >> 6, lane = threadIdx.x & 63, half = lane >> 5, c = lane & 31;
  size_t base = ((size_t)(b * HKV + kvh) * SK + (SK - W) + sp_i * KPS + w * 32) * DH;
  const u32x4* kp = (const u32x4*)(kc + base) + lane;
  const u32x4* vp = (const u32x4*)(vc + base) + lane;
  u32x4 kr[16], vr[16];
#pragma unroll
  for (int i = 0; i < 16; ++i) kr[i] = __builtin_nontemporal_load(kp + i * 64);
#pragma unroll
  for (int i = 0; i < 16; ++i) vr[i] = __builtin_nontemporal_load(vp + i * 64);
  u32 qp[2][4];
  for (int hh = 0; hh < 2; ++hh)
    for (int j = 0; j < 4; ++j) {
      const float* qq = q + b * HQ * DH + (kvh * 2 + hh) * DH + c * 8 + 2 * j;
      qp[hh][j] = (__float_as_uint(qq[0]) >> 16) | (__float_as_uint(qq[1]) & 0xffff0000u);
    }
#pragma unroll
  for (int i = 0; i < 16; ++i)
#pragma unroll
    for (int hh = 0; hh < 2; ++hh) {
      float s = 0.f;
#pragma unroll
      for (int j = 0; j < 4; ++j) s = dot2(kr[i][j], qp[hh][j], s);
      for (int o = 16; o; o >>= 1) s += __shfl_xor(s, o);
      if (c == 0) sp[w][hh][2 * i + half] = rbf(s) * 0.0625f;
    }
  __builtin_amdgcn_s_waitcnt(0xc07f);
  { int hh = half; float s = sp[w][hh][c];
    float m = s; for (int o = 16; o; o >>= 1) m = fmaxf(m, __shfl_xor(m, o));
    float p = __expf(s - m); float l = p; for (int o = 16; o; o >>= 1) l += __shfl_xor(l, o);
    sp[w][hh][c] = p; if (c == 0) { sml[w][hh][0] = m; sml[w][hh][1] = l; } }
  __builtin_amdgcn_s_waitcnt(0xc07f);
  float acc[2][8] = {};
#pragma unroll
  for (int i = 0; i < 16; ++i) {
    float p0 = sp[w][0][2 * i + half], p1 = sp[w][1][2 * i + half];
#pragma unroll
    for (int j = 0; j < 4; ++j) {
      acc[0][2 * j] += p0 * lo(vr[i][j]); acc[0][2 * j + 1] += p0 * hi(vr[i][j]);
      acc[1][2 * j] += p1 * lo(vr[i][j]); acc[1][2 * j + 1] += p1 * hi(vr[i][j]);
    }
  }
  for (int hh = 0; hh < 2; ++hh)
    for (int j = 0; j < 8; ++j) { float v = acc[hh][j] + __shfl_xor(acc[hh][j], 32); if (half == 0) so[w][hh][c * 8 + j] = v; }
  __syncthreads();
  { int hh = threadIdx.x >> 8, d = threadIdx.x & 255;
    float M = -INFINITY; for (int i = 0; i < 8; ++i) M = fmaxf(M, sml[i][hh][0]);
    float L = 0.f, o = 0.f;
    for (int i = 0; i < 8; ++i) { float e = __expf(sml[i][hh][0] - M); L += e * sml[i][hh][1]; o += e * so[i][hh][d]; }
    int slot = ((b * HQ + kvh * 2 + hh) * NSPLIT + sp_i);
    po[slot * DH + d] = o;
    if (d == 0) { pml[slot * 2] = M; pml[slot * 2 + 1] = L; } }
}

__global__ void __launch_bounds__(1024) k_out(const u16* x, const u16* wo, const u16* wd, const float* po, const float* pml, const float* h, u16* out) {
  __shared__ float o[B * HQ * DH];
  __shared__ float wsc[B * HQ * NSPLIT];
  __shared__ float red[2][16][2][16];
  int t = threadIdx.x;
  if (t < B * HQ) {
    float M = -INFINITY; for (int s = 0; s < NSPLIT; ++s) M = fmaxf(M, pml[(t * NSPLIT + s) * 2]);
    float L = 0.f; for (int s = 0; s < NSPLIT; ++s) L += __expf(pml[(t * NSPLIT + s) * 2] - M) * pml[(t * NSPLIT + s) * 2 + 1];
    for (int s = 0; s < NSPLIT; ++s) wsc[t * NSPLIT + s] = __expf(pml[(t * NSPLIT + s) * 2] - M) / L;
  }
  __syncthreads();
  for (int i = t; i < B * HQ * DH; i += 1024) {
    int bh = i >> 8, d = i & 255; float s = 0.f;
    for (int k = 0; k < NSPLIT; ++k) s += wsc[bh * NSPLIT + k] * po[(bh * NSPLIT + k) * DH + d];
    o[i] = rbf(s);
  }
  __syncthreads();
  int c8 = t & 1, r = t >> 1, col = blockIdx.x * 16 + c8 * 8;
  float a[2][2][8] = {};
  for (int k = r; k < HQ * DH; k += 512) {
    u32x4 wv = *(const u32x4*)(wo + (size_t)k * D + col);
    float x0 = o[k], x1 = o[HQ * DH + k];
    for (int j = 0; j < 4; ++j) { a[0][0][2*j] += x0 * lo(wv[j]); a[0][0][2*j+1] += x0 * hi(wv[j]); a[0][1][2*j] += x1 * lo(wv[j]); a[0][1][2*j+1] += x1 * hi(wv[j]); }
  }
  for (int k = r; k < DF; k += 512) {
    u32x4 wv = *(const u32x4*)(wd + (size_t)k * D + col);
    float x0 = h[k], x1 = h[DF + k];
    for (int j = 0; j < 4; ++j) { a[1][0][2*j] += x0 * lo(wv[j]); a[1][0][2*j+1] += x0 * hi(wv[j]); a[1][1][2*j] += x1 * lo(wv[j]); a[1][1][2*j+1] += x1 * hi(wv[j]); }
  }
  int w = t >> 6, lane = t & 63;
  for (int m = 0; m < 2; ++m) for (int b = 0; b < 2; ++b) for (int j = 0; j < 8; ++j) {
    float v = a[m][b][j]; for (int of = 2; of < 64; of <<= 1) v += __shfl_xor(v, of);
    if (lane < 2) red[m][w][b][lane * 8 + j] = v;
  }
  __syncthreads();
  if (t < 32) {
    int b = t >> 4, cc = t & 15; float sa = 0.f, sb = 0.f;
    for (int i = 0; i < 16; ++i) { sa += red[0][i][b][cc]; sb += red[1][i][b][cc]; }
    int idx = b * D + blockIdx.x * 16 + cc;
    float r1 = rbf(bf2f(x[idx]) + rbf(sa));
    __hip_bfloat16 res = __float2bfloat16(r1 + rbf(sb));
    out[idx] = *(u16*)&res;
  }
}

extern "C" void kernel_launch(void* const* d_in, const int* in_sizes, int n_in, void* d_out, int out_size, void* d_ws, size_t ws_size, hipStream_t stream) {
  const u16* x = (const u16*)d_in[0]; const u16* kc = (const u16*)d_in[1]; const u16* vc = (const u16*)d_in[2];
  const u16* wq = (const u16*)d_in[3]; const u16* wo = (const u16*)d_in[4];
  const u16* wg = (const u16*)d_in[6]; const u16* wu = (const u16*)d_in[7]; const u16* wd = (const u16*)d_in[8];
  float* ws = (float*)d_ws;
  float* q = ws; float* h = q + B * HQ * DH; float* po = h + 4096; float* pml = po + B * HQ * NSPLIT * DH;
  k_proj<<<64 + 28, 256, 0, stream>>>(x, wq, wg, wu, q, h);
  k_attn<<<256, 512, 0, stream>>>(kc, vc, q, po, pml);
  k_out<<<28, 1024, 0, stream>>>(x, wo, wd, po, pml, h, (u16*)d_out);
}
```

```cpp
#include <hip/hip_runtime.h>
#include <hip/hip_bf16.h>
#include <stdint.h>

typedef unsigned int u32;
typedef unsigned short u16;
typedef unsigned long long u64;
typedef u32 u32x2 __attribute__((ext_vector_type(2)));
typedef u32 u32x4 __attribute__((ext_vector_type(4)));
typedef float f32x4 __attribute__((ext_vector_type(4)));
typedef float f32x16 __attribute__((ext_vector_type(16)));
typedef __bf16 bf16x2_t __attribute__((ext_vector_type(2)));
typedef __bf16 bf16x8_t __attribute__((ext_vector_type(8)));
#define LAS __attribute__((address_space(3)))
typedef __amdgpu_buffer_rsrc_t rsrc_t;

#define STAMP 0
#define LATE 3
#define ABL 0
#define WS_OFF 0

namespace {
constexpr int D = 448, SK = 32768, W = 4096, DH = 256, DF = 1792;
constexpr int AUX_NT = 2, AUX_SC1 = 16;

constexpr int EP_OFF = 0;
constexpr int ST_OFF = 4096;
constexpr int QP_OFF = ST_OFF + 8192;
constexpr int GP_OFF = QP_OFF + 256 * 512;
constexpr int O1_OFF = GP_OFF + 256 * 1024;
constexpr int O2_OFF = O1_OFF + 256 * 2048;
constexpr int WS_BYTES = O2_OFF + 256 * 3584;
constexpr int TS_OFF = (WS_BYTES + 4095) / 4096 * 4096;

constexpr int L_XN = 0;
constexpr int L_QX = L_XN + 480;
constexpr int L_Q = L_QX + 128;
constexpr int L_P = L_Q + 256;
constexpr int L_OP = L_P + 512;
constexpr int L_WM = L_OP + 2048;
constexpr int L_WL = L_WM + 8;
constexpr int L_H = L_WL + 8;
constexpr int L_O = L_H + 8;
constexpr int L_R = L_O + 16;
constexpr int L_C = L_R + 128;
constexpr int C_QX = 0, C_Q = 1, C_MAX = 2, C_P = 3, C_V = 4, C_PUB = 5, C_H = 6, C_O = 7, C_TAG = 8, C_ACK = 9, C_VI = 10, C_O2 = 11, C_R = 12;
constexpr int L_END = L_C + 16;

constexpr u32 SPIN_LDS = 1u << 20, SPIN_MEM = 1u << 16;

__device__ __forceinline__ float asf(u32 v) { return __builtin_bit_cast(float, v); }
__device__ __forceinline__ u32 asu(float v) { return __builtin_bit_cast(u32, v); }
__device__ __forceinline__ float lo16(u32 v) { return asf(v << 16); }
__device__ __forceinline__ float hi16(u32 v) { return asf(v & 0xffff0000u); }
__device__ __forceinline__ u32 pkbf(float a, float b) {
  bf16x2_t r = {(__bf16)a, (__bf16)b};
  return __builtin_bit_cast(u32, r);
}
__device__ __forceinline__ float rbf(float a) { return (float)(__bf16)a; }
__device__ __forceinline__ float dot2(u32 a, u32 b, float c) {
  return __builtin_amdgcn_fdot2_f32_bf16(__builtin_bit_cast(bf16x2_t, a), __builtin_bit_cast(bf16x2_t, b), c, false);
}
template <int CTRL> __device__ __forceinline__ float dpp(float x) {
  return asf(__builtin_amdgcn_mov_dpp((int)asu(x), CTRL, 0xf, 0xf, true));
}
constexpr int XOR1 = 0xB1, XOR2 = 0x4E, HMIR = 0x141, ROR8 = 0x128, ROR4 = 0x124;
__device__ __forceinline__ float tr32(float a, float b) {
  auto r = __builtin_amdgcn_permlane32_swap(asu(a), asu(b), false, false);
  return asf(r[0]) + asf(r[1]);
}
__device__ __forceinline__ float tr16(float a, float b) {
  auto r = __builtin_amdgcn_permlane16_swap(asu(a), asu(b), false, false);
  return asf(r[0]) + asf(r[1]);
}
__device__ __forceinline__ float mx32(float a) {
  auto r = __builtin_amdgcn_permlane32_swap(asu(a), asu(a), false, false);
  return fmaxf(asf(r[0]), asf(r[1]));
}
__device__ __forceinline__ float mx16(float a) {
  auto r = __builtin_amdgcn_permlane16_swap(asu(a), asu(a), false, false);
  return fmaxf(asf(r[0]), asf(r[1]));
}
template <int CTRL> __device__ __forceinline__ float trd(float a, float b, bool bit) {
  const float keep = bit ? b : a, give = bit ? a : b;
  return keep + dpp<CTRL>(give);
}
__device__ __forceinline__ float row_sum(float v) { v += dpp<ROR8>(v); v += dpp<ROR4>(v); v += dpp<XOR2>(v); v += dpp<XOR1>(v); return v; }
__device__ __forceinline__ float row_max(float v) {
  v = fmaxf(v, dpp<ROR8>(v)); v = fmaxf(v, dpp<ROR4>(v)); v = fmaxf(v, dpp<XOR2>(v)); v = fmaxf(v, dpp<XOR1>(v)); return v;
}
__device__ __forceinline__ float wave_sum(float v) { v = row_sum(v); v = tr16(v, v); return tr32(v, v); }

__device__ __forceinline__ rsrc_t mkrs(const void* p, u32 bytes) { return __builtin_amdgcn_make_buffer_rsrc((void*)p, 0, bytes, 0x00020000); }
template <int AUX> __device__ __forceinline__ u32x4 ld128(rsrc_t r, int voff, int soff = 0) { return __builtin_amdgcn_raw_buffer_load_b128(r, voff, soff, AUX); }
__device__ __forceinline__ void st128(rsrc_t r, int voff, u32x4 v) { __builtin_amdgcn_raw_buffer_store_b128(v, r, voff, 0, AUX_SC1); }
__device__ __forceinline__ u32 tagw(float v, u32 tag8) { return (asu(v) & 0xffffff00u) | tag8; }
__device__ __forceinline__ u32x4 tag4(float a, float b, float c, float d, u32 t) { return u32x4{tagw(a, t), tagw(b, t), tagw(c, t), tagw(d, t)}; }
__device__ __forceinline__ u32 untag4(u32x4& v, u32 t) { v.x ^= t; v.y ^= t; v.z ^= t; v.w ^= t; return v.x | v.y | v.z | v.w; }
__device__ __forceinline__ bool all_ok(u32 bad) { return __builtin_amdgcn_ballot_w64((bad & 0xffu) != 0u) == 0ull; }

#define CBAR() asm volatile("" ::: "memory")
#define LGKM0() asm volatile("s_waitcnt lgkmcnt(0)" ::: "memory")
__device__ __forceinline__ u32 lds_peek(LAS u32* p) { return __builtin_amdgcn_readfirstlane(__hip_atomic_load(p, __ATOMIC_RELAXED, __HIP_MEMORY_SCOPE_WORKGROUP)); }
__device__ __forceinline__ void lds_wait(LAS u32* p, u32 want) {
  for (u32 s = 0; lds_peek(p) < want && s < SPIN_LDS; ++s) __builtin_amdgcn_s_sleep(1);
  CBAR();
}
__device__ __forceinline__ void lds_wait_hot(LAS u32* p, u32 want) {
  for (u32 s = 0; lds_peek(p) < want && s < SPIN_LDS * 4; ++s) {}
  CBAR();
}
__device__ __forceinline__ void lds_signal(LAS u32* p, int lane) {
  LGKM0();
  if (lane == 0) (void)__hip_atomic_fetch_add(p, 1u, __ATOMIC_RELAXED, __HIP_MEMORY_SCOPE_WORKGROUP);
}
__device__ __forceinline__ u32 lds_ticket(LAS u32* p, int lane) {
  LGKM0();
  u32 t = 0;
  if (lane == 0) t = __hip_atomic_fetch_add(p, 1u, __ATOMIC_RELAXED, __HIP_MEMORY_SCOPE_WORKGROUP);
  t = __builtin_amdgcn_readfirstlane(t);
  CBAR();
  return t;
}

#define TS(k)
#define TSV(k)
#define TSW(k)

__device__ __forceinline__ void final_reduce(LAS u32* L, rsrc_t rws, const u16* x, u16* out, char* ws, int wg, int w, int grp, int lane, u32 tag) {
  LAS float* const LF = (LAS float*)L;
  __builtin_amdgcn_s_setprio(3);
  const u32 tag8 = tag & 0xffu;
  const int rb = wg / 28, cb = wg % 28, qd = lane & 3;
  const float xres = lo16(x[rb * D + 16 * cb + (lane & 15)]);
  const int oo = O2_OFF + (grp + 8 * (lane >> 2)) * 3584 + rb * 1792 + cb * 64 + qd * 16;
  lds_wait(L + L_C + C_O, 1);
  lds_wait_hot(L + L_C + C_O2, 4);
  u32x4 a0, a1;
  for (u32 s = 0; s < SPIN_MEM; ++s) {
    CBAR();
    a0 = ld128<AUX_SC1>(rws, oo); a1 = ld128<AUX_SC1>(rws, oo, 128 * 3584);
    if (all_ok(untag4(a0, tag8) | untag4(a1, tag8))) break;
  }
  TSV(6);
  float f = tr16(tr32(asf(a0.x) + asf(a1.x), asf(a0.z) + asf(a1.z)), tr32(asf(a0.y) + asf(a1.y), asf(a0.w) + asf(a1.w)));
  f += dpp<ROR8>(f); f += dpp<ROR4>(f);
  if ((lane & 12) == 0) LF[L_R + grp * 16 + 4 * qd + (lane >> 4)] = f;
  if (lds_ticket(L + L_C + C_R, lane) == 7) {
    const LAS float* rp = LF + L_R + (lane & 15);
    const float t = ((rp[0] + rp[16]) + (rp[32] + rp[48])) + ((rp[64] + rp[80]) + (rp[96] + rp[112]));
    if (lane < 16) out[rb * D + 16 * cb + lane] = (u16)pkbf(xres + t, 0.f);
    if (wg == 0 && lane == 0) __builtin_amdgcn_raw_buffer_store_b32(tag, rws, EP_OFF, 0, AUX_SC1);
    TS(7);
  }
  TSW(6);
}

__global__ void __launch_bounds__(768, 3)
fused_decode(const u16* __restrict__ x, const u16* __restrict__ kc, const u16* __restrict__ vc, const u16* __restrict__ wq,
             const u16* __restrict__ wo, const u16* __restrict__ wg_, const u16* __restrict__ wu, const u16* __restrict__ wd,
             u16* __restrict__ out, char* __restrict__ ws) {
  __shared__ __attribute__((aligned(16))) u32 smem[L_END];
  LAS u32* const L = (LAS u32*)smem;
  LAS float* const LF = (LAS float*)smem;
  const int wg = blockIdx.x, h = wg & 7, j = wg >> 3, b = j & 1, sp = j >> 1, reg = wg & 15;
  const int w = __builtin_amdgcn_readfirstlane(threadIdx.x >> 6), lane = threadIdx.x & 63;
  TSV(0);
  asm volatile("" :: "s"(x), "s"(kc), "s"(vc), "s"(wq), "s"(wo), "s"(wg_), "s"(wu), "s"(wd), "s"(out), "s"(ws));
  if (threadIdx.x < 16) L[L_C + threadIdx.x] = 0u;
  const rsrc_t rws = mkrs(ws, WS_BYTES);

  if (w >= 4) {
    const bool isv = w >= 8;
    const int kw = w & 3;
    if (isv) __builtin_amdgcn_s_setprio(1); else __builtin_amdgcn_s_setprio(2);
    LGKM0();
    __builtin_amdgcn_s_barrier();
    const u32 u0 = sp * 128 + kw + reg * 37;
#define STREAM_LOADS(SRC) \
    const rsrc_t rkv = mkrs((SRC) + ((size_t)reg * SK + (SK - W)) * DH, W * DH * 2); \
    u32x4 d[32]; \
    _Pragma("unroll") for (int i = 0; i < 32; ++i) d[i] = ld128<AUX_NT>(rkv, lane * 16, (int)(((u0 + 4 * i) & 2047u) << 10)); \
    __builtin_amdgcn_sched_barrier(0); \
    TSV(1)

    if (!isv) {
      STREAM_LOADS(kc);
      lds_wait(L + L_C + C_Q, 2);
      TSV(2);
      const u32x4 q0 = *(LAS u32x4*)(L + L_Q + 4 * (lane & 31)), q1 = *(LAS u32x4*)(L + L_Q + 128 + 4 * (lane & 31));
      const bool b3 = lane & 8, b2 = lane & 4, b1 = lane & 2, b0 = lane & 1;
      float sc[2];
#pragma unroll
      for (int t = 0; t < 2; ++t) {
        float y[16];
#pragma unroll
        for (int i = 0; i < 16; ++i) {
          const u32x4 k = d[16 * t + i];
          float x0 = dot2(k.x, q0.x, 0.f), x1 = dot2(k.x, q1.x, 0.f);
          x0 = dot2(k.y, q0.y, x0); x1 = dot2(k.y, q1.y, x1);
          x0 = dot2(k.z, q0.z, x0); x1 = dot2(k.z, q1.z, x1);
          x0 = dot2(k.w, q0.w, x0); x1 = dot2(k.w, q1.w, x1);
          y[i] = tr16(x0, x1);
        }
#pragma unroll
        for (int i = 0; i < 8; ++i) y[i] = trd<ROR8>(y[2 * i], y[2 * i + 1], b3);
#pragma unroll
        for (int i = 0; i < 4; ++i) y[i] = trd<HMIR>(y[2 * i], y[2 * i + 1], b2);
#pragma unroll
        for (int i = 0; i < 2; ++i) y[i] = trd<XOR2>(y[2 * i], y[2 * i + 1], b1);
        sc[t] = rbf(trd<XOR1>(y[0], y[1], b0)) * 0.0625f;
      }
      const int head = (lane >> 4) & 1, hf = lane >> 5;
      const float wm = mx32(row_max(fmaxf(sc[0], sc[1])));
      if ((lane & 47) == 0) LF[L_WM + kw * 2 + head] = wm;
      lds_signal(L + L_C + C_MAX, lane);
      lds_wait_hot(L + L_C + C_MAX, 4);
      const float M = fmaxf(fmaxf(LF[L_WM + head], LF[L_WM + 2 + head]), fmaxf(LF[L_WM + 4 + head], LF[L_WM + 6 + head]));
      const float p0 = rbf(__builtin_amdgcn_exp2f((sc[0] - M) * 1.4426950408889634f));
      const float p1 = rbf(__builtin_amdgcn_exp2f((sc[1] - M) * 1.4426950408889634f));
      const int slot = (b3 ? 1 : 0) + (b2 ? 2 : 0) + (b1 ? 4 : 0) + (b0 ? 8 : 0);
      LAS u32* pp = L + L_P + ((kw * 2 + hf) * 2 + head) * 32 + slot;
      pp[0] = asu(p0) | (asu(p0) >> 16);
      pp[16] = asu(p1) | (asu(p1) >> 16);
      float ls = row_sum(p0 + p1);
      ls = tr32(ls, ls);
      if ((lane & 47) == 0) LF[L_WL + kw * 2 + head] = ls;
      const u32 tk = lds_ticket(L + L_C + C_P, lane);
      const u32 tag = L[L_C + C_TAG];
      const u32 tag8 = tag & 0xffu;
      if (tk == 3 && (lane & 47) == 0) {
        const float Ls = (LF[L_WL + head] + LF[L_WL + 2 + head]) + (LF[L_WL + 4 + head] + LF[L_WL + 6 + head]);
        st128(rws, ST_OFF + (((h * 2 + head) * 2 + b) * 16 + sp) * 16, u32x4{asu(M), tag, asu(Ls), tag});
      }
      TSV(3);
      TSW(0); TSW(1); TSW(2); TSW(3);

      if (kw == 1) {
        __builtin_amdgcn_s_setprio(3);
        const int s = lane >> 2, qd = lane & 3;
        const int so = ST_OFF + ((h * 2 + (j >> 4)) * 32 + s) * 16;
        const int oo = O1_OFF + (h + 16 * s) * 2048 + j * 64 + qd * 16;
        lds_wait(L + L_C + C_VI, 4);
        lds_wait_hot(L + L_C + C_PUB, 1);
        u32x4 s0, s1, a0, a1;
        for (u32 sp_ = 0; sp_ < SPIN_MEM; ++sp_) {
          CBAR();
          s0 = ld128<AUX_SC1>(rws, so); s1 = ld128<AUX_SC1>(rws, so + 256);
          const bool bad = (s0.y != tag) | (s0.w != tag) | (s1.y != tag) | (s1.w != tag);
          if (__builtin_amdgcn_ballot_w64(bad) == 0ull) break;
        }
        CBAR();
        a0 = ld128<AUX_SC1>(rws, oo); a1 = ld128<AUX_SC1>(rws, oo + 8 * 2048);
        __builtin_amdgcn_sched_barrier(0);
        float wgt[2];
#pragma unroll
        for (int bb = 0; bb < 2; ++bb) {
          const float m = asf(bb ? s1.x : s0.x), l = asf(bb ? s1.z : s0.z);
          float mg = fmaxf(m, dpp<ROR8>(m)); mg = fmaxf(mg, dpp<ROR4>(mg)); mg = mx32(mx16(mg));
          const float e = __builtin_amdgcn_exp2f((m - mg) * 1.4426950408889634f);
          float lg = e * l; lg += dpp<ROR8>(lg); lg += dpp<ROR4>(lg); lg = tr16(lg, lg); lg = tr32(lg, lg);
          wgt[bb] = e * __builtin_amdgcn_rcpf(lg);
        }
        asm volatile("" : "+v"(wgt[0]), "+v"(wgt[1]));
        __builtin_amdgcn_sched_barrier(0);
        TSV(4);
        for (u32 sp_ = 0; sp_ < SPIN_MEM; ++sp_) {
          if (all_ok(untag4(a0, tag8) | untag4(a1, tag8))) break;
          CBAR();
          a0 = ld128<AUX_SC1>(rws, oo); a1 = ld128<AUX_SC1>(rws, oo + 8 * 2048);
        }
        TSV(5);
        const float v0 = tr32(asf(a0.x) * wgt[0], asf(a1.x) * wgt[1]), v1 = tr32(asf(a0.y) * wgt[0], asf(a1.y) * wgt[1]);
        const float v2 = tr32(asf(a0.z) * wgt[0], asf(a1.z) * wgt[1]), v3 = tr32(asf(a0.w) * wgt[0], asf(a1.w) * wgt[1]);
        float e0 = tr16(v0, v2), e1 = tr16(v1, v3);
        e0 += dpp<ROR8>(e0); e1 += dpp<ROR8>(e1);
        e0 += dpp<ROR4>(e0); e1 += dpp<ROR4>(e1);
        if ((lane & 12) == 0) L[L_O + qd * 4 + (lane >> 5) * 2 + ((lane >> 4) & 1)] = pkbf(e0, e1);
        lds_signal(L + L_C + C_O, lane);
        TSW(4); TSW(5);
      }
      if (wg < 56) final_reduce(L, rws, x, out, ws, wg, w, kw, lane, tag);
    } else {
      STREAM_LOADS(vc);
      lds_signal(L + L_C + C_VI, lane);
      const int m = lane & 31, hf = lane >> 5;
      u32x4 msk;
#pragma unroll
      for (int r = 0; r < 4; ++r) msk[r] = (m < 16 && ((m & 7) >> 1) == r) ? ((m & 1) ? 0xffff0000u : 0x0000ffffu) : 0u;
      lds_wait(L + L_C + C_P, 4);
      TSV(2);
      const LAS u32x4* pp = (const LAS u32x4*)(L + L_P + ((kw * 2 + hf) * 2 + ((m >> 3) & 1)) * 32);
      f32x16 acc = {};
#pragma unroll
      for (int g = 0; g < 8; ++g) {
        const u32x4 p4 = pp[g];
#pragma unroll
        for (int k = 0; k < 4; ++k) {
          const u32x4 a = msk & p4[k];
          acc = __builtin_amdgcn_mfma_f32_32x32x16_bf16(__builtin_bit_cast(bf16x8_t, a), __builtin_bit_cast(bf16x8_t, d[4 * g + k]), acc, 0, 0, 0);
        }
        __builtin_amdgcn_sched_barrier(0);
      }
      TSV(3);
      LAS f32x4* op = (LAS f32x4*)(LF + L_OP) + 2 * m + hf;
      op[kw * 128] = f32x4{acc[0], acc[1], acc[2], acc[3]};
      op[kw * 128 + 64] = f32x4{acc[4], acc[5], acc[6], acc[7]};
      const u32 tag = L[L_C + C_TAG], tag8 = tag & 0xffu;
      if (lds_ticket(L + L_C + C_V, lane) == 3) {
        const LAS f32x4* rp = (const LAS f32x4*)(LF + L_OP) + lane;
        const f32x4 o0 = (rp[0] + rp[128]) + (rp[256] + rp[384]);
        const f32x4 o1 = (rp[64] + rp[192]) + (rp[320] + rp[448]);
        st128(rws, O1_OFF + wg * 2048 + lane * 16, tag4(o0.x, o0.y, o0.z, o0.w, tag8));
        st128(rws, O1_OFF + wg * 2048 + 1024 + lane * 16, tag4(o1.x, o1.y, o1.z, o1.w, tag8));
        if (lane == 0) __hip_atomic_store(L + L_C + C_PUB, 1u, __ATOMIC_RELAXED, __HIP_MEMORY_SCOPE_WORKGROUP);
        TS(4);
      }
      TSW(0); TSW(1); TSW(2); TSW(3);
      if (wg < 56) final_reduce(L, rws, x, out, ws, wg, w, 4 + kw, lane, tag);
    }
    return;
  }

  __builtin_amdgcn_s_setprio(3);
  const bool mlp = wg < 224;
  const int cb2 = wg % 28, ke = wg / 28;
  const int rr = lane >> 3, c8 = lane & 7;
  const rsrc_t rx = mkrs(x, 2 * D * 2);
  const u32x4 xa = ld128<0>(rx, lane < 56 ? lane * 16 : 1 << 20), xb = ld128<0>(rx, lane < 56 ? D * 2 + lane * 16 : 1 << 20);
  const u32 tag = __builtin_amdgcn_raw_buffer_load_b32(rws, EP_OFF, 0, AUX_SC1) + 1u;
  const int row0 = w < 2 ? 112 * (j >> 3) + 64 * w : 56 * ke;
  const int nrow = w == 0 ? 64 : w == 1 ? 48 : mlp ? 56 : 0;
  u32x4 wv[8];
  const rsrc_t rw = w < 2 ? mkrs(wq, D * 4096 * 2) : mkrs(w == 2 ? wg_ : wu, D * DF * 2);
  const int rs = w < 2 ? 8192 : DF * 2;
  const int vw0 = (row0 + 2 * rr) * rs + (w < 2 ? (h * 512 + (j & 7) * 64) * 2 : cb2 * 128) + c8 * 16;
#define EARLY_LOADS() \
  _Pragma("unroll") for (int i = 0; i < 8; ++i) wv[i] = ld128<AUX_NT>(rw, 16 * (i >> 1) + 2 * rr < nrow ? vw0 : 1 << 30, (16 * (i >> 1) + (i & 1)) * rs); \
  __builtin_amdgcn_sched_barrier(0)
  if (w < 2) { EARLY_LOADS(); LGKM0(); __builtin_amdgcn_s_barrier(); }
  else { __builtin_amdgcn_s_barrier(); EARLY_LOADS(); }

#pragma unroll
  for (int bb = 0; bb < 2; ++bb) {
    const u32x4 xv = bb ? xb : xa;
    float v[8];
#pragma unroll
    for (int k = 0; k < 4; ++k) { v[2 * k] = lo16(xv[k]); v[2 * k + 1] = hi16(xv[k]); }
    float s = 0.f;
#pragma unroll
    for (int k = 0; k < 8; ++k) s += v[k];
    const float mean = wave_sum(s) * (1.f / D);
    float q = 0.f;
#pragma unroll
    for (int k = 0; k < 8; ++k) { const float t = v[k] - mean; q += t * t; }
    q = lane < 56 ? q : 0.f;
    const float var = rbf(wave_sum(q) * (1.f / D));
    const float rden = __builtin_amdgcn_rcpf(rbf(__builtin_amdgcn_sqrtf(rbf(var + 1e-5f)))), mb = rbf(mean);
    u32x4 o;
#pragma unroll
    for (int k = 0; k < 4; ++k) o[k] = pkbf(rbf(v[2 * k] - mb) * rden, rbf(v[2 * k + 1] - mb) * rden);
    if (lane < 56) *(LAS u32x4*)(L + L_XN + bb * 224 + lane * 4) = o;
    else if (bb == 1) *(LAS u32x4*)(L + L_XN + 224 + lane * 4) = u32x4{0u, 0u, 0u, 0u};
  }
  const u32 tag8 = tag & 0xffu;
  if (w == 0 && lane == 0) L[L_C + C_TAG] = tag;
  LGKM0();
  TSV(1);

  {
    float a0[8] = {}, a1[8] = {};
#pragma unroll
    for (int k = 0; k < 4; ++k) {
      const u32 x0 = L[L_XN + row0 / 2 + rr + 8 * k], x1 = L[L_XN + 224 + row0 / 2 + rr + 8 * k];
#pragma unroll
      for (int c = 0; c < 4; ++c) {
        const u32 pl = __builtin_amdgcn_perm(wv[2 * k + 1][c], wv[2 * k][c], 0x05040100u), ph = __builtin_amdgcn_perm(wv[2 * k + 1][c], wv[2 * k][c], 0x07060302u);
        a0[2 * c] = dot2(pl, x0, a0[2 * c]); a0[2 * c + 1] = dot2(ph, x0, a0[2 * c + 1]);
        a1[2 * c] = dot2(pl, x1, a1[2 * c]); a1[2 * c + 1] = dot2(ph, x1, a1[2 * c + 1]);
      }
    }
    float t[8], u[4];
#pragma unroll
    for (int k = 0; k < 8; ++k) t[k] = tr32(a0[k], a1[k]);
#pragma unroll
    for (int k = 0; k < 4; ++k) { u[k] = tr16(t[k], t[k + 4]); u[k] += dpp<ROR8>(u[k]); }
    const int bb = lane >> 5, hf = (lane >> 4) & 1;
    LAS f32x4* qx = (LAS f32x4*)(LF + L_QX) + bb * 16 + c8 * 2 + hf;
    if (w == 1) {
      if (!(lane & 8)) *qx = f32x4{u[0], u[1], u[2], u[3]};
      lds_signal(L + L_C + C_QX, lane);
    } else if (w == 0) {
      lds_wait_hot(L + L_C + C_QX, 1);
      const f32x4 o = *qx;
      if (!(ABL & 1) && !(lane & 8)) st128(rws, QP_OFF + wg * 512 + bb * 256 + c8 * 32 + hf * 16, tag4(u[0] + o.x, u[1] + o.y, u[2] + o.z, u[3] + o.w, tag8));
    } else if (!(ABL & 2) && mlp && !(lane & 8)) {
      st128(rws, GP_OFF + wg * 1024 + c8 * 128 + (w - 2) * 64 + bb * 32 + hf * 16, tag4(u[0], u[1], u[2], u[3], tag8));
    }
  }
  TSV(2);

  if (w < 2) {
    if (w == 0) { asm volatile("s_waitcnt vmcnt(0)" ::: "memory"); lds_signal(L + L_C + C_ACK, lane); }
    else lds_wait(L + L_C + C_ACK, 1);
    const int qo = QP_OFF + (h + 8 * (4 * w + 8 * (lane >> 4))) * 512 + b * 256 + (lane & 15) * 16;
    u32x4 a[4];
    for (u32 s = 0; s < SPIN_MEM; ++s) {
      CBAR();
      if (ABL & 1) { a[0] = a[1] = a[2] = a[3] = xa; break; }
#pragma unroll
      for (int i = 0; i < 4; ++i) a[i] = ld128<AUX_SC1>(rws, qo, i * 8 * 512);
      if (all_ok((untag4(a[0], tag8) | untag4(a[1], tag8)) | (untag4(a[2], tag8) | untag4(a[3], tag8)))) break;
    }
#pragma unroll
    for (int i = 0; i < 2; ++i) {
      const u32x4 a0 = a[2 * i], a1 = a[2 * i + 1];
      const float v0 = tr32(asf(a0.x), asf(a1.x)), v1 = tr32(asf(a0.y), asf(a1.y)), v2 = tr32(asf(a0.z), asf(a1.z)), v3 = tr32(asf(a0.w), asf(a1.w));
      L[L_Q + (4 * w + 2 * i + (lane >> 5)) * 32 + 2 * (lane & 15) + ((lane >> 4) & 1)] = pkbf(tr16(v0, v2), tr16(v1, v3));
    }
    lds_signal(L + L_C + C_Q, lane);
  }
  TSV(3);

  if (w == 2) {
    u32 h0 = 0u, h1 = 0u;
    if (mlp) {
      const int go = GP_OFF + (cb2 + 28 * rr) * 1024 + ke * 128 + c8 * 16;
      u32x4 a;
      lds_wait(L + L_C + C_Q, 2);
      for (u32 s = 0; s < SPIN_MEM; ++s) {
        CBAR();
        if (ABL & 2) { a = xa; break; }
        a = ld128<AUX_SC1>(rws, go);
        if (all_ok(untag4(a, tag8))) break;
        __builtin_amdgcn_s_sleep(8);
      }
      float hv[4];
#pragma unroll
      for (int k = 0; k < 4; ++k) {
        float g = asf(a[k]);
        g += dpp<ROR8>(g); g = tr16(g, g); g = tr32(g, g);
        g = rbf(g);
        const float uu = dpp<ROR4>(g);
        const float z = 0.7978845608028654f * (g + 0.044715f * g * g * g);
        const float th = 1.f - 2.f / (1.f + __builtin_amdgcn_exp2f(z * 2.8853900817779268f));
        hv[k] = rbf(0.5f * g * (1.f + th)) * uu;
      }
      h0 = pkbf(hv[0], hv[1]); h1 = pkbf(hv[2], hv[3]);
    }
    if (lane < 4) { L[L_H + (2 * (lane & 1)) * 2 + (lane >> 1)] = h0; L[L_H + (2 * (lane & 1) + 1) * 2 + (lane >> 1)] = h1; }
    lds_signal(L + L_C + C_H, lane);
  }
  const int rg = lane >> 4, cl = lane & 15;
  u32x4 wa[4], wb[2];
  if (LATE == 0) lds_wait(L + L_C + C_Q, 2);
  if (LATE == 1) lds_wait(L + L_C + C_VI, 4);
  if (LATE == 2) { lds_wait(L + L_C + C_VI, 4); lds_wait_hot(L + L_C + C_V, 1); }
  if (LATE == 3) { lds_wait(L + L_C + C_VI, 4); lds_wait_hot(L + L_C + C_PUB, 1); }
  {
    const int co = (w * 128 + cl * 8) * 2;
    const bool on = co < D * 2;
    const rsrc_t ro = mkrs(wo, (ABL & 4) ? 0 : 4096 * D * 2), rd = mkrs(wd, mlp && !(ABL & 4) ? DF * D * 2 : 0);
    const int vo = on ? (h * 512 + j * 16 + 4 * rg) * (D * 2) + co : 1 << 30;
    const int vd = on ? (64 * cb2 + 8 * ke + 2 * rg) * (D * 2) + co : 1 << 30;
#pragma unroll
    for (int i = 0; i < 4; ++i) wa[i] = ld128<AUX_NT>(ro, vo, i * D * 2);
#pragma unroll
    for (int i = 0; i < 2; ++i) wb[i] = ld128<AUX_NT>(rd, vd, i * D * 2);
  }
  __builtin_amdgcn_sched_barrier(0);

  u32 pa[8], pb[8], pc[8];
#pragma unroll
  for (int k = 0; k < 4; ++k) {
    pa[2 * k] = __builtin_amdgcn_perm(wa[1][k], wa[0][k], 0x05040100u); pa[2 * k + 1] = __builtin_amdgcn_perm(wa[1][k], wa[0][k], 0x07060302u);
    pb[2 * k] = __builtin_amdgcn_perm(wa[3][k], wa[2][k], 0x05040100u); pb[2 * k + 1] = __builtin_amdgcn_perm(wa[3][k], wa[2][k], 0x07060302u);
    pc[2 * k] = __builtin_amdgcn_perm(wb[1][k], wb[0][k], 0x05040100u); pc[2 * k + 1] = __builtin_amdgcn_perm(wb[1][k], wb[0][k], 0x07060302u);
  }
  lds_wait(L + L_C + C_H, 1);
  float a0[8], a1[8];
  {
    const u32x2 hh = *(LAS u32x2*)(L + L_H + rg * 2);
#pragma unroll
    for (int k = 0; k < 8; ++k) { a0[k] = dot2(pc[k], hh.x, 0.f); a1[k] = dot2(pc[k], hh.y, 0.f); }
  }
#pragma unroll
  for (int k = 0; k < 8; ++k) asm volatile("" : "+v"(a0[k]), "+v"(a1[k]), "+v"(pa[k]), "+v"(pb[k]));
  TSV(4);
  lds_wait(L + L_C + C_VI, 4);
  lds_wait_hot(L + L_C + C_O, 1);
  {
    const u32x4 oo = *(LAS u32x4*)(L + L_O + rg * 4);
#pragma unroll
    for (int k = 0; k < 8; ++k) {
      a0[k] = dot2(pb[k], oo.y, dot2(pa[k], oo.x, a0[k]));
      a1[k] = dot2(pb[k], oo.w, dot2(pa[k], oo.z, a1[k]));
    }
    float t[8];
#pragma unroll
    for (int k = 0; k < 8; ++k) t[k] = tr32(a0[k], a1[k]);
    const int co = w * 128 + cl * 8;
    if (co < D) st128(rws, O2_OFF + wg * 3584 + (lane >> 5) * 1792 + (co + 4 * ((lane >> 4) & 1)) * 4,
                      tag4(tr16(t[0], t[4]), tr16(t[1], t[5]), tr16(t[2], t[6]), tr16(t[3], t[7]), tag8));
  }
  lds_signal(L + L_C + C_O2, lane);
  TS(5);
  TSW(0); TSW(1); TSW(2); TSW(3); TSW(4);
}
}

extern "C" void kernel_launch(void* const* d_in, const int* in_sizes, int n_in, void* d_out, int out_size, void* d_ws, size_t ws_size,
                              hipStream_t stream) {
  fused_decode<<<256, 768, 0, stream>>>((const u16*)d_in[0], (const u16*)d_in[1], (const u16*)d_in[2], (const u16*)d_in[3], (const u16*)d_in[4],
                                        (const u16*)d_in[6], (const u16*)d_in[7], (const u16*)d_in[8], (u16*)d_out, (char*)d_ws + WS_OFF);
}
```

```cpp
#include <hip/hip_runtime.h>
#include <hip/hip_bf16.h>
#include <stdint.h>

typedef unsigned int u32;
typedef unsigned short u16;
typedef unsigned long long u64;
typedef u32 u32x2 __attribute__((ext_vector_type(2)));
typedef u32 u32x4 __attribute__((ext_vector_type(4)));
typedef float f32x4 __attribute__((ext_vector_type(4)));
typedef float f32x16 __attribute__((ext_vector_type(16)));
typedef __bf16 bf16x2_t __attribute__((ext_vector_type(2)));
typedef __bf16 bf16x8_t __attribute__((ext_vector_type(8)));
#define LAS __attribute__((address_space(3)))
typedef __amdgpu_buffer_rsrc_t rsrc_t;

#define STAMP 0
#define LATE 1
#define ABL 0
#define DIAG 0
#define WS_OFF 0

namespace {
constexpr int D = 448, SK = 32768, W = 4096, DH = 256, DF = 1792;
constexpr int AUX_NT = 2, AUX_SC1 = 16;

constexpr int EP_OFF = 0;
constexpr int ST_OFF = 4096;
constexpr int QP_OFF = ST_OFF + 8192;
constexpr int GP_OFF = QP_OFF + 256 * 512;
constexpr int O1_OFF = GP_OFF + 256 * 1024;
constexpr int O2_OFF = O1_OFF + 256 * 2048;
constexpr int WS_BYTES = O2_OFF + 256 * 3584;
constexpr int TS_OFF = (WS_BYTES + 4095) / 4096 * 4096;

constexpr int L_XN = 0;
constexpr int L_QX = L_XN + 480;
constexpr int L_Q = L_QX + 128;
constexpr int L_P = L_Q + 256;
constexpr int L_OP = L_P + 512;
constexpr int L_WM = L_OP + 2048;
constexpr int L_WL = L_WM + 8;
constexpr int L_H = L_WL + 8;
constexpr int L_O = L_H + 8;
constexpr int L_R = L_O + 16;
constexpr int L_C = L_R + 128;
constexpr int C_QX = 0, C_Q = 1, C_MAX = 2, C_P = 3, C_V = 4, C_PUB = 5, C_H = 6, C_O = 7, C_TAG = 8, C_ACK = 9, C_VI = 10, C_O2 = 11, C_R = 12;
constexpr int L_END = L_C + 16;

constexpr u32 SPIN_LDS = 1u << 20, SPIN_MEM = 1u << 16;

__device__ __forceinline__ float asf(u32 v) { return __builtin_bit_cast(float, v); }
__device__ __forceinline__ u32 asu(float v) { return __builtin_bit_cast(u32, v); }
__device__ __forceinline__ float lo16(u32 v) { return asf(v << 16); }
__device__ __forceinline__ float hi16(u32 v) { return asf(v & 0xffff0000u); }
__device__ __forceinline__ u32 pkbf(float a, float b) {
  bf16x2_t r = {(__bf16)a, (__bf16)b};
  return __builtin_bit_cast(u32, r);
}
__device__ __forceinline__ float rbf(float a) { return (float)(__bf16)a; }
__device__ __forceinline__ float dot2(u32 a, u32 b, float c) {
  return __builtin_amdgcn_fdot2_f32_bf16(__builtin_bit_cast(bf16x2_t, a), __builtin_bit_cast(bf16x2_t, b), c, false);
}
template <int CTRL> __device__ __forceinline__ float dpp(float x) {
  return asf(__builtin_amdgcn_mov_dpp((int)asu(x), CTRL, 0xf, 0xf, true));
}
constexpr int XOR1 = 0xB1, XOR2 = 0x4E, HMIR = 0x141, ROR8 = 0x128, ROR4 = 0x124;
__device__ __forceinline__ float tr32(float a, float b) {
  auto r = __builtin_amdgcn_permlane32_swap(asu(a), asu(b), false, false);
  return asf(r[0]) + asf(r[1]);
}
__device__ __forceinline__ float tr16(float a, float b) {
  auto r = __builtin_amdgcn_permlane16_swap(asu(a), asu(b), false, false);
  return asf(r[0]) + asf(r[1]);
}
__device__ __forceinline__ float mx32(float a) {
  auto r = __builtin_amdgcn_permlane32_swap(asu(a), asu(a), false, false);
  return fmaxf(asf(r[0]), asf(r[1]));
}
__device__ __forceinline__ float mx16(float a) {
  auto r = __builtin_amdgcn_permlane16_swap(asu(a), asu(a), false, false);
  return fmaxf(asf(r[0]), asf(r[1]));
}
template <int CTRL> __device__ __forceinline__ float trd(float a, float b, bool bit) {
  const float keep = bit ? b : a, give = bit ? a : b;
  return keep + dpp<CTRL>(give);
}
__device__ __forceinline__ float row_sum(float v) { v += dpp<ROR8>(v); v += dpp<ROR4>(v); v += dpp<XOR2>(v); v += dpp<XOR1>(v); return v; }
__device__ __forceinline__ float row_max(float v) {
  v = fmaxf(v, dpp<ROR8>(v)); v = fmaxf(v, dpp<ROR4>(v)); v = fmaxf(v, dpp<XOR2>(v)); v = fmaxf(v, dpp<XOR1>(v)); return v;
}
__device__ __forceinline__ float wave_sum(float v) { v = row_sum(v); v = tr16(v, v); return tr32(v, v); }

__device__ __forceinline__ rsrc_t mkrs(const void* p, u32 bytes) { return __builtin_amdgcn_make_buffer_rsrc((void*)p, 0, bytes, 0x00020000); }
template <int AUX> __device__ __forceinline__ u32x4 ld128(rsrc_t r, int voff, int soff = 0) { return __builtin_amdgcn_raw_buffer_load_b128(r, voff, soff, AUX); }
__device__ __forceinline__ void st128(rsrc_t r, int voff, u32x4 v) { __builtin_amdgcn_raw_buffer_store_b128(v, r, voff, 0, AUX_SC1); }
__device__ __forceinline__ u32 tagw(float v, u32 tag8) { return (asu(v) & 0xffffff00u) | tag8; }
__device__ __forceinline__ u32x4 tag4(float a, float b, float c, float d, u32 t) { return u32x4{tagw(a, t), tagw(b, t), tagw(c, t), tagw(d, t)}; }
__device__ __forceinline__ u32 untag4(u32x4& v, u32 t) { v.x ^= t; v.y ^= t; v.z ^= t; v.w ^= t; return v.x | v.y | v.z | v.w; }
__device__ __forceinline__ bool all_ok(u32 bad) { return __builtin_amdgcn_ballot_w64((bad & 0xffu) != 0u) == 0ull; }

#define CBAR() asm volatile("" ::: "memory")
#define LGKM0() asm volatile("s_waitcnt lgkmcnt(0)" ::: "memory")
__device__ __forceinline__ u32 lds_peek(LAS u32* p) { return __builtin_amdgcn_readfirstlane(__hip_atomic_load(p, __ATOMIC_RELAXED, __HIP_MEMORY_SCOPE_WORKGROUP)); }
__device__ __forceinline__ void lds_wait(LAS u32* p, u32 want) {
  for (u32 s = 0; lds_peek(p) < want && s < SPIN_LDS; ++s) __builtin_amdgcn_s_sleep(1);
  CBAR();
}
__device__ __forceinline__ void lds_wait_hot(LAS u32* p, u32 want) {
  for (u32 s = 0; lds_peek(p) < want && s < SPIN_LDS * 4; ++s) {}
  CBAR();
}
__device__ __forceinline__ void lds_signal(LAS u32* p, int lane) {
  LGKM0();
  if (lane == 0) (void)__hip_atomic_fetch_add(p, 1u, __ATOMIC_RELAXED, __HIP_MEMORY_SCOPE_WORKGROUP);
}
__device__ __forceinline__ u32 lds_ticket(LAS u32* p, int lane) {
  LGKM0();
  u32 t = 0;
  if (lane == 0) t = __hip_atomic_fetch_add(p, 1u, __ATOMIC_RELAXED, __HIP_MEMORY_SCOPE_WORKGROUP);
  t = __builtin_amdgcn_readfirstlane(t);
  CBAR();
  return t;
}

#define TS(k)
#define TSV(k)
#define TSW(k)

__device__ __forceinline__ void final_reduce(LAS u32* L, rsrc_t rws, const u16* x, u16* out, char* ws, int wg, int w, int grp, int lane, u32 tag, u64 tent) {
  LAS float* const LF = (LAS float*)L;
  __builtin_amdgcn_s_setprio(3);
  const u32 tag8 = tag & 0xffu;
  const int rb = wg / 28, cb = wg % 28, qd = lane & 3;
  const float xres = lo16(x[rb * D + 16 * cb + (lane & 15)]);
  const int oo = O2_OFF + (grp + 8 * (lane >> 2)) * 3584 + rb * 1792 + cb * 64 + qd * 16;
  lds_wait(L + L_C + C_O, 1);
  lds_wait_hot(L + L_C + C_O2, 4);
  u32x4 a0, a1;
  for (u32 s = 0; s < SPIN_MEM; ++s) {
    CBAR();
    a0 = ld128<AUX_SC1>(rws, oo); a1 = ld128<AUX_SC1>(rws, oo, 128 * 3584);
    if (all_ok(untag4(a0, tag8) | untag4(a1, tag8))) break;
  }
  TSV(6);
  float f = tr16(tr32(asf(a0.x) + asf(a1.x), asf(a0.z) + asf(a1.z)), tr32(asf(a0.y) + asf(a1.y), asf(a0.w) + asf(a1.w)));
  f += dpp<ROR8>(f); f += dpp<ROR4>(f);
  if ((lane & 12) == 0) LF[L_R + grp * 16 + 4 * qd + (lane >> 4)] = f;
  if (lds_ticket(L + L_C + C_R, lane) == 7) {
    const LAS float* rp = LF + L_R + (lane & 15);
    const float t = ((rp[0] + rp[16]) + (rp[32] + rp[48])) + ((rp[64] + rp[80]) + (rp[96] + rp[112]));
    if (lane < 16) out[rb * D + 16 * cb + lane] = (u16)pkbf(xres + t, 0.f);
    if (wg == 0 && lane == 0) __builtin_amdgcn_raw_buffer_store_b32(tag, rws, EP_OFF, 0, AUX_SC1);
    if (wg == 0) {
      const u64 now = __builtin_amdgcn_s_memrealtime();
      u64 ev = DIAG == 0 ? tent : DIAG == 1 ? now : tent + lds_peek(L + L_C + 13);
      const u64 until = tent + 4000 + 4 * (ev - tent);
      while (__builtin_amdgcn_s_memrealtime() < until) __builtin_amdgcn_s_sleep(1);
    }
    TS(7);
  }
  TSW(6);
}

__global__ void __launch_bounds__(768, 3)
fused_decode(const u16* __restrict__ x, const u16* __restrict__ kc, const u16* __restrict__ vc, const u16* __restrict__ wq,
             const u16* __restrict__ wo, const u16* __restrict__ wg_, const u16* __restrict__ wu, const u16* __restrict__ wd,
             u16* __restrict__ out, char* __restrict__ ws) {
  __shared__ __attribute__((aligned(16))) u32 smem[L_END];
  LAS u32* const L = (LAS u32*)smem;
  LAS float* const LF = (LAS float*)smem;
  const int wg = blockIdx.x, h = wg & 7, j = wg >> 3, b = j & 1, sp = j >> 1, reg = wg & 15;
  const int w = __builtin_amdgcn_readfirstlane(threadIdx.x >> 6), lane = threadIdx.x & 63;
  TSV(0);
  const u64 tent = __builtin_amdgcn_s_memrealtime();
#define DG(k) do { if (DIAG == (k)) { const u32 dt_ = (u32)(__builtin_amdgcn_s_memrealtime() - tent); if (lane == 0) (void)__hip_atomic_fetch_max(L + L_C + 13, dt_, __ATOMIC_RELAXED, __HIP_MEMORY_SCOPE_WORKGROUP); } } while (0)
  asm volatile("" :: "s"(x), "s"(kc), "s"(vc), "s"(wq), "s"(wo), "s"(wg_), "s"(wu), "s"(wd), "s"(out), "s"(ws));
  if (threadIdx.x < 16) L[L_C + threadIdx.x] = 0u;
  const rsrc_t rws = mkrs(ws, WS_BYTES);

  if (w >= 4) {
    const bool isv = w >= 8;
    const int kw = w & 3;
    if (isv) __builtin_amdgcn_s_setprio(1); else __builtin_amdgcn_s_setprio(2);
    LGKM0();
    __builtin_amdgcn_s_barrier();
    const u32 u0 = sp * 128 + kw + reg * 37;
#define STREAM_LOADS(SRC) \
    const rsrc_t rkv = mkrs((SRC) + ((size_t)reg * SK + (SK - W)) * DH, W * DH * 2); \
    u32x4 d[32]; \
    _Pragma("unroll") for (int i = 0; i < 32; ++i) d[i] = ld128<AUX_NT>(rkv, lane * 16, (int)(((u0 + 4 * i) & 2047u) << 10)); \
    __builtin_amdgcn_sched_barrier(0); \
    TSV(1)

    if (!isv) {
      STREAM_LOADS(kc);
      lds_wait(L + L_C + C_Q, 2);
      TSV(2);
      const u32x4 q0 = *(LAS u32x4*)(L + L_Q + 4 * (lane & 31)), q1 = *(LAS u32x4*)(L + L_Q + 128 + 4 * (lane & 31));
      const bool b3 = lane & 8, b2 = lane & 4, b1 = lane & 2, b0 = lane & 1;
      float sc[2];
#pragma unroll
      for (int t = 0; t < 2; ++t) {
        float y[16];
#pragma unroll
        for (int i = 0; i < 16; ++i) {
          const u32x4 k = d[16 * t + i];
          float x0 = dot2(k.x, q0.x, 0.f), x1 = dot2(k.x, q1.x, 0.f);
          x0 = dot2(k.y, q0.y, x0); x1 = dot2(k.y, q1.y, x1);
          x0 = dot2(k.z, q0.z, x0); x1 = dot2(k.z, q1.z, x1);
          x0 = dot2(k.w, q0.w, x0); x1 = dot2(k.w, q1.w, x1);
          y[i] = tr16(x0, x1);
        }
#pragma unroll
        for (int i = 0; i < 8; ++i) y[i] = trd<ROR8>(y[2 * i], y[2 * i + 1], b3);
#pragma unroll
        for (int i = 0; i < 4; ++i) y[i] = trd<HMIR>(y[2 * i], y[2 * i + 1], b2);
#pragma unroll
        for (int i = 0; i < 2; ++i) y[i] = trd<XOR2>(y[2 * i], y[2 * i + 1], b1);
        sc[t] = rbf(trd<XOR1>(y[0], y[1], b0)) * 0.0625f;
      }
      const int head = (lane >> 4) & 1, hf = lane >> 5;
      const float wm = mx32(row_max(fmaxf(sc[0], sc[1])));
      if ((lane & 47) == 0) LF[L_WM + kw * 2 + head] = wm;
      lds_signal(L + L_C + C_MAX, lane);
      lds_wait_hot(L + L_C + C_MAX, 4);
      const float M = fmaxf(fmaxf(LF[L_WM + head], LF[L_WM + 2 + head]), fmaxf(LF[L_WM + 4 + head], LF[L_WM + 6 + head]));
      const float p0 = rbf(__builtin_amdgcn_exp2f((sc[0] - M) * 1.4426950408889634f));
      const float p1 = rbf(__builtin_amdgcn_exp2f((sc[1] - M) * 1.4426950408889634f));
      const int slot = (b3 ? 1 : 0) + (b2 ? 2 : 0) + (b1 ? 4 : 0) + (b0 ? 8 : 0);
      LAS u32* pp = L + L_P + ((kw * 2 + hf) * 2 + head) * 32 + slot;
      pp[0] = asu(p0) | (asu(p0) >> 16);
      pp[16] = asu(p1) | (asu(p1) >> 16);
      float ls = row_sum(p0 + p1);
      ls = tr32(ls, ls);
      if ((lane & 47) == 0) LF[L_WL + kw * 2 + head] = ls;
      const u32 tk = lds_ticket(L + L_C + C_P, lane);
      const u32 tag = L[L_C + C_TAG];
      const u32 tag8 = tag & 0xffu;
      if (tk == 3 && (lane & 47) == 0) {
        const float Ls = (LF[L_WL + head] + LF[L_WL + 2 + head]) + (LF[L_WL + 4 + head] + LF[L_WL + 6 + head]);
        st128(rws, ST_OFF + (((h * 2 + head) * 2 + b) * 16 + sp) * 16, u32x4{asu(M), tag, asu(Ls), tag});
      }
      DG(5);
      TSV(3);
      TSW(0); TSW(1); TSW(2); TSW(3);

      if (kw == 1) {
        __builtin_amdgcn_s_setprio(3);
        const int s = lane >> 2, qd = lane & 3;
        const int so = ST_OFF + ((h * 2 + (j >> 4)) * 32 + s) * 16;
        const int oo = O1_OFF + (h + 16 * s) * 2048 + j * 64 + qd * 16;
        lds_wait(L + L_C + C_VI, 4);
        lds_wait_hot(L + L_C + C_PUB, 1);
        u32x4 s0, s1, a0, a1;
        for (u32 sp_ = 0; sp_ < SPIN_MEM; ++sp_) {
          CBAR();
          s0 = ld128<AUX_SC1>(rws, so); s1 = ld128<AUX_SC1>(rws, so + 256);
          const bool bad = (s0.y != tag) | (s0.w != tag) | (s1.y != tag) | (s1.w != tag);
          if (__builtin_amdgcn_ballot_w64(bad) == 0ull) break;
        }
        CBAR();
        a0 = ld128<AUX_SC1>(rws, oo); a1 = ld128<AUX_SC1>(rws, oo + 8 * 2048);
        __builtin_amdgcn_sched_barrier(0);
        float wgt[2];
#pragma unroll
        for (int bb = 0; bb < 2; ++bb) {
          const float m = asf(bb ? s1.x : s0.x), l = asf(bb ? s1.z : s0.z);
          float mg = fmaxf(m, dpp<ROR8>(m)); mg = fmaxf(mg, dpp<ROR4>(mg)); mg = mx32(mx16(mg));
          const float e = __builtin_amdgcn_exp2f((m - mg) * 1.4426950408889634f);
          float lg = e * l; lg += dpp<ROR8>(lg); lg += dpp<ROR4>(lg); lg = tr16(lg, lg); lg = tr32(lg, lg);
          wgt[bb] = e * __builtin_amdgcn_rcpf(lg);
        }
        asm volatile("" : "+v"(wgt[0]), "+v"(wgt[1]));
        __builtin_amdgcn_sched_barrier(0);
        TSV(4);
        for (u32 sp_ = 0; sp_ < SPIN_MEM; ++sp_) {
          if (all_ok(untag4(a0, tag8) | untag4(a1, tag8))) break;
          CBAR();
          a0 = ld128<AUX_SC1>(rws, oo); a1 = ld128<AUX_SC1>(rws, oo + 8 * 2048);
        }
        TSV(5);
        const float v0 = tr32(asf(a0.x) * wgt[0], asf(a1.x) * wgt[1]), v1 = tr32(asf(a0.y) * wgt[0], asf(a1.y) * wgt[1]);
        const float v2 = tr32(asf(a0.z) * wgt[0], asf(a1.z) * wgt[1]), v3 = tr32(asf(a0.w) * wgt[0], asf(a1.w) * wgt[1]);
        float e0 = tr16(v0, v2), e1 = tr16(v1, v3);
        e0 += dpp<ROR8>(e0); e1 += dpp<ROR8>(e1);
        e0 += dpp<ROR4>(e0); e1 += dpp<ROR4>(e1);
        if ((lane & 12) == 0) L[L_O + qd * 4 + (lane >> 5) * 2 + ((lane >> 4) & 1)] = pkbf(e0, e1);
        lds_signal(L + L_C + C_O, lane);
        TSW(4); TSW(5);
      }
      if (wg < 56) final_reduce(L, rws, x, out, ws, wg, w, kw, lane, tag, tent);
    } else {
      STREAM_LOADS(vc);
      lds_signal(L + L_C + C_VI, lane);
      const int m = lane & 31, hf = lane >> 5;
      u32x4 msk;
#pragma unroll
      for (int r = 0; r < 4; ++r) msk[r] = (m < 16 && ((m & 7) >> 1) == r) ? ((m & 1) ? 0xffff0000u : 0x0000ffffu) : 0u;
      lds_wait(L + L_C + C_P, 4);
      TSV(2);
      const LAS u32x4* pp = (const LAS u32x4*)(L + L_P + ((kw * 2 + hf) * 2 + ((m >> 3) & 1)) * 32);
      f32x16 acc = {};
#pragma unroll
      for (int g = 0; g < 8; ++g) {
        const u32x4 p4 = pp[g];
#pragma unroll
        for (int k = 0; k < 4; ++k) {
          const u32x4 a = msk & p4[k];
          acc = __builtin_amdgcn_mfma_f32_32x32x16_bf16(__builtin_bit_cast(bf16x8_t, a), __builtin_bit_cast(bf16x8_t, d[4 * g + k]), acc, 0, 0, 0);
        }
        __builtin_amdgcn_sched_barrier(0);
      }
      DG(4);
      TSV(3);
      LAS f32x4* op = (LAS f32x4*)(LF + L_OP) + 2 * m + hf;
      op[kw * 128] = f32x4{acc[0], acc[1], acc[2], acc[3]};
      op[kw * 128 + 64] = f32x4{acc[4], acc[5], acc[6], acc[7]};
      const u32 tag = L[L_C + C_TAG], tag8 = tag & 0xffu;
      if (lds_ticket(L + L_C + C_V, lane) == 3) {
        const LAS f32x4* rp = (const LAS f32x4*)(LF + L_OP) + lane;
        const f32x4 o0 = (rp[0] + rp[128]) + (rp[256] + rp[384]);
        const f32x4 o1 = (rp[64] + rp[192]) + (rp[320] + rp[448]);
        st128(rws, O1_OFF + wg * 2048 + lane * 16, tag4(o0.x, o0.y, o0.z, o0.w, tag8));
        st128(rws, O1_OFF + wg * 2048 + 1024 + lane * 16, tag4(o1.x, o1.y, o1.z, o1.w, tag8));
        if (lane == 0) __hip_atomic_store(L + L_C + C_PUB, 1u, __ATOMIC_RELAXED, __HIP_MEMORY_SCOPE_WORKGROUP);
        TS(4);
      }
      TSW(0); TSW(1); TSW(2); TSW(3);
      if (wg < 56) final_reduce(L, rws, x, out, ws, wg, w, 4 + kw, lane, tag, tent);
    }
    return;
  }

  __builtin_amdgcn_s_setprio(3);
  const bool mlp = wg < 224;
  const int cb2 = wg % 28, ke = wg / 28;
  const int rr = lane >> 3, c8 = lane & 7;
  const rsrc_t rx = mkrs(x, 2 * D * 2);
  const u32x4 xa = ld128<0>(rx, lane < 56 ? lane * 16 : 1 << 20), xb = ld128<0>(rx, lane < 56 ? D * 2 + lane * 16 : 1 << 20);
  const u32 tag = __builtin_amdgcn_raw_buffer_load_b32(rws, EP_OFF, 0, AUX_SC1) + 1u;
  const int row0 = w < 2 ? 112 * (j >> 3) + 64 * w : 56 * ke;
  const int nrow = w == 0 ? 64 : w == 1 ? 48 : mlp ? 56 : 0;
  u32x4 wv[8];
  const rsrc_t rw = w < 2 ? mkrs(wq, D * 4096 * 2) : mkrs(w == 2 ? wg_ : wu, D * DF * 2);
  const int rs = w < 2 ? 8192 : DF * 2;
  const int vw0 = (row0 + 2 * rr) * rs + (w < 2 ? (h * 512 + (j & 7) * 64) * 2 : cb2 * 128) + c8 * 16;
#define EARLY_LOADS() \
  _Pragma("unroll") for (int i = 0; i < 8; ++i) wv[i] = ld128<AUX_NT>(rw, 16 * (i >> 1) + 2 * rr < nrow ? vw0 : 1 << 30, (16 * (i >> 1) + (i & 1)) * rs); \
  __builtin_amdgcn_sched_barrier(0)
  if (w < 2) { EARLY_LOADS(); LGKM0(); __builtin_amdgcn_s_barrier(); }
  else { __builtin_amdgcn_s_barrier(); EARLY_LOADS(); }

#pragma unroll
  for (int bb = 0; bb < 2; ++bb) {
    const u32x4 xv = bb ? xb : xa;
    float v[8];
#pragma unroll
    for (int k = 0; k < 4; ++k) { v[2 * k] = lo16(xv[k]); v[2 * k + 1] = hi16(xv[k]); }
    float s = 0.f;
#pragma unroll
    for (int k = 0; k < 8; ++k) s += v[k];
    const float mean = wave_sum(s) * (1.f / D);
    float q = 0.f;
#pragma unroll
    for (int k = 0; k < 8; ++k) { const float t = v[k] - mean; q += t * t; }
    q = lane < 56 ? q : 0.f;
    const float var = rbf(wave_sum(q) * (1.f / D));
    const float rden = __builtin_amdgcn_rcpf(rbf(__builtin_amdgcn_sqrtf(rbf(var + 1e-5f)))), mb = rbf(mean);
    u32x4 o;
#pragma unroll
    for (int k = 0; k < 4; ++k) o[k] = pkbf(rbf(v[2 * k] - mb) * rden, rbf(v[2 * k + 1] - mb) * rden);
    if (lane < 56) *(LAS u32x4*)(L + L_XN + bb * 224 + lane * 4) = o;
    else if (bb == 1) *(LAS u32x4*)(L + L_XN + 224 + lane * 4) = u32x4{0u, 0u, 0u, 0u};
  }
  const u32 tag8 = tag & 0xffu;
  if (w == 0 && lane == 0) L[L_C + C_TAG] = tag;
  LGKM0();
  if (w < 2) DG(2);
  TSV(1);

  {
    float a0[8] = {}, a1[8] = {};
#pragma unroll
    for (int k = 0; k < 4; ++k) {
      const u32 x0 = L[L_XN + row0 / 2 + rr + 8 * k], x1 = L[L_XN + 224 + row0 / 2 + rr + 8 * k];
#pragma unroll
      for (int c = 0; c < 4; ++c) {
        const u32 pl = __builtin_amdgcn_perm(wv[2 * k + 1][c], wv[2 * k][c], 0x05040100u), ph = __builtin_amdgcn_perm(wv[2 * k + 1][c], wv[2 * k][c], 0x07060302u);
        a0[2 * c] = dot2(pl, x0, a0[2 * c]); a0[2 * c + 1] = dot2(ph, x0, a0[2 * c + 1]);
        a1[2 * c] = dot2(pl, x1, a1[2 * c]); a1[2 * c + 1] = dot2(ph, x1, a1[2 * c + 1]);
      }
    }
    float t[8], u[4];
#pragma unroll
    for (int k = 0; k < 8; ++k) t[k] = tr32(a0[k], a1[k]);
#pragma unroll
    for (int k = 0; k < 4; ++k) { u[k] = tr16(t[k], t[k + 4]); u[k] += dpp<ROR8>(u[k]); }
    const int bb = lane >> 5, hf = (lane >> 4) & 1;
    LAS f32x4* qx = (LAS f32x4*)(LF + L_QX) + bb * 16 + c8 * 2 + hf;
    if (w == 1) {
      if (!(lane & 8)) *qx = f32x4{u[0], u[1], u[2], u[3]};
      lds_signal(L + L_C + C_QX, lane);
    } else if (w == 0) {
      lds_wait_hot(L + L_C + C_QX, 1);
      const f32x4 o = *qx;
      if (!(ABL & 1) && !(lane & 8)) st128(rws, QP_OFF + wg * 512 + bb * 256 + c8 * 32 + hf * 16, tag4(u[0] + o.x, u[1] + o.y, u[2] + o.z, u[3] + o.w, tag8));
    } else if (!(ABL & 2) && mlp && !(lane & 8)) {
      st128(rws, GP_OFF + wg * 1024 + c8 * 128 + (w - 2) * 64 + bb * 32 + hf * 16, tag4(u[0], u[1], u[2], u[3], tag8));
    }
  }
  TSV(2);

  if (w < 2) {
    if (w == 0) { asm volatile("s_waitcnt vmcnt(0)" ::: "memory"); lds_signal(L + L_C + C_ACK, lane); }
    else lds_wait(L + L_C + C_ACK, 1);
    const int qo = QP_OFF + (h + 8 * (4 * w + 8 * (lane >> 4))) * 512 + b * 256 + (lane & 15) * 16;
    u32x4 a[4];
    for (u32 s = 0; s < SPIN_MEM; ++s) {
      CBAR();
      if (ABL & 1) { a[0] = a[1] = a[2] = a[3] = xa; break; }
#pragma unroll
      for (int i = 0; i < 4; ++i) a[i] = ld128<AUX_SC1>(rws, qo, i * 8 * 512);
      if (all_ok((untag4(a[0], tag8) | untag4(a[1], tag8)) | (untag4(a[2], tag8) | untag4(a[3], tag8)))) break;
    }
#pragma unroll
    for (int i = 0; i < 2; ++i) {
      const u32x4 a0 = a[2 * i], a1 = a[2 * i + 1];
      const float v0 = tr32(asf(a0.x), asf(a1.x)), v1 = tr32(asf(a0.y), asf(a1.y)), v2 = tr32(asf(a0.z), asf(a1.z)), v3 = tr32(asf(a0.w), asf(a1.w));
      L[L_Q + (4 * w + 2 * i + (lane >> 5)) * 32 + 2 * (lane & 15) + ((lane >> 4) & 1)] = pkbf(tr16(v0, v2), tr16(v1, v3));
    }
    lds_signal(L + L_C + C_Q, lane);
    DG(3);
  }
  TSV(3);

  if (w == 2) {
    u32 h0 = 0u, h1 = 0u;
    if (mlp) {
      const int go = GP_OFF + (cb2 + 28 * rr) * 1024 + ke * 128 + c8 * 16;
      u32x4 a;
      lds_wait(L + L_C + C_Q, 2);
      for (u32 s = 0; s < SPIN_MEM; ++s) {
        CBAR();
        if (ABL & 2) { a = xa; break; }
        a = ld128<AUX_SC1>(rws, go);
        if (all_ok(untag4(a, tag8))) break;
        __builtin_amdgcn_s_sleep(8);
      }
      float hv[4];
#pragma unroll
      for (int k = 0; k < 4; ++k) {
        float g = asf(a[k]);
        g += dpp<ROR8>(g); g = tr16(g, g); g = tr32(g, g);
        g = rbf(g);
        const float uu = dpp<ROR4>(g);
        const float z = 0.7978845608028654f * (g + 0.044715f * g * g * g);
        const float th = 1.f - 2.f / (1.f + __builtin_amdgcn_exp2f(z * 2.8853900817779268f));
        hv[k] = rbf(0.5f * g * (1.f + th)) * uu;
      }
      h0 = pkbf(hv[0], hv[1]); h1 = pkbf(hv[2], hv[3]);
    }
    if (lane < 4) { L[L_H + (2 * (lane & 1)) * 2 + (lane >> 1)] = h0; L[L_H + (2 * (lane & 1) + 1) * 2 + (lane >> 1)] = h1; }
    lds_signal(L + L_C + C_H, lane);
  }
  const int rg = lane >> 4, cl = lane & 15;
  u32x4 wa[4], wb[2];
  if (LATE == 0) lds_wait(L + L_C + C_Q, 2);
  if (LATE == 1) lds_wait(L + L_C + C_VI, 4);
  if (LATE == 2) { lds_wait(L + L_C + C_VI, 4); lds_wait_hot(L + L_C + C_V, 1); }
  if (LATE == 3) { lds_wait(L + L_C + C_VI, 4); lds_wait_hot(L + L_C + C_PUB, 1); }
  {
    const int co = (w * 128 + cl * 8) * 2;
    const bool on = co < D * 2;
    const rsrc_t ro = mkrs(wo, (ABL & 4) ? 0 : 4096 * D * 2), rd = mkrs(wd, mlp && !(ABL & 4) ? DF * D * 2 : 0);
    const int vo = on ? (h * 512 + j * 16 + 4 * rg) * (D * 2) + co : 1 << 30;
    const int vd = on ? (64 * cb2 + 8 * ke + 2 * rg) * (D * 2) + co : 1 << 30;
#pragma unroll
    for (int i = 0; i < 4; ++i) wa[i] = ld128<AUX_NT>(ro, vo, i * D * 2);
#pragma unroll
    for (int i = 0; i < 2; ++i) wb[i] = ld128<AUX_NT>(rd, vd, i * D * 2);
  }
  __builtin_amdgcn_sched_barrier(0);

  u32 pa[8], pb[8], pc[8];
#pragma unroll
  for (int k = 0; k < 4; ++k) {
    pa[2 * k] = __builtin_amdgcn_perm(wa[1][k], wa[0][k], 0x05040100u); pa[2 * k + 1] = __builtin_amdgcn_perm(wa[1][k], wa[0][k], 0x07060302u);
    pb[2 * k] = __builtin_amdgcn_perm(wa[3][k], wa[2][k], 0x05040100u); pb[2 * k + 1] = __builtin_amdgcn_perm(wa[3][k], wa[2][k], 0x07060302u);
    pc[2 * k] = __builtin_amdgcn_perm(wb[1][k], wb[0][k], 0x05040100u); pc[2 * k + 1] = __builtin_amdgcn_perm(wb[1][k], wb[0][k], 0x07060302u);
  }
  lds_wait(L + L_C + C_H, 1);
  float a0[8], a1[8];
  {
    const u32x2 hh = *(LAS u32x2*)(L + L_H + rg * 2);
#pragma unroll
    for (int k = 0; k < 8; ++k) { a0[k] = dot2(pc[k], hh.x, 0.f); a1[k] = dot2(pc[k], hh.y, 0.f); }
  }
#pragma unroll
  for (int k = 0; k < 8; ++k) asm volatile("" : "+v"(a0[k]), "+v"(a1[k]), "+v"(pa[k]), "+v"(pb[k]));
  TSV(4);
  lds_wait(L + L_C + C_VI, 4);
  lds_wait_hot(L + L_C + C_O, 1);
  {
    const u32x4 oo = *(LAS u32x4*)(L + L_O + rg * 4);
#pragma unroll
    for (int k = 0; k < 8; ++k) {
      a0[k] = dot2(pb[k], oo.y, dot2(pa[k], oo.x, a0[k]));
      a1[k] = dot2(pb[k], oo.w, dot2(pa[k], oo.z, a1[k]));
    }
    float t[8];
#pragma unroll
    for (int k = 0; k < 8; ++k) t[k] = tr32(a0[k], a1[k]);
    const int co = w * 128 + cl * 8;
    if (co < D) st128(rws, O2_OFF + wg * 3584 + (lane >> 5) * 1792 + (co + 4 * ((lane >> 4) & 1)) * 4,
                      tag4(tr16(t[0], t[4]), tr16(t[1], t[5]), tr16(t[2], t[6]), tr16(t[3], t[7]), tag8));
  }
  lds_signal(L + L_C + C_O2, lane);
  TS(5);
  TSW(0); TSW(1); TSW(2); TSW(3); TSW(4);
}
}

extern "C" void kernel_launch(void* const* d_in, const int* in_sizes, int n_in, void* d_out, int out_size, void* d_ws, size_t ws_size,
                              hipStream_t stream) {
  fused_decode<<<256, 768, 0, stream>>>((const u16*)d_in[0], (const u16*)d_in[1], (const u16*)d_in[2], (const u16*)d_in[3], (const u16*)d_in[4],
                                        (const u16*)d_in[6], (const u16*)d_in[7], (const u16*)d_in[8], (u16*)d_out, (char*)d_ws + WS_OFF);
}
```

```cpp
#include <hip/hip_runtime.h>
#include <hip/hip_bf16.h>
#include <stdint.h>

typedef unsigned int u32;
typedef unsigned short u16;
typedef unsigned long long u64;
typedef u32 u32x2 __attribute__((ext_vector_type(2)));
typedef u32 u32x4 __attribute__((ext_vector_type(4)));
typedef float f32x4 __attribute__((ext_vector_type(4)));
typedef float f32x16 __attribute__((ext_vector_type(16)));
typedef __bf16 bf16x2_t __attribute__((ext_vector_type(2)));
typedef __bf16 bf16x8_t __attribute__((ext_vector_type(8)));
#define LAS __attribute__((address_space(3)))
typedef __amdgpu_buffer_rsrc_t rsrc_t;

#define STAMP 0
#define LATE 1
#define ABL 0
#define QDELAY 36
#define STATW 4
#define PFCODE 1
#define O1STR 2304
#define SLP1 0
#define SLP2 0
#define PRE 1
#define PREAUX 16
#define PRE0 1
#define ROT 37
#define DIAG 0
#define AMP 1
#define WS_OFF 0

namespace {
constexpr int D = 448, SK = 32768, W = 4096, DH = 256, DF = 1792;
constexpr int AUX_NT = 2, AUX_SC1 = 16;

constexpr int EP_OFF = 0;
constexpr int ST_OFF = 4096;
constexpr int QP_S = 768, GP_S = 1280, O1_S = O1STR, O2_S = 3840;
constexpr int QP_OFF = ST_OFF + 8192;
constexpr int GP_OFF = QP_OFF + 256 * QP_S;
constexpr int O1_OFF = GP_OFF + 256 * GP_S;
constexpr int O2_OFF = O1_OFF + 256 * O1_S;
constexpr int WS_BYTES = O2_OFF + 256 * O2_S;
constexpr int TS_OFF = 4 << 20;

constexpr int L_XN = 0;
constexpr int L_QX = L_XN + 480;
constexpr int L_Q = L_QX + 128;
constexpr int L_P = L_Q + 256;
constexpr int L_OP = L_P + 512;
constexpr int L_WM = L_OP + 2048;
constexpr int L_WL = L_WM + 8;
constexpr int L_H = L_WL + 8;
constexpr int L_O = L_H + 8;
constexpr int L_R = L_O + 16;
constexpr int L_C = L_R + 128;
constexpr int C_QX = 0, C_Q = 1, C_MAX = 2, C_P = 3, C_V = 4, C_PUB = 5, C_H = 6, C_O = 7, C_TAG = 8, C_ACK = 9, C_VI = 10, C_O2 = 11, C_R = 12;
constexpr int L_END = L_C + 16;

constexpr int CODE_SPAN = 15360;
constexpr u32 SPIN_LDS = 1u << 20, SPIN_MEM = 1u << 16;

__device__ __forceinline__ float asf(u32 v) { return __builtin_bit_cast(float, v); }
__device__ __forceinline__ u32 asu(float v) { return __builtin_bit_cast(u32, v); }
__device__ __forceinline__ float lo16(u32 v) { return asf(v << 16); }
__device__ __forceinline__ float hi16(u32 v) { return asf(v & 0xffff0000u); }
__device__ __forceinline__ u32 pkbf(float a, float b) {
  bf16x2_t r = {(__bf16)a, (__bf16)b};
  return __builtin_bit_cast(u32, r);
}
__device__ __forceinline__ float rbf(float a) { return (float)(__bf16)a; }
__device__ __forceinline__ float dot2(u32 a, u32 b, float c) {
  return __builtin_amdgcn_fdot2_f32_bf16(__builtin_bit_cast(bf16x2_t, a), __builtin_bit_cast(bf16x2_t, b), c, false);
}
template <int CTRL> __device__ __forceinline__ float dpp(float x) {
  return asf(__builtin_amdgcn_mov_dpp((int)asu(x), CTRL, 0xf, 0xf, true));
}
constexpr int XOR1 = 0xB1, XOR2 = 0x4E, HMIR = 0x141, ROR8 = 0x128, ROR4 = 0x124;
__device__ __forceinline__ float tr32(float a, float b) {
  auto r = __builtin_amdgcn_permlane32_swap(asu(a), asu(b), false, false);
  return asf(r[0]) + asf(r[1]);
}
__device__ __forceinline__ float tr16(float a, float b) {
  auto r = __builtin_amdgcn_permlane16_swap(asu(a), asu(b), false, false);
  return asf(r[0]) + asf(r[1]);
}
__device__ __forceinline__ float mx32(float a) {
  auto r = __builtin_amdgcn_permlane32_swap(asu(a), asu(a), false, false);
  return fmaxf(asf(r[0]), asf(r[1]));
}
__device__ __forceinline__ float mx16(float a) {
  auto r = __builtin_amdgcn_permlane16_swap(asu(a), asu(a), false, false);
  return fmaxf(asf(r[0]), asf(r[1]));
}
template <int CTRL> __device__ __forceinline__ float trd(float a, float b, bool bit) {
  const float keep = bit ? b : a, give = bit ? a : b;
  return keep + dpp<CTRL>(give);
}
__device__ __forceinline__ float row_sum(float v) { v += dpp<ROR8>(v); v += dpp<ROR4>(v); v += dpp<XOR2>(v); v += dpp<XOR1>(v); return v; }
__device__ __forceinline__ float row_max(float v) {
  v = fmaxf(v, dpp<ROR8>(v)); v = fmaxf(v, dpp<ROR4>(v)); v = fmaxf(v, dpp<XOR2>(v)); v = fmaxf(v, dpp<XOR1>(v)); return v;
}
__device__ __forceinline__ float wave_sum(float v) { v = row_sum(v); v = tr16(v, v); return tr32(v, v); }

__device__ __forceinline__ rsrc_t mkrs(const void* p, u32 bytes) { return __builtin_amdgcn_make_buffer_rsrc((void*)p, 0, bytes, 0x00020000); }
template <int AUX> __device__ __forceinline__ u32x4 ld128(rsrc_t r, int voff, int soff = 0) { return __builtin_amdgcn_raw_buffer_load_b128(r, voff, soff, AUX); }
__device__ __forceinline__ void st128(rsrc_t r, int voff, u32x4 v) { __builtin_amdgcn_raw_buffer_store_b128(v, r, voff, 0, AUX_SC1); }
__device__ __forceinline__ u32 tagw(float v, u32 tag8) { return (asu(v) & 0xffffff00u) | tag8; }
__device__ __forceinline__ u32x4 tag4(float a, float b, float c, float d, u32 t) { return u32x4{tagw(a, t), tagw(b, t), tagw(c, t), tagw(d, t)}; }
__device__ __forceinline__ u32 untag4(u32x4& v, u32 t) { v.x ^= t; v.y ^= t; v.z ^= t; v.w ^= t; return v.x | v.y | v.z | v.w; }
__device__ __forceinline__ bool all_ok(u32 bad) { return __builtin_amdgcn_ballot_w64((bad & 0xffu) != 0u) == 0ull; }

#define CBAR() asm volatile("" ::: "memory")
#define LGKM0() asm volatile("s_waitcnt lgkmcnt(0)" ::: "memory")
__device__ __forceinline__ u32 lds_peek(LAS u32* p) { return __builtin_amdgcn_readfirstlane(__hip_atomic_load(p, __ATOMIC_RELAXED, __HIP_MEMORY_SCOPE_WORKGROUP)); }
__device__ __forceinline__ void lds_wait(LAS u32* p, u32 want) {
  for (u32 s = 0; lds_peek(p) < want && s < SPIN_LDS; ++s) __builtin_amdgcn_s_sleep((ABL & 16) ? 8 : 1);
  CBAR();
}
__device__ __forceinline__ void lds_wait_hot(LAS u32* p, u32 want) {
  for (u32 s = 0; lds_peek(p) < want && s < SPIN_LDS * 4; ++s) {}
  CBAR();
}
__device__ __forceinline__ void lds_signal(LAS u32* p, int lane) {
  LGKM0();
  if (lane == 0) (void)__hip_atomic_fetch_add(p, 1u, __ATOMIC_RELAXED, __HIP_MEMORY_SCOPE_WORKGROUP);
}
__device__ __forceinline__ u32 lds_ticket(LAS u32* p, int lane) {
  LGKM0();
  u32 t = 0;
  if (lane == 0) t = __hip_atomic_fetch_add(p, 1u, __ATOMIC_RELAXED, __HIP_MEMORY_SCOPE_WORKGROUP);
  t = __builtin_amdgcn_readfirstlane(t);
  CBAR();
  return t;
}

#define MK(k)
#define TS(k)
#define TSV(k)
#define TSW(k)

__device__ __forceinline__ void final_reduce(LAS u32* L, rsrc_t rws, const u16* x, u16* out, char* ws, int wg, int w, int grp, int lane, u32 tag, u64 tent) {
  LAS float* const LF = (LAS float*)L;
  __builtin_amdgcn_s_setprio(3);
  const u32 tag8 = tag & 0xffu;
  const int rb = wg / 28, cb = wg % 28, qd = lane & 3;
  const float xres = lo16(x[rb * D + 16 * cb + (lane & 15)]);
  const int oo = O2_OFF + (grp * 32 + (lane >> 2)) * O2_S + rb * 1792 + cb * 64 + qd * 16;
  lds_wait(L + L_C + C_O, 1);
  lds_wait_hot(L + L_C + C_O2, 4);
  u32x4 a0, a1;
  for (u32 s = 0; s < SPIN_MEM; ++s) {
    CBAR();
    a0 = ld128<AUX_SC1>(rws, oo); a1 = ld128<AUX_SC1>(rws, oo, 16 * O2_S);
    if (all_ok(untag4(a0, tag8) | untag4(a1, tag8))) break;
    if (SLP2) __builtin_amdgcn_s_sleep(SLP2);
  }
  TSV(6);
  float f = tr16(tr32(asf(a0.x) + asf(a1.x), asf(a0.z) + asf(a1.z)), tr32(asf(a0.y) + asf(a1.y), asf(a0.w) + asf(a1.w)));
  f += dpp<ROR8>(f); f += dpp<ROR4>(f);
  if ((lane & 12) == 0) LF[L_R + grp * 16 + 4 * qd + (lane >> 4)] = f;
  if (lds_ticket(L + L_C + C_R, lane) == 7) {
    const LAS float* rp = LF + L_R + (lane & 15);
    const float t = ((rp[0] + rp[16]) + (rp[32] + rp[48])) + ((rp[64] + rp[80]) + (rp[96] + rp[112]));
    if (lane < 16) out[rb * D + 16 * cb + lane] = (u16)pkbf(xres + t, 0.f);
    if (wg == 0 && lane == 0) __builtin_amdgcn_raw_buffer_store_b32(tag, rws, EP_OFF, 0, AUX_SC1);
    if (wg == 0) {
      const u64 now = __builtin_amdgcn_s_memrealtime();
      u64 ev = DIAG == 0 ? tent : DIAG == 7 ? now : tent + lds_peek(L + L_C + 15);
      const u64 until = tent + 3000 + AMP * (ev - tent);
      while (__builtin_amdgcn_s_memrealtime() < until) __builtin_amdgcn_s_sleep(1);
    }
    TS(7);
  }
  TSW(6);
}

__global__ void __launch_bounds__(768, 3)
fused_decode(const u16* __restrict__ x, const u16* __restrict__ kc, const u16* __restrict__ vc, const u16* __restrict__ wq,
             const u16* __restrict__ wo, const u16* __restrict__ wg_, const u16* __restrict__ wu, const u16* __restrict__ wd,
             u16* __restrict__ out, char* __restrict__ ws) {
  __shared__ __attribute__((aligned(16))) u32 smem[L_END];
  LAS u32* const L = (LAS u32*)smem;
  LAS float* const LF = (LAS float*)smem;
  const int wg = blockIdx.x, h = wg & 7, j = wg >> 3, b = j & 1, sp = j >> 1, reg = wg & 15;
  const int w = __builtin_amdgcn_readfirstlane(threadIdx.x >> 6), lane = threadIdx.x & 63;
  const u64 tent = __builtin_amdgcn_s_memrealtime();
#undef MK
#define MK(k) do { if (DIAG == (k)) { const u32 dt_ = (u32)(__builtin_amdgcn_s_memrealtime() - tent); if (lane == 0) (void)__hip_atomic_fetch_max(L + L_C + 15, dt_, __ATOMIC_RELAXED, __HIP_MEMORY_SCOPE_WORKGROUP); } } while (0)
  u64 pc0;
  asm volatile("s_getpc_b64 %0" : "=s"(pc0));
  asm volatile("" :: "s"(x), "s"(kc), "s"(vc), "s"(wq), "s"(wo), "s"(wg_), "s"(wu), "s"(wd), "s"(out), "s"(ws));
  if (threadIdx.x < 16) L[L_C + threadIdx.x] = 0u;
  const rsrc_t rws = mkrs(ws, WS_BYTES);

  if (w >= 4) {
    const bool isv = w >= 8;
    const int kw = w & 3;
    if (isv) __builtin_amdgcn_s_setprio(1); else __builtin_amdgcn_s_setprio(2);
    const u32 code_touch = PFCODE ? __builtin_amdgcn_raw_buffer_load_b32(mkrs((const char*)(pc0 & ~255ull), w == 8 ? CODE_SPAN : 0), j * 512 + (lane & 3) * 128, 0, 0) : 0u;
    const int eo = lane < 8 ? QP_OFF + (h * 32 + j) * QP_S + lane * 64 : lane < 24 ? GP_OFF + wg * GP_S + (lane - 8) * 64 : ST_OFF + (wg & 127) * 64;
    const u32 box_touch = PRE0 ? __builtin_amdgcn_raw_buffer_load_b32(rws, (w == 9 && lane < 25) ? eo : 1 << 30, 0, PREAUX) : 0u;
    LGKM0();
    __builtin_amdgcn_s_barrier();
    if (w == 4) MK(9);
    const u32 blk0 = sp * 32 + reg * ROT;
#define STREAM_LOADS(SRC, VOFF, SOFF) \
    const rsrc_t rkv = mkrs((SRC) + ((size_t)reg * SK + (SK - W)) * DH, W * DH * 2); \
    u32x4 d[32]; \
    _Pragma("unroll") for (int i = 0; i < 32; ++i) d[i] = ld128<AUX_NT>(rkv, (VOFF), (int)(SOFF)); \
    __builtin_amdgcn_sched_barrier(0); \
    TSV(1)

    if (!isv) {
      const int g = lane >> 4, key = (lane >> 1) & 7;
      const bool hb = lane & 1;
      STREAM_LOADS(kc, key * 512 + (2 * g + (lane & 1)) * 16, (((blk0 + kw + 4 * (i >> 2)) & 511u) << 12) + (i & 3) * 128);
      lds_wait(L + L_C + C_Q, 2);
      TSV(2);
      u32x4 qa[4];
#pragma unroll
      for (int i = 0; i < 4; ++i) qa[i] = *(LAS u32x4*)(L + L_Q + (lane & 1) * 128 + i * 32 + (2 * g + ((lane >> 1) & 1)) * 4);
      f32x4 ea, eb;
#pragma unroll
      for (int n = 0; n < 4; ++n) {
        f32x4 ca = {}, cb = {};
#pragma unroll
        for (int i = 0; i < 4; ++i) {
          ca = __builtin_amdgcn_mfma_f32_16x16x32_bf16(__builtin_bit_cast(bf16x8_t, qa[i]), __builtin_bit_cast(bf16x8_t, d[8 * n + i]), ca, 0, 0, 0);
          cb = __builtin_amdgcn_mfma_f32_16x16x32_bf16(__builtin_bit_cast(bf16x8_t, qa[i]), __builtin_bit_cast(bf16x8_t, d[8 * n + 4 + i]), cb, 0, 0, 0);
        }
#pragma unroll
        for (int k = 0; k < 4; ++k) { ea[k] = (n == 0 || g == n) ? ca[k] : ea[k]; eb[k] = (n == 0 || g == n) ? cb[k] : eb[k]; }
      }
      asm volatile("" :: "v"(code_touch), "v"(box_touch));
      float sc[2];
#pragma unroll
      for (int hd = 0; hd < 2; ++hd) sc[hd] = rbf((hb ? eb[2 + hd] : ea[hd]) + dpp<XOR1>(hb ? ea[2 + hd] : eb[hd])) * 0.0625f;
      const float wm0 = mx32(mx16(row_max(sc[0]))), wm1 = mx32(mx16(row_max(sc[1])));
      if (lane == 0) { LF[L_WM + kw * 2] = wm0; LF[L_WM + kw * 2 + 1] = wm1; }
      lds_signal(L + L_C + C_MAX, lane);
      lds_wait_hot(L + L_C + C_MAX, 4);
      const f32x4 ma = *(LAS f32x4*)(LF + L_WM), mb = *(LAS f32x4*)(LF + L_WM + 4);
      const float M0 = fmaxf(fmaxf(ma.x, ma.z), fmaxf(mb.x, mb.z)), M1 = fmaxf(fmaxf(ma.y, ma.w), fmaxf(mb.y, mb.w));
      const float p0 = rbf(__builtin_amdgcn_exp2f((sc[0] - M0) * 1.4426950408889634f));
      const float p1 = rbf(__builtin_amdgcn_exp2f((sc[1] - M1) * 1.4426950408889634f));
      LAS u32* pp = L + L_P + key * 64 + kw + 4 * (2 * g + (lane & 1));
      pp[0] = asu(p0) | (asu(p0) >> 16);
      pp[32] = asu(p1) | (asu(p1) >> 16);
      float ls = tr32(p0, p1);
      ls = tr16(ls, ls);
      ls = row_sum(ls);
      if ((lane & 31) == 0) LF[L_WL + kw * 2 + (lane >> 5)] = ls;
      const u32 tk = lds_ticket(L + L_C + C_P, lane);
      const u32 tag = L[L_C + C_TAG];
      const u32 tag8 = tag & 0xffu;
      if (tk == 3 && lane < 2) {
        const float Ls = (LF[L_WL + lane] + LF[L_WL + 2 + lane]) + (LF[L_WL + 4 + lane] + LF[L_WL + 6 + lane]);
        st128(rws, ST_OFF + (((h * 2 + lane) * 2 + b) * 16 + sp) * 16, u32x4{asu(lane ? M1 : M0), tag, asu(Ls), tag});
      }
      MK(2);
      TSV(3);
      TSW(0); TSW(1); TSW(2); TSW(3);

      if (PRE && kw >= 2) {
        const int po = kw == 2 ? O1_OFF + (h * 32 + j) * O1_S : O2_OFF + (h * 32 + j) * O2_S;
        const int nl = kw == 2 ? 16 : (PRE == 2 ? 0 : 28);
        const u32 t0 = __builtin_amdgcn_raw_buffer_load_b32(rws, lane < 2 * nl ? po + lane * 64 : 1 << 30, 0, PREAUX);
        asm volatile("" :: "v"(t0));
      }
      if (kw == 1) {
        __builtin_amdgcn_s_setprio(3);
        const int s = lane >> 2, qd = lane & 3;
        const int so = ST_OFF + ((h * 2 + (j >> 4)) * 32 + s) * 16;
        const int oo = O1_OFF + (h * 32 + 2 * s) * O1_S + j * 64 + qd * 16;
        u32x4 s0, s1, a0, a1;
        lds_wait(L + L_C + C_VI, STATW);
        for (u32 sp_ = 0; sp_ < SPIN_MEM; ++sp_) {
          CBAR();
          s0 = ld128<AUX_SC1>(rws, so); s1 = ld128<AUX_SC1>(rws, so + 256);
          const bool bad = (s0.y != tag) | (s0.w != tag) | (s1.y != tag) | (s1.w != tag);
          if (__builtin_amdgcn_ballot_w64(bad) == 0ull) break;
          lds_wait(L + L_C + C_PUB, 1);
        }
        float wgt[2];
#pragma unroll
        for (int bb = 0; bb < 2; ++bb) {
          const float m = asf(bb ? s1.x : s0.x), l = asf(bb ? s1.z : s0.z);
          float mg = fmaxf(m, dpp<ROR8>(m)); mg = fmaxf(mg, dpp<ROR4>(mg)); mg = mx32(mx16(mg));
          const float e = __builtin_amdgcn_exp2f((m - mg) * 1.4426950408889634f);
          float lg = e * l; lg += dpp<ROR8>(lg); lg += dpp<ROR4>(lg); lg = tr16(lg, lg); lg = tr32(lg, lg);
          wgt[bb] = e * __builtin_amdgcn_rcpf(lg);
        }
        asm volatile("" : "+v"(wgt[0]), "+v"(wgt[1]));
        TSV(4);
        lds_wait(L + L_C + C_VI, 4);
        lds_wait_hot(L + L_C + C_PUB, 1);
        for (u32 sp_ = 0; sp_ < SPIN_MEM; ++sp_) {
          CBAR();
          a0 = ld128<AUX_SC1>(rws, oo); a1 = ld128<AUX_SC1>(rws, oo + O1_S);
          if (all_ok(untag4(a0, tag8) | untag4(a1, tag8))) break;
          if (SLP1) __builtin_amdgcn_s_sleep(SLP1);
        }
        TSV(5);
        const float v0 = tr32(asf(a0.x) * wgt[0], asf(a1.x) * wgt[1]), v1 = tr32(asf(a0.y) * wgt[0], asf(a1.y) * wgt[1]);
        const float v2 = tr32(asf(a0.z) * wgt[0], asf(a1.z) * wgt[1]), v3 = tr32(asf(a0.w) * wgt[0], asf(a1.w) * wgt[1]);
        float e0 = tr16(v0, v2), e1 = tr16(v1, v3);
        e0 += dpp<ROR8>(e0); e1 += dpp<ROR8>(e1);
        e0 += dpp<ROR4>(e0); e1 += dpp<ROR4>(e1);
        if ((lane & 12) == 0) L[L_O + qd * 4 + (lane >> 5) * 2 + ((lane >> 4) & 1)] = pkbf(e0, e1);
        lds_signal(L + L_C + C_O, lane);
        MK(4);
        TSW(4); TSW(5);
      }
      if (wg < 56) final_reduce(L, rws, x, out, ws, wg, w, kw, lane, tag, tent);
    } else {
      STREAM_LOADS(vc, lane * 16 + kw * 1024, ((blk0 + i) & 511u) << 12);
      lds_signal(L + L_C + C_VI, lane);
      const int m = lane & 31, hf = lane >> 5;
      u32x4 msk;
#pragma unroll
      for (int r = 0; r < 4; ++r) msk[r] = (m < 16 && ((m & 7) >> 1) == r) ? ((m & 1) ? 0xffff0000u : 0x0000ffffu) : 0u;
      lds_wait(L + L_C + C_P, 4);
      TSV(2);
      const LAS u32x4* pp = (const LAS u32x4*)(L + L_P + ((kw * 2 + hf) * 2 + ((m >> 3) & 1)) * 32);
      f32x16 acc = {};
#pragma unroll
      for (int g = 0; g < 8; ++g) {
        const u32x4 p4 = pp[g];
#pragma unroll
        for (int k = 0; k < 4; ++k) {
          const u32x4 a = msk & p4[k];
          acc = __builtin_amdgcn_mfma_f32_32x32x16_bf16(__builtin_bit_cast(bf16x8_t, a), __builtin_bit_cast(bf16x8_t, d[4 * g + k]), acc, 0, 0, 0);
        }
        __builtin_amdgcn_sched_barrier(0);
      }
      asm volatile("" :: "v"(code_touch), "v"(box_touch));
      TSV(3);
      LAS f32x4* op = (LAS f32x4*)(LF + L_OP) + 2 * m + hf;
      op[kw * 128] = f32x4{acc[0], acc[1], acc[2], acc[3]};
      op[kw * 128 + 64] = f32x4{acc[4], acc[5], acc[6], acc[7]};
      const u32 tag = L[L_C + C_TAG], tag8 = tag & 0xffu;
      if (lds_ticket(L + L_C + C_V, lane) == 3) {
        const LAS f32x4* rp = (const LAS f32x4*)(LF + L_OP) + lane;
        const f32x4 o0 = (rp[0] + rp[128]) + (rp[256] + rp[384]);
        const f32x4 o1 = (rp[64] + rp[192]) + (rp[320] + rp[448]);
        st128(rws, O1_OFF + (h * 32 + j) * O1_S + lane * 16, tag4(o0.x, o0.y, o0.z, o0.w, tag8));
        st128(rws, O1_OFF + (h * 32 + j) * O1_S + 1024 + lane * 16, tag4(o1.x, o1.y, o1.z, o1.w, tag8));
        if (lane == 0) __hip_atomic_store(L + L_C + C_PUB, 1u, __ATOMIC_RELAXED, __HIP_MEMORY_SCOPE_WORKGROUP);
        MK(3);
        TS(4);
      }
      TSW(0); TSW(1); TSW(2); TSW(3);
      if (wg < 56) final_reduce(L, rws, x, out, ws, wg, w, 4 + kw, lane, tag, tent);
    }
    return;
  }

  if (!(ABL & 32)) __builtin_amdgcn_s_setprio(3);
  const bool mlp = wg < 224;
  const int cb2 = wg % 28, ke = wg / 28;
  const int rr = lane >> 3, c8 = lane & 7;
  const rsrc_t rx = mkrs(x, 2 * D * 2);
  const u32x4 xa = ld128<0>(rx, lane < 56 ? lane * 16 : 1 << 20), xb = ld128<0>(rx, lane < 56 ? D * 2 + lane * 16 : 1 << 20);
  const u32 tag = __builtin_amdgcn_raw_buffer_load_b32(rws, EP_OFF, 0, AUX_SC1) + 1u;
  const int row0 = w < 2 ? 112 * (j >> 3) + 64 * w : 56 * ke;
  const int nrow = w == 0 ? 64 : w == 1 ? 48 : mlp ? 56 : 0;
  u32x4 wv[8];
  const rsrc_t rw = w < 2 ? mkrs(wq, D * 4096 * 2) : mkrs(w == 2 ? wg_ : wu, D * DF * 2);
  const int rs = w < 2 ? 8192 : DF * 2;
  const int vw0 = (row0 + 2 * rr) * rs + (w < 2 ? (h * 512 + (j & 7) * 64) * 2 : cb2 * 128) + c8 * 16;
#define EARLY_LOADS() \
  _Pragma("unroll") for (int i = 0; i < 8; ++i) wv[i] = ld128<AUX_NT>(rw, 16 * (i >> 1) + 2 * rr < nrow ? vw0 : 1 << 30, (16 * (i >> 1) + (i & 1)) * rs); \
  __builtin_amdgcn_sched_barrier(0)
  EARLY_LOADS();
  LGKM0();
  __builtin_amdgcn_s_barrier();

#pragma unroll
  for (int bb = 0; bb < 2; ++bb) {
    const u32x4 xv = bb ? xb : xa;
    float v[8];
#pragma unroll
    for (int k = 0; k < 4; ++k) { v[2 * k] = lo16(xv[k]); v[2 * k + 1] = hi16(xv[k]); }
    float s = 0.f;
#pragma unroll
    for (int k = 0; k < 8; ++k) s += v[k];
    const float mean = wave_sum(s) * (1.f / D);
    float q = 0.f;
#pragma unroll
    for (int k = 0; k < 8; ++k) { const float t = v[k] - mean; q += t * t; }
    q = lane < 56 ? q : 0.f;
    const float var = rbf(wave_sum(q) * (1.f / D));
    const float rden = __builtin_amdgcn_rcpf(rbf(__builtin_amdgcn_sqrtf(rbf(var + 1e-5f)))), mb = rbf(mean);
    u32x4 o;
#pragma unroll
    for (int k = 0; k < 4; ++k) o[k] = pkbf(rbf(v[2 * k] - mb) * rden, rbf(v[2 * k + 1] - mb) * rden);
    if (lane < 56) *(LAS u32x4*)(L + L_XN + bb * 224 + lane * 4) = o;
    else if (bb == 1) *(LAS u32x4*)(L + L_XN + 224 + lane * 4) = u32x4{0u, 0u, 0u, 0u};
  }
  const u32 tag8 = tag & 0xffu;
  if (w == 0 && lane == 0) L[L_C + C_TAG] = tag;
  LGKM0();
  if (w == 0) MK(10);
  TSV(1);

  {
    float a0[8] = {}, a1[8] = {};
#pragma unroll
    for (int k = 0; k < 4; ++k) {
      const u32 x0 = L[L_XN + row0 / 2 + rr + 8 * k], x1 = L[L_XN + 224 + row0 / 2 + rr + 8 * k];
#pragma unroll
      for (int c = 0; c < 4; ++c) {
        const u32 pl = __builtin_amdgcn_perm(wv[2 * k + 1][c], wv[2 * k][c], 0x05040100u), ph = __builtin_amdgcn_perm(wv[2 * k + 1][c], wv[2 * k][c], 0x07060302u);
        a0[2 * c] = dot2(pl, x0, a0[2 * c]); a0[2 * c + 1] = dot2(ph, x0, a0[2 * c + 1]);
        a1[2 * c] = dot2(pl, x1, a1[2 * c]); a1[2 * c + 1] = dot2(ph, x1, a1[2 * c + 1]);
      }
    }
    float t[8], u[4];
#pragma unroll
    for (int k = 0; k < 8; ++k) t[k] = tr32(a0[k], a1[k]);
#pragma unroll
    for (int k = 0; k < 4; ++k) { u[k] = tr16(t[k], t[k + 4]); u[k] += dpp<ROR8>(u[k]); }
    const int bb = lane >> 5, hf = (lane >> 4) & 1;
    LAS f32x4* qx = (LAS f32x4*)(LF + L_QX) + bb * 16 + c8 * 2 + hf;
    if (w == 1) {
      if (!(lane & 8)) *qx = f32x4{u[0], u[1], u[2], u[3]};
      lds_signal(L + L_C + C_QX, lane);
    } else if (w == 0) {
      lds_wait_hot(L + L_C + C_QX, 1);
      const f32x4 o = *qx;
      if (!(ABL & 1) && !(lane & 8)) st128(rws, QP_OFF + (h * 32 + j) * QP_S + bb * 256 + c8 * 32 + hf * 16, tag4(u[0] + o.x, u[1] + o.y, u[2] + o.z, u[3] + o.w, tag8));
    } else if (!(ABL & 2) && mlp && !(lane & 8)) {
      st128(rws, GP_OFF + wg * GP_S + c8 * 128 + (w - 2) * 64 + bb * 32 + hf * 16, tag4(u[0], u[1], u[2], u[3], tag8));
    }
  }
  TSV(2);

  if (w < 2) {
    if (QDELAY == 0 && w == 0) asm volatile("s_waitcnt vmcnt(0)" ::: "memory");
    if (w == 0) lds_signal(L + L_C + C_ACK, lane);
    else lds_wait(L + L_C + C_ACK, 1);
    if (QDELAY) __builtin_amdgcn_s_sleep(QDELAY);
    const int qo = QP_OFF + (h * 32 + 4 * w + 8 * (lane >> 4)) * QP_S + b * 256 + (lane & 15) * 16;
    u32x4 a[4];
    for (u32 s = 0; s < SPIN_MEM; ++s) {
      CBAR();
      if (ABL & 1) { a[0] = a[1] = a[2] = a[3] = xa; break; }
#pragma unroll
      for (int i = 0; i < 4; ++i) a[i] = ld128<AUX_SC1>(rws, qo, i * QP_S);
      if (all_ok((untag4(a[0], tag8) | untag4(a[1], tag8)) | (untag4(a[2], tag8) | untag4(a[3], tag8)))) break;
    }
#pragma unroll
    for (int i = 0; i < 2; ++i) {
      const u32x4 a0 = a[2 * i], a1 = a[2 * i + 1];
      const float v0 = tr32(asf(a0.x), asf(a1.x)), v1 = tr32(asf(a0.y), asf(a1.y)), v2 = tr32(asf(a0.z), asf(a1.z)), v3 = tr32(asf(a0.w), asf(a1.w));
      L[L_Q + (4 * w + 2 * i + (lane >> 5)) * 32 + 2 * (lane & 15) + ((lane >> 4) & 1)] = pkbf(tr16(v0, v2), tr16(v1, v3));
    }
    lds_signal(L + L_C + C_Q, lane);
    MK(1);
  }
  TSV(3);

  if (w == 2) {
    u32 h0 = 0u, h1 = 0u;
    if (mlp) {
      const int go = GP_OFF + (cb2 + 28 * rr) * GP_S + ke * 128 + c8 * 16;
      u32x4 a;
      lds_wait(L + L_C + C_Q, 2);
      for (u32 s = 0; s < SPIN_MEM; ++s) {
        CBAR();
        if (ABL & 2) { a = xa; break; }
        a = ld128<AUX_SC1>(rws, go);
        if (all_ok(untag4(a, tag8))) break;
        __builtin_amdgcn_s_sleep(8);
      }
      float hv[4];
#pragma unroll
      for (int k = 0; k < 4; ++k) {
        float g = asf(a[k]);
        g += dpp<ROR8>(g); g = tr16(g, g); g = tr32(g, g);
        g = rbf(g);
        const float uu = dpp<ROR4>(g);
        const float z = 0.7978845608028654f * (g + 0.044715f * g * g * g);
        const float th = 1.f - 2.f / (1.f + __builtin_amdgcn_exp2f(z * 2.8853900817779268f));
        hv[k] = rbf(0.5f * g * (1.f + th)) * uu;
      }
      h0 = pkbf(hv[0], hv[1]); h1 = pkbf(hv[2], hv[3]);
    }
    if (lane < 4) { L[L_H + (2 * (lane & 1)) * 2 + (lane >> 1)] = h0; L[L_H + (2 * (lane & 1) + 1) * 2 + (lane >> 1)] = h1; }
    lds_signal(L + L_C + C_H, lane);
  }
  const int rg = lane >> 4, cl = lane & 15;
  u32x4 wa[4], wb[2];
  if (LATE == 0) lds_wait(L + L_C + C_Q, 2);
  if (LATE == 1) lds_wait(L + L_C + C_VI, 4);
  if (LATE == 2) { lds_wait(L + L_C + C_VI, 4); lds_wait_hot(L + L_C + C_V, 1); }
  if (LATE == 3) { lds_wait(L + L_C + C_VI, 4); lds_wait_hot(L + L_C + C_PUB, 1); }
  {
    const int co = (w * 128 + cl * 8) * 2;
    const bool on = co < D * 2;
    const rsrc_t ro = mkrs(wo, (ABL & 4) ? 0 : 4096 * D * 2), rd = mkrs(wd, mlp && !(ABL & 4) ? DF * D * 2 : 0);
    const int vo = on ? (h * 512 + j * 16 + 4 * rg) * (D * 2) + co : 1 << 30;
    const int vd = on ? (64 * cb2 + 8 * ke + 2 * rg) * (D * 2) + co : 1 << 30;
#pragma unroll
    for (int i = 0; i < 4; ++i) wa[i] = ld128<AUX_NT>(ro, vo, i * D * 2);
#pragma unroll
    for (int i = 0; i < 2; ++i) wb[i] = ld128<AUX_NT>(rd, vd, i * D * 2);
  }
  __builtin_amdgcn_sched_barrier(0);

  u32 pa[8], pb[8], pc[8];
#pragma unroll
  for (int k = 0; k < 4; ++k) {
    pa[2 * k] = __builtin_amdgcn_perm(wa[1][k], wa[0][k], 0x05040100u); pa[2 * k + 1] = __builtin_amdgcn_perm(wa[1][k], wa[0][k], 0x07060302u);
    pb[2 * k] = __builtin_amdgcn_perm(wa[3][k], wa[2][k], 0x05040100u); pb[2 * k + 1] = __builtin_amdgcn_perm(wa[3][k], wa[2][k], 0x07060302u);
    pc[2 * k] = __builtin_amdgcn_perm(wb[1][k], wb[0][k], 0x05040100u); pc[2 * k + 1] = __builtin_amdgcn_perm(wb[1][k], wb[0][k], 0x07060302u);
  }
  lds_wait(L + L_C + C_H, 1);
  float a0[8], a1[8];
  {
    const u32x2 hh = *(LAS u32x2*)(L + L_H + rg * 2);
#pragma unroll
    for (int k = 0; k < 8; ++k) { a0[k] = dot2(pc[k], hh.x, 0.f); a1[k] = dot2(pc[k], hh.y, 0.f); }
  }
#pragma unroll
  for (int k = 0; k < 8; ++k) asm volatile("" : "+v"(a0[k]), "+v"(a1[k]), "+v"(pa[k]), "+v"(pb[k]));
  TSV(4);
  lds_wait(L + L_C + C_VI, 4);
  lds_wait_hot(L + L_C + C_O, 1);
  {
    const u32x4 oo = *(LAS u32x4*)(L + L_O + rg * 4);
#pragma unroll
    for (int k = 0; k < 8; ++k) {
      a0[k] = dot2(pb[k], oo.y, dot2(pa[k], oo.x, a0[k]));
      a1[k] = dot2(pb[k], oo.w, dot2(pa[k], oo.z, a1[k]));
    }
    float t[8];
#pragma unroll
    for (int k = 0; k < 8; ++k) t[k] = tr32(a0[k], a1[k]);
    const int co = w * 128 + cl * 8;
    if (co < D) st128(rws, O2_OFF + (h * 32 + j) * O2_S + (lane >> 5) * 1792 + (co + 4 * ((lane >> 4) & 1)) * 4,
                      tag4(tr16(t[0], t[4]), tr16(t[1], t[5]), tr16(t[2], t[6]), tr16(t[3], t[7]), tag8));
  }
  lds_signal(L + L_C + C_O2, lane);
  MK(5);
  TS(5);
  TSW(0); TSW(1); TSW(2); TSW(3); TSW(4);
}
}

extern "C" void kernel_launch(void* const* d_in, const int* in_sizes, int n_in, void* d_out, int out_size, void* d_ws, size_t ws_size,
                              hipStream_t stream) {
  fused_decode<<<256, 768, 0, stream>>>((const u16*)d_in[0], (const u16*)d_in[1], (const u16*)d_in[2], (const u16*)d_in[3], (const u16*)d_in[4],
                                        (const u16*)d_in[6], (const u16*)d_in[7], (const u16*)d_in[8], (u16*)d_out, (char*)d_ws + WS_OFF);
}
```

```cpp
#include <hip/hip_runtime.h>
#include <hip/hip_bf16.h>
#include <stdint.h>

typedef unsigned int u32;
typedef unsigned short u16;
typedef unsigned long long u64;
typedef u32 u32x2 __attribute__((ext_vector_type(2)));
typedef u32 u32x4 __attribute__((ext_vector_type(4)));
typedef float f32x4 __attribute__((ext_vector_type(4)));
typedef float f32x16 __attribute__((ext_vector_type(16)));
typedef __bf16 bf16x2_t __attribute__((ext_vector_type(2)));
typedef __bf16 bf16x8_t __attribute__((ext_vector_type(8)));
#define LAS __attribute__((address_space(3)))
#define CAS __attribute__((address_space(4)))
typedef __amdgpu_buffer_rsrc_t rsrc_t;

#define STAMP 0
#define LATE 1
#define QDELAY 30
#define TOUCH 0
#define PRE 1
#define ROT 37
#define CODE_SPAN 14720
#define CTW 0
#define WS_OFF 0

namespace {
constexpr int D = 448, SK = 32768, W = 4096, DH = 256, DF = 1792;
constexpr int AUX_NT = 2, AUX_SC1 = 16;
constexpr int OOB = 1 << 30;

constexpr int QP_S = 768, GP_S = 1280, O1_S = 2304, O2_S = 3840;
constexpr int EP_OFF = 0;
constexpr int ST_OFF = 4096;
constexpr int QP_OFF = ST_OFF + 8192;
constexpr int GP_OFF = QP_OFF + 256 * QP_S;
constexpr int O1_OFF = GP_OFF + 256 * GP_S;
constexpr int O2_OFF = O1_OFF + 256 * O1_S;
constexpr int WS_BYTES = O2_OFF + 256 * O2_S;
constexpr int TS_OFF = 4 << 20;

constexpr int L_XN = 0;
constexpr int L_QX = L_XN + 480;
constexpr int L_Q = L_QX + 128;
constexpr int L_P = L_Q + 256;
constexpr int L_OP = L_P + 512;
constexpr int L_WM = L_OP + 2048;
constexpr int L_WL = L_WM + 8;
constexpr int L_H = L_WL + 8;
constexpr int L_O = L_H + 16;
constexpr int L_R = L_O + 32;
constexpr int F_P = L_R + 128;
constexpr int F_VI = F_P + 4;
constexpr int F_O2 = F_VI + 4;
constexpr int F_Q = F_O2 + 4;
constexpr int F_QX = F_Q + 2, F_PUB = F_QX + 1;
constexpr int C_V = F_PUB + 1, C_R = C_V + 1, L_DG = C_R + 1;
constexpr int L_END = L_DG + 1;

constexpr u32 SPIN_LDS = 1u << 21, SPIN_MEM = 1u << 16;

__device__ __forceinline__ float asf(u32 v) { return __builtin_bit_cast(float, v); }
__device__ __forceinline__ u32 asu(float v) { return __builtin_bit_cast(u32, v); }
__device__ __forceinline__ float lo16(u32 v) { return asf(v << 16); }
__device__ __forceinline__ float hi16(u32 v) { return asf(v & 0xffff0000u); }
__device__ __forceinline__ u32 pkbf(float a, float b) {
  bf16x2_t r = {(__bf16)a, (__bf16)b};
  return __builtin_bit_cast(u32, r);
}
__device__ __forceinline__ float rbf(float a) { return (float)(__bf16)a; }
__device__ __forceinline__ float dot2(u32 a, u32 b, float c) {
  return __builtin_amdgcn_fdot2_f32_bf16(__builtin_bit_cast(bf16x2_t, a), __builtin_bit_cast(bf16x2_t, b), c, false);
}
template <int CTRL> __device__ __forceinline__ float dpp(float x) {
  return asf(__builtin_amdgcn_mov_dpp((int)asu(x), CTRL, 0xf, 0xf, true));
}
constexpr int XOR1 = 0xB1, XOR2 = 0x4E, ROR8 = 0x128, ROR4 = 0x124;
__device__ __forceinline__ float tr32(float a, float b) {
  auto r = __builtin_amdgcn_permlane32_swap(asu(a), asu(b), false, false);
  return asf(r[0]) + asf(r[1]);
}
__device__ __forceinline__ float tr16(float a, float b) {
  auto r = __builtin_amdgcn_permlane16_swap(asu(a), asu(b), false, false);
  return asf(r[0]) + asf(r[1]);
}
__device__ __forceinline__ float mx32(float a) {
  auto r = __builtin_amdgcn_permlane32_swap(asu(a), asu(a), false, false);
  return fmaxf(asf(r[0]), asf(r[1]));
}
__device__ __forceinline__ float mx16(float a) {
  auto r = __builtin_amdgcn_permlane16_swap(asu(a), asu(a), false, false);
  return fmaxf(asf(r[0]), asf(r[1]));
}
__device__ __forceinline__ float row_sum(float v) { v += dpp<ROR8>(v); v += dpp<ROR4>(v); v += dpp<XOR2>(v); v += dpp<XOR1>(v); return v; }
__device__ __forceinline__ float row_max(float v) {
  v = fmaxf(v, dpp<ROR8>(v)); v = fmaxf(v, dpp<ROR4>(v)); v = fmaxf(v, dpp<XOR2>(v)); v = fmaxf(v, dpp<XOR1>(v)); return v;
}
__device__ __forceinline__ float wave_sum(float v) { v = row_sum(v); v = tr16(v, v); return tr32(v, v); }

__device__ __forceinline__ rsrc_t mkrs(const void* p, u32 bytes) { return __builtin_amdgcn_make_buffer_rsrc((void*)p, 0, bytes, 0x00020000); }
template <int AUX> __device__ __forceinline__ u32x4 ld128(rsrc_t r, int voff, int soff = 0) { return __builtin_amdgcn_raw_buffer_load_b128(r, voff, soff, AUX); }
template <int AUX> __device__ __forceinline__ u32 ld32(rsrc_t r, int voff) { return __builtin_amdgcn_raw_buffer_load_b32(r, voff, 0, AUX); }
__device__ __forceinline__ void st128(rsrc_t r, int voff, u32x4 v) { __builtin_amdgcn_raw_buffer_store_b128(v, r, voff, 0, AUX_SC1); }
__device__ __forceinline__ u32 tagw(float v, u32 tag8) { return (asu(v) & 0xffffff00u) | tag8; }
__device__ __forceinline__ u32x4 tag4(float a, float b, float c, float d, u32 t) { return u32x4{tagw(a, t), tagw(b, t), tagw(c, t), tagw(d, t)}; }
__device__ __forceinline__ u32 untag4(u32x4& v, u32 t) { v.x ^= t; v.y ^= t; v.z ^= t; v.w ^= t; return v.x | v.y | v.z | v.w; }
__device__ __forceinline__ bool none(bool bad) { return __builtin_amdgcn_ballot_w64(bad) == 0ull; }
__device__ __forceinline__ bool all_ok(u32 bad) { return none((bad & 0xffu) != 0u); }

#define CBAR() asm volatile("" ::: "memory")
#define LGKM0() asm volatile("s_waitcnt lgkmcnt(0)" ::: "memory")
template <int N, int SLEEP> __device__ __forceinline__ void flag_wait(LAS u32* p, u32 tag, int lane) {
  u32 v, cnt;
  const u32 addr = (u32)(uintptr_t)(p + (lane & (N - 1)));
  asm volatile("s_mov_b32 %1, %4\n"
               "1:\n\t"
               "ds_read_b32 %0, %2\n\t"
               "s_waitcnt lgkmcnt(0)\n\t"
               "v_cmp_ne_u32 vcc, %3, %0\n\t"
               "s_cbranch_vccz 2f\n\t"
               "s_sleep %5\n\t"
               "s_sub_u32 %1, %1, 1\n\t"
               "s_cbranch_scc0 1b\n"
               "2:"
               : "=&v"(v), "=&s"(cnt) : "v"(addr), "s"(tag), "i"(SPIN_LDS), "i"(SLEEP) : "vcc", "scc", "memory");
}
__device__ __forceinline__ void flag_set(LAS u32* p, u32 tag) { CBAR(); *p = tag; CBAR(); }
__device__ __forceinline__ u32 ticket(LAS u32* p, int lane) {
  CBAR();
  u32 t = 0;
  if (lane == 0) t = __hip_atomic_fetch_add(p, 1u, __ATOMIC_RELAXED, __HIP_MEMORY_SCOPE_WORKGROUP);
  t = __builtin_amdgcn_readfirstlane(t);
  CBAR();
  return t;
}

#define MK(k)
#define MK0()

__device__ __forceinline__ void final_reduce(LAS u32* L, rsrc_t rws, const u16* x, u16* out, char* ws, int wg, int w, int grp, int lane, u32 tag) {
  LAS float* const LF = (LAS float*)L;
  __builtin_amdgcn_s_setprio(3);
  const u32 tag8 = tag & 0xffu;
  const int rb = wg / 28, cb = wg % 28, qd = lane & 3;
  const float xres = lo16(x[rb * D + 16 * cb + (lane & 15)]);
  const int oo = O2_OFF + (grp * 32 + (lane >> 2)) * O2_S + rb * 1792 + cb * 64 + qd * 16;
  flag_wait<1, 1>(L + F_PUB, tag, lane);
  flag_wait<4, 0>(L + F_O2, tag, lane);
  u32x4 a0, a1;
  for (u32 s = 0; s < SPIN_MEM; ++s) {
    CBAR();
    a0 = ld128<AUX_SC1>(rws, oo); a1 = ld128<AUX_SC1>(rws, oo, 16 * O2_S);
    if (all_ok(untag4(a0, tag8) | untag4(a1, tag8))) break;
  }
  MK(6);
  float f = tr16(tr32(asf(a0.x) + asf(a1.x), asf(a0.z) + asf(a1.z)), tr32(asf(a0.y) + asf(a1.y), asf(a0.w) + asf(a1.w)));
  f += dpp<ROR8>(f); f += dpp<ROR4>(f);
  if ((lane & 12) == 0) LF[L_R + grp * 16 + 4 * qd + (lane >> 4)] = f;
  if (ticket(L + C_R, lane) == 7) {
    const LAS float* rp = LF + L_R + (lane & 15);
    const float t = ((rp[0] + rp[16]) + (rp[32] + rp[48])) + ((rp[64] + rp[80]) + (rp[96] + rp[112]));
    if (lane < 16) out[rb * D + 16 * cb + lane] = (u16)pkbf(xres + t, 0.f);
    if (wg == 0 && lane == 0) __builtin_amdgcn_raw_buffer_store_b32(tag, rws, EP_OFF, 0, AUX_SC1);
    MK(7);
  }
}

__global__ void __launch_bounds__(768, 3)
fused_decode(const u16* __restrict__ x, const u16* __restrict__ kc, const u16* __restrict__ vc, const u16* __restrict__ wq,
             const u16* __restrict__ wo, const u16* __restrict__ wg_, const u16* __restrict__ wu, const u16* __restrict__ wd,
             u16* __restrict__ out, char* __restrict__ ws) {
  __shared__ __attribute__((aligned(16))) u32 smem[L_END];
  LAS u32* const L = (LAS u32*)smem;
  LAS float* const LF = (LAS float*)smem;
  const int wg = blockIdx.x, h = wg & 7, j = wg >> 3, b = j & 1, sp = j >> 1, reg = wg & 15;
  const int w = __builtin_amdgcn_readfirstlane(threadIdx.x >> 6), lane = threadIdx.x & 63, kw = w & 3;
  asm volatile("s_load_dword s94, %0, 0x0\n\ts_load_dword s95, %0, 0x40" :: "s"(__builtin_amdgcn_kernarg_segment_ptr()) : "s94", "s95");
  u64 pc0;
  asm volatile("s_getpc_b64 %0" : "=s"(pc0));
  const u32 code_touch = ld32<0>(mkrs((const char*)(pc0 & ~255ull), w == CTW ? CODE_SPAN : 0), lane < 5 ? j * 640 + lane * 128 : OOB);
  __builtin_amdgcn_sched_barrier(0);
#define ROLE_ENTRY() \
  const rsrc_t rws = mkrs(ws, WS_BYTES); \
  const u32 epoch = *(const CAS u32*)(ws + EP_OFF)
#define ROLE_TAG() const u32 tag = epoch + 1u, tag8 = tag & 0xffu
  const u32 blk0 = sp * 32 + reg * ROT;
#define STREAM_LOADS(SRC, VOFF, SOFF) \
  const rsrc_t rkv = mkrs((SRC) + ((size_t)reg * SK + (SK - W)) * DH, W * DH * 2); \
  u32x4 d[32]; \
  __builtin_amdgcn_sched_barrier(0); \
  __builtin_amdgcn_s_barrier(); \
  __builtin_amdgcn_sched_barrier(0); \
  _Pragma("unroll") for (int i = 0; i < 32; ++i) d[i] = ld128<AUX_NT>(rkv, (VOFF), (int)(SOFF)); \
  __builtin_amdgcn_sched_barrier(0)

  if (__builtin_expect(w >= 4 && w < 8, 0)) {
    __builtin_amdgcn_s_setprio(2);
    ROLE_ENTRY();
    const int g = lane >> 4, key = (lane >> 1) & 7;
    const bool hb = lane & 1;
    const int kvo = key * 512 + (2 * g + (lane & 1)) * 16;
    STREAM_LOADS(kc, kvo + (i & 3) * 128, ((blk0 + kw + 4 * (i >> 2)) & 511u) << 12);
    MK0();
    ROLE_TAG();
    if (lane < 2) L[L_WM + kw * 2 + lane] = 0u;
    if (lane == 0) L[F_P + kw] = 0u;
    if (kw == 0 && lane < 2) L[C_V + lane] = 0u;
    if (kw == 1 && lane < 32) L[L_O + lane] = 0u;
    flag_wait<2, 1>(L + F_Q, tag, lane);
    u32x4 qa[4];
#pragma unroll
    for (int i = 0; i < 4; ++i) qa[i] = *(LAS u32x4*)(L + L_Q + (lane & 1) * 128 + i * 32 + (2 * g + ((lane >> 1) & 1)) * 4);
    f32x4 ea, eb;
#pragma unroll
    for (int n = 0; n < 4; ++n) {
      f32x4 ca = {}, cb = {};
#pragma unroll
      for (int i = 0; i < 4; ++i) {
        ca = __builtin_amdgcn_mfma_f32_16x16x32_bf16(__builtin_bit_cast(bf16x8_t, qa[i]), __builtin_bit_cast(bf16x8_t, d[8 * n + i]), ca, 0, 0, 0);
        cb = __builtin_amdgcn_mfma_f32_16x16x32_bf16(__builtin_bit_cast(bf16x8_t, qa[i]), __builtin_bit_cast(bf16x8_t, d[8 * n + 4 + i]), cb, 0, 0, 0);
      }
#pragma unroll
      for (int k = 0; k < 4; ++k) { ea[k] = (n == 0 || g == n) ? ca[k] : ea[k]; eb[k] = (n == 0 || g == n) ? cb[k] : eb[k]; }
    }
    asm volatile("" :: "v"(code_touch));
    float sc[2];
#pragma unroll
    for (int hd = 0; hd < 2; ++hd) sc[hd] = rbf((hb ? eb[2 + hd] : ea[hd]) + dpp<XOR1>(hb ? ea[2 + hd] : eb[hd])) * 0.0625f;
    const float wm0 = mx32(mx16(row_max(sc[0]))), wm1 = mx32(mx16(row_max(sc[1])));
    const u32 tag16 = (tag & 0x7fffu) | 0x8000u;
    CBAR();
    if (lane < 2) L[L_WM + kw * 2 + lane] = (asu(lane ? wm1 : wm0) & 0xffff0000u) | tag16;
    u32x4 ma, mb;
    for (u32 s = 0; s < SPIN_LDS; ++s) {
      CBAR();
      ma = *(LAS u32x4*)(L + L_WM) ^ tag16; mb = *(LAS u32x4*)(L + L_WM + 4) ^ tag16;
      if (none((((ma.x | ma.y | ma.z | ma.w) | (mb.x | mb.y | mb.z | mb.w)) & 0xffffu) != 0u)) break;
    }
    const float M0 = fmaxf(fmaxf(asf(ma.x), asf(ma.z)), fmaxf(asf(mb.x), asf(mb.z)));
    const float M1 = fmaxf(fmaxf(asf(ma.y), asf(ma.w)), fmaxf(asf(mb.y), asf(mb.w)));
    const float p0 = rbf(__builtin_amdgcn_exp2f((sc[0] - M0) * 1.4426950408889634f));
    const float p1 = rbf(__builtin_amdgcn_exp2f((sc[1] - M1) * 1.4426950408889634f));
    LAS u32* pp = L + L_P + key * 64 + kw + 4 * (2 * g + (lane & 1));
    pp[0] = asu(p0) | (asu(p0) >> 16);
    pp[32] = asu(p1) | (asu(p1) >> 16);
    float ls = tr32(p0, p1);
    ls = row_sum(tr16(ls, ls));
    if ((lane & 31) == 0) LF[L_WL + kw * 2 + (lane >> 5)] = ls;
    flag_set(L + F_P + kw, tag);
    MK(2);

    if (kw == 0) {
      flag_wait<4, 0>(L + F_P, tag, lane);
      if (lane < 2) {
        const float Ls = (LF[L_WL + lane] + LF[L_WL + 2 + lane]) + (LF[L_WL + 4 + lane] + LF[L_WL + 6 + lane]);
        st128(rws, ST_OFF + (((h * 2 + lane) * 2 + b) * 16 + sp) * 16, u32x4{asu(lane ? M1 : M0), tag, asu(Ls), tag});
      }
    } else if (kw == 1) {
      __builtin_amdgcn_s_setprio(3);
      const int s = lane >> 2, qd = lane & 3;
      const int so = ST_OFF + ((h * 2 + (j >> 4)) * 32 + s) * 16;
      const int oo = O1_OFF + (h * 32 + 2 * s) * O1_S + j * 64 + qd * 16;
      u32x4 s0, s1, a0, a1;
      flag_wait<4, 1>(L + F_VI, tag, lane);
      for (u32 it = 0; it < SPIN_MEM; ++it) {
        CBAR();
        s0 = ld128<AUX_SC1>(rws, so); s1 = ld128<AUX_SC1>(rws, so + 256);
        if (none((s0.y != tag) | (s0.w != tag) | (s1.y != tag) | (s1.w != tag))) break;
        flag_wait<1, 1>(L + F_PUB, tag, lane);
      }
      float wgt[2];
#pragma unroll
      for (int bb = 0; bb < 2; ++bb) {
        const float m = asf(bb ? s1.x : s0.x), l = asf(bb ? s1.z : s0.z);
        float mg = fmaxf(m, dpp<ROR8>(m)); mg = fmaxf(mg, dpp<ROR4>(mg)); mg = mx32(mx16(mg));
        const float e = __builtin_amdgcn_exp2f((m - mg) * 1.4426950408889634f);
        float lg = e * l; lg += dpp<ROR8>(lg); lg += dpp<ROR4>(lg); lg = tr16(lg, lg); lg = tr32(lg, lg);
        wgt[bb] = e * __builtin_amdgcn_rcpf(lg);
      }
      asm volatile("" : "+v"(wgt[0]), "+v"(wgt[1]));
      flag_wait<1, 0>(L + F_PUB, tag, lane);
      for (u32 it = 0; it < SPIN_MEM; ++it) {
        CBAR();
        a0 = ld128<AUX_SC1>(rws, oo); a1 = ld128<AUX_SC1>(rws, oo + O1_S);
        if (all_ok(untag4(a0, tag8) | untag4(a1, tag8))) break;
      }
      const float v0 = tr32(asf(a0.x) * wgt[0], asf(a1.x) * wgt[1]), v1 = tr32(asf(a0.y) * wgt[0], asf(a1.y) * wgt[1]);
      const float v2 = tr32(asf(a0.z) * wgt[0], asf(a1.z) * wgt[1]), v3 = tr32(asf(a0.w) * wgt[0], asf(a1.w) * wgt[1]);
      float e0 = tr16(v0, v2), e1 = tr16(v1, v3);
      e0 += dpp<ROR8>(e0); e1 += dpp<ROR8>(e1);
      e0 += dpp<ROR4>(e0); e1 += dpp<ROR4>(e1);
      if ((lane & 12) == 0) *(LAS u32x2*)(L + L_O + qd * 8 + (lane >> 4) * 2) = u32x2{pkbf(e0, e1), tag};
      CBAR();
      MK(4);
    } else if (PRE) {
      const int po = kw == 2 ? O1_OFF + (h * 32 + j) * O1_S : O2_OFF + (h * 32 + j) * O2_S;
      const u32 t0 = ld32<AUX_SC1>(rws, lane < (kw == 2 ? 32 : PRE == 2 ? 0 : 56) ? po + lane * 64 : OOB);
      asm volatile("" :: "v"(t0));
    }
    if (wg < 56) final_reduce(L, rws, x, out, ws, wg, w, kw, lane, tag);
    return;
  }

  if (__builtin_expect(w >= 8, 0)) {
    __builtin_amdgcn_s_setprio(1);
    ROLE_ENTRY();
    const int eo = lane < 8 ? QP_OFF + (h * 32 + j) * QP_S + lane * 64 : lane < 24 ? GP_OFF + wg * GP_S + (lane - 8) * 64 : ST_OFF + (wg & 127) * 64;
    const u32 box_touch = ld32<AUX_SC1>(rws, (w == 9 && lane < 25) ? eo : OOB);
    u32 page_touch = 0u;
    if (TOUCH && w == 10 && lane < 5) {
      const u16* tp = lane == 0 ? vc + ((size_t)reg * SK + (SK - W) + ((blk0 & 511u) << 3)) * DH
                        : lane == 1 ? wo + (h * 512 + j * 16) * D : lane == 2 ? wd + (wg < 224 ? wg * 8 * D : 0)
                        : lane == 3 ? out : (const u16*)(ws + O2_OFF + (h * 32 + j) * O2_S);
      page_touch = __builtin_nontemporal_load(tp);
    }
    if (lane == 0) { L[F_VI + kw] = 0u; L[F_PUB] = 0u; }
    STREAM_LOADS(vc, lane * 16 + kw * 1024, ((blk0 + i) & 511u) << 12);
    ROLE_TAG();
    flag_set(L + F_VI + kw, tag);
    MK0();
    const int m = lane & 31, hf = lane >> 5;
    u32x4 msk;
#pragma unroll
    for (int r = 0; r < 4; ++r) msk[r] = (m < 16 && ((m & 7) >> 1) == r) ? ((m & 1) ? 0xffff0000u : 0x0000ffffu) : 0u;
    flag_wait<4, 1>(L + F_P, tag, lane);
    const LAS u32x4* pp = (const LAS u32x4*)(L + L_P + ((kw * 2 + hf) * 2 + ((m >> 3) & 1)) * 32);
    f32x16 acc = {};
#pragma unroll
    for (int g = 0; g < 8; ++g) {
      const u32x4 p4 = pp[g];
#pragma unroll
      for (int k = 0; k < 4; ++k) {
        const u32x4 a = msk & p4[k];
        acc = __builtin_amdgcn_mfma_f32_32x32x16_bf16(__builtin_bit_cast(bf16x8_t, a), __builtin_bit_cast(bf16x8_t, d[4 * g + k]), acc, 0, 0, 0);
      }
      __builtin_amdgcn_sched_barrier(0);
    }
    asm volatile("" :: "v"(code_touch), "v"(box_touch), "v"(page_touch));
    LAS f32x4* op = (LAS f32x4*)(LF + L_OP) + 2 * m + hf;
    op[kw * 128] = f32x4{acc[0], acc[1], acc[2], acc[3]};
    op[kw * 128 + 64] = f32x4{acc[4], acc[5], acc[6], acc[7]};
    if (ticket(L + C_V, lane) == 3) {
      const LAS f32x4* rp = (const LAS f32x4*)(LF + L_OP) + lane;
      const f32x4 o0 = (rp[0] + rp[128]) + (rp[256] + rp[384]);
      const f32x4 o1 = (rp[64] + rp[192]) + (rp[320] + rp[448]);
      st128(rws, O1_OFF + (h * 32 + j) * O1_S + lane * 16, tag4(o0.x, o0.y, o0.z, o0.w, tag8));
      st128(rws, O1_OFF + (h * 32 + j) * O1_S + 1024 + lane * 16, tag4(o1.x, o1.y, o1.z, o1.w, tag8));
      flag_set(L + F_PUB, tag);
      MK(3);
    }
    if (wg < 56) final_reduce(L, rws, x, out, ws, wg, w, 4 + kw, lane, tag);
    return;
  }

  __builtin_amdgcn_s_setprio(3);
  ROLE_ENTRY();
  const bool mlp = wg < 224;
  const int cb2 = wg % 28, ke = wg / 28;
  const int rr = lane >> 3, c8 = lane & 7;
  const rsrc_t rx = mkrs((const u16*)x, 2 * D * 2);
  const u32x4 xa = ld128<0>(rx, lane < 56 ? lane * 16 : OOB), xb = ld128<0>(rx, lane < 56 ? D * 2 + lane * 16 : OOB);
  const int row0 = kw < 2 ? 112 * (j >> 3) + 64 * kw : 56 * ke;
  const int nrow = kw == 0 ? 64 : kw == 1 ? 48 : mlp ? 56 : 0;
  u32x4 wv[8];
  {
    const rsrc_t rw = mkrs(kw < 2 ? wq : kw == 2 ? wg_ : wu, kw < 2 ? D * 4096 * 2 : D * DF * 2);
    const int rs = kw < 2 ? 8192 : DF * 2;
    const int vw0 = nrow ? (row0 + 2 * rr) * rs + (kw < 2 ? (h * 512 + (j & 7) * 64) * 2 : cb2 * 128) + c8 * 16 : OOB;
    const int vw3 = 48 + 2 * rr < nrow ? vw0 : OOB;
#pragma unroll
    for (int i = 0; i < 8; ++i) wv[i] = ld128<AUX_NT>(rw, i < 6 ? vw0 : vw3, (16 * (i >> 1) + (i & 1)) * rs);
  }
  __builtin_amdgcn_sched_barrier(0);
  __builtin_amdgcn_s_barrier();
  __builtin_amdgcn_sched_barrier(0);
  MK0();
  ROLE_TAG();
  if (lane == 0) { L[F_O2 + kw] = 0u; if (kw < 2) L[F_Q + kw] = 0u; if (kw == 1) L[F_QX] = 0u; }
  if (kw == 2 && lane < 16) L[L_H + lane] = 0u;

#pragma unroll
  for (int bb = 0; bb < 2; ++bb) {
    const u32x4 xv = bb ? xb : xa;
    float v[8];
#pragma unroll
    for (int k = 0; k < 4; ++k) { v[2 * k] = lo16(xv[k]); v[2 * k + 1] = hi16(xv[k]); }
    float s = 0.f;
#pragma unroll
    for (int k = 0; k < 8; ++k) s += v[k];
    const float mean = wave_sum(s) * (1.f / D);
    float q = 0.f;
#pragma unroll
    for (int k = 0; k < 8; ++k) { const float t = v[k] - mean; q += t * t; }
    q = lane < 56 ? q : 0.f;
    const float var = rbf(wave_sum(q) * (1.f / D));
    const float rden = __builtin_amdgcn_rcpf(rbf(__builtin_amdgcn_sqrtf(rbf(var + 1e-5f)))), mb = rbf(mean);
    u32x4 o;
#pragma unroll
    for (int k = 0; k < 4; ++k) o[k] = pkbf(rbf(v[2 * k] - mb) * rden, rbf(v[2 * k + 1] - mb) * rden);
    if (lane < 56) *(LAS u32x4*)(L + L_XN + bb * 224 + lane * 4) = o;
    else if (bb == 1) *(LAS u32x4*)(L + L_XN + 224 + lane * 4) = u32x4{0u, 0u, 0u, 0u};
  }
  asm volatile("" :: "v"(code_touch));
  MK(10);

  {
    float a0[8] = {}, a1[8] = {};
#pragma unroll
    for (int k = 0; k < 4; ++k) {
      const u32 x0 = L[L_XN + row0 / 2 + rr + 8 * k], x1 = L[L_XN + 224 + row0 / 2 + rr + 8 * k];
#pragma unroll
      for (int c = 0; c < 4; ++c) {
        const u32 pl = __builtin_amdgcn_perm(wv[2 * k + 1][c], wv[2 * k][c], 0x05040100u), ph = __builtin_amdgcn_perm(wv[2 * k + 1][c], wv[2 * k][c], 0x07060302u);
        a0[2 * c] = dot2(pl, x0, a0[2 * c]); a0[2 * c + 1] = dot2(ph, x0, a0[2 * c + 1]);
        a1[2 * c] = dot2(pl, x1, a1[2 * c]); a1[2 * c + 1] = dot2(ph, x1, a1[2 * c + 1]);
      }
    }
    float t[8], u[4];
#pragma unroll
    for (int k = 0; k < 8; ++k) t[k] = tr32(a0[k], a1[k]);
#pragma unroll
    for (int k = 0; k < 4; ++k) { u[k] = tr16(t[k], t[k + 4]); u[k] += dpp<ROR8>(u[k]); }
    const int bb = lane >> 5, hf = (lane >> 4) & 1;
    LAS f32x4* qx = (LAS f32x4*)(LF + L_QX) + bb * 16 + c8 * 2 + hf;
    if (kw == 1) {
      if (!(lane & 8)) *qx = f32x4{u[0], u[1], u[2], u[3]};
      flag_set(L + F_QX, tag);
    } else if (kw == 0) {
      flag_wait<1, 0>(L + F_QX, tag, lane);
      const f32x4 o = *qx;
      if (!(lane & 8)) st128(rws, QP_OFF + (h * 32 + j) * QP_S + bb * 256 + c8 * 32 + hf * 16, tag4(u[0] + o.x, u[1] + o.y, u[2] + o.z, u[3] + o.w, tag8));
    } else if (mlp && !(lane & 8)) {
      st128(rws, GP_OFF + wg * GP_S + c8 * 128 + (kw - 2) * 64 + bb * 32 + hf * 16, tag4(u[0], u[1], u[2], u[3], tag8));
    }
  }

  if (kw < 2) {
    __builtin_amdgcn_s_sleep(QDELAY);
    const int qo = QP_OFF + (h * 32 + 4 * kw + 8 * (lane >> 4)) * QP_S + b * 256 + (lane & 15) * 16;
    u32x4 a[4];
    for (u32 s = 0; s < SPIN_MEM; ++s) {
      CBAR();
#pragma unroll
      for (int i = 0; i < 4; ++i) a[i] = ld128<AUX_SC1>(rws, qo, i * QP_S);
      if (all_ok((untag4(a[0], tag8) | untag4(a[1], tag8)) | (untag4(a[2], tag8) | untag4(a[3], tag8)))) break;
    }
#pragma unroll
    for (int i = 0; i < 2; ++i) {
      const u32x4 a0 = a[2 * i], a1 = a[2 * i + 1];
      const float v0 = tr32(asf(a0.x), asf(a1.x)), v1 = tr32(asf(a0.y), asf(a1.y)), v2 = tr32(asf(a0.z), asf(a1.z)), v3 = tr32(asf(a0.w), asf(a1.w));
      L[L_Q + (4 * kw + 2 * i + (lane >> 5)) * 32 + 2 * (lane & 15) + ((lane >> 4) & 1)] = pkbf(tr16(v0, v2), tr16(v1, v3));
    }
    flag_set(L + F_Q + kw, tag);
    MK(1);
  } else if (kw == 2) {
    u32 h0 = 0u, h1 = 0u;
    if (mlp) {
      const int go = GP_OFF + (cb2 + 28 * rr) * GP_S + ke * 128 + c8 * 16;
      u32x4 a;
      flag_wait<2, 1>(L + F_Q, tag, lane);
      for (u32 s = 0; s < SPIN_MEM; ++s) {
        CBAR();
        a = ld128<AUX_SC1>(rws, go);
        if (all_ok(untag4(a, tag8))) break;
        __builtin_amdgcn_s_sleep(16);
      }
      float hv[4];
#pragma unroll
      for (int k = 0; k < 4; ++k) {
        float g = asf(a[k]);
        g += dpp<ROR8>(g); g = tr16(g, g); g = tr32(g, g);
        g = rbf(g);
        const float uu = dpp<ROR4>(g);
        const float z = 0.7978845608028654f * (g + 0.044715f * g * g * g);
        const float th = 1.f - 2.f * __builtin_amdgcn_rcpf(1.f + __builtin_amdgcn_exp2f(z * 2.8853900817779268f));
        hv[k] = rbf(0.5f * g * (1.f + th)) * uu;
      }
      h0 = pkbf(hv[0], hv[1]); h1 = pkbf(hv[2], hv[3]);
    }
    if (lane < 4) {
      *(LAS u32x2*)(L + L_H + (2 * (lane & 1)) * 4 + (lane >> 1) * 2) = u32x2{h0, tag};
      *(LAS u32x2*)(L + L_H + (2 * (lane & 1) + 1) * 4 + (lane >> 1) * 2) = u32x2{h1, tag};
    }
    CBAR();
  }

  const int rg = lane >> 4, cl = lane & 15;
  const int co = kw * 128 + cl * 8;
  u32x4 wa[4], wb[2];
  if (LATE == 0) flag_wait<2, 1>(L + F_Q, tag, lane);
  if (LATE >= 1) flag_wait<4, 1>(L + F_VI, tag, lane);
  if (LATE == 3) flag_wait<1, 0>(L + F_PUB, tag, lane);
  {
    const rsrc_t ro = mkrs(wo, 4096 * D * 2), rd = mkrs(wd, mlp ? DF * D * 2 : 0);
    const int vo = co < D ? (h * 512 + j * 16 + 4 * rg) * (D * 2) + co * 2 : OOB;
    const int vd = co < D ? (64 * cb2 + 8 * ke + 2 * rg) * (D * 2) + co * 2 : OOB;
#pragma unroll
    for (int i = 0; i < 2; ++i) wb[i] = ld128<AUX_NT>(rd, vd, i * D * 2);
#pragma unroll
    for (int i = 0; i < 4; ++i) wa[i] = ld128<AUX_NT>(ro, vo, i * D * 2);
  }
  __builtin_amdgcn_sched_barrier(0);
  float a0[8], a1[8];
  u32 pa[8], pb[8];
  {
    u32x4 hh;
    for (u32 s = 0; s < SPIN_LDS; ++s) {
      CBAR();
      hh = *(LAS u32x4*)(L + L_H + rg * 4);
      if (none((hh.y != tag) | (hh.w != tag))) break;
      __builtin_amdgcn_s_sleep(1);
    }
#pragma unroll
    for (int k = 0; k < 4; ++k) {
      const u32 pl = __builtin_amdgcn_perm(wb[1][k], wb[0][k], 0x05040100u), ph = __builtin_amdgcn_perm(wb[1][k], wb[0][k], 0x07060302u);
      a0[2 * k] = dot2(pl, hh.x, 0.f); a0[2 * k + 1] = dot2(ph, hh.x, 0.f);
      a1[2 * k] = dot2(pl, hh.z, 0.f); a1[2 * k + 1] = dot2(ph, hh.z, 0.f);
    }
#pragma unroll
    for (int k = 0; k < 4; ++k) {
      pa[2 * k] = __builtin_amdgcn_perm(wa[1][k], wa[0][k], 0x05040100u); pa[2 * k + 1] = __builtin_amdgcn_perm(wa[1][k], wa[0][k], 0x07060302u);
      pb[2 * k] = __builtin_amdgcn_perm(wa[3][k], wa[2][k], 0x05040100u); pb[2 * k + 1] = __builtin_amdgcn_perm(wa[3][k], wa[2][k], 0x07060302u);
    }
  }
#pragma unroll
  for (int k = 0; k < 8; ++k) asm volatile("" : "+v"(a0[k]), "+v"(a1[k]), "+v"(pa[k]), "+v"(pb[k]));
  const int o2o = co < D ? O2_OFF + (h * 32 + j) * O2_S + (lane >> 5) * 1792 + (co + 4 * ((lane >> 4) & 1)) * 4 : OOB;
  if (LATE == 0) flag_wait<4, 1>(L + F_VI, tag, lane);
  {
    u32x4 oa, ob;
    for (u32 s = 0; s < SPIN_LDS; ++s) {
      CBAR();
      oa = *(LAS u32x4*)(L + L_O + rg * 8); ob = *(LAS u32x4*)(L + L_O + rg * 8 + 4);
      if (none(((oa.y != tag) | (oa.w != tag)) | ((ob.y != tag) | (ob.w != tag)))) break;
    }
#pragma unroll
    for (int k = 0; k < 8; ++k) {
      a0[k] = dot2(pb[k], oa.z, dot2(pa[k], oa.x, a0[k]));
      a1[k] = dot2(pb[k], ob.z, dot2(pa[k], ob.x, a1[k]));
    }
    float t[8];
#pragma unroll
    for (int k = 0; k < 8; ++k) t[k] = tr32(a0[k], a1[k]);
    st128(rws, o2o, tag4(tr16(t[0], t[4]), tr16(t[1], t[5]), tr16(t[2], t[6]), tr16(t[3], t[7]), tag8));
  }
  flag_set(L + F_O2 + kw, tag);
  MK(5);
}
}

extern "C" void kernel_launch(void* const* d_in, const int* in_sizes, int n_in, void* d_out, int out_size, void* d_ws, size_t ws_size,
                              hipStream_t stream) {
  fused_decode<<<256, 768, 0, stream>>>((const u16*)d_in[0], (const u16*)d_in[1], (const u16*)d_in[2], (const u16*)d_in[3], (const u16*)d_in[4],
                                        (const u16*)d_in[6], (const u16*)d_in[7], (const u16*)d_in[8], (u16*)d_out, (char*)d_ws + WS_OFF);
}
```

```cpp
#include <hip/hip_runtime.h>
#include <hip/hip_bf16.h>
#include <stdint.h>

typedef unsigned int u32;
typedef unsigned short u16;
typedef unsigned long long u64;
typedef u32 u32x2 __attribute__((ext_vector_type(2)));
typedef u32 u32x4 __attribute__((ext_vector_type(4)));
typedef float f32x4 __attribute__((ext_vector_type(4)));
typedef float f32x16 __attribute__((ext_vector_type(16)));
typedef __bf16 bf16x2_t __attribute__((ext_vector_type(2)));
typedef __bf16 bf16x8_t __attribute__((ext_vector_type(8)));
#define LAS __attribute__((address_space(3)))
#define CAS __attribute__((address_space(4)))
typedef __amdgpu_buffer_rsrc_t rsrc_t;

#define STAMP 0
#define LATE 4
#define QDELAY 40
#define PRE 2
#define PSTR 64
#define ABL 0
#define SLP1 0
#define SLP2 0
#define PSTAG 5
#define DBL 0
#define PA 55
#define PD 625
#define PK0 2
#define D1 8
#define D2 8
#define PV 0
#define LD 0
#define ROT 37
#define CODE_SPAN 13440
#define CTW 0
#define WS_OFF 0

namespace {
constexpr int D = 448, SK = 32768, W = 4096, DH = 256, DF = 1792;
constexpr int AUX_NT = 2, AUX_SC1 = 16;
constexpr int OOB = 1 << 30;
constexpr u32 TMASK = 0xfu;

constexpr int QP_S = 768, GP_S = 1280, O1_S = 2304, O2_S = 3840;
constexpr int EP_OFF = 0;
constexpr int ST_OFF = 4096;
constexpr int QP_OFF = ST_OFF + 8192;
constexpr int GP_OFF = QP_OFF + 256 * QP_S;
constexpr int O1_OFF = GP_OFF + 256 * GP_S;
constexpr int O2_OFF = O1_OFF + 256 * O1_S;
constexpr int WS_BYTES = O2_OFF + 256 * O2_S;
constexpr int TS_OFF = 4 << 20;

constexpr int L_XN = 0;
constexpr int L_QX = L_XN + 480;
constexpr int L_Q = L_QX + 128;
constexpr int L_P = L_Q + 256;
constexpr int L_OP = L_P + 512;
constexpr int L_WM = L_OP + 2048;
constexpr int L_WL = L_WM + 8;
constexpr int L_H = L_WL + 8;
constexpr int L_O = L_H + 16;
constexpr int L_R = L_O + 32;
constexpr int F_P = L_R + 128;
constexpr int F_VI = F_P + 4;
constexpr int F_O2 = F_VI + 4;
constexpr int F_Q = F_O2 + 4;
constexpr int F_QX = F_Q + 2, F_PUB = F_QX + 1;
constexpr int F_V = F_PUB + 1;
constexpr int F_R = F_V + 4;
constexpr int L_DG = F_R + 8;
constexpr int L_END = L_DG + 4;

constexpr u32 SPIN_LDS = 1u << 21, SPIN_MEM = 1u << 16;

__device__ __forceinline__ float asf(u32 v) { return __builtin_bit_cast(float, v); }
__device__ __forceinline__ u32 asu(float v) { return __builtin_bit_cast(u32, v); }
__device__ __forceinline__ float lo16(u32 v) { return asf(v << 16); }
__device__ __forceinline__ float hi16(u32 v) { return asf(v & 0xffff0000u); }
__device__ __forceinline__ u32 pkbf(float a, float b) {
  bf16x2_t r = {(__bf16)a, (__bf16)b};
  return __builtin_bit_cast(u32, r);
}
__device__ __forceinline__ float rbf(float a) { return (float)(__bf16)a; }
__device__ __forceinline__ float dot2(u32 a, u32 b, float c) {
  return __builtin_amdgcn_fdot2_f32_bf16(__builtin_bit_cast(bf16x2_t, a), __builtin_bit_cast(bf16x2_t, b), c, false);
}
template <int CTRL> __device__ __forceinline__ float dpp(float x) {
  return asf(__builtin_amdgcn_mov_dpp((int)asu(x), CTRL, 0xf, 0xf, true));
}
constexpr int XOR1 = 0xB1, XOR2 = 0x4E, ROR8 = 0x128, ROR4 = 0x124;
__device__ __forceinline__ float tr32(float a, float b) {
  auto r = __builtin_amdgcn_permlane32_swap(asu(a), asu(b), false, false);
  return asf(r[0]) + asf(r[1]);
}
__device__ __forceinline__ float tr16(float a, float b) {
  auto r = __builtin_amdgcn_permlane16_swap(asu(a), asu(b), false, false);
  return asf(r[0]) + asf(r[1]);
}
__device__ __forceinline__ float mx32(float a) {
  auto r = __builtin_amdgcn_permlane32_swap(asu(a), asu(a), false, false);
  return fmaxf(asf(r[0]), asf(r[1]));
}
__device__ __forceinline__ float mx16(float a) {
  auto r = __builtin_amdgcn_permlane16_swap(asu(a), asu(a), false, false);
  return fmaxf(asf(r[0]), asf(r[1]));
}
__device__ __forceinline__ float row_sum(float v) { v += dpp<ROR8>(v); v += dpp<ROR4>(v); v += dpp<XOR2>(v); v += dpp<XOR1>(v); return v; }
__device__ __forceinline__ float row_max(float v) {
  v = fmaxf(v, dpp<ROR8>(v)); v = fmaxf(v, dpp<ROR4>(v)); v = fmaxf(v, dpp<XOR2>(v)); v = fmaxf(v, dpp<XOR1>(v)); return v;
}
__device__ __forceinline__ float wave_sum(float v) { v = row_sum(v); v = tr16(v, v); return tr32(v, v); }

__device__ __forceinline__ rsrc_t mkrs(const void* p, u32 bytes) { return __builtin_amdgcn_make_buffer_rsrc((void*)p, 0, bytes, 0x00020000); }
template <int AUX> __device__ __forceinline__ u32x4 ld128(rsrc_t r, int voff, int soff = 0) { return __builtin_amdgcn_raw_buffer_load_b128(r, voff, soff, AUX); }
template <int AUX> __device__ __forceinline__ u32 ld32(rsrc_t r, int voff) { return __builtin_amdgcn_raw_buffer_load_b32(r, voff, 0, AUX); }
__device__ __forceinline__ void st128(rsrc_t r, int voff, u32x4 v) { __builtin_amdgcn_raw_buffer_store_b128(v, r, voff, 0, AUX_SC1); }
__device__ __forceinline__ u32 tagw(float v, u32 tag4) { return (asu(v) & ~TMASK) | tag4; }
__device__ __forceinline__ u32x4 tag4(float a, float b, float c, float d, u32 t) { return u32x4{tagw(a, t), tagw(b, t), tagw(c, t), tagw(d, t)}; }
__device__ __forceinline__ u32 untag4(u32x4& v, u32 t) { v.x ^= t; v.y ^= t; v.z ^= t; v.w ^= t; return v.x | v.y | v.z | v.w; }
__device__ __forceinline__ bool none(bool bad) { return __builtin_amdgcn_ballot_w64(bad) == 0ull; }
__device__ __forceinline__ bool all_ok(u32 bad) { return none((bad & TMASK) != 0u); }

#define CBAR() asm volatile("" ::: "memory")
#define PIN(v) asm volatile("" : "+v"(v))
#define LGKM0() asm volatile("s_waitcnt lgkmcnt(0)" ::: "memory")
template <int N, int SLEEP> __device__ __forceinline__ void flag_wait(LAS u32* p, u32 tag, int lane) {
  u32 v, cnt;
  const u32 addr = (u32)(uintptr_t)(p + (lane & (N - 1)));
  asm volatile("s_mov_b32 %1, %4\n"
               "1:\n\t"
               "ds_read_b32 %0, %2\n\t"
               "s_waitcnt lgkmcnt(0)\n\t"
               "v_cmp_ne_u32 vcc, %3, %0\n\t"
               "s_cbranch_vccz 2f\n\t"
               "s_sleep %5\n\t"
               "s_sub_u32 %1, %1, 1\n\t"
               "s_cbranch_scc0 1b\n"
               "2:"
               : "=&v"(v), "=&s"(cnt) : "v"(addr), "s"(tag), "i"(SPIN_LDS), "i"(SLEEP) : "vcc", "scc", "memory");
}
template <int DUE> __device__ __forceinline__ void pace(u64 org) {
  asm volatile("1:\n\t"
               "s_memrealtime vcc\n\t"
               "s_waitcnt lgkmcnt(0)\n\t"
               "s_sub_u32 vcc_lo, vcc_lo, %0\n\t"
               "s_cmp_lt_i32 vcc_lo, %1\n\t"
               "s_cbranch_scc0 2f\n\t"
               "s_sleep 1\n\t"
               "s_branch 1b\n"
               "2:"
               :: "s"((u32)org), "i"(DUE), "s"(org) : "vcc", "scc", "memory");
}
__device__ __forceinline__ void flag_set(LAS u32* p, u32 tag) { CBAR(); *p = tag; CBAR(); }

__device__ __forceinline__ void poll2(rsrc_t rs, int voff, int soff, u32 t4, u32x4& r0, u32x4& r1) {
  u32 o0, o1, o2, o3, o4, o5, o6, o7, m, cnt;
  asm volatile("s_mov_b32 %9, %14\n\t"
               "buffer_load_dwordx4 v[144:147], %10, %11, 0 offen sc1\n\t"
               "buffer_load_dwordx4 v[148:151], %10, %11, %12 offen sc1\n\t"
               "s_sleep %15\n"
               "1:\n\t"
               "buffer_load_dwordx4 v[152:155], %10, %11, 0 offen sc1\n\t"
               "buffer_load_dwordx4 v[156:159], %10, %11, %12 offen sc1\n\t"
               "s_waitcnt vmcnt(2)\n\t"
               "v_xor_b32 %0, %13, v144\n\t" "v_xor_b32 %1, %13, v145\n\t" "v_xor_b32 %2, %13, v146\n\t" "v_xor_b32 %3, %13, v147\n\t" "v_xor_b32 %4, %13, v148\n\t" "v_xor_b32 %5, %13, v149\n\t" "v_xor_b32 %6, %13, v150\n\t" "v_xor_b32 %7, %13, v151\n\t" "v_or3_b32 %8, %0, %1, %2\n\t" "v_or3_b32 %8, %8, %3, %4\n\t" "v_or3_b32 %8, %8, %5, %6\n\t" "v_or_b32 %8, %8, %7\n\t" "v_and_b32 %8, 15, %8\n\t" "v_cmp_ne_u32 vcc, 0, %8\n\t"
               "s_cbranch_vccz 2f\n\t"
               "buffer_load_dwordx4 v[144:147], %10, %11, 0 offen sc1\n\t"
               "buffer_load_dwordx4 v[148:151], %10, %11, %12 offen sc1\n\t"
               "s_waitcnt vmcnt(2)\n\t"
               "v_xor_b32 %0, %13, v152\n\t" "v_xor_b32 %1, %13, v153\n\t" "v_xor_b32 %2, %13, v154\n\t" "v_xor_b32 %3, %13, v155\n\t" "v_xor_b32 %4, %13, v156\n\t" "v_xor_b32 %5, %13, v157\n\t" "v_xor_b32 %6, %13, v158\n\t" "v_xor_b32 %7, %13, v159\n\t" "v_or3_b32 %8, %0, %1, %2\n\t" "v_or3_b32 %8, %8, %3, %4\n\t" "v_or3_b32 %8, %8, %5, %6\n\t" "v_or_b32 %8, %8, %7\n\t" "v_and_b32 %8, 15, %8\n\t" "v_cmp_ne_u32 vcc, 0, %8\n\t"
               "s_cbranch_vccz 2f\n\t"
               "s_sub_u32 %9, %9, 1\n\t"
               "s_cbranch_scc0 1b\n"
               "2:"
               : "=&v"(o0), "=&v"(o1), "=&v"(o2), "=&v"(o3), "=&v"(o4), "=&v"(o5), "=&v"(o6), "=&v"(o7), "=&v"(m), "=&s"(cnt)
               : "v"(voff), "s"(rs), "s"(soff), "s"(t4), "i"(SPIN_MEM), "i"(PSTAG)
               : "vcc", "scc", "memory", "v144", "v145", "v146", "v147", "v148", "v149", "v150", "v151", "v152", "v153", "v154", "v155",
                 "v156", "v157", "v158", "v159");
  r0 = u32x4{o0, o1, o2, o3};
  r1 = u32x4{o4, o5, o6, o7};
}

#define MK(k)
#define MV(k)
#define MW(k)
#define MK0()

__device__ __forceinline__ void final_reduce(LAS u32* L, rsrc_t rws, const u16* x, u16* out, char* ws, int wg, int w, int grp, int lane, u32 tag) {
  LAS float* const LF = (LAS float*)L;
  __builtin_amdgcn_s_setprio(3);
  const u32 tag8 = tag & TMASK;
  const int rb = wg / 28, cb = wg % 28, qd = lane & 3;
  float xres = lo16(x[rb * D + 16 * cb + (lane & 15)]);
  int oo = O2_OFF + (grp * 32 + (lane >> 2)) * O2_S + rb * 1792 + cb * 64 + qd * 16;
  LAS float* wp = LF + ((lane & 12) == 0 ? L_R + (4 * qd + (lane >> 4)) * 8 + grp : L_XN + 128 + lane);
  LAS u32* fp = L + F_R + grp;
  LAS u32* fr = L + F_R + (lane & 7);
  LAS f32x4* rp = (LAS f32x4*)(LF + L_R + (lane & 15) * 8);
  int so = lane < 16 ? (rb * D + 16 * cb + lane) * 2 : OOB;
  u32 tagv = tag;
  const rsrc_t ro = mkrs(out, 2 * D * 2);
  PIN(xres); PIN(oo); PIN(wp); PIN(fp); PIN(fr); PIN(rp); PIN(so); PIN(tagv);
  flag_wait<1, 1>(L + F_PUB, tag, lane);
  flag_wait<4, 0>(L + F_O2, tag, lane);
  if (D2) __builtin_amdgcn_s_sleep(D2);
  u32x4 a0, a1;
  if (DBL & 2) poll2(rws, oo, 16 * O2_S, tag8, a0, a1);
  else for (u32 s = 0; s < SPIN_MEM; ++s) {
    CBAR();
    a0 = ld128<AUX_SC1>(rws, oo); a1 = ld128<AUX_SC1>(rws, oo, 16 * O2_S);
    if (all_ok(untag4(a0, tag8) | untag4(a1, tag8))) break;
    if (SLP2) __builtin_amdgcn_s_sleep(SLP2);
  }
  MK(6);
  float f = tr16(tr32(asf(a0.x) + asf(a1.x), asf(a0.z) + asf(a1.z)), tr32(asf(a0.y) + asf(a1.y), asf(a0.w) + asf(a1.w)));
  f += dpp<ROR8>(f); f += dpp<ROR4>(f);
  *wp = f;
  flag_set(fp, tagv);
  u32 fl = *fr;
  CBAR();
  const f32x4 r0 = rp[0], r1 = rp[1];
  u32 res = pkbf(xres + (((r0.x + r0.y) + (r0.z + r0.w)) + ((r1.x + r1.y) + (r1.z + r1.w))), 0.f);
  PIN(res);
  if (none(fl != tag)) {
    __builtin_amdgcn_raw_buffer_store_b16((u16)res, ro, so, 0, 0);
    if (wg == 0 && lane == 0) __builtin_amdgcn_raw_buffer_store_b32(tag, rws, EP_OFF, 0, AUX_SC1);
    MK(7);
  }
}

__global__ void __launch_bounds__(768, 3)
fused_decode(const u16* __restrict__ x, const u16* __restrict__ kc, const u16* __restrict__ vc, const u16* __restrict__ wq,
             const u16* __restrict__ wo, const u16* __restrict__ wg_, const u16* __restrict__ wu, const u16* __restrict__ wd,
             u16* __restrict__ out, char* __restrict__ ws) {
  __shared__ __attribute__((aligned(16))) u32 smem[L_END];
  LAS u32* const L = (LAS u32*)smem;
  LAS float* const LF = (LAS float*)smem;
  const int wg = blockIdx.x, h = wg & 7, j = wg >> 3, b = j & 1, sp = j >> 1, reg = wg & 15;
  const int w = __builtin_amdgcn_readfirstlane(threadIdx.x >> 6), lane = threadIdx.x & 63, kw = w & 3;
  asm volatile("s_load_dword s94, %0, 0x0\n\ts_load_dword s95, %0, 0x40" :: "s"(__builtin_amdgcn_kernarg_segment_ptr()) : "s94", "s95");
  u64 pc0;
  asm volatile("s_getpc_b64 %0" : "=s"(pc0));
  const u32 code_touch = ld32<0>(mkrs((const char*)(pc0 & ~255ull), w == CTW ? CODE_SPAN : 0), lane < 5 ? j * 640 + lane * 128 : OOB);
  __builtin_amdgcn_sched_barrier(0);
#define ROLE_ENTRY() \
  const rsrc_t rws = mkrs(ws, WS_BYTES); \
  const u32 epoch = *(const CAS u32*)(ws + EP_OFF)
#define ROLE_TAG() const u32 tag = epoch + 1u, tag8 = tag & TMASK
  const u32 blk0 = sp * 32 + reg * ROT;
  u64 org;
#define STREAM_LOADS(SRC, VOFF, SOFF, BAR) \
  const rsrc_t rkv = mkrs((SRC) + ((size_t)reg * SK + (SK - W)) * DH, W * DH * 2); \
  u32x4 d[32]; \
  if (BAR) { __builtin_amdgcn_sched_barrier(0); __builtin_amdgcn_s_barrier(); org = __builtin_amdgcn_s_memrealtime(); __builtin_amdgcn_sched_barrier(0); } \
  MV(2); \
  STREAM_GROUP(0, VOFF, SOFF, BAR) STREAM_GROUP(1, VOFF, SOFF, BAR) STREAM_GROUP(2, VOFF, SOFF, BAR) STREAM_GROUP(3, VOFF, SOFF, BAR) \
  STREAM_GROUP(4, VOFF, SOFF, BAR) STREAM_GROUP(5, VOFF, SOFF, BAR) STREAM_GROUP(6, VOFF, SOFF, BAR) STREAM_GROUP(7, VOFF, SOFF, BAR)
#define STREAM_GROUP(G, VOFF, SOFF, ISK) \
  if (PD > 0 && (!(ISK) || (G) >= PK0)) pace<PA + ((ISK) ? 0 : PV) + (((ISK) ? 0 : 8) + (G)) * PD / 10>(org); \
  _Pragma("unroll") for (int i = 4 * (G); i < 4 * (G) + 4; ++i) d[i] = ld128<AUX_NT>(rkv, (VOFF), (int)(SOFF)); \
  __builtin_amdgcn_sched_barrier(0);

  if (__builtin_expect(w >= 4, 0)) {
  u32 tag_out;
  if (w < 8) {
    __builtin_amdgcn_s_setprio(2);
    const int g = lane >> 4, key = (lane >> 1) & 7;
    const bool hb = lane & 1;
    const int kvo = key * 512 + (2 * g + (lane & 1)) * 16;
    STREAM_LOADS(kc, kvo + (i & 3) * 128, ((blk0 + kw + 4 * (i >> 2)) & 511u) << 12, true);
    MV(3);
    ROLE_ENTRY();
    MK0(); MW(2); MW(3);
    ROLE_TAG();
    if (lane < 2) L[L_WM + kw * 2 + lane] = 0u;
    if (lane == 0) { L[F_P + kw] = 0u; L[F_R + kw] = 0u; }
    if (kw == 1 && lane < 32) L[L_O + lane] = 0u;
    flag_wait<2, 1>(L + F_Q, tag, lane);
    u32x4 qa[4];
#pragma unroll
    for (int i = 0; i < 4; ++i) qa[i] = *(LAS u32x4*)(L + L_Q + (lane & 1) * 128 + i * 32 + (2 * g + ((lane >> 1) & 1)) * 4);
    f32x4 ea, eb;
#pragma unroll
    for (int n = 0; n < 4; ++n) {
      f32x4 ca = {}, cb = {};
#pragma unroll
      for (int i = 0; i < 4; ++i) {
        ca = __builtin_amdgcn_mfma_f32_16x16x32_bf16(__builtin_bit_cast(bf16x8_t, qa[i]), __builtin_bit_cast(bf16x8_t, d[8 * n + i]), ca, 0, 0, 0);
        cb = __builtin_amdgcn_mfma_f32_16x16x32_bf16(__builtin_bit_cast(bf16x8_t, qa[i]), __builtin_bit_cast(bf16x8_t, d[8 * n + 4 + i]), cb, 0, 0, 0);
      }
#pragma unroll
      for (int k = 0; k < 4; ++k) { ea[k] = (n == 0 || g == n) ? ca[k] : ea[k]; eb[k] = (n == 0 || g == n) ? cb[k] : eb[k]; }
    }
    asm volatile("" :: "v"(code_touch));
    float sc[2];
#pragma unroll
    for (int hd = 0; hd < 2; ++hd) sc[hd] = rbf((hb ? eb[2 + hd] : ea[hd]) + dpp<XOR1>(hb ? ea[2 + hd] : eb[hd])) * 0.0625f;
    const float wm0 = mx32(mx16(row_max(sc[0]))), wm1 = mx32(mx16(row_max(sc[1])));
    const u32 tag16 = (tag & 0x7fffu) | 0x8000u;
    CBAR();
    if (lane < 2) L[L_WM + kw * 2 + lane] = (asu(lane ? wm1 : wm0) & 0xffff0000u) | tag16;
    u32x4 ma, mb;
    for (u32 s = 0; s < SPIN_LDS; ++s) {
      CBAR();
      ma = *(LAS u32x4*)(L + L_WM) ^ tag16; mb = *(LAS u32x4*)(L + L_WM + 4) ^ tag16;
      if (none((((ma.x | ma.y | ma.z | ma.w) | (mb.x | mb.y | mb.z | mb.w)) & 0xffffu) != 0u)) break;
    }
    const float M0 = fmaxf(fmaxf(asf(ma.x), asf(ma.z)), fmaxf(asf(mb.x), asf(mb.z)));
    const float M1 = fmaxf(fmaxf(asf(ma.y), asf(ma.w)), fmaxf(asf(mb.y), asf(mb.w)));
    const float p0 = rbf(__builtin_amdgcn_exp2f((sc[0] - M0) * 1.4426950408889634f));
    const float p1 = rbf(__builtin_amdgcn_exp2f((sc[1] - M1) * 1.4426950408889634f));
    LAS u32* pp = L + L_P + key * 64 + kw + 4 * (2 * g + (lane & 1));
    pp[0] = asu(p0) | (asu(p0) >> 16);
    pp[32] = asu(p1) | (asu(p1) >> 16);
    float ls = tr32(p0, p1);
    ls = row_sum(tr16(ls, ls));
    if ((lane & 31) == 0) LF[L_WL + kw * 2 + (lane >> 5)] = ls;
    flag_set(L + F_P + kw, tag);
    MK(2);

    if (kw == 0) {
      flag_wait<4, 0>(L + F_P, tag, lane);
      if (lane < 2) {
        const float Ls = (LF[L_WL + lane] + LF[L_WL + 2 + lane]) + (LF[L_WL + 4 + lane] + LF[L_WL + 6 + lane]);
        st128(rws, ST_OFF + (((h * 2 + lane) * 2 + b) * 16 + sp) * 16, u32x4{asu(lane ? M1 : M0), tag, asu(Ls), tag});
      }
    } else if (kw == 1) {
      __builtin_amdgcn_s_setprio(3);
      const int s = lane >> 2, qd = lane & 3;
      const int so = ST_OFF + ((h * 2 + (j >> 4)) * 32 + s) * 16;
      const int oo = O1_OFF + (h * 32 + 2 * s) * O1_S + j * 64 + qd * 16;
      u32x4 s0, s1, a0, a1;
      flag_wait<4, 1>(L + F_VI, tag, lane);
      for (u32 it = 0; it < SPIN_MEM; ++it) {
        CBAR();
        s0 = ld128<AUX_SC1>(rws, so); s1 = ld128<AUX_SC1>(rws, so + 256);
        if (none((s0.y != tag) | (s0.w != tag) | (s1.y != tag) | (s1.w != tag))) break;
        flag_wait<1, 1>(L + F_PUB, tag, lane);
      }
      float wgt[2];
#pragma unroll
      for (int bb = 0; bb < 2; ++bb) {
        const float m = asf(bb ? s1.x : s0.x), l = asf(bb ? s1.z : s0.z);
        float mg = fmaxf(m, dpp<ROR8>(m)); mg = fmaxf(mg, dpp<ROR4>(mg)); mg = mx32(mx16(mg));
        const float e = __builtin_amdgcn_exp2f((m - mg) * 1.4426950408889634f);
        float lg = e * l; lg += dpp<ROR8>(lg); lg += dpp<ROR4>(lg); lg = tr16(lg, lg); lg = tr32(lg, lg);
        wgt[bb] = e * __builtin_amdgcn_rcpf(lg);
      }
      LAS u32x2* ow = (LAS u32x2*)(L + ((lane & 12) == 0 ? L_O + qd * 8 + (lane >> 4) * 2 : L_XN + 2 * lane));
      u32 tagv = tag;
      PIN(wgt[0]); PIN(wgt[1]); PIN(ow); PIN(tagv);
      flag_wait<1, 0>(L + F_PUB, tag, lane);
      if (D1) __builtin_amdgcn_s_sleep(D1);
      if (DBL & 1) poll2(rws, oo, O1_S, tag8, a0, a1);
      else for (u32 it = 0; it < SPIN_MEM; ++it) {
        CBAR();
        a0 = ld128<AUX_SC1>(rws, oo); a1 = ld128<AUX_SC1>(rws, oo + O1_S);
        if (all_ok(untag4(a0, tag8) | untag4(a1, tag8))) break;
        if (SLP1) __builtin_amdgcn_s_sleep(SLP1);
      }
      const float v0 = tr32(asf(a0.x) * wgt[0], asf(a1.x) * wgt[1]), v1 = tr32(asf(a0.y) * wgt[0], asf(a1.y) * wgt[1]);
      const float v2 = tr32(asf(a0.z) * wgt[0], asf(a1.z) * wgt[1]), v3 = tr32(asf(a0.w) * wgt[0], asf(a1.w) * wgt[1]);
      float e0 = tr16(v0, v2), e1 = tr16(v1, v3);
      e0 += dpp<ROR8>(e0); e1 += dpp<ROR8>(e1);
      e0 += dpp<ROR4>(e0); e1 += dpp<ROR4>(e1);
      *ow = u32x2{pkbf(e0, e1), tagv};
      asm volatile("s_waitcnt vmcnt(0)" ::: "memory");
      MK(4);
    } else if (PRE) {
      const int po = kw == 2 ? O1_OFF + (h * 32 + j) * O1_S : O2_OFF + (h * 32 + j) * O2_S;
      const u32 t0 = ld32<AUX_SC1>(rws, lane * PSTR < (kw == 2 ? 2048 : PRE == 2 ? 0 : 3584) ? po + lane * PSTR : OOB);
      asm volatile("" :: "v"(t0));
    }
    tag_out = tag;
  } else {
    __builtin_amdgcn_s_barrier();
    org = __builtin_amdgcn_s_memrealtime();
    __builtin_amdgcn_s_setprio(1);
    __builtin_amdgcn_sched_barrier(0);
    ROLE_ENTRY();
    if (lane == 0) { L[F_VI + kw] = 0u; L[F_V + kw] = 0u; L[F_R + 4 + kw] = 0u; L[F_PUB] = 0u; }
    STREAM_LOADS(vc, lane * 16 + kw * 1024, ((blk0 + i) & 511u) << 12, false);
    MV(4);
    ROLE_TAG();
    flag_set(L + F_VI + kw, tag);
    MK0();
    const int m = lane & 31, hf = lane >> 5;
    u32x4 msk;
#pragma unroll
    for (int r = 0; r < 4; ++r) msk[r] = (m < 16 && ((m & 7) >> 1) == r) ? ((m & 1) ? 0xffff0000u : 0x0000ffffu) : 0u;
    LAS f32x4* op = (LAS f32x4*)(LF + L_OP) + kw * 128 + 2 * m + hf;
    LAS f32x4* rp = (LAS f32x4*)(LF + L_OP) + lane;
    LAS u32* fp = L + F_V + kw;
    LAS u32* fr = L + F_V + (lane & 3);
    int o1o = O1_OFF + (h * 32 + j) * O1_S + lane * 16;
    PIN(op); PIN(rp); PIN(fp); PIN(fr); PIN(o1o);
    flag_wait<4, 1>(L + F_P, tag, lane);
    const LAS u32x4* pp = (const LAS u32x4*)(L + L_P + ((kw * 2 + hf) * 2 + ((m >> 3) & 1)) * 32);
    f32x16 acc = {};
#pragma unroll
    for (int g = 0; g < 8; ++g) {
      const u32x4 p4 = pp[g];
#pragma unroll
      for (int k = 0; k < 4; ++k) {
        const u32x4 a = msk & p4[k];
        acc = __builtin_amdgcn_mfma_f32_32x32x16_bf16(__builtin_bit_cast(bf16x8_t, a), __builtin_bit_cast(bf16x8_t, d[4 * g + k]), acc, 0, 0, 0);
      }
      __builtin_amdgcn_sched_barrier(0);
    }
    asm volatile("" :: "v"(code_touch));
    MV(6);
    op[0] = f32x4{acc[0], acc[1], acc[2], acc[3]};
    op[64] = f32x4{acc[4], acc[5], acc[6], acc[7]};
    flag_set(fp, tag);
    const u32 fl = *fr;
    CBAR();
    const f32x4 o0 = (rp[0] + rp[128]) + (rp[256] + rp[384]);
    const f32x4 o1 = (rp[64] + rp[192]) + (rp[320] + rp[448]);
    u32x4 t0 = tag4(o0.x, o0.y, o0.z, o0.w, tag8), t1 = tag4(o1.x, o1.y, o1.z, o1.w, tag8);
    PIN(t0); PIN(t1);
    if (none(fl != tag)) {
      st128(rws, o1o, t0);
      st128(rws, o1o + 1024, t1);
      flag_set(L + F_PUB, tag);
      MK(3);
    }
    MW(4); MW(6);
    tag_out = tag;
  }
  if (wg < 56 && (wg % 28) < ((ABL & 64) ? 16 : (ABL & 128) ? 8 : 28)) final_reduce(L, mkrs(ws, WS_BYTES), x, out, ws, wg, w, w - 4, lane, tag_out);
  return;
  }

  __builtin_amdgcn_s_setprio(3);
  ROLE_ENTRY();
  const bool mlp = wg < 224;
  const int cb2 = wg % 28, ke = wg / 28;
  const int rr = lane >> 3, c8 = lane & 7;
  const rsrc_t rx = mkrs((const u16*)x, 2 * D * 2);
  const u32x4 xa = ld128<0>(rx, lane < 56 ? lane * 16 : OOB), xb = ld128<0>(rx, lane < 56 ? D * 2 + lane * 16 : OOB);
  const int row0 = kw < 2 ? 112 * (j >> 3) + 64 * kw : 56 * ke;
  const int nrow = kw == 0 ? 64 : kw == 1 ? 48 : mlp ? 56 : 0;
  u32x4 wv[8];
  {
    const rsrc_t rw = mkrs(kw < 2 ? wq : kw == 2 ? wg_ : wu, kw < 2 ? ((ABL & 32) ? 0 : D * 4096 * 2) : ((ABL & 8) ? 0 : D * DF * 2));
    const int rs = kw < 2 ? 8192 : DF * 2;
    const int vw0 = nrow ? (row0 + 2 * rr) * rs + (kw < 2 ? (h * 512 + (j & 7) * 64) * 2 : cb2 * 128) + c8 * 16 : OOB;
    const int vw3 = 48 + 2 * rr < nrow ? vw0 : OOB;
#pragma unroll
    for (int i = 0; i < 8; ++i) wv[i] = ld128<AUX_NT>(rw, i < 6 ? vw0 : vw3, (16 * (i >> 1) + (i & 1)) * rs);
  }
  const int eo = lane < 8 ? QP_OFF + (h * 32 + j) * QP_S + lane * 64 : lane < 24 ? GP_OFF + wg * GP_S + (lane - 8) * 64 : ST_OFF + (wg & 127) * 64;
  const u32 box_touch = ld32<AUX_SC1>(rws, (!(ABL & 16) && kw == 1 && lane < 25) ? eo : OOB);
  __builtin_amdgcn_sched_barrier(0);
  __builtin_amdgcn_s_barrier();
  org = __builtin_amdgcn_s_memrealtime();
  __builtin_amdgcn_sched_barrier(0);
  MK0();
  ROLE_TAG();
  if (lane == 0) { L[F_O2 + kw] = 0u; if (kw < 2) L[F_Q + kw] = 0u; if (kw == 1) L[F_QX] = 0u; }
  if (kw == 2 && lane < 16) L[L_H + lane] = 0u;

#pragma unroll
  for (int bb = 0; bb < 2; ++bb) {
    const u32x4 xv = bb ? xb : xa;
    float v[8];
#pragma unroll
    for (int k = 0; k < 4; ++k) { v[2 * k] = lo16(xv[k]); v[2 * k + 1] = hi16(xv[k]); }
    float s = 0.f;
#pragma unroll
    for (int k = 0; k < 8; ++k) s += v[k];
    const float mean = wave_sum(s) * (1.f / D);
    float q = 0.f;
#pragma unroll
    for (int k = 0; k < 8; ++k) { const float t = v[k] - mean; q += t * t; }
    q = lane < 56 ? q : 0.f;
    const float var = rbf(wave_sum(q) * (1.f / D));
    const float den = rbf(__builtin_amdgcn_sqrtf(rbf(var + 1e-5f))), rden = __builtin_amdgcn_rcpf(den), mb = rbf(mean);
#pragma unroll
    for (int k = 0; k < 8; ++k) { const float c = rbf(v[k] - mb), r = c * rden; v[k] = fmaf(fmaf(-r, den, c), rden, r); }
    u32x4 o;
#pragma unroll
    for (int k = 0; k < 4; ++k) o[k] = pkbf(v[2 * k], v[2 * k + 1]);
    if (lane < 56) *(LAS u32x4*)(L + L_XN + bb * 224 + lane * 4) = o;
    else if (bb == 1) *(LAS u32x4*)(L + L_XN + 224 + lane * 4) = u32x4{0u, 0u, 0u, 0u};
  }
  asm volatile("" :: "v"(code_touch));
  MK(10);

  {
    float a0[8] = {}, a1[8] = {};
#pragma unroll
    for (int k = 0; k < 4; ++k) {
      const u32 x0 = L[L_XN + row0 / 2 + rr + 8 * k], x1 = L[L_XN + 224 + row0 / 2 + rr + 8 * k];
#pragma unroll
      for (int c = 0; c < 4; ++c) {
        const u32 pl = __builtin_amdgcn_perm(wv[2 * k + 1][c], wv[2 * k][c], 0x05040100u), ph = __builtin_amdgcn_perm(wv[2 * k + 1][c], wv[2 * k][c], 0x07060302u);
        a0[2 * c] = dot2(pl, x0, a0[2 * c]); a0[2 * c + 1] = dot2(ph, x0, a0[2 * c + 1]);
        a1[2 * c] = dot2(pl, x1, a1[2 * c]); a1[2 * c + 1] = dot2(ph, x1, a1[2 * c + 1]);
      }
    }
    asm volatile("" :: "v"(box_touch));
    MV(5);
    float t[8], u[4];
#pragma unroll
    for (int k = 0; k < 8; ++k) t[k] = tr32(a0[k], a1[k]);
#pragma unroll
    for (int k = 0; k < 4; ++k) { u[k] = tr16(t[k], t[k + 4]); u[k] += dpp<ROR8>(u[k]); }
    const int bb = lane >> 5, hf = (lane >> 4) & 1;
    LAS f32x4* qx = (LAS f32x4*)(LF + L_QX) + bb * 16 + c8 * 2 + hf;
    if (kw == 1) {
      if (!(lane & 8)) *qx = f32x4{u[0], u[1], u[2], u[3]};
      flag_set(L + F_QX, tag);
    } else if (kw == 0) {
      flag_wait<1, 0>(L + F_QX, tag, lane);
      const f32x4 o = *qx;
      if (!(ABL & 1) && !(lane & 8)) st128(rws, QP_OFF + (h * 32 + j) * QP_S + bb * 256 + c8 * 32 + hf * 16, tag4(u[0] + o.x, u[1] + o.y, u[2] + o.z, u[3] + o.w, tag8));
    } else if (!(ABL & 2) && mlp && !(lane & 8)) {
      st128(rws, GP_OFF + wg * GP_S + c8 * 128 + (kw - 2) * 64 + bb * 32 + hf * 16, tag4(u[0], u[1], u[2], u[3], tag8));
    }
  }

  if (kw < 2) {
    __builtin_amdgcn_s_sleep(QDELAY);
    const int qo = QP_OFF + (h * 32 + 4 * kw + 8 * (lane >> 4)) * QP_S + b * 256 + (lane & 15) * 16;
    u32x4 a[4];
    for (u32 s = 0; s < SPIN_MEM; ++s) {
      CBAR();
      if (ABL & 1) { a[0] = a[1] = a[2] = a[3] = xa; break; }
#pragma unroll
      for (int i = 0; i < 4; ++i) a[i] = ld128<AUX_SC1>(rws, qo, i * QP_S);
      if (all_ok((untag4(a[0], tag8) | untag4(a[1], tag8)) | (untag4(a[2], tag8) | untag4(a[3], tag8)))) break;
    }
#pragma unroll
    for (int i = 0; i < 2; ++i) {
      const u32x4 a0 = a[2 * i], a1 = a[2 * i + 1];
      const float v0 = tr32(asf(a0.x), asf(a1.x)), v1 = tr32(asf(a0.y), asf(a1.y)), v2 = tr32(asf(a0.z), asf(a1.z)), v3 = tr32(asf(a0.w), asf(a1.w));
      L[L_Q + (4 * kw + 2 * i + (lane >> 5)) * 32 + 2 * (lane & 15) + ((lane >> 4) & 1)] = pkbf(tr16(v0, v2), tr16(v1, v3));
    }
    flag_set(L + F_Q + kw, tag);
    MK(1);
  } else if (kw == 2) {
    u32 h0 = 0u, h1 = 0u;
    if (mlp) {
      const int go = GP_OFF + (cb2 + 28 * rr) * GP_S + ke * 128 + c8 * 16;
      u32x4 a;
      flag_wait<2, 1>(L + F_Q, tag, lane);
      for (u32 s = 0; s < SPIN_MEM; ++s) {
        CBAR();
        if (ABL & 2) { a = xa; break; }
        a = ld128<AUX_SC1>(rws, go);
        if (all_ok(untag4(a, tag8))) break;
        __builtin_amdgcn_s_sleep(16);
      }
      float hv[4];
#pragma unroll
      for (int k = 0; k < 4; ++k) {
        float g = asf(a[k]);
        g += dpp<ROR8>(g); g = tr16(g, g); g = tr32(g, g);
        g = rbf(g);
        const float uu = dpp<ROR4>(g);
        const float z = 0.7978845608028654f * (g + 0.044715f * g * g * g);
        const float th = 1.f - 2.f * __builtin_amdgcn_rcpf(1.f + __builtin_amdgcn_exp2f(z * 2.8853900817779268f));
        hv[k] = rbf(0.5f * g * (1.f + th)) * uu;
      }
      h0 = pkbf(hv[0], hv[1]); h1 = pkbf(hv[2], hv[3]);
    }
    if (lane < 4) {
      *(LAS u32x2*)(L + L_H + (2 * (lane & 1)) * 4 + (lane >> 1) * 2) = u32x2{h0, tag};
      *(LAS u32x2*)(L + L_H + (2 * (lane & 1) + 1) * 4 + (lane >> 1) * 2) = u32x2{h1, tag};
    }
    CBAR();
  }

  const int rg = lane >> 4, cl = lane & 15;
  const int co = kw * 128 + cl * 8;
  u32x4 wa[4], wb[2];
  if (LATE == 0) flag_wait<2, 1>(L + F_Q, tag, lane);
  if (LATE >= 1) flag_wait<4, 1>(L + F_VI, tag, lane);
  if (LATE == 3) flag_wait<1, 0>(L + F_PUB, tag, lane);
  if (LATE == 4) pace<PA + PV + 16 * PD / 10 + LD>(org);
  if (LATE == 5) __builtin_amdgcn_s_sleep(LD);
  {
    const rsrc_t ro = mkrs(wo, (ABL & 4) ? 0 : 4096 * D * 2), rd = mkrs(wd, mlp && !(ABL & 4) ? DF * D * 2 : 0);
    const int vo = co < D ? (h * 512 + j * 16 + 4 * rg) * (D * 2) + co * 2 : OOB;
    const int vd = co < D ? (64 * cb2 + 8 * ke + 2 * rg) * (D * 2) + co * 2 : OOB;
#pragma unroll
    for (int i = 0; i < 2; ++i) wb[i] = ld128<AUX_NT>(rd, vd, i * D * 2);
#pragma unroll
    for (int i = 0; i < 4; ++i) wa[i] = ld128<AUX_NT>(ro, vo, i * D * 2);
  }
  __builtin_amdgcn_sched_barrier(0);
  float a0[8], a1[8];
  u32 pa[8], pb[8];
  {
    u32x4 hh;
    for (u32 s = 0; s < SPIN_LDS; ++s) {
      CBAR();
      hh = *(LAS u32x4*)(L + L_H + rg * 4);
      if (none((hh.y != tag) | (hh.w != tag))) break;
      __builtin_amdgcn_s_sleep(1);
    }
#pragma unroll
    for (int k = 0; k < 4; ++k) {
      const u32 pl = __builtin_amdgcn_perm(wb[1][k], wb[0][k], 0x05040100u), ph = __builtin_amdgcn_perm(wb[1][k], wb[0][k], 0x07060302u);
      a0[2 * k] = dot2(pl, hh.x, 0.f); a0[2 * k + 1] = dot2(ph, hh.x, 0.f);
      a1[2 * k] = dot2(pl, hh.z, 0.f); a1[2 * k + 1] = dot2(ph, hh.z, 0.f);
    }
#pragma unroll
    for (int k = 0; k < 4; ++k) {
      pa[2 * k] = __builtin_amdgcn_perm(wa[1][k], wa[0][k], 0x05040100u); pa[2 * k + 1] = __builtin_amdgcn_perm(wa[1][k], wa[0][k], 0x07060302u);
      pb[2 * k] = __builtin_amdgcn_perm(wa[3][k], wa[2][k], 0x05040100u); pb[2 * k + 1] = __builtin_amdgcn_perm(wa[3][k], wa[2][k], 0x07060302u);
    }
  }
#pragma unroll
  for (int k = 0; k < 8; ++k) asm volatile("" : "+v"(a0[k]), "+v"(a1[k]), "+v"(pa[k]), "+v"(pb[k]));
  int o2o = co < ((ABL & 64) ? 256 : (ABL & 128) ? 128 : D) ? O2_OFF + (h * 32 + j) * O2_S + (lane >> 5) * 1792 + (co + 4 * ((lane >> 4) & 1)) * 4 : OOB;
  LAS u32x4* orp = (LAS u32x4*)(L + L_O + rg * 8);
  PIN(o2o); PIN(orp);
  if (LATE == 0) flag_wait<4, 1>(L + F_VI, tag, lane);
  {
    u32x4 oa, ob;
    for (u32 s = 0; s < SPIN_LDS; ++s) {
      CBAR();
      oa = orp[0]; ob = orp[1];
      if (none(((oa.y != tag) | (oa.w != tag)) | ((ob.y != tag) | (ob.w != tag)))) break;
    }
#pragma unroll
    for (int k = 0; k < 8; ++k) {
      a0[k] = dot2(pb[k], oa.z, dot2(pa[k], oa.x, a0[k]));
      a1[k] = dot2(pb[k], ob.z, dot2(pa[k], ob.x, a1[k]));
    }
    float t[8];
#pragma unroll
    for (int k = 0; k < 8; ++k) t[k] = tr32(a0[k], a1[k]);
    st128(rws, o2o, tag4(tr16(t[0], t[4]), tr16(t[1], t[5]), tr16(t[2], t[6]), tr16(t[3], t[7]), tag8));
  }
  flag_set(L + F_O2 + kw, tag);
  MK(5);
}
}

extern "C" void kernel_launch(void* const* d_in, const int* in_sizes, int n_in, void* d_out, int out_size, void* d_ws, size_t ws_size,
                              hipStream_t stream) {
  fused_decode<<<256, 768, 0, stream>>>((const u16*)d_in[0], (const u16*)d_in[1], (const u16*)d_in[2], (const u16*)d_in[3], (const u16*)d_in[4],
                                        (const u16*)d_in[6], (const u16*)d_in[7], (const u16*)d_in[8], (u16*)d_out, (char*)d_ws + WS_OFF);
}
```

```cpp
#include <hip/hip_runtime.h>
#include <hip/hip_bf16.h>
#include <stdint.h>

typedef unsigned int u32;
typedef unsigned short u16;
typedef unsigned long long u64;
typedef u32 u32x2 __attribute__((ext_vector_type(2)));
typedef u32 u32x4 __attribute__((ext_vector_type(4)));
typedef float f32x4 __attribute__((ext_vector_type(4)));
typedef float f32x16 __attribute__((ext_vector_type(16)));
typedef __bf16 bf16x2_t __attribute__((ext_vector_type(2)));
typedef __bf16 bf16x8_t __attribute__((ext_vector_type(8)));
#define LAS __attribute__((address_space(3)))
typedef __amdgpu_buffer_rsrc_t rsrc_t;

#define STAMP 0
#define LATE 4
#define QDELAY 40
#define PRE 2
#define PSTR 64
#define ABL 0
#define SLP1 0
#define SLP2 0
#define PSTAG 5
#define DBL 0
#define PA 35
#define PD 625
#define PK0 2
#define D1 12
#define D2 10
#define PV 0
#define LD 0
#define ROT 37
#define CODE_SPAN 13440
#define CTW 0
#define DIAG 0
#define WS_OFF 0

namespace {
constexpr int D = 448, SK = 32768, W = 4096, DH = 256, DF = 1792;
constexpr int AUX_NT = 2, AUX_SC1 = 16;
constexpr int OOB = 1 << 30;
constexpr u32 TMASK = 0xfu;

constexpr int QP_S = 768, GP_S = 1280, O1_S = 2304, O2_S = 3840;
constexpr int EP_OFF = 0;
constexpr int ST_OFF = 4096;
constexpr int QP_OFF = ST_OFF + 8192;
constexpr int GP_OFF = QP_OFF + 256 * QP_S;
constexpr int O1_OFF = GP_OFF + 256 * GP_S;
constexpr int O2_OFF = O1_OFF + 256 * O1_S;
constexpr int WS_BYTES = O2_OFF + 256 * O2_S;
constexpr int TS_OFF = 4 << 20;

constexpr int L_XN = 0;
constexpr int L_QX = L_XN + 480;
constexpr int L_GX = L_QX + 128;
constexpr int L_Q = L_GX + 256;
constexpr int L_P = L_Q + 256;
constexpr int L_OP = L_P + 512;
constexpr int L_WM = L_OP + 2048;
constexpr int L_WL = L_WM + 8;
constexpr int L_H = L_WL + 8;
constexpr int L_O = L_H + 16;
constexpr int L_R = L_O + 32;
constexpr int F_P = L_R + 128;
constexpr int F_VI = F_P + 4;
constexpr int F_O2 = F_VI + 4;
constexpr int F_Q = F_O2 + 4;
constexpr int F_QX = F_Q + 2, F_PUB = F_QX + 1;
constexpr int F_V = F_PUB + 1;
constexpr int F_R = F_V + 4;
constexpr int L_TAG = F_R + 8;
constexpr int L_DG = L_TAG + 1;
constexpr int L_END = L_TAG + 4;
constexpr u32 SET = 1u;
static_assert((L_END - L_WM) % 4 == 0 && (L_END - L_WM) / 4 <= 64, "one 16-byte store per lane clears the flag area");

constexpr u32 SPIN_LDS = 1u << 21, SPIN_MEM = 1u << 16;

__device__ __forceinline__ float asf(u32 v) { return __builtin_bit_cast(float, v); }
__device__ __forceinline__ u32 asu(float v) { return __builtin_bit_cast(u32, v); }
__device__ __forceinline__ float lo16(u32 v) { return asf(v << 16); }
__device__ __forceinline__ float hi16(u32 v) { return asf(v & 0xffff0000u); }
__device__ __forceinline__ u32 pkbf(float a, float b) {
  bf16x2_t r = {(__bf16)a, (__bf16)b};
  return __builtin_bit_cast(u32, r);
}
__device__ __forceinline__ float rbf(float a) { return (float)(__bf16)a; }
__device__ __forceinline__ float dot2(u32 a, u32 b, float c) {
  return __builtin_amdgcn_fdot2_f32_bf16(__builtin_bit_cast(bf16x2_t, a), __builtin_bit_cast(bf16x2_t, b), c, false);
}
template <int CTRL> __device__ __forceinline__ float dpp(float x) {
  return asf(__builtin_amdgcn_mov_dpp((int)asu(x), CTRL, 0xf, 0xf, true));
}
constexpr int XOR1 = 0xB1, XOR2 = 0x4E, ROR8 = 0x128, ROR4 = 0x124;
__device__ __forceinline__ float tr32(float a, float b) {
  auto r = __builtin_amdgcn_permlane32_swap(asu(a), asu(b), false, false);
  return asf(r[0]) + asf(r[1]);
}
__device__ __forceinline__ float tr16(float a, float b) {
  auto r = __builtin_amdgcn_permlane16_swap(asu(a), asu(b), false, false);
  return asf(r[0]) + asf(r[1]);
}
__device__ __forceinline__ float mx32(float a) {
  auto r = __builtin_amdgcn_permlane32_swap(asu(a), asu(a), false, false);
  return fmaxf(asf(r[0]), asf(r[1]));
}
__device__ __forceinline__ float mx16(float a) {
  auto r = __builtin_amdgcn_permlane16_swap(asu(a), asu(a), false, false);
  return fmaxf(asf(r[0]), asf(r[1]));
}
__device__ __forceinline__ float row_sum(float v) { v += dpp<ROR8>(v); v += dpp<ROR4>(v); v += dpp<XOR2>(v); v += dpp<XOR1>(v); return v; }
__device__ __forceinline__ float row_max(float v) {
  v = fmaxf(v, dpp<ROR8>(v)); v = fmaxf(v, dpp<ROR4>(v)); v = fmaxf(v, dpp<XOR2>(v)); v = fmaxf(v, dpp<XOR1>(v)); return v;
}
__device__ __forceinline__ float wave_sum(float v) { v = row_sum(v); v = tr16(v, v); return tr32(v, v); }

__device__ __forceinline__ rsrc_t mkrs(const void* p, u32 bytes) { return __builtin_amdgcn_make_buffer_rsrc((void*)p, 0, bytes, 0x00020000); }
template <int AUX> __device__ __forceinline__ u32x4 ld128(rsrc_t r, int voff, int soff = 0) { return __builtin_amdgcn_raw_buffer_load_b128(r, voff, soff, AUX); }
template <int AUX> __device__ __forceinline__ u32 ld32(rsrc_t r, int voff) { return __builtin_amdgcn_raw_buffer_load_b32(r, voff, 0, AUX); }
__device__ __forceinline__ void st128(rsrc_t r, int voff, u32x4 v) { __builtin_amdgcn_raw_buffer_store_b128(v, r, voff, 0, AUX_SC1); }
__device__ __forceinline__ u32 tagw(float v, u32 tag4) { return (asu(v) & ~TMASK) | tag4; }
__device__ __forceinline__ u32x4 tag4(float a, float b, float c, float d, u32 t) { return u32x4{tagw(a, t), tagw(b, t), tagw(c, t), tagw(d, t)}; }
__device__ __forceinline__ u32 untag4(u32x4& v, u32 t) { v.x ^= t; v.y ^= t; v.z ^= t; v.w ^= t; return v.x | v.y | v.z | v.w; }
__device__ __forceinline__ bool none(bool bad) { return __builtin_amdgcn_ballot_w64(bad) == 0ull; }
__device__ __forceinline__ bool all_ok(u32 bad) { return none((bad & TMASK) != 0u); }

#define CBAR() asm volatile("" ::: "memory")
#define PIN(v) asm volatile("" : "+v"(v))
#define LGKM0() asm volatile("s_waitcnt lgkmcnt(0)" ::: "memory")
template <int N, int SLEEP> __device__ __forceinline__ void flag_wait(LAS u32* p, int lane) {
  u32 v, cnt;
  const u32 addr = (u32)(uintptr_t)(p + (lane & (N - 1)));
  asm volatile("s_mov_b32 %1, %4\n"
               "1:\n\t"
               "ds_read_b32 %0, %2\n\t"
               "s_waitcnt lgkmcnt(0)\n\t"
               "v_cmp_ne_u32 vcc, %3, %0\n\t"
               "s_cbranch_vccz 2f\n\t"
               "s_sleep %5\n\t"
               "s_sub_u32 %1, %1, 1\n\t"
               "s_cbranch_scc0 1b\n"
               "2:"
               : "=&v"(v), "=&s"(cnt) : "v"(addr), "i"(SET), "i"(SPIN_LDS), "i"(SLEEP) : "vcc", "scc", "memory");
}
template <int DUE> __device__ __forceinline__ void pace(u64 org) {
  asm volatile("1:\n\t"
               "s_memrealtime vcc\n\t"
               "s_waitcnt lgkmcnt(0)\n\t"
               "s_sub_u32 vcc_lo, vcc_lo, %0\n\t"
               "s_cmp_lt_i32 vcc_lo, %1\n\t"
               "s_cbranch_scc0 2f\n\t"
               "s_sleep 1\n\t"
               "s_branch 1b\n"
               "2:"
               :: "s"((u32)org), "i"(DUE), "s"(org) : "vcc", "scc", "memory");
}
__device__ __forceinline__ void flag_set(LAS u32* p, u32 set = SET) { CBAR(); *p = set; CBAR(); }

__device__ __forceinline__ void poll2(rsrc_t rs, int voff, int soff, u32 t4, u32x4& r0, u32x4& r1) {
  u32 o0, o1, o2, o3, o4, o5, o6, o7, m, cnt;
  asm volatile("s_mov_b32 %9, %14\n\t"
               "buffer_load_dwordx4 v[144:147], %10, %11, 0 offen sc1\n\t"
               "buffer_load_dwordx4 v[148:151], %10, %11, %12 offen sc1\n\t"
               "s_sleep %15\n"
               "1:\n\t"
               "buffer_load_dwordx4 v[152:155], %10, %11, 0 offen sc1\n\t"
               "buffer_load_dwordx4 v[156:159], %10, %11, %12 offen sc1\n\t"
               "s_waitcnt vmcnt(2)\n\t"
               "v_xor_b32 %0, %13, v144\n\t" "v_xor_b32 %1, %13, v145\n\t" "v_xor_b32 %2, %13, v146\n\t" "v_xor_b32 %3, %13, v147\n\t" "v_xor_b32 %4, %13, v148\n\t" "v_xor_b32 %5, %13, v149\n\t" "v_xor_b32 %6, %13, v150\n\t" "v_xor_b32 %7, %13, v151\n\t" "v_or3_b32 %8, %0, %1, %2\n\t" "v_or3_b32 %8, %8, %3, %4\n\t" "v_or3_b32 %8, %8, %5, %6\n\t" "v_or_b32 %8, %8, %7\n\t" "v_and_b32 %8, 15, %8\n\t" "v_cmp_ne_u32 vcc, 0, %8\n\t"
               "s_cbranch_vccz 2f\n\t"
               "buffer_load_dwordx4 v[144:147], %10, %11, 0 offen sc1\n\t"
               "buffer_load_dwordx4 v[148:151], %10, %11, %12 offen sc1\n\t"
               "s_waitcnt vmcnt(2)\n\t"
               "v_xor_b32 %0, %13, v152\n\t" "v_xor_b32 %1, %13, v153\n\t" "v_xor_b32 %2, %13, v154\n\t" "v_xor_b32 %3, %13, v155\n\t" "v_xor_b32 %4, %13, v156\n\t" "v_xor_b32 %5, %13, v157\n\t" "v_xor_b32 %6, %13, v158\n\t" "v_xor_b32 %7, %13, v159\n\t" "v_or3_b32 %8, %0, %1, %2\n\t" "v_or3_b32 %8, %8, %3, %4\n\t" "v_or3_b32 %8, %8, %5, %6\n\t" "v_or_b32 %8, %8, %7\n\t" "v_and_b32 %8, 15, %8\n\t" "v_cmp_ne_u32 vcc, 0, %8\n\t"
               "s_cbranch_vccz 2f\n\t"
               "s_sub_u32 %9, %9, 1\n\t"
               "s_cbranch_scc0 1b\n"
               "2:"
               : "=&v"(o0), "=&v"(o1), "=&v"(o2), "=&v"(o3), "=&v"(o4), "=&v"(o5), "=&v"(o6), "=&v"(o7), "=&v"(m), "=&s"(cnt)
               : "v"(voff), "s"(rs), "s"(soff), "s"(t4), "i"(SPIN_MEM), "i"(PSTAG)
               : "vcc", "scc", "memory", "v144", "v145", "v146", "v147", "v148", "v149", "v150", "v151", "v152", "v153", "v154", "v155",
                 "v156", "v157", "v158", "v159");
  r0 = u32x4{o0, o1, o2, o3};
  r1 = u32x4{o4, o5, o6, o7};
}

#define MK(k)
#define MV(k)
#define MW(k)
#define MK0()

__device__ __forceinline__ void final_reduce(LAS u32* L, rsrc_t rws, const u16* x, u16* out, char* ws, int wg, int w, int grp, int lane, u32 tag, u64 tent) {
  LAS float* const LF = (LAS float*)L;
  __builtin_amdgcn_s_setprio(3);
  const u32 tag8 = tag & TMASK;
  const int rb = wg / 28, cb = wg % 28, qd = lane & 3;
  const int pi = cb & 7, ph = cb < 24 ? pi >> 2 : pi >> 1, pg = cb < 24 ? pi & 3 : pi & 1;
  const int col = rb * D + 128 * (cb >> 3) + 8 * (4 * pg + ((lane >> 2) & 3)) + 4 * ph + (lane & 3);
  float xres = lo16(x[col]);
  int oo = O2_OFF + (grp * 32 + (lane >> 2)) * O2_S + rb * 1792 + cb * 64 + qd * 16;
  LAS float* wp = LF + ((lane & 12) == 0 ? L_R + (4 * qd + (lane >> 4)) * 8 + grp : L_XN + 128 + lane);
  LAS u32* fp = L + F_R + grp;
  LAS u32* fr = L + F_R + (lane & 7);
  LAS f32x4* rp = (LAS f32x4*)(LF + L_R + (lane & 15) * 8);
  int so = lane < 16 ? col * 2 : OOB;
  u32 tagv = SET;
  const rsrc_t ro = mkrs(out, 2 * D * 2);
  PIN(xres); PIN(oo); PIN(wp); PIN(fp); PIN(fr); PIN(rp); PIN(so); PIN(tagv);
  flag_wait<1, 1>(L + F_PUB, lane);
  flag_wait<4, 0>(L + F_O2, lane);
  if (D2) __builtin_amdgcn_s_sleep(D2);
  u32x4 a0, a1;
  if (DBL & 2) poll2(rws, oo, 16 * O2_S, tag8, a0, a1);
  else for (u32 s = 0; s < SPIN_MEM; ++s) {
    CBAR();
    a0 = ld128<AUX_SC1>(rws, oo); a1 = ld128<AUX_SC1>(rws, oo, 16 * O2_S);
    if (all_ok(untag4(a0, tag8) | untag4(a1, tag8))) break;
    if (SLP2) __builtin_amdgcn_s_sleep(SLP2);
  }
  MK(6);
  float f = tr16(tr32(asf(a0.x) + asf(a1.x), asf(a0.z) + asf(a1.z)), tr32(asf(a0.y) + asf(a1.y), asf(a0.w) + asf(a1.w)));
  f += dpp<ROR8>(f); f += dpp<ROR4>(f);
  *wp = f;
  flag_set(fp, tagv);
  u32 fl = *fr;
  CBAR();
  const f32x4 r0 = rp[0], r1 = rp[1];
  u32 res = pkbf(xres + (((r0.x + r0.y) + (r0.z + r0.w)) + ((r1.x + r1.y) + (r1.z + r1.w))), 0.f);
  PIN(res);
  if (none(fl != SET)) {
    __builtin_amdgcn_raw_buffer_store_b16((u16)res, ro, so, 0, 0);
    if (wg == 0 && lane == 0) __builtin_amdgcn_raw_buffer_store_b32(tag, rws, EP_OFF, 0, AUX_SC1);
    if (wg == 0) {
      const rsrc_t rt = mkrs(ws + TS_OFF, 8192);
      const u32x4 tv = ld128<AUX_SC1>(rt, (DIAG & 8 ? 1024 : 0) + lane * 16);
      const u32 t0 = (u32)tent;
      float a = (float)(int)(tv.x - t0), b2 = (float)(int)(tv.y - t0), c = (float)(int)(tv.z - t0), d2 = (float)(int)(tv.w - t0);
      float mx = mx32(mx16(row_max(fmaxf(fmaxf(a, b2), fmaxf(c, d2))))), mn = -mx32(mx16(row_max(fmaxf(fmaxf(-a, -b2), fmaxf(-c, -d2)))));
      float av = wave_sum((a + b2) + (c + d2)) * (1.f / 256);
      float st = (DIAG & 7) == 0 ? 0.f : (DIAG & 7) == 1 ? mx - mn : (DIAG & 7) == 2 ? av - mn : (DIAG & 7) == 3 ? -mn : mx;
      const u64 until = tent + 3000 + (u64)(int)__builtin_amdgcn_readfirstlane((int)(st * 10.f));
      while (__builtin_amdgcn_s_memrealtime() < until) __builtin_amdgcn_s_sleep(1);
    }
    MK(7);
  }
}

__global__ void __launch_bounds__(768, 3)
fused_decode(const u16* __restrict__ x, const u16* __restrict__ kc, const u16* __restrict__ vc, const u16* __restrict__ wq,
             const u16* __restrict__ wo, const u16* __restrict__ wg_, const u16* __restrict__ wu, const u16* __restrict__ wd,
             u16* __restrict__ out, char* __restrict__ ws) {
  __shared__ __attribute__((aligned(16))) u32 smem[L_END];
  LAS u32* const L = (LAS u32*)smem;
  LAS float* const LF = (LAS float*)smem;
  const int wg = blockIdx.x, h = wg & 7, j = wg >> 3, b = j & 1, sp = j >> 1, reg = wg & 15;
  const int w = __builtin_amdgcn_readfirstlane(threadIdx.x >> 6), lane = threadIdx.x & 63, kw = w & 3;
  const u64 tent = __builtin_amdgcn_s_memrealtime();
  asm volatile("s_load_dword s94, %0, 0x0\n\ts_load_dword s95, %0, 0x40" :: "s"(__builtin_amdgcn_kernarg_segment_ptr()) : "s94", "s95");
  u64 pc0;
  asm volatile("s_getpc_b64 %0" : "=s"(pc0));
  const u32 code_touch = ld32<0>(mkrs((const char*)(pc0 & ~255ull), w == CTW ? CODE_SPAN : 0), lane < 5 ? j * 640 + lane * 128 : OOB);
  __builtin_amdgcn_sched_barrier(0);
#define ROLE_ENTRY() const rsrc_t rws = mkrs(ws, WS_BYTES)
#define ROLE_TAG() const u32 tag = __builtin_amdgcn_readfirstlane(L[L_TAG]), tag8 = tag & TMASK
  const u32 blk0 = sp * 32 + reg * ROT;
  u64 org;
#define STREAM_LOADS(SRC, VOFF, SOFF, BAR) \
  const rsrc_t rkv = mkrs((SRC) + ((size_t)reg * SK + (SK - W)) * DH, W * DH * 2); \
  u32x4 d[32]; \
  if (BAR) { __builtin_amdgcn_sched_barrier(0); __builtin_amdgcn_s_barrier(); org = __builtin_amdgcn_s_memrealtime(); __builtin_amdgcn_sched_barrier(0); } \
  MV(2); \
  STREAM_GROUP(0, VOFF, SOFF, BAR) STREAM_GROUP(1, VOFF, SOFF, BAR) STREAM_GROUP(2, VOFF, SOFF, BAR) STREAM_GROUP(3, VOFF, SOFF, BAR) \
  STREAM_GROUP(4, VOFF, SOFF, BAR) STREAM_GROUP(5, VOFF, SOFF, BAR) STREAM_GROUP(6, VOFF, SOFF, BAR) STREAM_GROUP(7, VOFF, SOFF, BAR)
#define STREAM_GROUP(G, VOFF, SOFF, ISK) \
  if (PD > 0 && (!(ISK) || (G) >= PK0)) pace<PA + ((ISK) ? 0 : PV) + (((ISK) ? 0 : 8) + (G)) * PD / 10>(org); \
  _Pragma("unroll") for (int i = 4 * (G); i < 4 * (G) + 4; ++i) d[i] = ld128<AUX_NT>(rkv, (VOFF), (int)(SOFF)); \
  __builtin_amdgcn_sched_barrier(0);

  if (__builtin_expect(w >= 4, 0)) {
  u32 tag_out;
  if (w < 8) {
    __builtin_amdgcn_s_setprio(2);
    const int g = lane >> 4, key = (lane >> 1) & 7;
    const bool hb = lane & 1;
    const int kvo = key * 512 + (2 * g + (lane & 1)) * 16;
    STREAM_LOADS(kc, kvo + (i & 3) * 128, ((blk0 + kw + 4 * (i >> 2)) & 511u) << 12, true);
    MV(3);
    ROLE_ENTRY();
    MK0(); MW(2); MW(3);
    flag_wait<2, 1>(L + F_Q, lane);
    ROLE_TAG();
    u32x4 qa[4];
#pragma unroll
    for (int i = 0; i < 4; ++i) qa[i] = *(LAS u32x4*)(L + L_Q + (lane & 1) * 128 + i * 32 + (2 * g + ((lane >> 1) & 1)) * 4);
    f32x4 ea, eb;
#pragma unroll
    for (int n = 0; n < 4; ++n) {
      f32x4 ca = {}, cb = {};
#pragma unroll
      for (int i = 0; i < 4; ++i) {
        ca = __builtin_amdgcn_mfma_f32_16x16x32_bf16(__builtin_bit_cast(bf16x8_t, qa[i]), __builtin_bit_cast(bf16x8_t, d[8 * n + i]), ca, 0, 0, 0);
        cb = __builtin_amdgcn_mfma_f32_16x16x32_bf16(__builtin_bit_cast(bf16x8_t, qa[i]), __builtin_bit_cast(bf16x8_t, d[8 * n + 4 + i]), cb, 0, 0, 0);
      }
#pragma unroll
      for (int k = 0; k < 4; ++k) { ea[k] = (n == 0 || g == n) ? ca[k] : ea[k]; eb[k] = (n == 0 || g == n) ? cb[k] : eb[k]; }
    }
    asm volatile("" :: "v"(code_touch));
    float sc[2];
#pragma unroll
    for (int hd = 0; hd < 2; ++hd) sc[hd] = rbf((hb ? eb[2 + hd] : ea[hd]) + dpp<XOR1>(hb ? ea[2 + hd] : eb[hd])) * 0.0625f;
    const float wm0 = mx32(mx16(row_max(sc[0]))), wm1 = mx32(mx16(row_max(sc[1])));
    const u32 tag16 = SET;
    CBAR();
    if (lane < 2) L[L_WM + kw * 2 + lane] = (asu(lane ? wm1 : wm0) & 0xffff0000u) | tag16;
    u32x4 ma, mb;
    for (u32 s = 0; s < SPIN_LDS; ++s) {
      CBAR();
      ma = *(LAS u32x4*)(L + L_WM) ^ tag16; mb = *(LAS u32x4*)(L + L_WM + 4) ^ tag16;
      if (none((((ma.x | ma.y | ma.z | ma.w) | (mb.x | mb.y | mb.z | mb.w)) & 0xffffu) != 0u)) break;
    }
    const float M0 = fmaxf(fmaxf(asf(ma.x), asf(ma.z)), fmaxf(asf(mb.x), asf(mb.z)));
    const float M1 = fmaxf(fmaxf(asf(ma.y), asf(ma.w)), fmaxf(asf(mb.y), asf(mb.w)));
    const float p0 = rbf(__builtin_amdgcn_exp2f((sc[0] - M0) * 1.4426950408889634f));
    const float p1 = rbf(__builtin_amdgcn_exp2f((sc[1] - M1) * 1.4426950408889634f));
    LAS u32* pp = L + L_P + key * 64 + kw + 4 * (2 * g + (lane & 1));
    pp[0] = asu(p0) | (asu(p0) >> 16);
    pp[32] = asu(p1) | (asu(p1) >> 16);
    float ls = tr32(p0, p1);
    ls = row_sum(tr16(ls, ls));
    if ((lane & 31) == 0) LF[L_WL + kw * 2 + (lane >> 5)] = ls;
    flag_set(L + F_P + kw);
    MK(2);

    if (kw == 0) {
      flag_wait<4, 0>(L + F_P, lane);
      if (lane < 2) {
        const float Ls = (LF[L_WL + lane] + LF[L_WL + 2 + lane]) + (LF[L_WL + 4 + lane] + LF[L_WL + 6 + lane]);
        st128(rws, ST_OFF + (((h * 2 + lane) * 2 + b) * 16 + sp) * 16, u32x4{asu(lane ? M1 : M0), tag, asu(Ls), tag});
      }
    } else if (kw == 1) {
      __builtin_amdgcn_s_setprio(3);
      const int s = lane >> 2, qd = lane & 3;
      const int so = ST_OFF + ((h * 2 + (j >> 4)) * 32 + s) * 16;
      const int oo = O1_OFF + (h * 32 + 2 * s) * O1_S + j * 64 + qd * 16;
      u32x4 s0, s1, a0, a1;
      flag_wait<4, 1>(L + F_VI, lane);
      for (u32 it = 0; it < SPIN_MEM; ++it) {
        CBAR();
        s0 = ld128<AUX_SC1>(rws, so); s1 = ld128<AUX_SC1>(rws, so + 256);
        if (none((s0.y != tag) | (s0.w != tag) | (s1.y != tag) | (s1.w != tag))) break;
        flag_wait<1, 1>(L + F_PUB, lane);
      }
      float wgt[2];
#pragma unroll
      for (int bb = 0; bb < 2; ++bb) {
        const float m = asf(bb ? s1.x : s0.x), l = asf(bb ? s1.z : s0.z);
        float mg = fmaxf(m, dpp<ROR8>(m)); mg = fmaxf(mg, dpp<ROR4>(mg)); mg = mx32(mx16(mg));
        const float e = __builtin_amdgcn_exp2f((m - mg) * 1.4426950408889634f);
        float lg = e * l; lg += dpp<ROR8>(lg); lg += dpp<ROR4>(lg); lg = tr16(lg, lg); lg = tr32(lg, lg);
        wgt[bb] = e * __builtin_amdgcn_rcpf(lg);
      }
      LAS u32x2* ow = (LAS u32x2*)(L + ((lane & 12) == 0 ? L_O + qd * 8 + (lane >> 4) * 2 : L_XN + 2 * lane));
      u32 tagv = SET;
      PIN(wgt[0]); PIN(wgt[1]); PIN(ow); PIN(tagv);
      flag_wait<1, 0>(L + F_PUB, lane);
      if (D1) __builtin_amdgcn_s_sleep(D1);
      if (DBL & 1) poll2(rws, oo, O1_S, tag8, a0, a1);
      else for (u32 it = 0; it < SPIN_MEM; ++it) {
        CBAR();
        a0 = ld128<AUX_SC1>(rws, oo); a1 = ld128<AUX_SC1>(rws, oo + O1_S);
        if (all_ok(untag4(a0, tag8) | untag4(a1, tag8))) break;
        if (SLP1) __builtin_amdgcn_s_sleep(SLP1);
      }
      const float v0 = tr32(asf(a0.x) * wgt[0], asf(a1.x) * wgt[1]), v1 = tr32(asf(a0.y) * wgt[0], asf(a1.y) * wgt[1]);
      const float v2 = tr32(asf(a0.z) * wgt[0], asf(a1.z) * wgt[1]), v3 = tr32(asf(a0.w) * wgt[0], asf(a1.w) * wgt[1]);
      float e0 = tr16(v0, v2), e1 = tr16(v1, v3);
      e0 += dpp<ROR8>(e0); e1 += dpp<ROR8>(e1);
      e0 += dpp<ROR4>(e0); e1 += dpp<ROR4>(e1);
      *ow = u32x2{pkbf(e0, e1), tagv};
      asm volatile("s_waitcnt vmcnt(0)" ::: "memory");
      MK(4);
    } else if (PRE) {
      const int po = kw == 2 ? O1_OFF + (h * 32 + j) * O1_S : O2_OFF + (h * 32 + j) * O2_S;
      const u32 t0 = ld32<AUX_SC1>(rws, lane * PSTR < (kw == 2 ? 2048 : PRE == 2 ? 0 : 3584) ? po + lane * PSTR : OOB);
      asm volatile("" :: "v"(t0));
    }
    tag_out = tag;
  } else {
    if (kw == 0 && lane < (L_END - L_WM) / 4) *(LAS u32x4*)(L + L_WM + 4 * lane) = u32x4{0u, 0u, 0u, 0u};
    LGKM0();
    __builtin_amdgcn_s_barrier();
    org = __builtin_amdgcn_s_memrealtime();
    __builtin_amdgcn_s_setprio(1);
    __builtin_amdgcn_sched_barrier(0);
    ROLE_ENTRY();
    STREAM_LOADS(vc, lane * 16 + kw * 1024, ((blk0 + i) & 511u) << 12, false);
    MV(4);
    flag_set(L + F_VI + kw);
    MK0();
    const int m = lane & 31, hf = lane >> 5;
    u32x4 msk;
#pragma unroll
    for (int r = 0; r < 4; ++r) msk[r] = (m < 16 && ((m & 7) >> 1) == r) ? ((m & 1) ? 0xffff0000u : 0x0000ffffu) : 0u;
    LAS f32x4* op = (LAS f32x4*)(LF + L_OP) + kw * 128 + 2 * m + hf;
    LAS f32x4* rp = (LAS f32x4*)(LF + L_OP) + lane;
    LAS u32* fp = L + F_V + kw;
    LAS u32* fr = L + F_V + (lane & 3);
    int o1o = O1_OFF + (h * 32 + j) * O1_S + lane * 16;
    PIN(op); PIN(rp); PIN(fp); PIN(fr); PIN(o1o);
    flag_wait<4, 1>(L + F_P, lane);
    ROLE_TAG();
    const LAS u32x4* pp = (const LAS u32x4*)(L + L_P + ((kw * 2 + hf) * 2 + ((m >> 3) & 1)) * 32);
    f32x16 acc = {};
#pragma unroll
    for (int g = 0; g < 8; ++g) {
      const u32x4 p4 = pp[g];
#pragma unroll
      for (int k = 0; k < 4; ++k) {
        const u32x4 a = msk & p4[k];
        acc = __builtin_amdgcn_mfma_f32_32x32x16_bf16(__builtin_bit_cast(bf16x8_t, a), __builtin_bit_cast(bf16x8_t, d[4 * g + k]), acc, 0, 0, 0);
      }
      __builtin_amdgcn_sched_barrier(0);
    }
    asm volatile("" :: "v"(code_touch));
    MV(6);
    op[0] = f32x4{acc[0], acc[1], acc[2], acc[3]};
    op[64] = f32x4{acc[4], acc[5], acc[6], acc[7]};
    flag_set(fp);
    const u32 fl = *fr;
    CBAR();
    const f32x4 o0 = (rp[0] + rp[128]) + (rp[256] + rp[384]);
    const f32x4 o1 = (rp[64] + rp[192]) + (rp[320] + rp[448]);
    u32x4 t0 = tag4(o0.x, o0.y, o0.z, o0.w, tag8), t1 = tag4(o1.x, o1.y, o1.z, o1.w, tag8);
    PIN(t0); PIN(t1);
    if (none(fl != SET)) {
      st128(rws, o1o, t0);
      st128(rws, o1o + 1024, t1);
      flag_set(L + F_PUB);
      MK(3);
    }
    MW(4); MW(6);
    tag_out = tag;
  }
  if (wg < 56 && (wg % 28) < ((ABL & 64) ? 16 : (ABL & 128) ? 8 : 28)) final_reduce(L, mkrs(ws, WS_BYTES), x, out, ws, wg, w, w - 4, lane, tag_out, tent);
  return;
  }

  __builtin_amdgcn_s_setprio(3);
  ROLE_ENTRY();
  const bool mlp = wg < 224;
  const int cb2 = wg % 28, ke = wg / 28;
  const int rr = lane >> 3, c8 = lane & 7;
  const rsrc_t rx = mkrs((const u16*)x, 2 * D * 2);
  const u32x4 xa = ld128<0>(rx, lane < 56 ? lane * 16 : OOB), xb = ld128<0>(rx, lane < 56 ? D * 2 + lane * 16 : OOB);
  const u32 epoch = ld32<AUX_SC1>(rws, EP_OFF);
  const int row0 = kw < 2 ? 112 * (j >> 3) + 64 * kw : 56 * ke;
  const int nrow = kw == 0 ? 64 : kw == 1 ? 48 : mlp ? 56 : 0;
  u32x4 wv[8];
  {
    const rsrc_t rw = mkrs(kw < 2 ? wq : kw == 2 ? wg_ : wu, kw < 2 ? ((ABL & 32) ? 0 : D * 4096 * 2) : ((ABL & 8) ? 0 : D * DF * 2));
    const int rs = kw < 2 ? 8192 : DF * 2;
    const int vw0 = nrow ? (row0 + 2 * rr) * rs + (kw < 2 ? (h * 512 + (j & 7) * 64) * 2 : cb2 * 128) + c8 * 16 : OOB;
    const int vw3 = 48 + 2 * rr < nrow ? vw0 : OOB;
#pragma unroll
    for (int i = 0; i < 8; ++i) wv[i] = ld128<AUX_NT>(rw, i < 6 ? vw0 : vw3, (16 * (i >> 1) + (i & 1)) * rs);
  }
  const int eo = lane < 8 ? QP_OFF + (h * 32 + j) * QP_S + lane * 64 : lane < 24 ? GP_OFF + wg * GP_S + (lane - 8) * 64 : ST_OFF + (wg & 127) * 64;
  const u32 box_touch = ld32<AUX_SC1>(rws, (!(ABL & 16) && kw == 1 && lane < 25) ? eo : OOB);
  __builtin_amdgcn_sched_barrier(0);
  __builtin_amdgcn_s_barrier();
  org = __builtin_amdgcn_s_memrealtime();
  __builtin_amdgcn_sched_barrier(0);
  MK0();

#pragma unroll
  for (int bb = 0; bb < 2; ++bb) {
    const u32x4 xv = bb ? xb : xa;
    float v[8];
#pragma unroll
    for (int k = 0; k < 4; ++k) { v[2 * k] = lo16(xv[k]); v[2 * k + 1] = hi16(xv[k]); }
    float s = 0.f;
#pragma unroll
    for (int k = 0; k < 8; ++k) s += v[k];
    const float mean = wave_sum(s) * (1.f / D);
    float q = 0.f;
#pragma unroll
    for (int k = 0; k < 8; ++k) { const float t = v[k] - mean; q += t * t; }
    q = lane < 56 ? q : 0.f;
    const float var = rbf(wave_sum(q) * (1.f / D));
    const float den = rbf(__builtin_amdgcn_sqrtf(rbf(var + 1e-5f))), rden = __builtin_amdgcn_rcpf(den), mb = rbf(mean);
#pragma unroll
    for (int k = 0; k < 8; ++k) { const float c = rbf(v[k] - mb), r = c * rden; v[k] = fmaf(fmaf(-r, den, c), rden, r); }
    u32x4 o;
#pragma unroll
    for (int k = 0; k < 4; ++k) o[k] = pkbf(v[2 * k], v[2 * k + 1]);
    if (lane < 56) *(LAS u32x4*)(L + L_XN + bb * 224 + lane * 4) = o;
    else if (bb == 1) *(LAS u32x4*)(L + L_XN + 224 + lane * 4) = u32x4{0u, 0u, 0u, 0u};
  }
  asm volatile("" :: "v"(code_touch));
  const u32 tag = __builtin_amdgcn_readfirstlane(epoch) + 1u, tag8 = tag & TMASK;
  if (kw == 0) L[L_TAG] = tag;
  if (kw == 3 && lane == 0) { const rsrc_t rt = mkrs(ws + TS_OFF, 8192); __builtin_amdgcn_raw_buffer_store_b32((u32)tent, rt, wg * 4, 0, AUX_SC1); __builtin_amdgcn_raw_buffer_store_b32((u32)org, rt, 1024 + wg * 4, 0, AUX_SC1); }
  MK(10);

  {
    float a0[8] = {}, a1[8] = {};
#pragma unroll
    for (int k = 0; k < 4; ++k) {
      const u32 x0 = L[L_XN + row0 / 2 + rr + 8 * k], x1 = L[L_XN + 224 + row0 / 2 + rr + 8 * k];
#pragma unroll
      for (int c = 0; c < 4; ++c) {
        const u32 pl = __builtin_amdgcn_perm(wv[2 * k + 1][c], wv[2 * k][c], 0x05040100u), ph = __builtin_amdgcn_perm(wv[2 * k + 1][c], wv[2 * k][c], 0x07060302u);
        a0[2 * c] = dot2(pl, x0, a0[2 * c]); a0[2 * c + 1] = dot2(ph, x0, a0[2 * c + 1]);
        a1[2 * c] = dot2(pl, x1, a1[2 * c]); a1[2 * c + 1] = dot2(ph, x1, a1[2 * c + 1]);
      }
    }
    asm volatile("" :: "v"(box_touch));
    MV(5);
    float t[8], u[4];
#pragma unroll
    for (int k = 0; k < 8; ++k) t[k] = tr32(a0[k], a1[k]);
#pragma unroll
    for (int k = 0; k < 4; ++k) { u[k] = tr16(t[k], t[k + 4]); u[k] += dpp<ROR8>(u[k]); }
    const int bb = lane >> 5, hf = (lane >> 4) & 1;
    LAS f32x4* qx = (LAS f32x4*)(LF + L_QX) + bb * 16 + c8 * 2 + hf;
    if (kw == 1) {
      if (!(lane & 8)) *qx = f32x4{u[0], u[1], u[2], u[3]};
      flag_set(L + F_QX);
    } else if (kw == 0) {
      flag_wait<1, 0>(L + F_QX, lane);
      const f32x4 o = *qx;
      if (!(ABL & 1) && !(lane & 8)) st128(rws, QP_OFF + (h * 32 + j) * QP_S + bb * 256 + hf * 128 + c8 * 16, tag4(u[0] + o.x, u[1] + o.y, u[2] + o.z, u[3] + o.w, tag8));
    } else if (!(ABL & 2) && mlp) {
      LAS u32x4* gx = (LAS u32x4*)(L + L_GX) + (kw - 2) * 32;
      if (!(lane & 8)) gx[c8 * 4 + bb * 2 + hf] = tag4(u[0], u[1], u[2], u[3], tag8);
      CBAR();
      if (lane < 32) st128(rws, GP_OFF + wg * GP_S + (lane >> 2) * 128 + (kw - 2) * 64 + (lane & 3) * 16, gx[lane]);
    }
  }

  if (kw < 2) {
    __builtin_amdgcn_s_sleep(QDELAY);
    const int qo = QP_OFF + (h * 32 + 4 * kw + 8 * (lane >> 4)) * QP_S + b * 256 + (lane & 15) * 16;
    u32x4 a[4];
    for (u32 s = 0; s < SPIN_MEM; ++s) {
      CBAR();
      if (ABL & 1) { a[0] = a[1] = a[2] = a[3] = xa; break; }
#pragma unroll
      for (int i = 0; i < 4; ++i) a[i] = ld128<AUX_SC1>(rws, qo, i * QP_S);
      if (all_ok((untag4(a[0], tag8) | untag4(a[1], tag8)) | (untag4(a[2], tag8) | untag4(a[3], tag8)))) break;
    }
#pragma unroll
    for (int i = 0; i < 2; ++i) {
      const u32x4 a0 = a[2 * i], a1 = a[2 * i + 1];
      const float v0 = tr32(asf(a0.x), asf(a1.x)), v1 = tr32(asf(a0.y), asf(a1.y)), v2 = tr32(asf(a0.z), asf(a1.z)), v3 = tr32(asf(a0.w), asf(a1.w));
      L[L_Q + (4 * kw + 2 * i + (lane >> 5)) * 32 + 4 * (lane & 7) + 2 * ((lane >> 3) & 1) + ((lane >> 4) & 1)] = pkbf(tr16(v0, v2), tr16(v1, v3));
    }
    flag_set(L + F_Q + kw);
    MK(1);
  } else if (kw == 2) {
    u32 h0 = 0u, h1 = 0u;
    if (mlp) {
      const int go = GP_OFF + (cb2 + 28 * rr) * GP_S + ke * 128 + c8 * 16;
      u32x4 a;
      flag_wait<2, 1>(L + F_Q, lane);
      for (u32 s = 0; s < SPIN_MEM; ++s) {
        CBAR();
        if (ABL & 2) { a = xa; break; }
        a = ld128<AUX_SC1>(rws, go);
        if (all_ok(untag4(a, tag8))) break;
        __builtin_amdgcn_s_sleep(16);
      }
      float hv[4];
#pragma unroll
      for (int k = 0; k < 4; ++k) {
        float g = asf(a[k]);
        g += dpp<ROR8>(g); g = tr16(g, g); g = tr32(g, g);
        g = rbf(g);
        const float uu = dpp<ROR4>(g);
        const float z = 0.7978845608028654f * (g + 0.044715f * g * g * g);
        const float th = 1.f - 2.f * __builtin_amdgcn_rcpf(1.f + __builtin_amdgcn_exp2f(z * 2.8853900817779268f));
        hv[k] = rbf(0.5f * g * (1.f + th)) * uu;
      }
      h0 = pkbf(hv[0], hv[1]); h1 = pkbf(hv[2], hv[3]);
    }
    if (lane < 4) {
      *(LAS u32x2*)(L + L_H + (2 * (lane & 1)) * 4 + (lane >> 1) * 2) = u32x2{h0, SET};
      *(LAS u32x2*)(L + L_H + (2 * (lane & 1) + 1) * 4 + (lane >> 1) * 2) = u32x2{h1, SET};
    }
    CBAR();
  }

  const int rg = lane >> 4, cl = lane & 15;
  const int co = kw * 128 + cl * 8;
  u32x4 wa[4], wb[2];
  if (LATE == 0) flag_wait<2, 1>(L + F_Q, lane);
  if (LATE >= 1) flag_wait<4, 1>(L + F_VI, lane);
  if (LATE == 3) flag_wait<1, 0>(L + F_PUB, lane);
  if (LATE == 4) pace<PA + PV + 16 * PD / 10 + LD>(org);
  if (LATE == 5) __builtin_amdgcn_s_sleep(LD);
  {
    const rsrc_t ro = mkrs(wo, (ABL & 4) ? 0 : 4096 * D * 2), rd = mkrs(wd, mlp && !(ABL & 4) ? DF * D * 2 : 0);
    const int vo = co < D ? (h * 512 + j * 16 + 4 * rg) * (D * 2) + co * 2 : OOB;
    const int vd = co < D ? (64 * cb2 + 8 * ke + 2 * rg) * (D * 2) + co * 2 : OOB;
#pragma unroll
    for (int i = 0; i < 2; ++i) wb[i] = ld128<AUX_NT>(rd, vd, i * D * 2);
#pragma unroll
    for (int i = 0; i < 4; ++i) wa[i] = ld128<AUX_NT>(ro, vo, i * D * 2);
  }
  __builtin_amdgcn_sched_barrier(0);
  float a0[8], a1[8];
  u32 pa[8], pb[8];
  {
    u32x4 hh;
    for (u32 s = 0; s < SPIN_LDS; ++s) {
      CBAR();
      hh = *(LAS u32x4*)(L + L_H + rg * 4);
      if (none((hh.y != SET) | (hh.w != SET))) break;
      __builtin_amdgcn_s_sleep(1);
    }
#pragma unroll
    for (int k = 0; k < 4; ++k) {
      const u32 pl = __builtin_amdgcn_perm(wb[1][k], wb[0][k], 0x05040100u), ph = __builtin_amdgcn_perm(wb[1][k], wb[0][k], 0x07060302u);
      a0[2 * k] = dot2(pl, hh.x, 0.f); a0[2 * k + 1] = dot2(ph, hh.x, 0.f);
      a1[2 * k] = dot2(pl, hh.z, 0.f); a1[2 * k + 1] = dot2(ph, hh.z, 0.f);
    }
#pragma unroll
    for (int k = 0; k < 4; ++k) {
      pa[2 * k] = __builtin_amdgcn_perm(wa[1][k], wa[0][k], 0x05040100u); pa[2 * k + 1] = __builtin_amdgcn_perm(wa[1][k], wa[0][k], 0x07060302u);
      pb[2 * k] = __builtin_amdgcn_perm(wa[3][k], wa[2][k], 0x05040100u); pb[2 * k + 1] = __builtin_amdgcn_perm(wa[3][k], wa[2][k], 0x07060302u);
    }
  }
#pragma unroll
  for (int k = 0; k < 8; ++k) asm volatile("" : "+v"(a0[k]), "+v"(a1[k]), "+v"(pa[k]), "+v"(pb[k]));
  int o2o = co < ((ABL & 64) ? 256 : (ABL & 128) ? 128 : D) ? O2_OFF + (h * 32 + j) * O2_S + (lane >> 5) * 1792 + kw * 512 + (((lane >> 4) & 1) * (kw == 3 ? 8 : 16) + cl) * 16 : OOB;
  LAS u32x4* orp = (LAS u32x4*)(L + L_O + rg * 8);
  PIN(o2o); PIN(orp);
  if (LATE == 0) flag_wait<4, 1>(L + F_VI, lane);
  {
    u32x4 oa, ob;
    for (u32 s = 0; s < SPIN_LDS; ++s) {
      CBAR();
      oa = orp[0]; ob = orp[1];
      if (none(((oa.y != SET) | (oa.w != SET)) | ((ob.y != SET) | (ob.w != SET)))) break;
    }
#pragma unroll
    for (int k = 0; k < 8; ++k) {
      a0[k] = dot2(pb[k], oa.z, dot2(pa[k], oa.x, a0[k]));
      a1[k] = dot2(pb[k], ob.z, dot2(pa[k], ob.x, a1[k]));
    }
    float t[8];
#pragma unroll
    for (int k = 0; k < 8; ++k) t[k] = tr32(a0[k], a1[k]);
    st128(rws, o2o, tag4(tr16(t[0], t[4]), tr16(t[1], t[5]), tr16(t[2], t[6]), tr16(t[3], t[7]), tag8));
  }
  flag_set(L + F_O2 + kw);
  MK(5);
}
}

extern "C" void kernel_launch(void* const* d_in, const int* in_sizes, int n_in, void* d_out, int out_size, void* d_ws, size_t ws_size,
                              hipStream_t stream) {
  fused_decode<<<256, 768, 0, stream>>>((const u16*)d_in[0], (const u16*)d_in[1], (const u16*)d_in[2], (const u16*)d_in[3], (const u16*)d_in[4],
                                        (const u16*)d_in[6], (const u16*)d_in[7], (const u16*)d_in[8], (u16*)d_out, (char*)d_ws + WS_OFF);
}
```

```cpp
#include <hip/hip_runtime.h>
#include <hip/hip_bf16.h>
#include <stdint.h>

typedef unsigned int u32;
typedef unsigned short u16;
typedef unsigned long long u64;
typedef u32 u32x2 __attribute__((ext_vector_type(2)));
typedef u32 u32x4 __attribute__((ext_vector_type(4)));
typedef float f32x4 __attribute__((ext_vector_type(4)));
typedef float f32x16 __attribute__((ext_vector_type(16)));
typedef __bf16 bf16x2_t __attribute__((ext_vector_type(2)));
typedef __bf16 bf16x8_t __attribute__((ext_vector_type(8)));
#define LAS __attribute__((address_space(3)))
typedef __amdgpu_buffer_rsrc_t rsrc_t;

#define STAMP 0
#define LATE 4
#define QDELAY 40
#define PRE 2
#define PSTR 64
#define ABL 0
#define SLP1 0
#define SLP2 0
#define PSTAG 5
#define DBL 0
#define PA 35
#define PD 620
#define PK0 2
#define D1 12
#define D2 10
#define PV 0
#define LD 0
#define PDV PD
#define STG 0
#define SR 3
#define SR2 0
#define GO 1
#define GB 5
#define TP1 0
#define TP2 0
#define PREAUX 16
#define STAUX 16
#define ROT 37
#define CODE_SPAN 13440
#define CTW 0
#define DIAG 0
#define AMP 1
#define WS_OFF 0

namespace {
constexpr int D = 448, SK = 32768, W = 4096, DH = 256, DF = 1792;
constexpr int AUX_NT = 2, AUX_SC1 = 16;
constexpr int OOB = 1 << 30;
constexpr u32 TMASK = 0xfu;

constexpr int QP_S = 768, GP_S = 1280, O1_S = 2304, O2_S = 3840;
constexpr int EP_OFF = 0;
constexpr int OR_OFF = 2048;
constexpr int ST_OFF = 4096;
constexpr int QP_OFF = ST_OFF + 8192;
constexpr int GP_OFF = QP_OFF + 256 * QP_S;
constexpr int O1_OFF = GP_OFF + 256 * GP_S;
constexpr int O2_OFF = O1_OFF + 256 * O1_S;
constexpr int WS_BYTES = O2_OFF + 256 * O2_S;
constexpr int TS_OFF = 4 << 20;

constexpr int L_XN = 0;
constexpr int L_QX = L_XN + 480;
constexpr int L_GX = L_QX + 128;
constexpr int L_Q = L_GX + 256;
constexpr int L_P = L_Q + 256;
constexpr int L_OP = L_P + 512;
constexpr int L_WM = L_OP + 2048;
constexpr int L_WL = L_WM + 8;
constexpr int L_H = L_WL + 8;
constexpr int L_O = L_H + 16;
constexpr int L_R = L_O + 32;
constexpr int F_P = L_R + 128;
constexpr int F_VI = F_P + 4;
constexpr int F_O2 = F_VI + 4;
constexpr int F_Q = F_O2 + 4;
constexpr int F_QX = F_Q + 2, F_PUB = F_QX + 1;
constexpr int F_V = F_PUB + 1;
constexpr int F_R = F_V + 4;
constexpr int L_TAG = F_R + 8;
constexpr int L_DG = L_TAG + 1;
constexpr int L_SH = L_TAG + 2, F_SH = L_TAG + 3;
constexpr int L_END = L_TAG + 4;
constexpr u32 SET = 1u;
static_assert((L_END - L_WM) % 4 == 0 && (L_END - L_WM) / 4 <= 64, "one 16-byte store per lane clears the flag area");

constexpr int T_LAST = PA + PV + (8 * PD + 7 * PDV) / 10;
constexpr u32 SPIN_LDS = 1u << 21, SPIN_MEM = 1u << 16;

__device__ __forceinline__ float asf(u32 v) { return __builtin_bit_cast(float, v); }
__device__ __forceinline__ u32 asu(float v) { return __builtin_bit_cast(u32, v); }
__device__ __forceinline__ float lo16(u32 v) { return asf(v << 16); }
__device__ __forceinline__ float hi16(u32 v) { return asf(v & 0xffff0000u); }
__device__ __forceinline__ u32 pkbf(float a, float b) {
  bf16x2_t r = {(__bf16)a, (__bf16)b};
  return __builtin_bit_cast(u32, r);
}
__device__ __forceinline__ float rbf(float a) { return (float)(__bf16)a; }
__device__ __forceinline__ float dot2(u32 a, u32 b, float c) {
  return __builtin_amdgcn_fdot2_f32_bf16(__builtin_bit_cast(bf16x2_t, a), __builtin_bit_cast(bf16x2_t, b), c, false);
}
template <int CTRL> __device__ __forceinline__ float dpp(float x) {
  return asf(__builtin_amdgcn_mov_dpp((int)asu(x), CTRL, 0xf, 0xf, true));
}
constexpr int XOR1 = 0xB1, XOR2 = 0x4E, ROR8 = 0x128, ROR4 = 0x124;
__device__ __forceinline__ float tr32(float a, float b) {
  auto r = __builtin_amdgcn_permlane32_swap(asu(a), asu(b), false, false);
  return asf(r[0]) + asf(r[1]);
}
__device__ __forceinline__ float tr16(float a, float b) {
  auto r = __builtin_amdgcn_permlane16_swap(asu(a), asu(b), false, false);
  return asf(r[0]) + asf(r[1]);
}
__device__ __forceinline__ float mx32(float a) {
  auto r = __builtin_amdgcn_permlane32_swap(asu(a), asu(a), false, false);
  return fmaxf(asf(r[0]), asf(r[1]));
}
__device__ __forceinline__ float mx16(float a) {
  auto r = __builtin_amdgcn_permlane16_swap(asu(a), asu(a), false, false);
  return fmaxf(asf(r[0]), asf(r[1]));
}
__device__ __forceinline__ float row_sum(float v) { v += dpp<ROR8>(v); v += dpp<ROR4>(v); v += dpp<XOR2>(v); v += dpp<XOR1>(v); return v; }
__device__ __forceinline__ float row_max(float v) {
  v = fmaxf(v, dpp<ROR8>(v)); v = fmaxf(v, dpp<ROR4>(v)); v = fmaxf(v, dpp<XOR2>(v)); v = fmaxf(v, dpp<XOR1>(v)); return v;
}
__device__ __forceinline__ float wave_sum(float v) { v = row_sum(v); v = tr16(v, v); return tr32(v, v); }

__device__ __forceinline__ rsrc_t mkrs(const void* p, u32 bytes) { return __builtin_amdgcn_make_buffer_rsrc((void*)p, 0, bytes, 0x00020000); }
template <int AUX> __device__ __forceinline__ u32x4 ld128(rsrc_t r, int voff, int soff = 0) { return __builtin_amdgcn_raw_buffer_load_b128(r, voff, soff, AUX); }
template <int AUX> __device__ __forceinline__ u32 ld32(rsrc_t r, int voff) { return __builtin_amdgcn_raw_buffer_load_b32(r, voff, 0, AUX); }
__device__ __forceinline__ void st128(rsrc_t r, int voff, u32x4 v) { __builtin_amdgcn_raw_buffer_store_b128(v, r, voff, 0, STAUX); }
__device__ __forceinline__ u32 tagw(float v, u32 tag4) { return (asu(v) & ~TMASK) | tag4; }
__device__ __forceinline__ u32x4 tag4(float a, float b, float c, float d, u32 t) { return u32x4{tagw(a, t), tagw(b, t), tagw(c, t), tagw(d, t)}; }
__device__ __forceinline__ u32 untag4(u32x4& v, u32 t) { v.x ^= t; v.y ^= t; v.z ^= t; v.w ^= t; return v.x | v.y | v.z | v.w; }
__device__ __forceinline__ bool none(bool bad) { return __builtin_amdgcn_ballot_w64(bad) == 0ull; }
__device__ __forceinline__ bool all_ok(u32 bad) { return none((bad & TMASK) != 0u); }

#define CBAR() asm volatile("" ::: "memory")
#define PIN(v) asm volatile("" : "+v"(v))
#define LGKM0() asm volatile("s_waitcnt lgkmcnt(0)" ::: "memory")
template <int N, int SLEEP> __device__ __forceinline__ void flag_wait(LAS u32* p, int lane) {
  u32 v, cnt;
  const u32 addr = (u32)(uintptr_t)(p + (lane & (N - 1)));
  asm volatile("s_mov_b32 %1, %4\n"
               "1:\n\t"
               "ds_read_b32 %0, %2\n\t"
               "s_waitcnt lgkmcnt(0)\n\t"
               "v_cmp_ne_u32 vcc, %3, %0\n\t"
               "s_cbranch_vccz 2f\n\t"
               "s_sleep %5\n\t"
               "s_sub_u32 %1, %1, 1\n\t"
               "s_cbranch_scc0 1b\n"
               "2:"
               : "=&v"(v), "=&s"(cnt) : "v"(addr), "i"(SET), "i"(SPIN_LDS), "i"(SLEEP) : "vcc", "scc", "memory");
}
template <int DUE> __device__ __forceinline__ void pace(u64 org) {
  asm volatile("1:\n\t"
               "s_memrealtime vcc\n\t"
               "s_waitcnt lgkmcnt(0)\n\t"
               "s_sub_u32 vcc_lo, vcc_lo, %0\n\t"
               "s_cmp_lt_i32 vcc_lo, %1\n\t"
               "s_cbranch_scc0 2f\n\t"
               "s_sleep 1\n\t"
               "s_branch 1b\n"
               "2:"
               :: "s"((u32)org), "i"(DUE), "s"(org) : "vcc", "scc", "memory");
}
__device__ __forceinline__ void flag_set(LAS u32* p, u32 set = SET) { CBAR(); *p = set; CBAR(); }

#define MK(k)
#define MV(k)
#define MW(k)
#define NV(k)
#define NW(k)
#define NC(k, c)
#define MK0()

__device__ __forceinline__ void final_reduce(LAS u32* L, rsrc_t rws, const u16* x, u16* out, char* ws, int wg, int w, int grp, int lane, u32 tag, u64 org, u64 tent) {
  LAS float* const LF = (LAS float*)L;
  __builtin_amdgcn_s_setprio(3);
  const u32 tag8 = tag & TMASK;
  const int rb = wg / 28, cb = wg % 28, qd = lane & 3;
  const int pi = cb & 7, ph = cb < 24 ? pi >> 2 : pi >> 1, pg = cb < 24 ? pi & 3 : pi & 1;
  const int col = rb * D + 128 * (cb >> 3) + 8 * (4 * pg + ((lane >> 2) & 3)) + 4 * ph + (lane & 3);
  float xres = lo16(x[col]);
  int oo = O2_OFF + (grp * 32 + (lane >> 2)) * O2_S + rb * 1792 + cb * 64 + qd * 16;
  LAS float* wp = LF + ((lane & 12) == 0 ? L_R + (4 * qd + (lane >> 4)) * 8 + grp : L_XN + 128 + lane);
  LAS u32* fp = L + F_R + grp;
  LAS u32* fr = L + F_R + (lane & 7);
  LAS f32x4* rp = (LAS f32x4*)(LF + L_R + (lane & 15) * 8);
  int so = lane < 16 ? col * 2 : OOB;
  u32 tagv = SET;
  const rsrc_t ro = mkrs(out, 2 * D * 2);
  PIN(xres); PIN(oo); PIN(wp); PIN(fp); PIN(fr); PIN(rp); PIN(so); PIN(tagv);
  flag_wait<1, 1>(L + F_PUB, lane);
  flag_wait<4, 0>(L + F_O2, lane);
  if (D2) __builtin_amdgcn_s_sleep(D2);
  if (TP2) pace<T_LAST + TP2>(org);
  u32x4 a0, a1;
  u32 np2 = 0;
  for (u32 s = 0; s < SPIN_MEM; ++s) {
    CBAR();
    ++np2;
    a0 = ld128<AUX_SC1>(rws, oo); a1 = ld128<AUX_SC1>(rws, oo, 16 * O2_S);
    if (all_ok(untag4(a0, tag8) | untag4(a1, tag8))) break;
    if (SLP2) __builtin_amdgcn_s_sleep(SLP2);
  }
  MK(6);
  NV(4);
  float f = tr16(tr32(asf(a0.x) + asf(a1.x), asf(a0.z) + asf(a1.z)), tr32(asf(a0.y) + asf(a1.y), asf(a0.w) + asf(a1.w)));
  f += dpp<ROR8>(f); f += dpp<ROR4>(f);
  *wp = f;
  flag_set(fp, tagv);
  u32 fl = *fr;
  CBAR();
  const f32x4 r0 = rp[0], r1 = rp[1];
  u32 res = pkbf(xres + (((r0.x + r0.y) + (r0.z + r0.w)) + ((r1.x + r1.y) + (r1.z + r1.w))), 0.f);
  PIN(res);
  if (none(fl != SET)) {
    __builtin_amdgcn_raw_buffer_store_b16((u16)res, ro, so, 0, 0);
    if (wg == 0 && lane == 0) __builtin_amdgcn_raw_buffer_store_b32(tag, rws, EP_OFF, 0, AUX_SC1);
    if (wg == 0) {
      const u64 now = __builtin_amdgcn_s_memrealtime();
      u64 ev = DIAG == 0 ? tent : DIAG == 7 ? now : tent + __builtin_amdgcn_readfirstlane(L[L_DG]);
      const u64 until = tent + 3000 + AMP * (ev - tent);
      while (__builtin_amdgcn_s_memrealtime() < until) __builtin_amdgcn_s_sleep(1);
    }
    MK(7);
  }
  NW(4); NC(6, np2);
}

__global__ void __launch_bounds__(768, 3)
fused_decode(const u16* __restrict__ x, const u16* __restrict__ kc, const u16* __restrict__ vc, const u16* __restrict__ wq,
             const u16* __restrict__ wo, const u16* __restrict__ wg_, const u16* __restrict__ wu, const u16* __restrict__ wd,
             u16* __restrict__ out, char* __restrict__ ws) {
  __shared__ __attribute__((aligned(16))) u32 smem[L_END];
  LAS u32* const L = (LAS u32*)smem;
  LAS float* const LF = (LAS float*)smem;
  const int wg = blockIdx.x, h = wg & 7, j = wg >> 3, b = j & 1, sp = j >> 1, reg = wg & 15;
  const int w = __builtin_amdgcn_readfirstlane(threadIdx.x >> 6), lane = threadIdx.x & 63, kw = w & 3;
  const u64 tent = __builtin_amdgcn_s_memrealtime();
#undef MK
#define MK(k) do { if (DIAG == (k)) { const u32 dt_ = (u32)(__builtin_amdgcn_s_memrealtime() - tent); if (lane == 0) (void)__hip_atomic_fetch_max(L + L_DG, dt_, __ATOMIC_RELAXED, __HIP_MEMORY_SCOPE_WORKGROUP); } } while (0)
  asm volatile("s_load_dword s94, %0, 0x0\n\ts_load_dword s95, %0, 0x40" :: "s"(__builtin_amdgcn_kernarg_segment_ptr()) : "s94", "s95");
  u64 pc0;
  asm volatile("s_getpc_b64 %0" : "=s"(pc0));
  const u32 code_touch = ld32<0>(mkrs((const char*)(pc0 & ~255ull), w == CTW ? CODE_SPAN : 0), lane < 5 ? j * 640 + lane * 128 : OOB);
  __builtin_amdgcn_sched_barrier(0);
#define ROLE_ENTRY() const rsrc_t rws = mkrs(ws, WS_BYTES)
#define ROLE_TAG() const u32 tag = __builtin_amdgcn_readfirstlane(L[L_TAG]), tag8 = tag & TMASK
  const u32 blk0 = sp * 32 + reg * ROT;
  const u32 srot = sp * SR + reg * SR2;
  u64 org;
#define STREAM_LOADS(SRC, VOFF, SOFF, BAR) \
  const rsrc_t rkv = mkrs((SRC) + ((size_t)reg * SK + (SK - W)) * DH, W * DH * 2); \
  u32x4 d[32]; \
  if (BAR) { __builtin_amdgcn_sched_barrier(0); __builtin_amdgcn_s_barrier(); org = __builtin_amdgcn_s_memrealtime(); __builtin_amdgcn_sched_barrier(0); } \
  MV(2); \
  STREAM_GROUP(0, VOFF, SOFF, BAR) STREAM_GROUP(1, VOFF, SOFF, BAR) STREAM_GROUP(2, VOFF, SOFF, BAR) STREAM_GROUP(3, VOFF, SOFF, BAR) \
  STREAM_GROUP(4, VOFF, SOFF, BAR) STREAM_GROUP(5, VOFF, SOFF, BAR) STREAM_GROUP(6, VOFF, SOFF, BAR) STREAM_GROUP(7, VOFF, SOFF, BAR)
#define STREAM_GROUP(G, VOFF, SOFF, ISK) \
  if (PD > 0 && (!(ISK) || (G) >= PK0)) pace<PA + ((ISK) ? (G) * PD / 10 : PV + (8 * PD + (G) * PDV) / 10)>(org + kw * STG); \
  _Pragma("unroll") for (int i = 4 * (G); i < 4 * (G) + 4; ++i) d[i] = ld128<AUX_NT>(rkv, (VOFF), (int)(SOFF)); \
  __builtin_amdgcn_sched_barrier(0);

  if (__builtin_expect(w >= 4, 0)) {
  u32 tag_out;
  if (w < 8) {
    __builtin_amdgcn_s_setprio(2);
    const int g = lane >> 4, key = (lane >> 1) & 7;
    const bool hb = lane & 1;
    const int kvo = key * 512 + (2 * g + (lane & 1)) * 16;
    STREAM_LOADS(kc, kvo + (i & 3) * 128, ((blk0 + ((kw + 4 * (i >> 2) + srot) & 31u)) & 511u) << 12, true);
    MV(3);
    ROLE_ENTRY();
    MK0(); MW(2); MW(3);
    flag_wait<2, 1>(L + F_Q, lane);
    ROLE_TAG();
    u32x4 qa[4];
#pragma unroll
    for (int i = 0; i < 4; ++i) qa[i] = *(LAS u32x4*)(L + L_Q + (lane & 1) * 128 + i * 32 + (2 * g + ((lane >> 1) & 1)) * 4);
    f32x4 ea, eb;
#pragma unroll
    for (int n = 0; n < 4; ++n) {
      f32x4 ca = {}, cb = {};
#pragma unroll
      for (int i = 0; i < 4; ++i) {
        ca = __builtin_amdgcn_mfma_f32_16x16x32_bf16(__builtin_bit_cast(bf16x8_t, qa[i]), __builtin_bit_cast(bf16x8_t, d[8 * n + i]), ca, 0, 0, 0);
        cb = __builtin_amdgcn_mfma_f32_16x16x32_bf16(__builtin_bit_cast(bf16x8_t, qa[i]), __builtin_bit_cast(bf16x8_t, d[8 * n + 4 + i]), cb, 0, 0, 0);
      }
#pragma unroll
      for (int k = 0; k < 4; ++k) { ea[k] = (n == 0 || g == n) ? ca[k] : ea[k]; eb[k] = (n == 0 || g == n) ? cb[k] : eb[k]; }
    }
    asm volatile("" :: "v"(code_touch));
    float sc[2];
#pragma unroll
    for (int hd = 0; hd < 2; ++hd) sc[hd] = rbf((hb ? eb[2 + hd] : ea[hd]) + dpp<XOR1>(hb ? ea[2 + hd] : eb[hd])) * 0.0625f;
    const float wm0 = mx32(mx16(row_max(sc[0]))), wm1 = mx32(mx16(row_max(sc[1])));
    const u32 tag16 = SET;
    CBAR();
    if (lane < 2) L[L_WM + kw * 2 + lane] = (asu(lane ? wm1 : wm0) & 0xffff0000u) | tag16;
    u32x4 ma, mb;
    for (u32 s = 0; s < SPIN_LDS; ++s) {
      CBAR();
      ma = *(LAS u32x4*)(L + L_WM) ^ tag16; mb = *(LAS u32x4*)(L + L_WM + 4) ^ tag16;
      if (none((((ma.x | ma.y | ma.z | ma.w) | (mb.x | mb.y | mb.z | mb.w)) & 0xffffu) != 0u)) break;
    }
    const float M0 = fmaxf(fmaxf(asf(ma.x), asf(ma.z)), fmaxf(asf(mb.x), asf(mb.z)));
    const float M1 = fmaxf(fmaxf(asf(ma.y), asf(ma.w)), fmaxf(asf(mb.y), asf(mb.w)));
    const float p0 = rbf(__builtin_amdgcn_exp2f((sc[0] - M0) * 1.4426950408889634f));
    const float p1 = rbf(__builtin_amdgcn_exp2f((sc[1] - M1) * 1.4426950408889634f));
    LAS u32* pp = L + L_P + key * 64 + kw + 4 * (2 * g + (lane & 1));
    pp[0] = asu(p0) | (asu(p0) >> 16);
    pp[32] = asu(p1) | (asu(p1) >> 16);
    float ls = tr32(p0, p1);
    ls = row_sum(tr16(ls, ls));
    if ((lane & 31) == 0) LF[L_WL + kw * 2 + (lane >> 5)] = ls;
    flag_set(L + F_P + kw);
    MK(2);

    if (kw == 0) {
      flag_wait<4, 0>(L + F_P, lane);
      if (lane < 2) {
        const float Ls = (LF[L_WL + lane] + LF[L_WL + 2 + lane]) + (LF[L_WL + 4 + lane] + LF[L_WL + 6 + lane]);
        st128(rws, ST_OFF + (((h * 2 + lane) * 2 + b) * 16 + sp) * 16, u32x4{asu(lane ? M1 : M0), tag, asu(Ls), tag});
      }
    } else if (kw == 1) {
      __builtin_amdgcn_s_setprio(3);
      const int s = lane >> 2, qd = lane & 3;
      const int so = ST_OFF + ((h * 2 + (j >> 4)) * 32 + s) * 16;
      const int oo = O1_OFF + (h * 32 + 2 * s) * O1_S + j * 64 + qd * 16;
      u32x4 s0, s1, a0, a1;
      flag_wait<4, 1>(L + F_VI, lane);
      for (u32 it = 0; it < SPIN_MEM; ++it) {
        CBAR();
        s0 = ld128<AUX_SC1>(rws, so); s1 = ld128<AUX_SC1>(rws, so + 256);
        if (none((s0.y != tag) | (s0.w != tag) | (s1.y != tag) | (s1.w != tag))) break;
        flag_wait<1, 1>(L + F_PUB, lane);
      }
      float wgt[2];
#pragma unroll
      for (int bb = 0; bb < 2; ++bb) {
        const float m = asf(bb ? s1.x : s0.x), l = asf(bb ? s1.z : s0.z);
        float mg = fmaxf(m, dpp<ROR8>(m)); mg = fmaxf(mg, dpp<ROR4>(mg)); mg = mx32(mx16(mg));
        const float e = __builtin_amdgcn_exp2f((m - mg) * 1.4426950408889634f);
        float lg = e * l; lg += dpp<ROR8>(lg); lg += dpp<ROR4>(lg); lg = tr16(lg, lg); lg = tr32(lg, lg);
        wgt[bb] = e * __builtin_amdgcn_rcpf(lg);
      }
      LAS u32x2* ow = (LAS u32x2*)(L + ((lane & 12) == 0 ? L_O + qd * 8 + (lane >> 4) * 2 : L_XN + 2 * lane));
      u32 tagv = SET;
      PIN(wgt[0]); PIN(wgt[1]); PIN(ow); PIN(tagv);
      if (GO) org += (u64)(long long)(int)__builtin_amdgcn_readfirstlane(L[L_SH]);
      flag_wait<1, 0>(L + F_PUB, lane);
      if (D1) __builtin_amdgcn_s_sleep(D1);
      if (TP1) pace<T_LAST + TP1>(org);
      u32 np1 = 0;
      for (u32 it = 0; it < SPIN_MEM; ++it) {
        CBAR();
        ++np1;
        a0 = ld128<AUX_SC1>(rws, oo); a1 = ld128<AUX_SC1>(rws, oo + O1_S);
        if (all_ok(untag4(a0, tag8) | untag4(a1, tag8))) break;
        if (SLP1) __builtin_amdgcn_s_sleep(SLP1);
      }
      NV(2);
      const float v0 = tr32(asf(a0.x) * wgt[0], asf(a1.x) * wgt[1]), v1 = tr32(asf(a0.y) * wgt[0], asf(a1.y) * wgt[1]);
      const float v2 = tr32(asf(a0.z) * wgt[0], asf(a1.z) * wgt[1]), v3 = tr32(asf(a0.w) * wgt[0], asf(a1.w) * wgt[1]);
      float e0 = tr16(v0, v2), e1 = tr16(v1, v3);
      e0 += dpp<ROR8>(e0); e1 += dpp<ROR8>(e1);
      e0 += dpp<ROR4>(e0); e1 += dpp<ROR4>(e1);
      *ow = u32x2{pkbf(e0, e1), tagv};
      asm volatile("s_waitcnt vmcnt(0)" ::: "memory");
      MK(4); NW(2); NC(5, np1);
    }
    if (GO != 0 && TP2 != 0 && kw != 1) { flag_wait<1, 4>(L + F_SH, lane); org += (u64)(long long)(int)__builtin_amdgcn_readfirstlane(L[L_SH]); }
    if (PRE && PRE < 3 && kw >= 2) {
      const int po = kw == 2 ? O1_OFF + (h * 32 + j) * O1_S : O2_OFF + (h * 32 + j) * O2_S;
      const u32 t0 = ld32<AUX_SC1>(rws, lane * PSTR < (kw == 2 ? 2048 : PRE == 2 ? 0 : 3584) ? po + lane * PSTR : OOB);
      asm volatile("" :: "v"(t0));
    }
    if (PRE >= 3 && kw >= 2) {
      u32 t0 = 0u;
      if (kw == 2) t0 = ld32<PREAUX>(rws, lane < 32 ? O1_OFF + (h * 32 + lane) * O1_S + j * 64 : OOB);
      else if (PRE == 4 && wg < 56) {
        const int po = O2_OFF + lane * O2_S + (wg / 28) * 1792 + (wg % 28) * 64;
#pragma unroll
        for (int i = 0; i < 4; ++i) t0 |= __builtin_amdgcn_raw_buffer_load_b32(rws, po, i * 64 * O2_S, PREAUX);
      }
      asm volatile("" :: "v"(t0));
    }
    tag_out = tag;
  } else {
    if (kw == 0 && lane < (L_END - L_WM) / 4) *(LAS u32x4*)(L + L_WM + 4 * lane) = u32x4{0u, 0u, 0u, 0u};
    LGKM0();
    __builtin_amdgcn_s_barrier();
    org = __builtin_amdgcn_s_memrealtime();
    __builtin_amdgcn_s_setprio(1);
    __builtin_amdgcn_sched_barrier(0);
    ROLE_ENTRY();
    if (GO) { flag_wait<1, 4>(L + F_SH, lane); org += (u64)(long long)(int)__builtin_amdgcn_readfirstlane(L[L_SH]); }
    STREAM_LOADS(vc, lane * 16 + kw * 1024, ((blk0 + ((i + srot) & 31u)) & 511u) << 12, false);
    MV(4);
    flag_set(L + F_VI + kw);
    MK0();
    const int m = lane & 31, hf = lane >> 5;
    u32x4 msk;
#pragma unroll
    for (int r = 0; r < 4; ++r) msk[r] = (m < 16 && ((m & 7) >> 1) == r) ? ((m & 1) ? 0xffff0000u : 0x0000ffffu) : 0u;
    LAS f32x4* op = (LAS f32x4*)(LF + L_OP) + kw * 128 + 2 * m + hf;
    LAS f32x4* rp = (LAS f32x4*)(LF + L_OP) + lane;
    LAS u32* fp = L + F_V + kw;
    LAS u32* fr = L + F_V + (lane & 3);
    int o1o = O1_OFF + (h * 32 + j) * O1_S + lane * 16;
    PIN(op); PIN(rp); PIN(fp); PIN(fr); PIN(o1o);
    flag_wait<4, 1>(L + F_P, lane);
    ROLE_TAG();
    const LAS u32x4* pp = (const LAS u32x4*)(L + L_P + ((kw * 2 + hf) * 2 + ((m >> 3) & 1)) * 32);
    f32x16 acc = {};
#pragma unroll
    for (int g = 0; g < 8; ++g) {
      const u32x4 p4 = pp[g];
#pragma unroll
      for (int k = 0; k < 4; ++k) {
        const u32x4 a = msk & p4[k];
        acc = __builtin_amdgcn_mfma_f32_32x32x16_bf16(__builtin_bit_cast(bf16x8_t, a), __builtin_bit_cast(bf16x8_t, d[4 * g + k]), acc, 0, 0, 0);
      }
      __builtin_amdgcn_sched_barrier(0);
    }
    asm volatile("" :: "v"(code_touch));
    MV(6); NV(1);
    op[0] = f32x4{acc[0], acc[1], acc[2], acc[3]};
    op[64] = f32x4{acc[4], acc[5], acc[6], acc[7]};
    flag_set(fp);
    const u32 fl = *fr;
    CBAR();
    const f32x4 o0 = (rp[0] + rp[128]) + (rp[256] + rp[384]);
    const f32x4 o1 = (rp[64] + rp[192]) + (rp[320] + rp[448]);
    u32x4 t0 = tag4(o0.x, o0.y, o0.z, o0.w, tag8), t1 = tag4(o1.x, o1.y, o1.z, o1.w, tag8);
    PIN(t0); PIN(t1);
    if (none(fl != SET)) {
      st128(rws, o1o, t0);
      st128(rws, o1o + 1024, t1);
      flag_set(L + F_PUB);
      MK(3);
    }
    MW(4); MW(6); NW(1);
    tag_out = tag;
  }
  if (wg < 56 && (wg % 28) < ((ABL & 64) ? 16 : (ABL & 128) ? 8 : 28)) final_reduce(L, mkrs(ws, WS_BYTES), x, out, ws, wg, w, w - 4, lane, tag_out, org, tent);
  return;
  }

  __builtin_amdgcn_s_setprio(3);
  ROLE_ENTRY();
  const bool mlp = wg < 224;
  const int cb2 = wg % 28, ke = wg / 28;
  const int rr = lane >> 3, c8 = lane & 7;
  const rsrc_t rx = mkrs((const u16*)x, 2 * D * 2);
  const u32x4 xa = ld128<0>(rx, lane < 56 ? lane * 16 : OOB), xb = ld128<0>(rx, lane < 56 ? D * 2 + lane * 16 : OOB);
  const u32 epoch = ld32<AUX_SC1>(rws, EP_OFF);
  const int row0 = kw < 2 ? 112 * (j >> 3) + 64 * kw : 56 * ke;
  const int nrow = kw == 0 ? 64 : kw == 1 ? 48 : mlp ? 56 : 0;
  u32x4 wv[8];
  {
    const rsrc_t rw = mkrs(kw < 2 ? wq : kw == 2 ? wg_ : wu, kw < 2 ? ((ABL & 32) ? 0 : D * 4096 * 2) : ((ABL & 8) ? 0 : D * DF * 2));
    const int rs = kw < 2 ? 8192 : DF * 2;
    const int vw0 = nrow ? (row0 + 2 * rr) * rs + (kw < 2 ? (h * 512 + (j & 7) * 64) * 2 : cb2 * 128) + c8 * 16 : OOB;
    const int vw3 = 48 + 2 * rr < nrow ? vw0 : OOB;
#pragma unroll
    for (int i = 0; i < 8; ++i) wv[i] = ld128<AUX_NT>(rw, i < 6 ? vw0 : vw3, (16 * (i >> 1) + (i & 1)) * rs);
  }
  const int eo = lane < 8 ? QP_OFF + (h * 32 + j) * QP_S + lane * 64 : lane < 24 ? GP_OFF + wg * GP_S + (lane - 8) * 64 : ST_OFF + (wg & 127) * 64;
  const u32 box_touch = ld32<AUX_SC1>(rws, (!(ABL & 16) && kw == 1 && lane < 25) ? eo : OOB);
  __builtin_amdgcn_sched_barrier(0);
  __builtin_amdgcn_s_barrier();
  org = __builtin_amdgcn_s_memrealtime();
  __builtin_amdgcn_sched_barrier(0);
  MK0();

#pragma unroll
  for (int bb = 0; bb < 2; ++bb) {
    const u32x4 xv = bb ? xb : xa;
    float v[8];
#pragma unroll
    for (int k = 0; k < 4; ++k) { v[2 * k] = lo16(xv[k]); v[2 * k + 1] = hi16(xv[k]); }
    float s = 0.f;
#pragma unroll
    for (int k = 0; k < 8; ++k) s += v[k];
    const float mean = wave_sum(s) * (1.f / D);
    float q = 0.f;
#pragma unroll
    for (int k = 0; k < 8; ++k) { const float t = v[k] - mean; q += t * t; }
    q = lane < 56 ? q : 0.f;
    const float var = rbf(wave_sum(q) * (1.f / D));
    const float den = rbf(__builtin_amdgcn_sqrtf(rbf(var + 1e-5f))), rden = __builtin_amdgcn_rcpf(den), mb = rbf(mean);
#pragma unroll
    for (int k = 0; k < 8; ++k) { const float c = rbf(v[k] - mb), r = c * rden; v[k] = fmaf(fmaf(-r, den, c), rden, r); }
    u32x4 o;
#pragma unroll
    for (int k = 0; k < 4; ++k) o[k] = pkbf(v[2 * k], v[2 * k + 1]);
    if (lane < 56) *(LAS u32x4*)(L + L_XN + bb * 224 + lane * 4) = o;
    else if (bb == 1) *(LAS u32x4*)(L + L_XN + 224 + lane * 4) = u32x4{0u, 0u, 0u, 0u};
  }
  asm volatile("" :: "v"(code_touch));
  const u32 tag = __builtin_amdgcn_readfirstlane(epoch) + 1u, tag8 = tag & TMASK;
  if (kw == 0) L[L_TAG] = tag;
  if (GO && kw == 3 && wg < 8 && lane == 0) __builtin_amdgcn_raw_buffer_store_b64(u32x2{(u32)org, tag}, rws, OR_OFF + wg * 8, 0, AUX_SC1);
  MK(10);

  {
    float a0[8] = {}, a1[8] = {};
#pragma unroll
    for (int k = 0; k < 4; ++k) {
      const u32 x0 = L[L_XN + row0 / 2 + rr + 8 * k], x1 = L[L_XN + 224 + row0 / 2 + rr + 8 * k];
#pragma unroll
      for (int c = 0; c < 4; ++c) {
        const u32 pl = __builtin_amdgcn_perm(wv[2 * k + 1][c], wv[2 * k][c], 0x05040100u), ph = __builtin_amdgcn_perm(wv[2 * k + 1][c], wv[2 * k][c], 0x07060302u);
        a0[2 * c] = dot2(pl, x0, a0[2 * c]); a0[2 * c + 1] = dot2(ph, x0, a0[2 * c + 1]);
        a1[2 * c] = dot2(pl, x1, a1[2 * c]); a1[2 * c + 1] = dot2(ph, x1, a1[2 * c + 1]);
      }
    }
    asm volatile("" :: "v"(box_touch));
    MV(5);
    float t[8], u[4];
#pragma unroll
    for (int k = 0; k < 8; ++k) t[k] = tr32(a0[k], a1[k]);
#pragma unroll
    for (int k = 0; k < 4; ++k) { u[k] = tr16(t[k], t[k + 4]); u[k] += dpp<ROR8>(u[k]); }
    const int bb = lane >> 5, hf = (lane >> 4) & 1;
    LAS f32x4* qx = (LAS f32x4*)(LF + L_QX) + bb * 16 + c8 * 2 + hf;
    if (kw == 1) {
      if (!(lane & 8)) *qx = f32x4{u[0], u[1], u[2], u[3]};
      flag_set(L + F_QX);
    } else if (kw == 0) {
      flag_wait<1, 0>(L + F_QX, lane);
      const f32x4 o = *qx;
      if (!(ABL & 1) && !(lane & 8)) st128(rws, QP_OFF + (h * 32 + j) * QP_S + bb * 256 + hf * 128 + c8 * 16, tag4(u[0] + o.x, u[1] + o.y, u[2] + o.z, u[3] + o.w, tag8));
    } else if (!(ABL & 2) && mlp) {
      LAS u32x4* gx = (LAS u32x4*)(L + L_GX) + (kw - 2) * 32;
      if (!(lane & 8)) gx[c8 * 4 + bb * 2 + hf] = tag4(u[0], u[1], u[2], u[3], tag8);
      CBAR();
      if (lane < 32) st128(rws, GP_OFF + wg * GP_S + (lane >> 2) * 128 + (kw - 2) * 64 + (lane & 3) * 16, gx[lane]);
    }
  }

  if (kw < 2) {
    __builtin_amdgcn_s_sleep(QDELAY);
    const int qo = QP_OFF + (h * 32 + 4 * kw + 8 * (lane >> 4)) * QP_S + b * 256 + (lane & 15) * 16;
    u32x4 a[4];
    for (u32 s = 0; s < SPIN_MEM; ++s) {
      CBAR();
      if (ABL & 1) { a[0] = a[1] = a[2] = a[3] = xa; break; }
#pragma unroll
      for (int i = 0; i < 4; ++i) a[i] = ld128<AUX_SC1>(rws, qo, i * QP_S);
      if (all_ok((untag4(a[0], tag8) | untag4(a[1], tag8)) | (untag4(a[2], tag8) | untag4(a[3], tag8)))) break;
    }
#pragma unroll
    for (int i = 0; i < 2; ++i) {
      const u32x4 a0 = a[2 * i], a1 = a[2 * i + 1];
      const float v0 = tr32(asf(a0.x), asf(a1.x)), v1 = tr32(asf(a0.y), asf(a1.y)), v2 = tr32(asf(a0.z), asf(a1.z)), v3 = tr32(asf(a0.w), asf(a1.w));
      L[L_Q + (4 * kw + 2 * i + (lane >> 5)) * 32 + 4 * (lane & 7) + 2 * ((lane >> 3) & 1) + ((lane >> 4) & 1)] = pkbf(tr16(v0, v2), tr16(v1, v3));
    }
    flag_set(L + F_Q + kw);
    MK(1);
  } else if (kw == 2) {
    u32 h0 = 0u, h1 = 0u;
    if (mlp) {
      const int go = GP_OFF + (cb2 + 28 * rr) * GP_S + ke * 128 + c8 * 16;
      u32x4 a;
      flag_wait<2, 1>(L + F_Q, lane);
      for (u32 s = 0; s < SPIN_MEM; ++s) {
        CBAR();
        if (ABL & 2) { a = xa; break; }
        a = ld128<AUX_SC1>(rws, go);
        if (all_ok(untag4(a, tag8))) break;
        __builtin_amdgcn_s_sleep(16);
      }
      float hv[4];
#pragma unroll
      for (int k = 0; k < 4; ++k) {
        float g = asf(a[k]);
        g += dpp<ROR8>(g); g = tr16(g, g); g = tr32(g, g);
        g = rbf(g);
        const float uu = dpp<ROR4>(g);
        const float z = 0.7978845608028654f * (g + 0.044715f * g * g * g);
        const float th = 1.f - 2.f * __builtin_amdgcn_rcpf(1.f + __builtin_amdgcn_exp2f(z * 2.8853900817779268f));
        hv[k] = rbf(0.5f * g * (1.f + th)) * uu;
      }
      h0 = pkbf(hv[0], hv[1]); h1 = pkbf(hv[2], hv[3]);
    }
    if (lane < 4) {
      *(LAS u32x2*)(L + L_H + (2 * (lane & 1)) * 4 + (lane >> 1) * 2) = u32x2{h0, SET};
      *(LAS u32x2*)(L + L_H + (2 * (lane & 1) + 1) * 4 + (lane >> 1) * 2) = u32x2{h1, SET};
    }
    CBAR();
  } else if (GO) {
    pace<300>(org);
    const u32x2 o = __builtin_amdgcn_raw_buffer_load_b64(rws, OR_OFF + (lane & 7) * 8, 0, AUX_SC1);
    const bool ok = o.y == tag;
    const float n = row_sum(ok ? 1.f : 0.f), dsum = row_sum(ok ? (float)(int)(o.x - (u32)org) : 0.f);
    const float sh = n > 0.f ? fminf(fmaxf(dsum * __builtin_amdgcn_rcpf(n) + GB, -80.f), 80.f) : 0.f;
    L[L_SH] = (u32)(int)sh;
    flag_set(L + F_SH);
  }

  const int rg = lane >> 4, cl = lane & 15;
  const int co = kw * 128 + cl * 8;
  u32x4 wa[4], wb[2];
  if (LATE == 0) flag_wait<2, 1>(L + F_Q, lane);
  if (LATE >= 1) flag_wait<4, 1>(L + F_VI, lane);
  if (LATE == 3) flag_wait<1, 0>(L + F_PUB, lane);
  if (GO) org += (u64)(long long)(int)__builtin_amdgcn_readfirstlane(L[L_SH]);
  if (LATE == 4) pace<PA + PV + (8 * PD + 8 * PDV) / 10 + LD>(org + kw * STG);
  if (LATE == 5) __builtin_amdgcn_s_sleep(LD);
  {
    const rsrc_t ro = mkrs(wo, (ABL & 4) ? 0 : 4096 * D * 2), rd = mkrs(wd, mlp && !(ABL & 4) ? DF * D * 2 : 0);
    const int vo = co < D ? (h * 512 + j * 16 + 4 * rg) * (D * 2) + co * 2 : OOB;
    const int vd = co < D ? (64 * cb2 + 8 * ke + 2 * rg) * (D * 2) + co * 2 : OOB;
#pragma unroll
    for (int i = 0; i < 2; ++i) wb[i] = ld128<AUX_NT>(rd, vd, i * D * 2);
#pragma unroll
    for (int i = 0; i < 4; ++i) wa[i] = ld128<AUX_NT>(ro, vo, i * D * 2);
  }
  __builtin_amdgcn_sched_barrier(0);
  float a0[8], a1[8];
  u32 pa[8], pb[8];
  {
    u32x4 hh;
    for (u32 s = 0; s < SPIN_LDS; ++s) {
      CBAR();
      hh = *(LAS u32x4*)(L + L_H + rg * 4);
      if (none((hh.y != SET) | (hh.w != SET))) break;
      __builtin_amdgcn_s_sleep(1);
    }
#pragma unroll
    for (int k = 0; k < 4; ++k) {
      const u32 pl = __builtin_amdgcn_perm(wb[1][k], wb[0][k], 0x05040100u), ph = __builtin_amdgcn_perm(wb[1][k], wb[0][k], 0x07060302u);
      a0[2 * k] = dot2(pl, hh.x, 0.f); a0[2 * k + 1] = dot2(ph, hh.x, 0.f);
      a1[2 * k] = dot2(pl, hh.z, 0.f); a1[2 * k + 1] = dot2(ph, hh.z, 0.f);
    }
#pragma unroll
    for (int k = 0; k < 4; ++k) {
      pa[2 * k] = __builtin_amdgcn_perm(wa[1][k], wa[0][k], 0x05040100u); pa[2 * k + 1] = __builtin_amdgcn_perm(wa[1][k], wa[0][k], 0x07060302u);
      pb[2 * k] = __builtin_amdgcn_perm(wa[3][k], wa[2][k], 0x05040100u); pb[2 * k + 1] = __builtin_amdgcn_perm(wa[3][k], wa[2][k], 0x07060302u);
    }
  }
#pragma unroll
  for (int k = 0; k < 8; ++k) asm volatile("" : "+v"(a0[k]), "+v"(a1[k]), "+v"(pa[k]), "+v"(pb[k]));
  int o2o = co < ((ABL & 64) ? 256 : (ABL & 128) ? 128 : D) ? O2_OFF + (h * 32 + j) * O2_S + (lane >> 5) * 1792 + kw * 512 + (((lane >> 4) & 1) * (kw == 3 ? 8 : 16) + cl) * 16 : OOB;
  LAS u32x4* orp = (LAS u32x4*)(L + L_O + rg * 8);
  PIN(o2o); PIN(orp);
  if (LATE == 0) flag_wait<4, 1>(L + F_VI, lane);
  {
    u32x4 oa, ob;
    for (u32 s = 0; s < SPIN_LDS; ++s) {
      CBAR();
      oa = orp[0]; ob = orp[1];
      if (none(((oa.y != SET) | (oa.w != SET)) | ((ob.y != SET) | (ob.w != SET)))) break;
    }
#pragma unroll
    for (int k = 0; k < 8; ++k) {
      a0[k] = dot2(pb[k], oa.z, dot2(pa[k], oa.x, a0[k]));
      a1[k] = dot2(pb[k], ob.z, dot2(pa[k], ob.x, a1[k]));
    }
    float t[8];
#pragma unroll
    for (int k = 0; k < 8; ++k) t[k] = tr32(a0[k], a1[k]);
    st128(rws, o2o, tag4(tr16(t[0], t[4]), tr16(t[1], t[5]), tr16(t[2], t[6]), tr16(t[3], t[7]), tag8));
  }
  flag_set(L + F_O2 + kw);
  NV(3); NW(3);
  MK(5);
}
}

extern "C" void kernel_launch(void* const* d_in, const int* in_sizes, int n_in, void* d_out, int out_size, void* d_ws, size_t ws_size,
                              hipStream_t stream) {
  fused_decode<<<256, 768, 0, stream>>>((const u16*)d_in[0], (const u16*)d_in[1], (const u16*)d_in[2], (const u16*)d_in[3], (const u16*)d_in[4],
                                        (const u16*)d_in[6], (const u16*)d_in[7], (const u16*)d_in[8], (u16*)d_out, (char*)d_ws + WS_OFF);
}
```

```cpp
#include <hip/hip_runtime.h>
#include <hip/hip_bf16.h>
#include <stdint.h>

typedef unsigned int u32;
typedef unsigned short u16;
typedef unsigned long long u64;
typedef u32 u32x2 __attribute__((ext_vector_type(2)));
typedef u32 u32x4 __attribute__((ext_vector_type(4)));
typedef float f32x4 __attribute__((ext_vector_type(4)));
typedef float f32x16 __attribute__((ext_vector_type(16)));
typedef __bf16 bf16x2_t __attribute__((ext_vector_type(2)));
typedef __bf16 bf16x8_t __attribute__((ext_vector_type(8)));
#define LAS __attribute__((address_space(3)))
typedef __amdgpu_buffer_rsrc_t rsrc_t;

#define STAMP 0
#define LATE 4
#define QDELAY 40
#define PRE 2
#define PSTR 64
#define ABL 0
#define SLP1 0
#define SLP2 0
#define PSTAG 5
#define DBL 0
#define PA 35
#define PD 625
#define PK0 2
#define D1 12
#define D2 10
#define PV 0
#define LD 0
#define PDV PD
#define STG 0
#define SR 3
#define SR2 0
#define GO 1
#define GB 5
#define TP1 0
#define TP2 0
#define PREAUX 16
#define STAUX 16
#define WIN 4
#define ROT 37
#define CODE_SPAN 13440
#define CTW 0
#define WS_OFF 0

namespace {
constexpr int D = 448, SK = 32768, W = 4096, DH = 256, DF = 1792;
constexpr int AUX_NT = 2, AUX_SC1 = 16;
constexpr int OOB = 1 << 30;
constexpr u32 TMASK = 0xfu;

constexpr int QP_S = 768, GP_S = 1280, O1_S = 2304, O2_S = 3840;
constexpr int EP_OFF = 0;
constexpr int OR_OFF = 2048;
constexpr int ST_OFF = 4096;
constexpr int QP_OFF = ST_OFF + 8192;
constexpr int GP_OFF = QP_OFF + 256 * QP_S;
constexpr int O1_OFF = GP_OFF + 256 * GP_S;
constexpr int O2_OFF = O1_OFF + 256 * O1_S;
constexpr int WS_BYTES = O2_OFF + 256 * O2_S;
constexpr int TS_OFF = 4 << 20;

constexpr int L_XN = 0;
constexpr int L_QX = L_XN + 480;
constexpr int L_GX = L_QX + 128;
constexpr int L_Q = L_GX + 256;
constexpr int L_P = L_Q + 256;
constexpr int L_OP = L_P + 512;
constexpr int L_WM = L_OP + 2048;
constexpr int L_WL = L_WM + 8;
constexpr int L_H = L_WL + 8;
constexpr int L_O = L_H + 16;
constexpr int L_R = L_O + 32;
constexpr int F_P = L_R + 128;
constexpr int F_VI = F_P + 4;
constexpr int F_O2 = F_VI + 4;
constexpr int F_Q = F_O2 + 4;
constexpr int F_QX = F_Q + 2, F_PUB = F_QX + 1;
constexpr int F_V = F_PUB + 1;
constexpr int F_R = F_V + 4;
constexpr int L_TAG = F_R + 8;
constexpr int L_DG = L_TAG + 1;
constexpr int L_SH = L_TAG + 2, F_SH = L_TAG + 3;
constexpr int L_END = L_TAG + 4;
constexpr u32 SET = 1u;
static_assert((L_END - L_WM) % 4 == 0 && (L_END - L_WM) / 4 <= 64, "one 16-byte store per lane clears the flag area");

constexpr int T_LAST = PA + PV + (8 * PD + 7 * PDV) / 10;
constexpr u32 SPIN_LDS = 1u << 21, SPIN_MEM = 1u << 16;

__device__ __forceinline__ float asf(u32 v) { return __builtin_bit_cast(float, v); }
__device__ __forceinline__ u32 asu(float v) { return __builtin_bit_cast(u32, v); }
__device__ __forceinline__ float lo16(u32 v) { return asf(v << 16); }
__device__ __forceinline__ float hi16(u32 v) { return asf(v & 0xffff0000u); }
__device__ __forceinline__ u32 pkbf(float a, float b) {
  bf16x2_t r = {(__bf16)a, (__bf16)b};
  return __builtin_bit_cast(u32, r);
}
__device__ __forceinline__ float rbf(float a) { return (float)(__bf16)a; }
__device__ __forceinline__ float dot2(u32 a, u32 b, float c) {
  return __builtin_amdgcn_fdot2_f32_bf16(__builtin_bit_cast(bf16x2_t, a), __builtin_bit_cast(bf16x2_t, b), c, false);
}
template <int CTRL> __device__ __forceinline__ float dpp(float x) {
  return asf(__builtin_amdgcn_mov_dpp((int)asu(x), CTRL, 0xf, 0xf, true));
}
constexpr int XOR1 = 0xB1, XOR2 = 0x4E, ROR8 = 0x128, ROR4 = 0x124;
__device__ __forceinline__ float tr32(float a, float b) {
  auto r = __builtin_amdgcn_permlane32_swap(asu(a), asu(b), false, false);
  return asf(r[0]) + asf(r[1]);
}
__device__ __forceinline__ float tr16(float a, float b) {
  auto r = __builtin_amdgcn_permlane16_swap(asu(a), asu(b), false, false);
  return asf(r[0]) + asf(r[1]);
}
__device__ __forceinline__ float mx32(float a) {
  auto r = __builtin_amdgcn_permlane32_swap(asu(a), asu(a), false, false);
  return fmaxf(asf(r[0]), asf(r[1]));
}
__device__ __forceinline__ float mx16(float a) {
  auto r = __builtin_amdgcn_permlane16_swap(asu(a), asu(a), false, false);
  return fmaxf(asf(r[0]), asf(r[1]));
}
__device__ __forceinline__ float row_sum(float v) { v += dpp<ROR8>(v); v += dpp<ROR4>(v); v += dpp<XOR2>(v); v += dpp<XOR1>(v); return v; }
__device__ __forceinline__ float row_max(float v) {
  v = fmaxf(v, dpp<ROR8>(v)); v = fmaxf(v, dpp<ROR4>(v)); v = fmaxf(v, dpp<XOR2>(v)); v = fmaxf(v, dpp<XOR1>(v)); return v;
}
__device__ __forceinline__ float wave_sum(float v) { v = row_sum(v); v = tr16(v, v); return tr32(v, v); }

__device__ __forceinline__ rsrc_t mkrs(const void* p, u32 bytes) { return __builtin_amdgcn_make_buffer_rsrc((void*)p, 0, bytes, 0x00020000); }
template <int AUX> __device__ __forceinline__ u32x4 ld128(rsrc_t r, int voff, int soff = 0) { return __builtin_amdgcn_raw_buffer_load_b128(r, voff, soff, AUX); }
template <int AUX> __device__ __forceinline__ u32 ld32(rsrc_t r, int voff) { return __builtin_amdgcn_raw_buffer_load_b32(r, voff, 0, AUX); }
__device__ __forceinline__ void st128(rsrc_t r, int voff, u32x4 v) { __builtin_amdgcn_raw_buffer_store_b128(v, r, voff, 0, STAUX); }
__device__ __forceinline__ u32 tagw(float v, u32 tag4) { return (asu(v) & ~TMASK) | tag4; }
__device__ __forceinline__ u32x4 tag4(float a, float b, float c, float d, u32 t) { return u32x4{tagw(a, t), tagw(b, t), tagw(c, t), tagw(d, t)}; }
__device__ __forceinline__ u32 untag4(u32x4& v, u32 t) { v.x ^= t; v.y ^= t; v.z ^= t; v.w ^= t; return v.x | v.y | v.z | v.w; }
__device__ __forceinline__ bool none(bool bad) { return __builtin_amdgcn_ballot_w64(bad) == 0ull; }
__device__ __forceinline__ bool all_ok(u32 bad) { return none((bad & TMASK) != 0u); }

#define CBAR() asm volatile("" ::: "memory")
#define PIN(v) asm volatile("" : "+v"(v))
#define LGKM0() asm volatile("s_waitcnt lgkmcnt(0)" ::: "memory")
template <int N, int SLEEP> __device__ __forceinline__ void flag_wait(LAS u32* p, int lane) {
  u32 v, cnt;
  const u32 addr = (u32)(uintptr_t)(p + (lane & (N - 1)));
  asm volatile("s_mov_b32 %1, %4\n"
               "1:\n\t"
               "ds_read_b32 %0, %2\n\t"
               "s_waitcnt lgkmcnt(0)\n\t"
               "v_cmp_ne_u32 vcc, %3, %0\n\t"
               "s_cbranch_vccz 2f\n\t"
               "s_sleep %5\n\t"
               "s_sub_u32 %1, %1, 1\n\t"
               "s_cbranch_scc0 1b\n"
               "2:"
               : "=&v"(v), "=&s"(cnt) : "v"(addr), "i"(SET), "i"(SPIN_LDS), "i"(SLEEP) : "vcc", "scc", "memory");
}
template <int DUE> __device__ __forceinline__ void pace(u64 org) {
  asm volatile("1:\n\t"
               "s_memrealtime vcc\n\t"
               "s_waitcnt lgkmcnt(0)\n\t"
               "s_sub_u32 vcc_lo, vcc_lo, %0\n\t"
               "s_cmp_lt_i32 vcc_lo, %1\n\t"
               "s_cbranch_scc0 2f\n\t"
               "s_sleep 1\n\t"
               "s_branch 1b\n"
               "2:"
               :: "s"((u32)org), "i"(DUE), "s"(org) : "vcc", "scc", "memory");
}
__device__ __forceinline__ void flag_set(LAS u32* p, u32 set = SET) { CBAR(); *p = set; CBAR(); }

#define MK(k)
#define MV(k)
#define MW(k)
#define NV(k)
#define NW(k)
#define NC(k, c)
#define MK0()

__device__ __forceinline__ void final_reduce(LAS u32* L, rsrc_t rws, const u16* x, u16* out, char* ws, int wg, int w, int grp, int lane, u32 tag, u64 org) {
  LAS float* const LF = (LAS float*)L;
  __builtin_amdgcn_s_setprio(3);
  const u32 tag8 = tag & TMASK;
  const int rb = wg / 28, cb = wg % 28, qd = lane & 3;
  const int pi = cb & 7, ph = cb < 24 ? pi >> 2 : pi >> 1, pg = cb < 24 ? pi & 3 : pi & 1;
  const int col = rb * D + 128 * (cb >> 3) + 8 * (4 * pg + ((lane >> 2) & 3)) + 4 * ph + (lane & 3);
  float xres = lo16(x[col]);
  int oo = O2_OFF + (grp * 32 + (lane >> 2)) * O2_S + rb * 1792 + cb * 64 + qd * 16;
  LAS float* wp = LF + ((lane & 12) == 0 ? L_R + (4 * qd + (lane >> 4)) * 8 + grp : L_XN + 128 + lane);
  LAS u32* fp = L + F_R + grp;
  LAS u32* fr = L + F_R + (lane & 7);
  LAS f32x4* rp = (LAS f32x4*)(LF + L_R + (lane & 15) * 8);
  int so = lane < 16 ? col * 2 : OOB;
  u32 tagv = SET;
  const rsrc_t ro = mkrs(out, 2 * D * 2);
  PIN(xres); PIN(oo); PIN(wp); PIN(fp); PIN(fr); PIN(rp); PIN(so); PIN(tagv);
  flag_wait<1, 1>(L + F_PUB, lane);
  flag_wait<4, 0>(L + F_O2, lane);
  if (D2) __builtin_amdgcn_s_sleep(D2);
  if (TP2) pace<T_LAST + TP2>(org);
  u32x4 a0, a1;
  u32 np2 = 0;
  for (u32 s = 0; s < SPIN_MEM; ++s) {
    CBAR();
    ++np2;
    a0 = ld128<AUX_SC1>(rws, oo); a1 = ld128<AUX_SC1>(rws, oo, 16 * O2_S);
    if (all_ok(untag4(a0, tag8) | untag4(a1, tag8))) break;
    if (SLP2) __builtin_amdgcn_s_sleep(SLP2);
  }
  MK(6);
  NV(4);
  float f = tr16(tr32(asf(a0.x) + asf(a1.x), asf(a0.z) + asf(a1.z)), tr32(asf(a0.y) + asf(a1.y), asf(a0.w) + asf(a1.w)));
  f += dpp<ROR8>(f); f += dpp<ROR4>(f);
  *wp = f;
  flag_set(fp, tagv);
  u32 fl = *fr;
  CBAR();
  const f32x4 r0 = rp[0], r1 = rp[1];
  u32 res = pkbf(xres + (((r0.x + r0.y) + (r0.z + r0.w)) + ((r1.x + r1.y) + (r1.z + r1.w))), 0.f);
  PIN(res);
  if (none(fl != SET)) {
    __builtin_amdgcn_raw_buffer_store_b16((u16)res, ro, so, 0, 0);
    if (wg == 0 && lane == 0) __builtin_amdgcn_raw_buffer_store_b32(tag, rws, EP_OFF, 0, AUX_SC1);
    MK(7);
  }
  NW(4); NC(6, np2);
}

}

extern "C" __global__ void __launch_bounds__(768, 3)
fused_decode(const u16* __restrict__ x, const u16* __restrict__ wq, const u16* __restrict__ wg_, const u16* __restrict__ wu,
             const u16* __restrict__ kc, char* __restrict__ ws, const u16* __restrict__ vc, const u16* __restrict__ wo,
             const u16* __restrict__ wd, u16* __restrict__ out) {
  __shared__ __attribute__((aligned(16))) u32 smem[L_END];
  LAS u32* const L = (LAS u32*)smem;
  LAS float* const LF = (LAS float*)smem;
  const int wg = blockIdx.x, h = wg & 7, j = wg >> 3, b = j & 1, sp = j >> 1, reg = wg & 15;
  const int w = __builtin_amdgcn_readfirstlane(threadIdx.x >> 6), lane = threadIdx.x & 63, kw = w & 3;
  asm volatile("s_load_dword s94, %0, 0x0\n\ts_load_dword s95, %0, 0x40" :: "s"(__builtin_amdgcn_kernarg_segment_ptr()) : "s94", "s95");
  u64 pc0;
  asm volatile("s_getpc_b64 %0" : "=s"(pc0));
  const u32 code_touch = ld32<0>(mkrs((const char*)(pc0 & ~255ull), w == CTW ? CODE_SPAN : 0), lane < 5 ? j * 640 + lane * 128 : OOB);
  __builtin_amdgcn_sched_barrier(0);
#define ROLE_ENTRY() const rsrc_t rws = mkrs(ws, WS_BYTES)
#define ROLE_TAG() const u32 tag = __builtin_amdgcn_readfirstlane(L[L_TAG]), tag8 = tag & TMASK
  const u32 blk0 = sp * 32 + reg * ROT;
  const u32 srot = sp * SR + reg * SR2;
  u64 org;
#define STREAM_LOADS(SRC, VOFF, SOFF, BAR) \
  const rsrc_t rkv = mkrs((SRC) + ((size_t)reg * SK + (SK - W)) * DH, W * DH * 2); \
  u32x4 d[32]; \
  if (BAR) { __builtin_amdgcn_sched_barrier(0); __builtin_amdgcn_s_barrier(); org = __builtin_amdgcn_s_memrealtime(); __builtin_amdgcn_sched_barrier(0); } \
  MV(2); \
  STREAM_GROUP(0, VOFF, SOFF, BAR) STREAM_GROUP(1, VOFF, SOFF, BAR) STREAM_GROUP(2, VOFF, SOFF, BAR) STREAM_GROUP(3, VOFF, SOFF, BAR) \
  STREAM_GROUP(4, VOFF, SOFF, BAR) STREAM_GROUP(5, VOFF, SOFF, BAR) STREAM_GROUP(6, VOFF, SOFF, BAR) STREAM_GROUP(7, VOFF, SOFF, BAR)
#define STREAM_GROUP(G, VOFF, SOFF, ISK) \
  if (PD > 0 && (!(ISK) || (G) >= PK0)) pace<PA + ((ISK) ? (G) * PD / 10 : PV + (8 * PD + (G) * PDV) / 10)>(org + kw * STG); \
  if (WIN > 0 && 4 * (G) > WIN) asm volatile("s_waitcnt vmcnt(%0)" :: "i"(WIN) : "memory"); \
  _Pragma("unroll") for (int i = 4 * (G); i < 4 * (G) + 4; ++i) d[i] = ld128<AUX_NT>(rkv, (VOFF), (int)(SOFF)); \
  __builtin_amdgcn_sched_barrier(0);

  if (__builtin_expect(w >= 4, 0)) {
  u32 tag_out;
  if (w < 8) {
    __builtin_amdgcn_s_setprio(2);
    const int g = lane >> 4, key = (lane >> 1) & 7;
    const bool hb = lane & 1;
    const int kvo = key * 512 + (2 * g + (lane & 1)) * 16;
    STREAM_LOADS(kc, kvo + (i & 3) * 128, ((blk0 + ((kw + 4 * (i >> 2) + srot) & 31u)) & 511u) << 12, true);
    MV(3);
    ROLE_ENTRY();
    MK0(); MW(2); MW(3);
    flag_wait<2, 1>(L + F_Q, lane);
    ROLE_TAG();
    u32x4 qa[4];
#pragma unroll
    for (int i = 0; i < 4; ++i) qa[i] = *(LAS u32x4*)(L + L_Q + (lane & 1) * 128 + i * 32 + (2 * g + ((lane >> 1) & 1)) * 4);
    f32x4 ea, eb;
#pragma unroll
    for (int n = 0; n < 4; ++n) {
      f32x4 ca = {}, cb = {};
#pragma unroll
      for (int i = 0; i < 4; ++i) {
        ca = __builtin_amdgcn_mfma_f32_16x16x32_bf16(__builtin_bit_cast(bf16x8_t, qa[i]), __builtin_bit_cast(bf16x8_t, d[8 * n + i]), ca, 0, 0, 0);
        cb = __builtin_amdgcn_mfma_f32_16x16x32_bf16(__builtin_bit_cast(bf16x8_t, qa[i]), __builtin_bit_cast(bf16x8_t, d[8 * n + 4 + i]), cb, 0, 0, 0);
      }
#pragma unroll
      for (int k = 0; k < 4; ++k) { ea[k] = (n == 0 || g == n) ? ca[k] : ea[k]; eb[k] = (n == 0 || g == n) ? cb[k] : eb[k]; }
    }
    asm volatile("" :: "v"(code_touch));
    float sc[2];
#pragma unroll
    for (int hd = 0; hd < 2; ++hd) sc[hd] = rbf((hb ? eb[2 + hd] : ea[hd]) + dpp<XOR1>(hb ? ea[2 + hd] : eb[hd])) * 0.0625f;
    const float wm0 = mx32(mx16(row_max(sc[0]))), wm1 = mx32(mx16(row_max(sc[1])));
    const u32 tag16 = SET;
    CBAR();
    if (lane < 2) L[L_WM + kw * 2 + lane] = (asu(lane ? wm1 : wm0) & 0xffff0000u) | tag16;
    u32x4 ma, mb;
    for (u32 s = 0; s < SPIN_LDS; ++s) {
      CBAR();
      ma = *(LAS u32x4*)(L + L_WM) ^ tag16; mb = *(LAS u32x4*)(L + L_WM + 4) ^ tag16;
      if (none((((ma.x | ma.y | ma.z | ma.w) | (mb.x | mb.y | mb.z | mb.w)) & 0xffffu) != 0u)) break;
    }
    const float M0 = fmaxf(fmaxf(asf(ma.x), asf(ma.z)), fmaxf(asf(mb.x), asf(mb.z)));
    const float M1 = fmaxf(fmaxf(asf(ma.y), asf(ma.w)), fmaxf(asf(mb.y), asf(mb.w)));
    const float p0 = rbf(__builtin_amdgcn_exp2f((sc[0] - M0) * 1.4426950408889634f));
    const float p1 = rbf(__builtin_amdgcn_exp2f((sc[1] - M1) * 1.4426950408889634f));
    LAS u32* pp = L + L_P + key * 64 + kw + 4 * (2 * g + (lane & 1));
    pp[0] = asu(p0) | (asu(p0) >> 16);
    pp[32] = asu(p1) | (asu(p1) >> 16);
    float ls = tr32(p0, p1);
    ls = row_sum(tr16(ls, ls));
    if ((lane & 31) == 0) LF[L_WL + kw * 2 + (lane >> 5)] = ls;
    flag_set(L + F_P + kw);
    MK(2);

    if (kw == 0) {
      flag_wait<4, 0>(L + F_P, lane);
      if (lane < 2) {
        const float Ls = (LF[L_WL + lane] + LF[L_WL + 2 + lane]) + (LF[L_WL + 4 + lane] + LF[L_WL + 6 + lane]);
        st128(rws, ST_OFF + (((h * 2 + lane) * 2 + b) * 16 + sp) * 16, u32x4{asu(lane ? M1 : M0), tag, asu(Ls), tag});
      }
    } else if (kw == 1) {
      __builtin_amdgcn_s_setprio(3);
      const int s = lane >> 2, qd = lane & 3;
      const int so = ST_OFF + ((h * 2 + (j >> 4)) * 32 + s) * 16;
      const int oo = O1_OFF + (h * 32 + 2 * s) * O1_S + j * 64 + qd * 16;
      u32x4 s0, s1, a0, a1;
      flag_wait<4, 1>(L + F_VI, lane);
      for (u32 it = 0; it < SPIN_MEM; ++it) {
        CBAR();
        s0 = ld128<AUX_SC1>(rws, so); s1 = ld128<AUX_SC1>(rws, so + 256);
        if (none((s0.y != tag) | (s0.w != tag) | (s1.y != tag) | (s1.w != tag))) break;
        flag_wait<1, 1>(L + F_PUB, lane);
      }
      float wgt[2];
#pragma unroll
      for (int bb = 0; bb < 2; ++bb) {
        const float m = asf(bb ? s1.x : s0.x), l = asf(bb ? s1.z : s0.z);
        float mg = fmaxf(m, dpp<ROR8>(m)); mg = fmaxf(mg, dpp<ROR4>(mg)); mg = mx32(mx16(mg));
        const float e = __builtin_amdgcn_exp2f((m - mg) * 1.4426950408889634f);
        float lg = e * l; lg += dpp<ROR8>(lg); lg += dpp<ROR4>(lg); lg = tr16(lg, lg); lg = tr32(lg, lg);
        wgt[bb] = e * __builtin_amdgcn_rcpf(lg);
      }
      LAS u32x2* ow = (LAS u32x2*)(L + ((lane & 12) == 0 ? L_O + qd * 8 + (lane >> 4) * 2 : L_XN + 2 * lane));
      u32 tagv = SET;
      PIN(wgt[0]); PIN(wgt[1]); PIN(ow); PIN(tagv);
      if (GO) org += (u64)(long long)(int)__builtin_amdgcn_readfirstlane(L[L_SH]);
      flag_wait<1, 0>(L + F_PUB, lane);
      if (D1) __builtin_amdgcn_s_sleep(D1);
      if (TP1) pace<T_LAST + TP1>(org);
      u32 np1 = 0;
      for (u32 it = 0; it < SPIN_MEM; ++it) {
        CBAR();
        ++np1;
        a0 = ld128<AUX_SC1>(rws, oo); a1 = ld128<AUX_SC1>(rws, oo + O1_S);
        if (all_ok(untag4(a0, tag8) | untag4(a1, tag8))) break;
        if (SLP1) __builtin_amdgcn_s_sleep(SLP1);
      }
      NV(2);
      const float v0 = tr32(asf(a0.x) * wgt[0], asf(a1.x) * wgt[1]), v1 = tr32(asf(a0.y) * wgt[0], asf(a1.y) * wgt[1]);
      const float v2 = tr32(asf(a0.z) * wgt[0], asf(a1.z) * wgt[1]), v3 = tr32(asf(a0.w) * wgt[0], asf(a1.w) * wgt[1]);
      float e0 = tr16(v0, v2), e1 = tr16(v1, v3);
      e0 += dpp<ROR8>(e0); e1 += dpp<ROR8>(e1);
      e0 += dpp<ROR4>(e0); e1 += dpp<ROR4>(e1);
      *ow = u32x2{pkbf(e0, e1), tagv};
      asm volatile("s_waitcnt vmcnt(0)" ::: "memory");
      MK(4); NW(2); NC(5, np1);
    }
    if (GO != 0 && TP2 != 0 && kw != 1) { flag_wait<1, 4>(L + F_SH, lane); org += (u64)(long long)(int)__builtin_amdgcn_readfirstlane(L[L_SH]); }
    if (PRE && PRE < 3 && kw >= 2) {
      const int po = kw == 2 ? O1_OFF + (h * 32 + j) * O1_S : O2_OFF + (h * 32 + j) * O2_S;
      const u32 t0 = ld32<AUX_SC1>(rws, lane * PSTR < (kw == 2 ? 2048 : PRE == 2 ? 0 : 3584) ? po + lane * PSTR : OOB);
      asm volatile("" :: "v"(t0));
    }
    if (PRE >= 3 && kw >= 2) {
      u32 t0 = 0u;
      if (kw == 2) t0 = ld32<PREAUX>(rws, lane < 32 ? O1_OFF + (h * 32 + lane) * O1_S + j * 64 : OOB);
      else if (PRE == 4 && wg < 56) {
        const int po = O2_OFF + lane * O2_S + (wg / 28) * 1792 + (wg % 28) * 64;
#pragma unroll
        for (int i = 0; i < 4; ++i) t0 |= __builtin_amdgcn_raw_buffer_load_b32(rws, po, i * 64 * O2_S, PREAUX);
      }
      asm volatile("" :: "v"(t0));
    }
    tag_out = tag;
  } else {
    if (kw == 0 && lane < (L_END - L_WM) / 4) *(LAS u32x4*)(L + L_WM + 4 * lane) = u32x4{0u, 0u, 0u, 0u};
    LGKM0();
    __builtin_amdgcn_s_barrier();
    org = __builtin_amdgcn_s_memrealtime();
    __builtin_amdgcn_s_setprio(1);
    __builtin_amdgcn_sched_barrier(0);
    ROLE_ENTRY();
    if (GO) { flag_wait<1, 4>(L + F_SH, lane); org += (u64)(long long)(int)__builtin_amdgcn_readfirstlane(L[L_SH]); }
    STREAM_LOADS(vc, lane * 16 + kw * 1024, ((blk0 + ((i + srot) & 31u)) & 511u) << 12, false);
    MV(4);
    flag_set(L + F_VI + kw);
    MK0();
    const int m = lane & 31, hf = lane >> 5;
    u32x4 msk;
#pragma unroll
    for (int r = 0; r < 4; ++r) msk[r] = (m < 16 && ((m & 7) >> 1) == r) ? ((m & 1) ? 0xffff0000u : 0x0000ffffu) : 0u;
    LAS f32x4* op = (LAS f32x4*)(LF + L_OP) + kw * 128 + 2 * m + hf;
    LAS f32x4* rp = (LAS f32x4*)(LF + L_OP) + lane;
    LAS u32* fp = L + F_V + kw;
    LAS u32* fr = L + F_V + (lane & 3);
    int o1o = O1_OFF + (h * 32 + j) * O1_S + lane * 16;
    PIN(op); PIN(rp); PIN(fp); PIN(fr); PIN(o1o);
    flag_wait<4, 1>(L + F_P, lane);
    ROLE_TAG();
    const LAS u32x4* pp = (const LAS u32x4*)(L + L_P + ((kw * 2 + hf) * 2 + ((m >> 3) & 1)) * 32);
    f32x16 acc = {};
#pragma unroll
    for (int g = 0; g < 8; ++g) {
      const u32x4 p4 = pp[g];
#pragma unroll
      for (int k = 0; k < 4; ++k) {
        const u32x4 a = msk & p4[k];
        acc = __builtin_amdgcn_mfma_f32_32x32x16_bf16(__builtin_bit_cast(bf16x8_t, a), __builtin_bit_cast(bf16x8_t, d[4 * g + k]), acc, 0, 0, 0);
      }
      __builtin_amdgcn_sched_barrier(0);
    }
    asm volatile("" :: "v"(code_touch));
    MV(6); NV(1);
    op[0] = f32x4{acc[0], acc[1], acc[2], acc[3]};
    op[64] = f32x4{acc[4], acc[5], acc[6], acc[7]};
    flag_set(fp);
    const u32 fl = *fr;
    CBAR();
    const f32x4 o0 = (rp[0] + rp[128]) + (rp[256] + rp[384]);
    const f32x4 o1 = (rp[64] + rp[192]) + (rp[320] + rp[448]);
    u32x4 t0 = tag4(o0.x, o0.y, o0.z, o0.w, tag8), t1 = tag4(o1.x, o1.y, o1.z, o1.w, tag8);
    PIN(t0); PIN(t1);
    if (none(fl != SET)) {
      st128(rws, o1o, t0);
      st128(rws, o1o + 1024, t1);
      flag_set(L + F_PUB);
      MK(3);
    }
    MW(4); MW(6); NW(1);
    tag_out = tag;
  }
  if (wg < 56 && (wg % 28) < ((ABL & 64) ? 16 : (ABL & 128) ? 8 : 28)) final_reduce(L, mkrs(ws, WS_BYTES), x, out, ws, wg, w, w - 4, lane, tag_out, org);
  return;
  }

  __builtin_amdgcn_s_setprio(3);
  ROLE_ENTRY();
  const bool mlp = wg < 224;
  const int cb2 = wg % 28, ke = wg / 28;
  const int rr = lane >> 3, c8 = lane & 7;
  const rsrc_t rx = mkrs((const u16*)x, 2 * D * 2);
  const u32x4 xa = ld128<0>(rx, lane < 56 ? lane * 16 : OOB), xb = ld128<0>(rx, lane < 56 ? D * 2 + lane * 16 : OOB);
  const u32 epoch = ld32<AUX_SC1>(rws, EP_OFF);
  const int row0 = kw < 2 ? 112 * (j >> 3) + 64 * kw : 56 * ke;
  const int nrow = kw == 0 ? 64 : kw == 1 ? 48 : mlp ? 56 : 0;
  u32x4 wv[8];
  {
    const rsrc_t rw = mkrs(kw < 2 ? wq : kw == 2 ? wg_ : wu, kw < 2 ? ((ABL & 32) ? 0 : D * 4096 * 2) : ((ABL & 8) ? 0 : D * DF * 2));
    const int rs = kw < 2 ? 8192 : DF * 2;
    const int vw0 = nrow ? (row0 + 2 * rr) * rs + (kw < 2 ? (h * 512 + (j & 7) * 64) * 2 : cb2 * 128) + c8 * 16 : OOB;
    const int vw3 = 48 + 2 * rr < nrow ? vw0 : OOB;
#pragma unroll
    for (int i = 0; i < 8; ++i) wv[i] = ld128<AUX_NT>(rw, i < 6 ? vw0 : vw3, (16 * (i >> 1) + (i & 1)) * rs);
  }
  const int eo = lane < 8 ? QP_OFF + (h * 32 + j) * QP_S + lane * 64 : lane < 24 ? GP_OFF + wg * GP_S + (lane - 8) * 64 : ST_OFF + (wg & 127) * 64;
  const u32 box_touch = ld32<AUX_SC1>(rws, (!(ABL & 16) && kw == 1 && lane < 25) ? eo : OOB);
  __builtin_amdgcn_sched_barrier(0);
  __builtin_amdgcn_s_barrier();
  org = __builtin_amdgcn_s_memrealtime();
  __builtin_amdgcn_sched_barrier(0);
  MK0();

#pragma unroll
  for (int bb = 0; bb < 2; ++bb) {
    const u32x4 xv = bb ? xb : xa;
    float v[8];
#pragma unroll
    for (int k = 0; k < 4; ++k) { v[2 * k] = lo16(xv[k]); v[2 * k + 1] = hi16(xv[k]); }
    float s = 0.f;
#pragma unroll
    for (int k = 0; k < 8; ++k) s += v[k];
    const float mean = wave_sum(s) * (1.f / D);
    float q = 0.f;
#pragma unroll
    for (int k = 0; k < 8; ++k) { const float t = v[k] - mean; q += t * t; }
    q = lane < 56 ? q : 0.f;
    const float var = rbf(wave_sum(q) * (1.f / D));
    const float den = rbf(__builtin_amdgcn_sqrtf(rbf(var + 1e-5f))), rden = __builtin_amdgcn_rcpf(den), mb = rbf(mean);
#pragma unroll
    for (int k = 0; k < 8; ++k) { const float c = rbf(v[k] - mb), r = c * rden; v[k] = fmaf(fmaf(-r, den, c), rden, r); }
    u32x4 o;
#pragma unroll
    for (int k = 0; k < 4; ++k) o[k] = pkbf(v[2 * k], v[2 * k + 1]);
    if (lane < 56) *(LAS u32x4*)(L + L_XN + bb * 224 + lane * 4) = o;
    else if (bb == 1) *(LAS u32x4*)(L + L_XN + 224 + lane * 4) = u32x4{0u, 0u, 0u, 0u};
  }
  asm volatile("" :: "v"(code_touch));
  const u32 tag = __builtin_amdgcn_readfirstlane(epoch) + 1u, tag8 = tag & TMASK;
  if (kw == 0) L[L_TAG] = tag;
  if (GO && kw == 3 && wg < 8 && lane == 0) __builtin_amdgcn_raw_buffer_store_b64(u32x2{(u32)org, tag}, rws, OR_OFF + wg * 8, 0, AUX_SC1);
  MK(10);

  {
    float a0[8] = {}, a1[8] = {};
#pragma unroll
    for (int k = 0; k < 4; ++k) {
      const u32 x0 = L[L_XN + row0 / 2 + rr + 8 * k], x1 = L[L_XN + 224 + row0 / 2 + rr + 8 * k];
#pragma unroll
      for (int c = 0; c < 4; ++c) {
        const u32 pl = __builtin_amdgcn_perm(wv[2 * k + 1][c], wv[2 * k][c], 0x05040100u), ph = __builtin_amdgcn_perm(wv[2 * k + 1][c], wv[2 * k][c], 0x07060302u);
        a0[2 * c] = dot2(pl, x0, a0[2 * c]); a0[2 * c + 1] = dot2(ph, x0, a0[2 * c + 1]);
        a1[2 * c] = dot2(pl, x1, a1[2 * c]); a1[2 * c + 1] = dot2(ph, x1, a1[2 * c + 1]);
      }
    }
    asm volatile("" :: "v"(box_touch));
    MV(5);
    float t[8], u[4];
#pragma unroll
    for (int k = 0; k < 8; ++k) t[k] = tr32(a0[k], a1[k]);
#pragma unroll
    for (int k = 0; k < 4; ++k) { u[k] = tr16(t[k], t[k + 4]); u[k] += dpp<ROR8>(u[k]); }
    const int bb = lane >> 5, hf = (lane >> 4) & 1;
    LAS f32x4* qx = (LAS f32x4*)(LF + L_QX) + bb * 16 + c8 * 2 + hf;
    if (kw == 1) {
      if (!(lane & 8)) *qx = f32x4{u[0], u[1], u[2], u[3]};
      flag_set(L + F_QX);
    } else if (kw == 0) {
      flag_wait<1, 0>(L + F_QX, lane);
      const f32x4 o = *qx;
      if (!(ABL & 1) && !(lane & 8)) st128(rws, QP_OFF + (h * 32 + j) * QP_S + bb * 256 + hf * 128 + c8 * 16, tag4(u[0] + o.x, u[1] + o.y, u[2] + o.z, u[3] + o.w, tag8));
    } else if (!(ABL & 2) && mlp) {
      LAS u32x4* gx = (LAS u32x4*)(L + L_GX) + (kw - 2) * 32;
      if (!(lane & 8)) gx[c8 * 4 + bb * 2 + hf] = tag4(u[0], u[1], u[2], u[3], tag8);
      CBAR();
      if (lane < 32) st128(rws, GP_OFF + wg * GP_S + (lane >> 2) * 128 + (kw - 2) * 64 + (lane & 3) * 16, gx[lane]);
    }
  }

  if (kw < 2) {
    __builtin_amdgcn_s_sleep(QDELAY);
    const int qo = QP_OFF + (h * 32 + 4 * kw + 8 * (lane >> 4)) * QP_S + b * 256 + (lane & 15) * 16;
    u32x4 a[4];
    for (u32 s = 0; s < SPIN_MEM; ++s) {
      CBAR();
      if (ABL & 1) { a[0] = a[1] = a[2] = a[3] = xa; break; }
#pragma unroll
      for (int i = 0; i < 4; ++i) a[i] = ld128<AUX_SC1>(rws, qo, i * QP_S);
      if (all_ok((untag4(a[0], tag8) | untag4(a[1], tag8)) | (untag4(a[2], tag8) | untag4(a[3], tag8)))) break;
    }
#pragma unroll
    for (int i = 0; i < 2; ++i) {
      const u32x4 a0 = a[2 * i], a1 = a[2 * i + 1];
      const float v0 = tr32(asf(a0.x), asf(a1.x)), v1 = tr32(asf(a0.y), asf(a1.y)), v2 = tr32(asf(a0.z), asf(a1.z)), v3 = tr32(asf(a0.w), asf(a1.w));
      L[L_Q + (4 * kw + 2 * i + (lane >> 5)) * 32 + 4 * (lane & 7) + 2 * ((lane >> 3) & 1) + ((lane >> 4) & 1)] = pkbf(tr16(v0, v2), tr16(v1, v3));
    }
    flag_set(L + F_Q + kw);
    MK(1);
  } else if (kw == 2) {
    u32 h0 = 0u, h1 = 0u;
    if (mlp) {
      const int go = GP_OFF + (cb2 + 28 * rr) * GP_S + ke * 128 + c8 * 16;
      u32x4 a;
      flag_wait<2, 1>(L + F_Q, lane);
      for (u32 s = 0; s < SPIN_MEM; ++s) {
        CBAR();
        if (ABL & 2) { a = xa; break; }
        a = ld128<AUX_SC1>(rws, go);
        if (all_ok(untag4(a, tag8))) break;
        __builtin_amdgcn_s_sleep(16);
      }
      float hv[4];
#pragma unroll
      for (int k = 0; k < 4; ++k) {
        float g = asf(a[k]);
        g += dpp<ROR8>(g); g = tr16(g, g); g = tr32(g, g);
        g = rbf(g);
        const float uu = dpp<ROR4>(g);
        const float z = 0.7978845608028654f * (g + 0.044715f * g * g * g);
        const float th = 1.f - 2.f * __builtin_amdgcn_rcpf(1.f + __builtin_amdgcn_exp2f(z * 2.8853900817779268f));
        hv[k] = rbf(0.5f * g * (1.f + th)) * uu;
      }
      h0 = pkbf(hv[0], hv[1]); h1 = pkbf(hv[2], hv[3]);
    }
    if (lane < 4) {
      *(LAS u32x2*)(L + L_H + (2 * (lane & 1)) * 4 + (lane >> 1) * 2) = u32x2{h0, SET};
      *(LAS u32x2*)(L + L_H + (2 * (lane & 1) + 1) * 4 + (lane >> 1) * 2) = u32x2{h1, SET};
    }
    CBAR();
  } else if (GO) {
    pace<300>(org);
    const u32x2 o = __builtin_amdgcn_raw_buffer_load_b64(rws, OR_OFF + (lane & 7) * 8, 0, AUX_SC1);
    const bool ok = o.y == tag;
    const float n = row_sum(ok ? 1.f : 0.f), dsum = row_sum(ok ? (float)(int)(o.x - (u32)org) : 0.f);
    const float sh = n > 0.f ? fminf(fmaxf(dsum * __builtin_amdgcn_rcpf(n) + GB, -80.f), 80.f) : 0.f;
    L[L_SH] = (u32)(int)sh;
    flag_set(L + F_SH);
  }

  const int rg = lane >> 4, cl = lane & 15;
  const int co = kw * 128 + cl * 8;
  u32x4 wa[4], wb[2];
  if (LATE == 0) flag_wait<2, 1>(L + F_Q, lane);
  if (LATE >= 1) flag_wait<4, 1>(L + F_VI, lane);
  if (LATE == 3) flag_wait<1, 0>(L + F_PUB, lane);
  if (GO) org += (u64)(long long)(int)__builtin_amdgcn_readfirstlane(L[L_SH]);
  if (LATE == 4) pace<PA + PV + (8 * PD + 8 * PDV) / 10 + LD>(org + kw * STG);
  if (LATE == 5) __builtin_amdgcn_s_sleep(LD);
  {
    const rsrc_t ro = mkrs(wo, (ABL & 4) ? 0 : 4096 * D * 2), rd = mkrs(wd, mlp && !(ABL & 4) ? DF * D * 2 : 0);
    const int vo = co < D ? (h * 512 + j * 16 + 4 * rg) * (D * 2) + co * 2 : OOB;
    const int vd = co < D ? (64 * cb2 + 8 * ke + 2 * rg) * (D * 2) + co * 2 : OOB;
#pragma unroll
    for (int i = 0; i < 2; ++i) wb[i] = ld128<AUX_NT>(rd, vd, i * D * 2);
#pragma unroll
    for (int i = 0; i < 4; ++i) wa[i] = ld128<AUX_NT>(ro, vo, i * D * 2);
  }
  __builtin_amdgcn_sched_barrier(0);
  float a0[8], a1[8];
  u32 pa[8], pb[8];
  {
    u32x4 hh;
    for (u32 s = 0; s < SPIN_LDS; ++s) {
      CBAR();
      hh = *(LAS u32x4*)(L + L_H + rg * 4);
      if (none((hh.y != SET) | (hh.w != SET))) break;
      __builtin_amdgcn_s_sleep(1);
    }
#pragma unroll
    for (int k = 0; k < 4; ++k) {
      const u32 pl = __builtin_amdgcn_perm(wb[1][k], wb[0][k], 0x05040100u), ph = __builtin_amdgcn_perm(wb[1][k], wb[0][k], 0x07060302u);
      a0[2 * k] = dot2(pl, hh.x, 0.f); a0[2 * k + 1] = dot2(ph, hh.x, 0.f);
      a1[2 * k] = dot2(pl, hh.z, 0.f); a1[2 * k + 1] = dot2(ph, hh.z, 0.f);
    }
#pragma unroll
    for (int k = 0; k < 4; ++k) {
      pa[2 * k] = __builtin_amdgcn_perm(wa[1][k], wa[0][k], 0x05040100u); pa[2 * k + 1] = __builtin_amdgcn_perm(wa[1][k], wa[0][k], 0x07060302u);
      pb[2 * k] = __builtin_amdgcn_perm(wa[3][k], wa[2][k], 0x05040100u); pb[2 * k + 1] = __builtin_amdgcn_perm(wa[3][k], wa[2][k], 0x07060302u);
    }
  }
#pragma unroll
  for (int k = 0; k < 8; ++k) asm volatile("" : "+v"(a0[k]), "+v"(a1[k]), "+v"(pa[k]), "+v"(pb[k]));
  int o2o = co < ((ABL & 64) ? 256 : (ABL & 128) ? 128 : D) ? O2_OFF + (h * 32 + j) * O2_S + (lane >> 5) * 1792 + kw * 512 + (((lane >> 4) & 1) * (kw == 3 ? 8 : 16) + cl) * 16 : OOB;
  LAS u32x4* orp = (LAS u32x4*)(L + L_O + rg * 8);
  PIN(o2o); PIN(orp);
  if (LATE == 0) flag_wait<4, 1>(L + F_VI, lane);
  {
    u32x4 oa, ob;
    for (u32 s = 0; s < SPIN_LDS; ++s) {
      CBAR();
      oa = orp[0]; ob = orp[1];
      if (none(((oa.y != SET) | (oa.w != SET)) | ((ob.y != SET) | (ob.w != SET)))) break;
    }
#pragma unroll
    for (int k = 0; k < 8; ++k) {
      a0[k] = dot2(pb[k], oa.z, dot2(pa[k], oa.x, a0[k]));
      a1[k] = dot2(pb[k], ob.z, dot2(pa[k], ob.x, a1[k]));
    }
    float t[8];
#pragma unroll
    for (int k = 0; k < 8; ++k) t[k] = tr32(a0[k], a1[k]);
    st128(rws, o2o, tag4(tr16(t[0], t[4]), tr16(t[1], t[5]), tr16(t[2], t[6]), tr16(t[3], t[7]), tag8));
  }
  flag_set(L + F_O2 + kw);
  NV(3); NW(3);
  MK(5);
}

extern "C" void kernel_launch(void* const* d_in, const int* in_sizes, int n_in, void* d_out, int out_size, void* d_ws, size_t ws_size,
                              hipStream_t stream) {
  fused_decode<<<256, 768, 0, stream>>>((const u16*)d_in[0], (const u16*)d_in[3], (const u16*)d_in[6], (const u16*)d_in[7], (const u16*)d_in[1],
                                        (char*)d_ws + WS_OFF, (const u16*)d_in[2], (const u16*)d_in[4], (const u16*)d_in[8], (u16*)d_out);
}
```

```cpp
#include <hip/hip_runtime.h>
#include <hip/hip_bf16.h>
#include <stdint.h>

typedef unsigned int u32;
typedef unsigned short u16;
typedef unsigned long long u64;
typedef u32 u32x2 __attribute__((ext_vector_type(2)));
typedef u32 u32x4 __attribute__((ext_vector_type(4)));
typedef float f32x4 __attribute__((ext_vector_type(4)));
typedef float f32x16 __attribute__((ext_vector_type(16)));
typedef __bf16 bf16x2_t __attribute__((ext_vector_type(2)));
typedef __bf16 bf16x8_t __attribute__((ext_vector_type(8)));
#define LAS __attribute__((address_space(3)))
typedef __amdgpu_buffer_rsrc_t rsrc_t;

#define STAMP 0
#define LATE 4
#define QDELAY 40
#define PRE 0
#define PSTR 64
#define ABL 0
#define SLP1 0
#define SLP2 0
#define PSTAG 5
#define DBL 0
#define PA 30
#define PD 620
#define PK0 2
#define D1 10
#define D2 9
#define PV 0
#define LD -45
#define PDV 650
#define STG 0
#define SR 3
#define SR2 0
#define GO 1
#define GB 5
#define TP1 0
#define TP2 0
#define PREAUX 16
#define STAUX 16
#define WIN 6
#define LAUX 16
#define FINE 4
#define FO 25
#define TQ 0
#define TG 0
#define WINV WIN
#define MVM 126
#define HS 0
#define QS 0
#define PADAT 0
#define PADN 2
#define PAD(k) do { if (PADAT == (k)) asm volatile(".fill %c0, 4, 0xBF800000" :: "i"(PADN)); } while (0)
#define CT2 0
#define PERM 0
#define PT 0
#define ROT 37
#define CODE_SPAN 15872
#define CTW 0
#define DIAG 7
#define AMP 8
#define BASE 1200
#define WS_OFF 0

namespace {
constexpr int D = 448, SK = 32768, W = 4096, DH = 256, DF = 1792;
constexpr int AUX_NT = 2, AUX_SC1 = 16;
constexpr int OOB = 1 << 30;
constexpr u32 TMASK = 0xfu;

constexpr int QP_S = 768, GP_S = 1280, O1_S = 2304, O2_S = 3840;
constexpr int EP_OFF = 0;
constexpr int OR_OFF = 2048;
constexpr int ST_OFF = 4096;
constexpr int QP_OFF = ST_OFF + 8192;
constexpr int GP_OFF = QP_OFF + 256 * QP_S;
constexpr int O1_OFF = GP_OFF + 256 * GP_S;
constexpr int O2_OFF = O1_OFF + 256 * O1_S;
constexpr int WS_BYTES = O2_OFF + 256 * O2_S;
constexpr int TS_OFF = 4 << 20;

constexpr int L_XN = 0;
constexpr int L_QX = L_XN + 480;
constexpr int L_GX = L_QX + 128;
constexpr int L_Q = L_GX + 256;
constexpr int L_P = L_Q + 256;
constexpr int L_OP = L_P + 512;
constexpr int L_WM = L_OP + 2048;
constexpr int L_WL = L_WM + 8;
constexpr int L_H = L_WL + 8;
constexpr int L_O = L_H + 16;
constexpr int F_O = L_O + 16;
constexpr int L_R = L_O + 32;
constexpr int F_P = L_R + 128;
constexpr int F_VI = F_P + 4;
constexpr int F_O2 = F_VI + 4;
constexpr int F_Q = F_O2 + 4;
constexpr int F_QX = F_Q + 2, F_PUB = F_QX + 1;
constexpr int F_V = F_PUB + 1;
constexpr int F_R = F_V + 4;
constexpr int L_TAG = F_R + 8;
constexpr int L_DG = L_TAG + 1;
constexpr int L_SH = L_TAG + 2, F_SH = L_TAG + 3;
constexpr int L_END = L_TAG + 4;
constexpr u32 SET = 1u;
static_assert((L_END - L_WM) % 4 == 0 && (L_END - L_WM) / 4 <= 64, "one 16-byte store per lane clears the flag area");

constexpr int T_LAST = PA + PV + (8 * PD + 7 * PDV) / 10;
constexpr u32 SPIN_LDS = 1u << 21, SPIN_MEM = 1u << 16;

__device__ __forceinline__ float asf(u32 v) { return __builtin_bit_cast(float, v); }
__device__ __forceinline__ u32 asu(float v) { return __builtin_bit_cast(u32, v); }
__device__ __forceinline__ float lo16(u32 v) { return asf(v << 16); }
__device__ __forceinline__ float hi16(u32 v) { return asf(v & 0xffff0000u); }
__device__ __forceinline__ u32 pkbf(float a, float b) {
  bf16x2_t r = {(__bf16)a, (__bf16)b};
  return __builtin_bit_cast(u32, r);
}
__device__ __forceinline__ float rbf(float a) { return (float)(__bf16)a; }
__device__ __forceinline__ float dot2(u32 a, u32 b, float c) {
  return __builtin_amdgcn_fdot2_f32_bf16(__builtin_bit_cast(bf16x2_t, a), __builtin_bit_cast(bf16x2_t, b), c, false);
}
template <int CTRL> __device__ __forceinline__ float dpp(float x) {
  return asf(__builtin_amdgcn_mov_dpp((int)asu(x), CTRL, 0xf, 0xf, true));
}
constexpr int XOR1 = 0xB1, XOR2 = 0x4E, ROR8 = 0x128, ROR4 = 0x124;
__device__ __forceinline__ float tr32(float a, float b) {
  auto r = __builtin_amdgcn_permlane32_swap(asu(a), asu(b), false, false);
  return asf(r[0]) + asf(r[1]);
}
__device__ __forceinline__ float tr16(float a, float b) {
  auto r = __builtin_amdgcn_permlane16_swap(asu(a), asu(b), false, false);
  return asf(r[0]) + asf(r[1]);
}
__device__ __forceinline__ float mx32(float a) {
  auto r = __builtin_amdgcn_permlane32_swap(asu(a), asu(a), false, false);
  return fmaxf(asf(r[0]), asf(r[1]));
}
__device__ __forceinline__ float mx16(float a) {
  auto r = __builtin_amdgcn_permlane16_swap(asu(a), asu(a), false, false);
  return fmaxf(asf(r[0]), asf(r[1]));
}
__device__ __forceinline__ float row_sum(float v) { v += dpp<ROR8>(v); v += dpp<ROR4>(v); v += dpp<XOR2>(v); v += dpp<XOR1>(v); return v; }
__device__ __forceinline__ float row_max(float v) {
  v = fmaxf(v, dpp<ROR8>(v)); v = fmaxf(v, dpp<ROR4>(v)); v = fmaxf(v, dpp<XOR2>(v)); v = fmaxf(v, dpp<XOR1>(v)); return v;
}
__device__ __forceinline__ float wave_sum(float v) { v = row_sum(v); v = tr16(v, v); return tr32(v, v); }

__device__ __forceinline__ rsrc_t mkrs(const void* p, u32 bytes) { return __builtin_amdgcn_make_buffer_rsrc((void*)p, 0, bytes, 0x00020000); }
template <int AUX> __device__ __forceinline__ u32x4 ld128(rsrc_t r, int voff, int soff = 0) { return __builtin_amdgcn_raw_buffer_load_b128(r, voff, soff, AUX); }
template <int AUX> __device__ __forceinline__ u32 ld32(rsrc_t r, int voff) { return __builtin_amdgcn_raw_buffer_load_b32(r, voff, 0, AUX); }
__device__ __forceinline__ void st128(rsrc_t r, int voff, u32x4 v) { __builtin_amdgcn_raw_buffer_store_b128(v, r, voff, 0, STAUX); }
__device__ __forceinline__ u32 tagw(float v, u32 tag4) { return (asu(v) & ~TMASK) | tag4; }
__device__ __forceinline__ u32x4 tag4(float a, float b, float c, float d, u32 t) { return u32x4{tagw(a, t), tagw(b, t), tagw(c, t), tagw(d, t)}; }
__device__ __forceinline__ u32 untag4(u32x4& v, u32 t) { v.x ^= t; v.y ^= t; v.z ^= t; v.w ^= t; return v.x | v.y | v.z | v.w; }
__device__ __forceinline__ bool none(bool bad) { return __builtin_amdgcn_ballot_w64(bad) == 0ull; }
__device__ __forceinline__ bool all_ok(u32 bad) { return none((bad & TMASK) != 0u); }

#define CBAR() asm volatile("" ::: "memory")
#define PIN(v) asm volatile("" : "+v"(v))
#define LGKM0() asm volatile("s_waitcnt lgkmcnt(0)" ::: "memory")
template <int N, int SLEEP> __device__ __forceinline__ void flag_wait(LAS u32* p, int lane) {
  u32 v, cnt;
  const u32 addr = (u32)(uintptr_t)(p + (lane & (N - 1)));
  asm volatile("s_mov_b32 %1, %4\n"
               "1:\n\t"
               "ds_read_b32 %0, %2\n\t"
               "s_waitcnt lgkmcnt(0)\n\t"
               "v_cmp_ne_u32 vcc, %3, %0\n\t"
               "s_cbranch_vccz 2f\n\t"
               "s_sleep %5\n\t"
               "s_sub_u32 %1, %1, 1\n\t"
               "s_cbranch_scc0 1b\n"
               "2:"
               : "=&v"(v), "=&s"(cnt) : "v"(addr), "i"(SET), "i"(SPIN_LDS), "i"(SLEEP) : "vcc", "scc", "memory");
}
template <int DUE> __device__ __forceinline__ void pace(u64 org) {
  asm volatile("1:\n\t"
               "s_memrealtime vcc\n\t"
               "s_waitcnt lgkmcnt(0)\n\t"
               "s_sub_u32 vcc_lo, vcc_lo, %0\n\t"
               "s_cmp_lt_i32 vcc_lo, %1\n\t"
               "s_cbranch_scc0 2f\n\t"
               "s_sleep 1\n\t"
               "s_branch 1b\n"
               "2:"
               :: "s"((u32)org), "i"(DUE), "s"(org) : "vcc", "scc", "memory");
}
__device__ __forceinline__ void flag_set(LAS u32* p, u32 set = SET) { CBAR(); *p = set; CBAR(); }

template <int SLP> __device__ __forceinline__ void poll2(rsrc_t rs, int voff, int soff, u32 t4, u32x4& r0, u32x4& r1) {
  u32 o0, o1, o2, o3, o4, o5, o6, o7, m, cnt;
  asm volatile("s_mov_b32 %9, %14\n"
               "1:\n\t"
               "buffer_load_dwordx4 v[144:147], %10, %11, 0 offen sc1\n\t"
               "buffer_load_dwordx4 v[148:151], %10, %11, %12 offen sc1\n\t"
               "s_sub_u32 %9, %9, 1\n\t"
               "s_waitcnt vmcnt(1)\n\t"
               "v_xor_b32 %0, %13, v144\n\tv_xor_b32 %1, %13, v145\n\tv_xor_b32 %2, %13, v146\n\tv_xor_b32 %3, %13, v147\n\t"
               "v_or3_b32 %8, %0, %1, %2\n\t"
               "s_waitcnt vmcnt(0)\n\t"
               "v_xor_b32 %4, %13, v148\n\tv_xor_b32 %5, %13, v149\n\tv_xor_b32 %6, %13, v150\n\tv_xor_b32 %7, %13, v151\n\t"
               "v_or3_b32 %8, %8, %3, %4\n\t"
               "v_or3_b32 %8, %8, %5, %6\n\t"
               "v_bitop3_b32 %8, %8, 15, %7 bitop3:0xc8\n\t"
               "v_cmp_ne_u32 vcc, 0, %8\n\t"
               "s_cbranch_vccz 2f\n\t"
               "s_sleep %15\n\t"
               "s_cbranch_scc0 1b\n"
               "2:"
               : "=&v"(o0), "=&v"(o1), "=&v"(o2), "=&v"(o3), "=&v"(o4), "=&v"(o5), "=&v"(o6), "=&v"(o7), "=&v"(m), "=&s"(cnt)
               : "v"(voff), "s"(rs), "s"(soff), "s"(t4), "i"(SPIN_MEM), "i"(SLP)
               : "vcc", "scc", "memory", "v144", "v145", "v146", "v147", "v148", "v149", "v150", "v151");
  r0 = u32x4{o0, o1, o2, o3};
  r1 = u32x4{o4, o5, o6, o7};
}
__device__ __forceinline__ u32x4 data_wait(LAS u32* flag, LAS u32x4* p) {
  u32x4 r;
  u32 v, cnt;
  asm volatile("s_mov_b32 %2, %5\n"
               "1:\n\t"
               "ds_read_b32 %1, %3\n\t"
               "ds_read_b128 %0, %4\n\t"
               "s_sub_u32 %2, %2, 1\n\t"
               "s_waitcnt lgkmcnt(0)\n\t"
               "v_cmp_ne_u32 vcc, 1, %1\n\t"
               "s_cbranch_vccz 2f\n\t"
               "s_cbranch_scc0 1b\n"
               "2:"
               : "=&v"(r), "=&v"(v), "=&s"(cnt) : "v"((u32)(uintptr_t)flag), "v"((u32)(uintptr_t)p), "i"(SPIN_LDS) : "vcc", "scc", "memory");
  return r;
}

#define MK(k)
#define MV(k)
#define MW(k)
#define NV(k)
#define NW(k)
#define NC(k, c)
#define MK0()

__device__ __forceinline__ void final_reduce(LAS u32* L, rsrc_t rws, const u16* x, u16* out, char* ws, int wg, int w, int grp, int lane, u32 tag, u64 org, u64 tent) {
  LAS float* const LF = (LAS float*)L;
  __builtin_amdgcn_s_setprio(3);
  PAD(6);
  const u32 tag8 = tag & TMASK;
  const int rb = wg / 28, cb = wg % 28, qd = lane & 3;
  const int pi = cb & 7, ph = cb < 24 ? pi >> 2 : pi >> 1, pg = cb < 24 ? pi & 3 : pi & 1;
  const int col = rb * D + 128 * (cb >> 3) + 8 * (4 * pg + ((lane >> 2) & 3)) + 4 * ph + (lane & 3);
  float xres = lo16(x[col]);
  int oo = O2_OFF + (grp * 32 + (lane >> 2)) * O2_S + rb * 1792 + cb * 64 + qd * 16;
  LAS float* wp = LF + ((lane & 12) == 0 ? L_R + (4 * qd + (lane >> 4)) * 8 + grp : L_XN + 128 + lane);
  LAS u32* fp = L + F_R + grp;
  LAS u32* fr = L + F_R + (lane & 7);
  LAS f32x4* rp = (LAS f32x4*)(LF + L_R + (lane & 15) * 8);
  int so = lane < 16 ? col * 2 : OOB;
  u32 tagv = SET;
  rsrc_t ro = mkrs(out, 2 * D * 2);
  asm volatile("" : "+s"(ro));
  PIN(xres); PIN(oo); PIN(wp); PIN(fp); PIN(fr); PIN(rp); PIN(so); PIN(tagv);
  flag_wait<1, 1>(L + F_PUB, lane);
  flag_wait<4, 0>(L + F_O2, lane);
  if (D2) __builtin_amdgcn_s_sleep(D2);
  if (TP2) pace<T_LAST + TP2>(org);
  u32x4 a0, a1;
  poll2<SLP2>(rws, oo, 16 * O2_S, tag8, a0, a1);
  MK(6);
  NV(4);
  float f = tr16(tr32(asf(a0.x) + asf(a1.x), asf(a0.z) + asf(a1.z)), tr32(asf(a0.y) + asf(a1.y), asf(a0.w) + asf(a1.w)));
  f += dpp<ROR8>(f); f += dpp<ROR4>(f);
  *wp = f;
  flag_set(fp, tagv);
  u32 fl = *fr;
  CBAR();
  const f32x4 r0 = rp[0], r1 = rp[1];
  const f32x4 rs4 = r0 + r1;
  const float tot = xres + ((rs4.x + rs4.z) + (rs4.y + rs4.w));
  u32 res = pkbf(tot, tot);
  PIN(res);
  if (none(fl != SET)) {
    __builtin_amdgcn_raw_buffer_store_b16((u16)res, ro, so, 0, 0);
    if (wg == 0 && lane == 0) __builtin_amdgcn_raw_buffer_store_b32(tag, rws, EP_OFF, 0, AUX_SC1);
    if (wg == 0) {
      const u64 now = __builtin_amdgcn_s_memrealtime();
      u64 ev = DIAG == 0 ? tent : DIAG == 7 ? now : tent + __builtin_amdgcn_readfirstlane(L[L_DG]);
      const u64 until = tent + 3000 + AMP * (ev - tent - BASE);
      while (__builtin_amdgcn_s_memrealtime() < until) __builtin_amdgcn_s_sleep(1);
    }
    MK(7);
  }
  NW(4);
}

}

extern "C" __global__ void __launch_bounds__(768, 3)
fused_decode(const u16* __restrict__ x, const u16* __restrict__ wq, const u16* __restrict__ wg_, const u16* __restrict__ wu,
             const u16* __restrict__ kc, char* __restrict__ ws, const u16* __restrict__ vc, const u16* __restrict__ wo,
             const u16* __restrict__ wd, u16* __restrict__ out) {
  __shared__ __attribute__((aligned(16))) u32 smem[L_END];
  LAS u32* const L = (LAS u32*)smem;
  LAS float* const LF = (LAS float*)smem;
  const int wg = blockIdx.x, h = wg & 7, j = wg >> 3, b = j & 1, sp = j >> 1, reg = wg & 15;
  const int w = __builtin_amdgcn_readfirstlane(threadIdx.x >> 6), lane = threadIdx.x & 63, kw = w & 3;
  const u64 tent = __builtin_amdgcn_s_memrealtime();
#undef MK
#define MK(k) do { if (DIAG == (k)) { const u32 dt_ = (u32)(__builtin_amdgcn_s_memrealtime() - tent); if (lane == 0) (void)__hip_atomic_fetch_max(L + L_DG, dt_, __ATOMIC_RELAXED, __HIP_MEMORY_SCOPE_WORKGROUP); } } while (0)
  asm volatile("s_load_dword s94, %0, 0x0\n\ts_load_dword s95, %0, 0x40" :: "s"(__builtin_amdgcn_kernarg_segment_ptr()) : "s94", "s95");
  u64 pc0;
  asm volatile("s_getpc_b64 %0" : "=s"(pc0));
  const u32 code_touch = ld32<0>(mkrs((const char*)(pc0 & ~255ull), w == CTW ? CODE_SPAN : 0), lane < 5 ? j * 640 + lane * 128 : OOB);
  __builtin_amdgcn_sched_barrier(0);
#define ROLE_ENTRY() const rsrc_t rws = mkrs(ws, WS_BYTES)
#define ROLE_TAG() const u32 tag = __builtin_amdgcn_readfirstlane(L[L_TAG]), tag8 = tag & TMASK
  const u32 blk0 = sp * 32 + reg * ROT;
  const u32 srot = sp * SR + reg * SR2;
  const u32 gm = PERM ? 2 * ((sp + (PERM == 2 ? reg : 0)) & 3) + 1 : 1, gc = PERM ? (sp >> 2) + reg : 0;
#define BLK(slot) (PERM ? ((((((slot) >> 2) * gm + gc) & 7u) << 2) + ((slot) & 3u) + srot) & 31u : ((slot) + srot) & 31u)
  const int hq = (h + QS) & 7, ho = (h + HS) & 7;
  u64 org;
#define STREAM_LOADS(SRC, VOFF, SOFF, BAR) \
  const rsrc_t rkv = mkrs((SRC) + ((size_t)reg * SK + (SK - W)) * DH, W * DH * 2); \
  u32x4 d[32]; \
  if (BAR) { __builtin_amdgcn_sched_barrier(0); __builtin_amdgcn_s_barrier(); org = __builtin_amdgcn_s_memrealtime(); __builtin_amdgcn_sched_barrier(0); } \
  MV(2); \
  STREAM_GROUP(0, VOFF, SOFF, BAR) STREAM_GROUP(1, VOFF, SOFF, BAR) STREAM_GROUP(2, VOFF, SOFF, BAR) STREAM_GROUP(3, VOFF, SOFF, BAR) \
  STREAM_GROUP(4, VOFF, SOFF, BAR) STREAM_GROUP(5, VOFF, SOFF, BAR) STREAM_GROUP(6, VOFF, SOFF, BAR) STREAM_GROUP(7, VOFF, SOFF, BAR)
#define STREAM_GROUP(G, VOFF, SOFF, ISK) \
  if (PD > 0 && (!(ISK) || (G) >= PK0)) pace<PA + ((ISK) ? (G) * PD / 10 : PV + (8 * PD + (G) * PDV) / 10 - ((G) >= FINE ? FO : 0))>(org + kw * STG); \
  if (((ISK) ? WIN : WINV) > 0 && 4 * (G) > ((ISK) ? WIN : WINV)) asm volatile("s_waitcnt vmcnt(%0)" :: "i"((ISK) ? WIN : WINV) : "memory"); \
  _Pragma("unroll") for (int i = 4 * (G); i < 4 * (G) + 2; ++i) d[i] = ld128<AUX_NT>(rkv, (VOFF), (int)(SOFF)); \
  if (!(ISK) && (G) >= FINE) { __builtin_amdgcn_sched_barrier(0); pace<PA + PV + (8 * PD + (G) * PDV + PDV / 2) / 10 - FO>(org + kw * STG); } \
  _Pragma("unroll") for (int i = 4 * (G) + 2; i < 4 * (G) + 4; ++i) d[i] = ld128<AUX_NT>(rkv, (VOFF), (int)(SOFF)); \
  __builtin_amdgcn_sched_barrier(0);

  if (__builtin_expect(w >= 4, 0)) {
  u32 tag_out;
  if (w < 8) {
    __builtin_amdgcn_s_setprio(2);
    PAD(3);
    const int g = lane >> 4, key = (lane >> 1) & 7;
    const bool hb = lane & 1;
    const int kvo = key * 512 + (2 * g + (lane & 1)) * 16;
    STREAM_LOADS(kc, kvo + (i & 3) * 128, ((blk0 + BLK(kw + 4 * (i >> 2))) & 511u) << 12, true);
    MV(3);
    ROLE_ENTRY();
    MK0(); MW(2); MW(3);
    PAD(4);
    flag_wait<2, 1>(L + F_Q, lane);
    ROLE_TAG();
    u32x4 qa[4];
#pragma unroll
    for (int i = 0; i < 4; ++i) qa[i] = *(LAS u32x4*)(L + L_Q + (lane & 1) * 128 + i * 32 + (2 * g + ((lane >> 1) & 1)) * 4);
    f32x4 ea, eb;
#pragma unroll
    for (int n = 0; n < 4; ++n) {
      f32x4 ca = {}, cb = {};
#pragma unroll
      for (int i = 0; i < 4; ++i) {
        ca = __builtin_amdgcn_mfma_f32_16x16x32_bf16(__builtin_bit_cast(bf16x8_t, qa[i]), __builtin_bit_cast(bf16x8_t, d[8 * n + i]), ca, 0, 0, 0);
        cb = __builtin_amdgcn_mfma_f32_16x16x32_bf16(__builtin_bit_cast(bf16x8_t, qa[i]), __builtin_bit_cast(bf16x8_t, d[8 * n + 4 + i]), cb, 0, 0, 0);
      }
#pragma unroll
      for (int k = 0; k < 4; ++k) { ea[k] = (n == 0 || g == n) ? ca[k] : ea[k]; eb[k] = (n == 0 || g == n) ? cb[k] : eb[k]; }
    }
    asm volatile("" :: "v"(code_touch));
    float sc[2];
#pragma unroll
    for (int hd = 0; hd < 2; ++hd) sc[hd] = rbf((hb ? eb[2 + hd] : ea[hd]) + dpp<XOR1>(hb ? ea[2 + hd] : eb[hd])) * 0.0625f;
    const float wm0 = mx32(mx16(row_max(sc[0]))), wm1 = mx32(mx16(row_max(sc[1])));
    const u32 tag16 = SET;
    CBAR();
    if (lane < 2) L[L_WM + kw * 2 + lane] = (asu(lane ? wm1 : wm0) & 0xffff0000u) | tag16;
    u32x4 ma, mb;
    for (u32 s = 0; s < SPIN_LDS; ++s) {
      CBAR();
      ma = *(LAS u32x4*)(L + L_WM) ^ tag16; mb = *(LAS u32x4*)(L + L_WM + 4) ^ tag16;
      if (none((((ma.x | ma.y | ma.z | ma.w) | (mb.x | mb.y | mb.z | mb.w)) & 0xffffu) != 0u)) break;
    }
    const float M0 = fmaxf(fmaxf(asf(ma.x), asf(ma.z)), fmaxf(asf(mb.x), asf(mb.z)));
    const float M1 = fmaxf(fmaxf(asf(ma.y), asf(ma.w)), fmaxf(asf(mb.y), asf(mb.w)));
    const float p0 = rbf(__builtin_amdgcn_exp2f((sc[0] - M0) * 1.4426950408889634f));
    const float p1 = rbf(__builtin_amdgcn_exp2f((sc[1] - M1) * 1.4426950408889634f));
    LAS u32* pp = L + L_P + key * 64 + kw + 4 * (2 * g + (lane & 1));
    pp[0] = asu(p0) | (asu(p0) >> 16);
    pp[32] = asu(p1) | (asu(p1) >> 16);
    float ls = tr32(p0, p1);
    ls = row_sum(tr16(ls, ls));
    if ((lane & 31) == 0) LF[L_WL + kw * 2 + (lane >> 5)] = ls;
    flag_set(L + F_P + kw);
    PAD(5);
    MK(2);

    if (kw == 0) {
      flag_wait<4, 0>(L + F_P, lane);
      if (lane < 2) {
        const float Ls = (LF[L_WL + lane] + LF[L_WL + 2 + lane]) + (LF[L_WL + 4 + lane] + LF[L_WL + 6 + lane]);
        st128(rws, ST_OFF + (((h * 2 + lane) * 2 + b) * 16 + sp) * 16, u32x4{asu(lane ? M1 : M0), tag, asu(Ls), tag});
      }
    } else if (kw == 1) {
      __builtin_amdgcn_s_setprio(3);
      PAD(7);
      const int s = lane >> 2, qd = lane & 3;
      const int so = ST_OFF + ((ho * 2 + (j >> 4)) * 32 + s) * 16;
      const int oo = O1_OFF + (ho * 32 + 2 * s) * O1_S + j * 64 + qd * 16;
      u32x4 s0, s1, a0, a1;
      flag_wait<4, 1>(L + F_VI, lane);
      for (u32 it = 0; it < SPIN_MEM; ++it) {
        CBAR();
        s0 = ld128<AUX_SC1>(rws, so); s1 = ld128<AUX_SC1>(rws, so + 256);
        if (none((s0.y != tag) | (s0.w != tag) | (s1.y != tag) | (s1.w != tag))) break;
        flag_wait<1, 1>(L + F_PUB, lane);
      }
      float wgt[2];
#pragma unroll
      for (int bb = 0; bb < 2; ++bb) {
        const float m = asf(bb ? s1.x : s0.x), l = asf(bb ? s1.z : s0.z);
        float mg = fmaxf(m, dpp<ROR8>(m)); mg = fmaxf(mg, dpp<ROR4>(mg)); mg = mx32(mx16(mg));
        const float e = __builtin_amdgcn_exp2f((m - mg) * 1.4426950408889634f);
        float lg = e * l; lg += dpp<ROR8>(lg); lg += dpp<ROR4>(lg); lg = tr16(lg, lg); lg = tr32(lg, lg);
        wgt[bb] = e * __builtin_amdgcn_rcpf(lg);
      }
      LAS u32* ow = L + ((lane & 12) == 0 ? L_O + qd * 4 + (lane >> 4) : L_XN + lane);
      LAS u32* of = L + F_O;
      u32 setv = SET;
      PIN(wgt[0]); PIN(wgt[1]); PIN(ow); PIN(of); PIN(setv);
      if (GO) org += (u64)(long long)(int)__builtin_amdgcn_readfirstlane(L[L_SH]);
      PAD(8);
      flag_wait<1, 0>(L + F_PUB, lane);
      if (D1) __builtin_amdgcn_s_sleep(D1);
      if (TP1) pace<T_LAST + TP1>(org);
      poll2<SLP1>(rws, oo, O1_S, tag8, a0, a1);
      NV(2);
      f32x4 m0 = __builtin_bit_cast(f32x4, a0) * wgt[0], m1 = __builtin_bit_cast(f32x4, a1) * wgt[1];
      PIN(m0); PIN(m1);
      const float v0 = tr32(m0.x, m1.x), v2 = tr32(m0.z, m1.z), v1 = tr32(m0.y, m1.y), v3 = tr32(m0.w, m1.w);
      float e0 = tr16(v0, v2), e1 = tr16(v1, v3);
      e0 += dpp<ROR8>(e0); e1 += dpp<ROR8>(e1);
      e0 += dpp<ROR4>(e0); e1 += dpp<ROR4>(e1);
      *ow = pkbf(e0, e1);
      flag_set(of, setv);
      MK(4); NW(2);
    }
    if (GO != 0 && TP2 != 0 && kw != 1) { flag_wait<1, 4>(L + F_SH, lane); org += (u64)(long long)(int)__builtin_amdgcn_readfirstlane(L[L_SH]); }
    if (PRE && PRE < 3 && kw >= 2) {
      const int po = kw == 2 ? O1_OFF + (h * 32 + j) * O1_S : O2_OFF + (h * 32 + j) * O2_S;
      const u32 t0 = ld32<AUX_SC1>(rws, lane * PSTR < (kw == 2 ? 2048 : PRE == 2 ? 0 : 3584) ? po + lane * PSTR : OOB);
      asm volatile("" :: "v"(t0));
    }
    if (CT2 && kw == 3) {
      if (CT2 == 2) flag_wait<1, 4>(L + F_VI, lane);
      if (CT2 == 4) pace<T_LAST - 150>(org);
      u32 t0;
      if (CT2 >= 3) { const rsrc_t rc = mkrs((const char*)(pc0 & ~255ull), CODE_SPAN); t0 = ld32<0>(rc, lane * 128) | ld32<0>(rc, 8192 + lane * 128); }
      else t0 = ld32<0>(mkrs((const char*)(pc0 & ~255ull), CODE_SPAN), lane < 5 ? j * 640 + lane * 128 : OOB);
      asm volatile("" :: "v"(t0));
    }
    if (PRE >= 3 && kw >= 2) {
      u32 t0 = 0u;
      if (kw == 2) t0 = ld32<PREAUX>(rws, lane < 32 ? O1_OFF + (h * 32 + lane) * O1_S + j * 64 : OOB);
      else if (PRE == 4 && wg < 56) {
        const int po = O2_OFF + lane * O2_S + (wg / 28) * 1792 + (wg % 28) * 64;
#pragma unroll
        for (int i = 0; i < 4; ++i) t0 |= __builtin_amdgcn_raw_buffer_load_b32(rws, po, i * 64 * O2_S, PREAUX);
      }
      asm volatile("" :: "v"(t0));
    }
    tag_out = tag;
  } else {
    if (kw == 0 && lane < (L_END - L_WM) / 4) *(LAS u32x4*)(L + L_WM + 4 * lane) = u32x4{0u, 0u, 0u, 0u};
    LGKM0();
    __builtin_amdgcn_s_barrier();
    org = __builtin_amdgcn_s_memrealtime();
    __builtin_amdgcn_s_setprio(1);
    __builtin_amdgcn_sched_barrier(0);
    ROLE_ENTRY();
    if (GO) { flag_wait<1, 4>(L + F_SH, lane); org += (u64)(long long)(int)__builtin_amdgcn_readfirstlane(L[L_SH]); }
    STREAM_LOADS(vc, lane * 16 + kw * 1024, ((blk0 + BLK((u32)i)) & 511u) << 12, false);
    MV(4);
    flag_set(L + F_VI + kw);
    PAD(1);
    MK0();
    const int m = lane & 31, hf = lane >> 5;
    u32x4 msk;
#pragma unroll
    for (int r = 0; r < 4; ++r) msk[r] = (m < 16 && ((m & 7) >> 1) == r) ? ((m & 1) ? 0xffff0000u : 0x0000ffffu) : 0u;
    LAS f32x4* op = (LAS f32x4*)(LF + L_OP) + kw * 128 + 2 * m + hf;
    LAS f32x4* rp = (LAS f32x4*)(LF + L_OP) + lane;
    LAS u32* fp = L + F_V + kw;
    LAS u32* fr = L + F_V + (lane & 3);
    int o1o = O1_OFF + (h * 32 + j) * O1_S + lane * 16;
    PIN(op); PIN(rp); PIN(fp); PIN(fr); PIN(o1o);
    flag_wait<4, 1>(L + F_P, lane);
    ROLE_TAG();
    const LAS u32x4* pp = (const LAS u32x4*)(L + L_P + ((kw * 2 + hf) * 2 + ((m >> 3) & 1)) * 32);
    f32x16 acc = {};
#pragma unroll
    for (int g = 0; g < 8; ++g) {
      const u32x4 p4 = pp[g];
#pragma unroll
      for (int k = 0; k < 4; ++k) {
        const u32x4 a = msk & p4[k];
        acc = __builtin_amdgcn_mfma_f32_32x32x16_bf16(__builtin_bit_cast(bf16x8_t, a), __builtin_bit_cast(bf16x8_t, d[4 * g + k]), acc, 0, 0, 0);
      }
      __builtin_amdgcn_sched_barrier(0);
    }
    asm volatile("" :: "v"(code_touch));
    PAD(2);
    MV(6); NV(1);
    op[0] = f32x4{acc[0], acc[1], acc[2], acc[3]};
    op[64] = f32x4{acc[4], acc[5], acc[6], acc[7]};
    flag_set(fp);
    const u32 fl = *fr;
    CBAR();
    const f32x4 o0 = (rp[0] + rp[128]) + (rp[256] + rp[384]);
    const f32x4 o1 = (rp[64] + rp[192]) + (rp[320] + rp[448]);
    u32x4 t0 = tag4(o0.x, o0.y, o0.z, o0.w, tag8), t1 = tag4(o1.x, o1.y, o1.z, o1.w, tag8);
    PIN(t0); PIN(t1);
    if (none(fl != SET)) {
      st128(rws, o1o, t0);
      st128(rws, o1o + 1024, t1);
      flag_set(L + F_PUB);
      MK(3);
    }
    MW(4); MW(6); NW(1);
    tag_out = tag;
  }
  if (wg < 56 && (wg % 28) < ((ABL & 64) ? 16 : (ABL & 128) ? 8 : 28)) final_reduce(L, mkrs(ws, WS_BYTES), x, out, ws, wg, w, w - 4, lane, tag_out, org, tent);
  return;
  }

  __builtin_amdgcn_s_setprio(3);
  ROLE_ENTRY();
  const bool mlp = wg < 224;
  const int cb2 = wg % 28, ke = wg / 28;
  const int rr = lane >> 3, c8 = lane & 7;
  const rsrc_t rx = mkrs((const u16*)x, 2 * D * 2);
  const u32x4 xa = ld128<0>(rx, lane < 56 ? lane * 16 : OOB), xb = ld128<0>(rx, lane < 56 ? D * 2 + lane * 16 : OOB);
  const u32 epoch = ld32<AUX_SC1>(rws, EP_OFF);
  const int row0 = kw < 2 ? 112 * (j >> 3) + 64 * kw : 56 * ke;
  const int nrow = kw == 0 ? 64 : kw == 1 ? 48 : mlp ? 56 : 0;
  u32x4 wv[8];
  {
    const rsrc_t rw = mkrs(kw < 2 ? wq : kw == 2 ? wg_ : wu, kw < 2 ? ((ABL & 32) ? 0 : D * 4096 * 2) : ((ABL & 8) ? 0 : D * DF * 2));
    const int rs = kw < 2 ? 8192 : DF * 2;
    const int vw0 = nrow ? (row0 + 2 * rr) * rs + (kw < 2 ? (hq * 512 + (j & 7) * 64) * 2 : cb2 * 128) + c8 * 16 : OOB;
    const int vw3 = 48 + 2 * rr < nrow ? vw0 : OOB;
#pragma unroll
    for (int i = 0; i < 8; ++i) wv[i] = ld128<AUX_NT>(rw, i < 6 ? vw0 : vw3, (16 * (i >> 1) + (i & 1)) * rs);
  }
  const int eo = lane < 8 ? QP_OFF + (hq * 32 + j) * QP_S + lane * 64 : lane < 24 ? GP_OFF + wg * GP_S + (lane - 8) * 64 : ST_OFF + (wg & 127) * 64;
  const u32 box_touch = ld32<AUX_SC1>(rws, (!(ABL & 16) && kw == 1 && lane < 25) ? eo : OOB);
  __builtin_amdgcn_sched_barrier(0);
  __builtin_amdgcn_s_barrier();
  org = __builtin_amdgcn_s_memrealtime();
  __builtin_amdgcn_sched_barrier(0);
  MK0();
  u32 page_touch = 0u;
  if (PT && kw == 3) {
    const char* pb = lane < 4 ? (const char*)wo + (size_t)(ho * 512 + j * 16) * (D * 2) : lane < 6 ? (const char*)wd + (size_t)(mlp ? 64 * cb2 + 8 * ke : 0) * (D * 2)
                   : lane == 6 ? (const char*)vc + (((size_t)reg * SK + (SK - W)) * DH * 2 + ((blk0 & 511u) << 12)) : lane == 7 ? (const char*)out : ws + O2_OFF + (h * 32 + j) * O2_S;
    const int po = lane < 4 ? (lane == 3 ? 14332 : lane * 4096) : lane == 5 ? 7164 : 0;
    if (lane < 9) page_touch = __builtin_nontemporal_load((const u32*)(pb + po));
  }
  __builtin_amdgcn_sched_barrier(0);

#pragma unroll
  for (int bb = 0; bb < 2; ++bb) {
    const u32x4 xv = bb ? xb : xa;
    float v[8];
#pragma unroll
    for (int k = 0; k < 4; ++k) { v[2 * k] = lo16(xv[k]); v[2 * k + 1] = hi16(xv[k]); }
    float s = 0.f;
#pragma unroll
    for (int k = 0; k < 8; ++k) s += v[k];
    const float mean = wave_sum(s) * (1.f / D);
    float q = 0.f;
#pragma unroll
    for (int k = 0; k < 8; ++k) { const float t = v[k] - mean; q += t * t; }
    q = lane < 56 ? q : 0.f;
    const float var = rbf(wave_sum(q) * (1.f / D));
    const float den = rbf(__builtin_amdgcn_sqrtf(rbf(var + 1e-5f))), rden = __builtin_amdgcn_rcpf(den), mb = rbf(mean);
#pragma unroll
    for (int k = 0; k < 8; ++k) { const float c = rbf(v[k] - mb), r = c * rden; v[k] = fmaf(fmaf(-r, den, c), rden, r); }
    u32x4 o;
#pragma unroll
    for (int k = 0; k < 4; ++k) o[k] = pkbf(v[2 * k], v[2 * k + 1]);
    if (lane < 56) *(LAS u32x4*)(L + L_XN + bb * 224 + lane * 4) = o;
    else if (bb == 1) *(LAS u32x4*)(L + L_XN + 224 + lane * 4) = u32x4{0u, 0u, 0u, 0u};
  }
  asm volatile("" :: "v"(code_touch));
  const u32 tag = __builtin_amdgcn_readfirstlane(epoch) + 1u, tag8 = tag & TMASK;
  if (kw == 0) L[L_TAG] = tag;
  if (GO && kw == 3 && wg < 8 && lane == 0) __builtin_amdgcn_raw_buffer_store_b64(u32x2{(u32)org, tag}, rws, OR_OFF + wg * 8, 0, AUX_SC1);
  MK(10);

  {
    float a0[8] = {}, a1[8] = {};
#pragma unroll
    for (int k = 0; k < 4; ++k) {
      const u32 x0 = L[L_XN + row0 / 2 + rr + 8 * k], x1 = L[L_XN + 224 + row0 / 2 + rr + 8 * k];
#pragma unroll
      for (int c = 0; c < 4; ++c) {
        const u32 pl = __builtin_amdgcn_perm(wv[2 * k + 1][c], wv[2 * k][c], 0x05040100u), ph = __builtin_amdgcn_perm(wv[2 * k + 1][c], wv[2 * k][c], 0x07060302u);
        a0[2 * c] = dot2(pl, x0, a0[2 * c]); a0[2 * c + 1] = dot2(ph, x0, a0[2 * c + 1]);
        a1[2 * c] = dot2(pl, x1, a1[2 * c]); a1[2 * c + 1] = dot2(ph, x1, a1[2 * c + 1]);
      }
    }
    asm volatile("" :: "v"(box_touch));
    MV(5);
    float t[8], u[4];
#pragma unroll
    for (int k = 0; k < 8; ++k) t[k] = tr32(a0[k], a1[k]);
#pragma unroll
    for (int k = 0; k < 4; ++k) { u[k] = tr16(t[k], t[k + 4]); u[k] += dpp<ROR8>(u[k]); }
    const int bb = lane >> 5, hf = (lane >> 4) & 1;
    LAS f32x4* qx = (LAS f32x4*)(LF + L_QX) + bb * 16 + c8 * 2 + hf;
    if (kw == 1) {
      if (!(lane & 8)) *qx = f32x4{u[0], u[1], u[2], u[3]};
      flag_set(L + F_QX);
    } else if (kw == 0) {
      flag_wait<1, 0>(L + F_QX, lane);
      const f32x4 o = *qx;
      if (!(ABL & 1) && !(lane & 8)) st128(rws, QP_OFF + (hq * 32 + j) * QP_S + bb * 256 + hf * 128 + c8 * 16, tag4(u[0] + o.x, u[1] + o.y, u[2] + o.z, u[3] + o.w, tag8));
    } else if (!(ABL & 2) && mlp) {
      LAS u32x4* gx = (LAS u32x4*)(L + L_GX) + (kw - 2) * 32;
      if (!(lane & 8)) gx[c8 * 4 + bb * 2 + hf] = tag4(u[0], u[1], u[2], u[3], tag8);
      CBAR();
      if (lane < 32) st128(rws, GP_OFF + wg * GP_S + (lane >> 2) * 128 + (kw - 2) * 64 + (lane & 3) * 16, gx[lane]);
    }
  }

  if (kw < 2) {
    __builtin_amdgcn_s_sleep(QDELAY);
    if (TQ) pace<TQ>(org);
    const int qo = QP_OFF + (h * 32 + 4 * kw + 8 * (lane >> 4)) * QP_S + b * 256 + (lane & 15) * 16;
    u32x4 a[4];
    for (u32 s = 0; s < SPIN_MEM; ++s) {
      CBAR();
      if (ABL & 1) { a[0] = a[1] = a[2] = a[3] = xa; break; }
#pragma unroll
      for (int i = 0; i < 4; ++i) a[i] = ld128<AUX_SC1>(rws, qo, i * QP_S);
      if (all_ok((untag4(a[0], tag8) | untag4(a[1], tag8)) | (untag4(a[2], tag8) | untag4(a[3], tag8)))) break;
    }
#pragma unroll
    for (int i = 0; i < 2; ++i) {
      const u32x4 a0 = a[2 * i], a1 = a[2 * i + 1];
      const float v0 = tr32(asf(a0.x), asf(a1.x)), v1 = tr32(asf(a0.y), asf(a1.y)), v2 = tr32(asf(a0.z), asf(a1.z)), v3 = tr32(asf(a0.w), asf(a1.w));
      L[L_Q + (4 * kw + 2 * i + (lane >> 5)) * 32 + 4 * (lane & 7) + 2 * ((lane >> 3) & 1) + ((lane >> 4) & 1)] = pkbf(tr16(v0, v2), tr16(v1, v3));
    }
    flag_set(L + F_Q + kw);
    MK(1);
  } else if (kw == 2) {
    u32 h0 = 0u, h1 = 0u;
    if (mlp) {
      const int go = GP_OFF + (cb2 + 28 * rr) * GP_S + ke * 128 + c8 * 16;
      u32x4 a;
      flag_wait<2, 1>(L + F_Q, lane);
      if (TG) pace<TG>(org);
      for (u32 s = 0; s < SPIN_MEM; ++s) {
        CBAR();
        if (ABL & 2) { a = xa; break; }
        a = ld128<AUX_SC1>(rws, go);
        if (all_ok(untag4(a, tag8))) break;
        __builtin_amdgcn_s_sleep(16);
      }
      float hv[4];
#pragma unroll
      for (int k = 0; k < 4; ++k) {
        float g = asf(a[k]);
        g += dpp<ROR8>(g); g = tr16(g, g); g = tr32(g, g);
        g = rbf(g);
        const float uu = dpp<ROR4>(g);
        const float z = 0.7978845608028654f * (g + 0.044715f * g * g * g);
        const float th = 1.f - 2.f * __builtin_amdgcn_rcpf(1.f + __builtin_amdgcn_exp2f(z * 2.8853900817779268f));
        hv[k] = rbf(0.5f * g * (1.f + th)) * uu;
      }
      h0 = pkbf(hv[0], hv[1]); h1 = pkbf(hv[2], hv[3]);
    }
    if (lane < 4) {
      *(LAS u32x2*)(L + L_H + (2 * (lane & 1)) * 4 + (lane >> 1) * 2) = u32x2{h0, SET};
      *(LAS u32x2*)(L + L_H + (2 * (lane & 1) + 1) * 4 + (lane >> 1) * 2) = u32x2{h1, SET};
    }
    CBAR();
  } else if (GO) {
    pace<300>(org);
    const u32x2 o = __builtin_amdgcn_raw_buffer_load_b64(rws, OR_OFF + (lane & 7) * 8, 0, AUX_SC1);
    const bool ok = o.y == tag;
    const float n = row_sum(ok ? 1.f : 0.f), dsum = row_sum(ok ? (float)(int)(o.x - (u32)org) : 0.f);
    const float sh = n > 0.f ? fminf(fmaxf(dsum * __builtin_amdgcn_rcpf(n) + GB, -80.f), 80.f) : 0.f;
    L[L_SH] = (u32)(int)sh;
    flag_set(L + F_SH);
  }

  asm volatile("" :: "v"(page_touch));
  const int rg = lane >> 4, cl = lane & 15;
  const int co = kw * 128 + cl * 8;
  u32x4 wa[4], wb[2];
  if (LATE == 0) flag_wait<2, 1>(L + F_Q, lane);
  if (LATE >= 1) flag_wait<4, 1>(L + F_VI, lane);
  if (LATE == 3) flag_wait<1, 0>(L + F_PUB, lane);
  if (GO) org += (u64)(long long)(int)__builtin_amdgcn_readfirstlane(L[L_SH]);
  if (LATE == 4) pace<PA + PV + (8 * PD + 8 * PDV) / 10 + LD>(org + kw * STG);
  if (LATE == 5) __builtin_amdgcn_s_sleep(LD);
  {
    const rsrc_t ro = mkrs(wo, (ABL & 4) ? 0 : 4096 * D * 2), rd = mkrs(wd, mlp && !(ABL & 4) ? DF * D * 2 : 0);
    const int vo = co < D ? (ho * 512 + j * 16 + 4 * rg) * (D * 2) + co * 2 : OOB;
    const int vd = co < D ? (64 * cb2 + 8 * ke + 2 * rg) * (D * 2) + co * 2 : OOB;
#pragma unroll
    for (int i = 0; i < 2; ++i) wb[i] = ld128<AUX_NT>(rd, vd, i * D * 2);
#pragma unroll
    for (int i = 0; i < 4; ++i) wa[i] = ld128<AUX_NT>(ro, vo, i * D * 2);
  }
  __builtin_amdgcn_sched_barrier(0);
  float a0[8], a1[8];
  u32 pa[8], pb[8];
  {
    u32x4 hh;
    for (u32 s = 0; s < SPIN_LDS; ++s) {
      CBAR();
      hh = *(LAS u32x4*)(L + L_H + rg * 4);
      if (none((hh.y != SET) | (hh.w != SET))) break;
      __builtin_amdgcn_s_sleep(1);
    }
#pragma unroll
    for (int k = 0; k < 4; ++k) {
      const u32 pl = __builtin_amdgcn_perm(wb[1][k], wb[0][k], 0x05040100u), ph = __builtin_amdgcn_perm(wb[1][k], wb[0][k], 0x07060302u);
      a0[2 * k] = dot2(pl, hh.x, 0.f); a0[2 * k + 1] = dot2(ph, hh.x, 0.f);
      a1[2 * k] = dot2(pl, hh.z, 0.f); a1[2 * k + 1] = dot2(ph, hh.z, 0.f);
    }
#pragma unroll
    for (int k = 0; k < 4; ++k) {
      pa[2 * k] = __builtin_amdgcn_perm(wa[1][k], wa[0][k], 0x05040100u); pa[2 * k + 1] = __builtin_amdgcn_perm(wa[1][k], wa[0][k], 0x07060302u);
      pb[2 * k] = __builtin_amdgcn_perm(wa[3][k], wa[2][k], 0x05040100u); pb[2 * k + 1] = __builtin_amdgcn_perm(wa[3][k], wa[2][k], 0x07060302u);
    }
  }
#pragma unroll
  for (int k = 0; k < 8; ++k) asm volatile("" : "+v"(a0[k]), "+v"(a1[k]), "+v"(pa[k]), "+v"(pb[k]));
  int o2o = !((ABL & 256) && j >= 16) && co < ((ABL & 64) ? 256 : (ABL & 128) ? 128 : D) ? O2_OFF + (h * 32 + j) * O2_S + (lane >> 5) * 1792 + kw * 512 + (((lane >> 4) & 1) * (kw == 3 ? 8 : 16) + cl) * 16 : OOB;
  LAS u32x4* orp = (LAS u32x4*)(L + L_O + rg * 4);
  LAS u32* ofp = L + F_O;
  PIN(o2o); PIN(orp); PIN(ofp);
  if (LATE == 0) flag_wait<4, 1>(L + F_VI, lane);
  {
    const u32x4 oo = data_wait(ofp, orp);
#pragma unroll
    for (int k = 0; k < 8; ++k) {
      a0[k] = dot2(pb[k], oo.y, dot2(pa[k], oo.x, a0[k]));
      a1[k] = dot2(pb[k], oo.w, dot2(pa[k], oo.z, a1[k]));
    }
    float t[8];
#pragma unroll
    for (int k = 0; k < 8; ++k) t[k] = tr32(a0[k], a1[k]);
    st128(rws, o2o, tag4(tr16(t[0], t[4]), tr16(t[1], t[5]), tr16(t[2], t[6]), tr16(t[3], t[7]), tag8));
  }
  flag_set(L + F_O2 + kw);
  NV(3); NW(3);
  MK(5);
}

extern "C" void kernel_launch(void* const* d_in, const int* in_sizes, int n_in, void* d_out, int out_size, void* d_ws, size_t ws_size,
                              hipStream_t stream) {
  fused_decode<<<256, 768, 0, stream>>>((const u16*)d_in[0], (const u16*)d_in[3], (const u16*)d_in[6], (const u16*)d_in[7], (const u16*)d_in[1],
                                        (char*)d_ws + WS_OFF, (const u16*)d_in[2], (const u16*)d_in[4], (const u16*)d_in[8], (u16*)d_out);
}
```

```cpp
#include <hip/hip_runtime.h>
#include <hip/hip_bf16.h>
#include <stdint.h>

typedef unsigned int u32;
typedef unsigned short u16;
typedef unsigned long long u64;
typedef u32 u32x2 __attribute__((ext_vector_type(2)));
typedef u32 u32x4 __attribute__((ext_vector_type(4)));
typedef float f32x4 __attribute__((ext_vector_type(4)));
typedef float f32x16 __attribute__((ext_vector_type(16)));
typedef __bf16 bf16x2_t __attribute__((ext_vector_type(2)));
typedef __bf16 bf16x8_t __attribute__((ext_vector_type(8)));
#define LAS __attribute__((address_space(3)))
typedef __amdgpu_buffer_rsrc_t rsrc_t;

#define STAMP 0
#define LATE 4
#define QDELAY 40
#define PRE 0
#define PSTR 64
#define ABL 0
#define SLP1 0
#define SLP2 0
#define PSTAG 5
#define DBL 0
#define PA 30
#define PD 620
#define PK0 2
#define D1 10
#define D2 9
#define PV 0
#define LD -45
#define PDV 650
#define STG 0
#define SR 3
#define SR2 0
#define GO 1
#define GB 5
#define TP1 0
#define TP2 0
#define PREAUX 16
#define STAUX 16
#define WIN 6
#define LAUX 16
#define FINE 4
#define FO 25
#define TQ 0
#define TG 0
#define WINV WIN
#define MVM 126
#define HS 0
#define QS 0
#define PADAT 0
#define PADN 2
#define PAD(k) do { if (PADAT == (k)) asm volatile(".fill %c0, 4, 0xBF800000" :: "i"(PADN)); } while (0)
#define CT2 0
#define PERM 0
#define PT 2
#define ROT 37
#define CODE_SPAN 15872
#define CTW 0
#define WS_OFF 0

namespace {
constexpr int D = 448, SK = 32768, W = 4096, DH = 256, DF = 1792;
constexpr int AUX_NT = 2, AUX_SC1 = 16;
constexpr int OOB = 1 << 30;
constexpr u32 TMASK = 0xfu;

constexpr int QP_S = 768, GP_S = 1280, O1_S = 2304, O2_S = 3840;
constexpr int EP_OFF = 0;
constexpr int OR_OFF = 2048;
constexpr int ST_OFF = 4096;
constexpr int QP_OFF = ST_OFF + 8192;
constexpr int GP_OFF = QP_OFF + 256 * QP_S;
constexpr int O1_OFF = GP_OFF + 256 * GP_S;
constexpr int O2_OFF = O1_OFF + 256 * O1_S;
constexpr int WS_BYTES = O2_OFF + 256 * O2_S;
constexpr int TS_OFF = 4 << 20;

constexpr int L_XN = 0;
constexpr int L_QX = L_XN + 480;
constexpr int L_GX = L_QX + 128;
constexpr int L_Q = L_GX + 256;
constexpr int L_P = L_Q + 256;
constexpr int L_OP = L_P + 512;
constexpr int L_WM = L_OP + 2048;
constexpr int L_WL = L_WM + 8;
constexpr int L_H = L_WL + 8;
constexpr int L_O = L_H + 16;
constexpr int F_O = L_O + 16;
constexpr int L_R = L_O + 32;
constexpr int F_P = L_R + 128;
constexpr int F_VI = F_P + 4;
constexpr int F_O2 = F_VI + 4;
constexpr int F_Q = F_O2 + 4;
constexpr int F_QX = F_Q + 2, F_PUB = F_QX + 1;
constexpr int F_V = F_PUB + 1;
constexpr int F_R = F_V + 4;
constexpr int L_TAG = F_R + 8;
constexpr int L_DG = L_TAG + 1;
constexpr int L_SH = L_TAG + 2, F_SH = L_TAG + 3;
constexpr int L_END = L_TAG + 4;
constexpr u32 SET = 1u;
static_assert((L_END - L_WM) % 4 == 0 && (L_END - L_WM) / 4 <= 64, "one 16-byte store per lane clears the flag area");

constexpr int T_LAST = PA + PV + (8 * PD + 7 * PDV) / 10;
constexpr u32 SPIN_LDS = 1u << 21, SPIN_MEM = 1u << 16;

__device__ __forceinline__ float asf(u32 v) { return __builtin_bit_cast(float, v); }
__device__ __forceinline__ u32 asu(float v) { return __builtin_bit_cast(u32, v); }
__device__ __forceinline__ float lo16(u32 v) { return asf(v << 16); }
__device__ __forceinline__ float hi16(u32 v) { return asf(v & 0xffff0000u); }
__device__ __forceinline__ u32 pkbf(float a, float b) {
  bf16x2_t r = {(__bf16)a, (__bf16)b};
  return __builtin_bit_cast(u32, r);
}
__device__ __forceinline__ float rbf(float a) { return (float)(__bf16)a; }
__device__ __forceinline__ float dot2(u32 a, u32 b, float c) {
  return __builtin_amdgcn_fdot2_f32_bf16(__builtin_bit_cast(bf16x2_t, a), __builtin_bit_cast(bf16x2_t, b), c, false);
}
template <int CTRL> __device__ __forceinline__ float dpp(float x) {
  return asf(__builtin_amdgcn_mov_dpp((int)asu(x), CTRL, 0xf, 0xf, true));
}
constexpr int XOR1 = 0xB1, XOR2 = 0x4E, ROR8 = 0x128, ROR4 = 0x124;
__device__ __forceinline__ float tr32(float a, float b) {
  auto r = __builtin_amdgcn_permlane32_swap(asu(a), asu(b), false, false);
  return asf(r[0]) + asf(r[1]);
}
__device__ __forceinline__ float tr16(float a, float b) {
  auto r = __builtin_amdgcn_permlane16_swap(asu(a), asu(b), false, false);
  return asf(r[0]) + asf(r[1]);
}
__device__ __forceinline__ float mx32(float a) {
  auto r = __builtin_amdgcn_permlane32_swap(asu(a), asu(a), false, false);
  return fmaxf(asf(r[0]), asf(r[1]));
}
__device__ __forceinline__ float mx16(float a) {
  auto r = __builtin_amdgcn_permlane16_swap(asu(a), asu(a), false, false);
  return fmaxf(asf(r[0]), asf(r[1]));
}
__device__ __forceinline__ float row_sum(float v) { v += dpp<ROR8>(v); v += dpp<ROR4>(v); v += dpp<XOR2>(v); v += dpp<XOR1>(v); return v; }
__device__ __forceinline__ float row_max(float v) {
  v = fmaxf(v, dpp<ROR8>(v)); v = fmaxf(v, dpp<ROR4>(v)); v = fmaxf(v, dpp<XOR2>(v)); v = fmaxf(v, dpp<XOR1>(v)); return v;
}
__device__ __forceinline__ float wave_sum(float v) { v = row_sum(v); v = tr16(v, v); return tr32(v, v); }

__device__ __forceinline__ rsrc_t mkrs(const void* p, u32 bytes) { return __builtin_amdgcn_make_buffer_rsrc((void*)p, 0, bytes, 0x00020000); }
template <int AUX> __device__ __forceinline__ u32x4 ld128(rsrc_t r, int voff, int soff = 0) { return __builtin_amdgcn_raw_buffer_load_b128(r, voff, soff, AUX); }
template <int AUX> __device__ __forceinline__ u32 ld32(rsrc_t r, int voff) { return __builtin_amdgcn_raw_buffer_load_b32(r, voff, 0, AUX); }
__device__ __forceinline__ void st128(rsrc_t r, int voff, u32x4 v) { __builtin_amdgcn_raw_buffer_store_b128(v, r, voff, 0, STAUX); }
__device__ __forceinline__ u32 tagw(float v, u32 tag4) { return (asu(v) & ~TMASK) | tag4; }
__device__ __forceinline__ u32x4 tag4(float a, float b, float c, float d, u32 t) { return u32x4{tagw(a, t), tagw(b, t), tagw(c, t), tagw(d, t)}; }
__device__ __forceinline__ u32 untag4(u32x4& v, u32 t) { v.x ^= t; v.y ^= t; v.z ^= t; v.w ^= t; return v.x | v.y | v.z | v.w; }
__device__ __forceinline__ bool none(bool bad) { return __builtin_amdgcn_ballot_w64(bad) == 0ull; }
__device__ __forceinline__ bool all_ok(u32 bad) { return none((bad & TMASK) != 0u); }

#define CBAR() asm volatile("" ::: "memory")
#define PIN(v) asm volatile("" : "+v"(v))
#define LGKM0() asm volatile("s_waitcnt lgkmcnt(0)" ::: "memory")
template <int N, int SLEEP> __device__ __forceinline__ void flag_wait(LAS u32* p, int lane) {
  u32 v, cnt;
  const u32 addr = (u32)(uintptr_t)(p + (lane & (N - 1)));
  asm volatile("s_mov_b32 %1, %4\n"
               "1:\n\t"
               "ds_read_b32 %0, %2\n\t"
               "s_waitcnt lgkmcnt(0)\n\t"
               "v_cmp_ne_u32 vcc, %3, %0\n\t"
               "s_cbranch_vccz 2f\n\t"
               "s_sleep %5\n\t"
               "s_sub_u32 %1, %1, 1\n\t"
               "s_cbranch_scc0 1b\n"
               "2:"
               : "=&v"(v), "=&s"(cnt) : "v"(addr), "i"(SET), "i"(SPIN_LDS), "i"(SLEEP) : "vcc", "scc", "memory");
}
template <int DUE> __device__ __forceinline__ void pace(u64 org) {
  asm volatile("1:\n\t"
               "s_memrealtime vcc\n\t"
               "s_waitcnt lgkmcnt(0)\n\t"
               "s_sub_u32 vcc_lo, vcc_lo, %0\n\t"
               "s_cmp_lt_i32 vcc_lo, %1\n\t"
               "s_cbranch_scc0 2f\n\t"
               "s_sleep 1\n\t"
               "s_branch 1b\n"
               "2:"
               :: "s"((u32)org), "i"(DUE), "s"(org) : "vcc", "scc", "memory");
}
__device__ __forceinline__ void flag_set(LAS u32* p, u32 set = SET) { CBAR(); *p = set; CBAR(); }

template <int SLP> __device__ __forceinline__ void poll2(rsrc_t rs, int voff, int soff, u32 t4, u32x4& r0, u32x4& r1) {
  u32 o0, o1, o2, o3, o4, o5, o6, o7, m, cnt;
  asm volatile("s_mov_b32 %9, %14\n"
               "1:\n\t"
               "buffer_load_dwordx4 v[144:147], %10, %11, 0 offen sc1\n\t"
               "buffer_load_dwordx4 v[148:151], %10, %11, %12 offen sc1\n\t"
               "s_sub_u32 %9, %9, 1\n\t"
               "s_waitcnt vmcnt(1)\n\t"
               "v_xor_b32 %0, %13, v144\n\tv_xor_b32 %1, %13, v145\n\tv_xor_b32 %2, %13, v146\n\tv_xor_b32 %3, %13, v147\n\t"
               "v_or3_b32 %8, %0, %1, %2\n\t"
               "s_waitcnt vmcnt(0)\n\t"
               "v_xor_b32 %4, %13, v148\n\tv_xor_b32 %5, %13, v149\n\tv_xor_b32 %6, %13, v150\n\tv_xor_b32 %7, %13, v151\n\t"
               "v_or3_b32 %8, %8, %3, %4\n\t"
               "v_or3_b32 %8, %8, %5, %6\n\t"
               "v_bitop3_b32 %8, %8, 15, %7 bitop3:0xc8\n\t"
               "v_cmp_ne_u32 vcc, 0, %8\n\t"
               "s_cbranch_vccz 2f\n\t"
               "s_sleep %15\n\t"
               "s_cbranch_scc0 1b\n"
               "2:"
               : "=&v"(o0), "=&v"(o1), "=&v"(o2), "=&v"(o3), "=&v"(o4), "=&v"(o5), "=&v"(o6), "=&v"(o7), "=&v"(m), "=&s"(cnt)
               : "v"(voff), "s"(rs), "s"(soff), "s"(t4), "i"(SPIN_MEM), "i"(SLP)
               : "vcc", "scc", "memory", "v144", "v145", "v146", "v147", "v148", "v149", "v150", "v151");
  r0 = u32x4{o0, o1, o2, o3};
  r1 = u32x4{o4, o5, o6, o7};
}
__device__ __forceinline__ u32x4 data_wait(LAS u32* flag, LAS u32x4* p) {
  u32x4 r;
  u32 v, cnt;
  asm volatile("s_mov_b32 %2, %5\n"
               "1:\n\t"
               "ds_read_b32 %1, %3\n\t"
               "ds_read_b128 %0, %4\n\t"
               "s_sub_u32 %2, %2, 1\n\t"
               "s_waitcnt lgkmcnt(0)\n\t"
               "v_cmp_ne_u32 vcc, 1, %1\n\t"
               "s_cbranch_vccz 2f\n\t"
               "s_cbranch_scc0 1b\n"
               "2:"
               : "=&v"(r), "=&v"(v), "=&s"(cnt) : "v"((u32)(uintptr_t)flag), "v"((u32)(uintptr_t)p), "i"(SPIN_LDS) : "vcc", "scc", "memory");
  return r;
}

#define MK(k)
#define MV(k)
#define MW(k)
#define NV(k)
#define NW(k)
#define NC(k, c)
#define MK0()

__device__ __forceinline__ void final_reduce(LAS u32* L, rsrc_t rws, const u16* x, u16* out, char* ws, int wg, int w, int grp, int lane, u32 tag, u64 org) {
  LAS float* const LF = (LAS float*)L;
  __builtin_amdgcn_s_setprio(3);
  PAD(6);
  const u32 tag8 = tag & TMASK;
  const int rb = wg / 28, cb = wg % 28, qd = lane & 3;
  const int pi = cb & 7, ph = cb < 24 ? pi >> 2 : pi >> 1, pg = cb < 24 ? pi & 3 : pi & 1;
  const int col = rb * D + 128 * (cb >> 3) + 8 * (4 * pg + ((lane >> 2) & 3)) + 4 * ph + (lane & 3);
  float xres = lo16(x[col]);
  int oo = O2_OFF + (grp * 32 + (lane >> 2)) * O2_S + rb * 1792 + cb * 64 + qd * 16;
  LAS float* wp = LF + ((lane & 12) == 0 ? L_R + (4 * qd + (lane >> 4)) * 8 + grp : L_XN + 128 + lane);
  LAS u32* fp = L + F_R + grp;
  LAS u32* fr = L + F_R + (lane & 7);
  LAS f32x4* rp = (LAS f32x4*)(LF + L_R + (lane & 15) * 8);
  int so = lane < 16 ? col * 2 : OOB;
  u32 tagv = SET;
  rsrc_t ro = mkrs(out, 2 * D * 2);
  asm volatile("" : "+s"(ro));
  PIN(xres); PIN(oo); PIN(wp); PIN(fp); PIN(fr); PIN(rp); PIN(so); PIN(tagv);
  flag_wait<1, 1>(L + F_PUB, lane);
  flag_wait<4, 0>(L + F_O2, lane);
  if (D2) __builtin_amdgcn_s_sleep(D2);
  if (TP2) pace<T_LAST + TP2>(org);
  u32x4 a0, a1;
  poll2<SLP2>(rws, oo, 16 * O2_S, tag8, a0, a1);
  MK(6);
  NV(4);
  float f = tr16(tr32(asf(a0.x) + asf(a1.x), asf(a0.z) + asf(a1.z)), tr32(asf(a0.y) + asf(a1.y), asf(a0.w) + asf(a1.w)));
  f += dpp<ROR8>(f); f += dpp<ROR4>(f);
  *wp = f;
  flag_set(fp, tagv);
  u32 fl = *fr;
  CBAR();
  const f32x4 r0 = rp[0], r1 = rp[1];
  const f32x4 rs4 = r0 + r1;
  const float tot = xres + ((rs4.x + rs4.z) + (rs4.y + rs4.w));
  u32 res = pkbf(tot, tot);
  PIN(res);
  if (none(fl != SET)) {
    __builtin_amdgcn_raw_buffer_store_b16((u16)res, ro, so, 0, 0);
    if (wg == 0 && lane == 0) __builtin_amdgcn_raw_buffer_store_b32(tag, rws, EP_OFF, 0, AUX_SC1);
    MK(7);
  }
  NW(4);
}

}

extern "C" __global__ void __launch_bounds__(768, 3)
fused_decode(const u16* __restrict__ x, const u16* __restrict__ wq, const u16* __restrict__ wg_, const u16* __restrict__ wu,
             const u16* __restrict__ kc, char* __restrict__ ws, const u16* __restrict__ vc, const u16* __restrict__ wo,
             const u16* __restrict__ wd, u16* __restrict__ out) {
  __shared__ __attribute__((aligned(16))) u32 smem[L_END];
  LAS u32* const L = (LAS u32*)smem;
  LAS float* const LF = (LAS float*)smem;
  const int wg = blockIdx.x, h = wg & 7, j = wg >> 3, b = j & 1, sp = j >> 1, reg = wg & 15;
  const int w = __builtin_amdgcn_readfirstlane(threadIdx.x >> 6), lane = threadIdx.x & 63, kw = w & 3;
  asm volatile("s_load_dword s94, %0, 0x0\n\ts_load_dword s95, %0, 0x40" :: "s"(__builtin_amdgcn_kernarg_segment_ptr()) : "s94", "s95");
  u64 pc0;
  asm volatile("s_getpc_b64 %0" : "=s"(pc0));
  const u32 code_touch = ld32<0>(mkrs((const char*)(pc0 & ~255ull), w == CTW ? CODE_SPAN : 0), lane < 5 ? j * 640 + lane * 128 : OOB);
  __builtin_amdgcn_sched_barrier(0);
#define ROLE_ENTRY() const rsrc_t rws = mkrs(ws, WS_BYTES)
#define ROLE_TAG() const u32 tag = __builtin_amdgcn_readfirstlane(L[L_TAG]), tag8 = tag & TMASK
  const u32 blk0 = sp * 32 + reg * ROT;
  const u32 srot = sp * SR + reg * SR2;
  const u32 gm = PERM ? 2 * ((sp + (PERM == 2 ? reg : 0)) & 3) + 1 : 1, gc = PERM ? (sp >> 2) + reg : 0;
#define BLK(slot) (PERM ? ((((((slot) >> 2) * gm + gc) & 7u) << 2) + ((slot) & 3u) + srot) & 31u : ((slot) + srot) & 31u)
  const int hq = (h + QS) & 7, ho = (h + HS) & 7;
  u64 org;
#define STREAM_LOADS(SRC, VOFF, SOFF, BAR) \
  const rsrc_t rkv = mkrs((SRC) + ((size_t)reg * SK + (SK - W)) * DH, W * DH * 2); \
  u32x4 d[32]; \
  if (BAR) { __builtin_amdgcn_sched_barrier(0); __builtin_amdgcn_s_barrier(); org = __builtin_amdgcn_s_memrealtime(); __builtin_amdgcn_sched_barrier(0); } \
  MV(2); \
  STREAM_GROUP(0, VOFF, SOFF, BAR) STREAM_GROUP(1, VOFF, SOFF, BAR) STREAM_GROUP(2, VOFF, SOFF, BAR) STREAM_GROUP(3, VOFF, SOFF, BAR) \
  STREAM_GROUP(4, VOFF, SOFF, BAR) STREAM_GROUP(5, VOFF, SOFF, BAR) STREAM_GROUP(6, VOFF, SOFF, BAR) STREAM_GROUP(7, VOFF, SOFF, BAR)
#define STREAM_GROUP(G, VOFF, SOFF, ISK) \
  if (PD > 0 && (!(ISK) || (G) >= PK0)) pace<PA + ((ISK) ? (G) * PD / 10 : PV + (8 * PD + (G) * PDV) / 10 - ((G) >= FINE ? FO : 0))>(org + kw * STG); \
  if (((ISK) ? WIN : WINV) > 0 && 4 * (G) > ((ISK) ? WIN : WINV)) asm volatile("s_waitcnt vmcnt(%0)" :: "i"((ISK) ? WIN : WINV) : "memory"); \
  _Pragma("unroll") for (int i = 4 * (G); i < 4 * (G) + 2; ++i) d[i] = ld128<AUX_NT>(rkv, (VOFF), (int)(SOFF)); \
  if (!(ISK) && (G) >= FINE) { __builtin_amdgcn_sched_barrier(0); pace<PA + PV + (8 * PD + (G) * PDV + PDV / 2) / 10 - FO>(org + kw * STG); } \
  _Pragma("unroll") for (int i = 4 * (G) + 2; i < 4 * (G) + 4; ++i) d[i] = ld128<AUX_NT>(rkv, (VOFF), (int)(SOFF)); \
  __builtin_amdgcn_sched_barrier(0);

  if (__builtin_expect(w >= 4, 0)) {
  u32 tag_out;
  if (w < 8) {
    __builtin_amdgcn_s_setprio(2);
    PAD(3);
    const int g = lane >> 4, key = (lane >> 1) & 7;
    const bool hb = lane & 1;
    const int kvo = key * 512 + (2 * g + (lane & 1)) * 16;
    STREAM_LOADS(kc, kvo + (i & 3) * 128, ((blk0 + BLK(kw + 4 * (i >> 2))) & 511u) << 12, true);
    MV(3);
    ROLE_ENTRY();
    MK0(); MW(2); MW(3);
    PAD(4);
    flag_wait<2, 1>(L + F_Q, lane);
    ROLE_TAG();
    u32x4 qa[4];
#pragma unroll
    for (int i = 0; i < 4; ++i) qa[i] = *(LAS u32x4*)(L + L_Q + (lane & 1) * 128 + i * 32 + (2 * g + ((lane >> 1) & 1)) * 4);
    f32x4 ea, eb;
#pragma unroll
    for (int n = 0; n < 4; ++n) {
      f32x4 ca = {}, cb = {};
#pragma unroll
      for (int i = 0; i < 4; ++i) {
        ca = __builtin_amdgcn_mfma_f32_16x16x32_bf16(__builtin_bit_cast(bf16x8_t, qa[i]), __builtin_bit_cast(bf16x8_t, d[8 * n + i]), ca, 0, 0, 0);
        cb = __builtin_amdgcn_mfma_f32_16x16x32_bf16(__builtin_bit_cast(bf16x8_t, qa[i]), __builtin_bit_cast(bf16x8_t, d[8 * n + 4 + i]), cb, 0, 0, 0);
      }
#pragma unroll
      for (int k = 0; k < 4; ++k) { ea[k] = (n == 0 || g == n) ? ca[k] : ea[k]; eb[k] = (n == 0 || g == n) ? cb[k] : eb[k]; }
    }
    asm volatile("" :: "v"(code_touch));
    float sc[2];
#pragma unroll
    for (int hd = 0; hd < 2; ++hd) sc[hd] = rbf((hb ? eb[2 + hd] : ea[hd]) + dpp<XOR1>(hb ? ea[2 + hd] : eb[hd])) * 0.0625f;
    const float wm0 = mx32(mx16(row_max(sc[0]))), wm1 = mx32(mx16(row_max(sc[1])));
    const u32 tag16 = SET;
    CBAR();
    if (lane < 2) L[L_WM + kw * 2 + lane] = (asu(lane ? wm1 : wm0) & 0xffff0000u) | tag16;
    u32x4 ma, mb;
    for (u32 s = 0; s < SPIN_LDS; ++s) {
      CBAR();
      ma = *(LAS u32x4*)(L + L_WM) ^ tag16; mb = *(LAS u32x4*)(L + L_WM + 4) ^ tag16;
      if (none((((ma.x | ma.y | ma.z | ma.w) | (mb.x | mb.y | mb.z | mb.w)) & 0xffffu) != 0u)) break;
    }
    const float M0 = fmaxf(fmaxf(asf(ma.x), asf(ma.z)), fmaxf(asf(mb.x), asf(mb.z)));
    const float M1 = fmaxf(fmaxf(asf(ma.y), asf(ma.w)), fmaxf(asf(mb.y), asf(mb.w)));
    const float p0 = rbf(__builtin_amdgcn_exp2f((sc[0] - M0) * 1.4426950408889634f));
    const float p1 = rbf(__builtin_amdgcn_exp2f((sc[1] - M1) * 1.4426950408889634f));
    LAS u32* pp = L + L_P + key * 64 + kw + 4 * (2 * g + (lane & 1));
    pp[0] = asu(p0) | (asu(p0) >> 16);
    pp[32] = asu(p1) | (asu(p1) >> 16);
    float ls = tr32(p0, p1);
    ls = row_sum(tr16(ls, ls));
    if ((lane & 31) == 0) LF[L_WL + kw * 2 + (lane >> 5)] = ls;
    flag_set(L + F_P + kw);
    PAD(5);
    MK(2);

    if (kw == 0) {
      flag_wait<4, 0>(L + F_P, lane);
      if (lane < 2) {
        const float Ls = (LF[L_WL + lane] + LF[L_WL + 2 + lane]) + (LF[L_WL + 4 + lane] + LF[L_WL + 6 + lane]);
        st128(rws, ST_OFF + (((h * 2 + lane) * 2 + b) * 16 + sp) * 16, u32x4{asu(lane ? M1 : M0), tag, asu(Ls), tag});
      }
    } else if (kw == 1) {
      __builtin_amdgcn_s_setprio(3);
      PAD(7);
      const int s = lane >> 2, qd = lane & 3;
      const int so = ST_OFF + ((ho * 2 + (j >> 4)) * 32 + s) * 16;
      const int oo = O1_OFF + (ho * 32 + 2 * s) * O1_S + j * 64 + qd * 16;
      u32x4 s0, s1, a0, a1;
      flag_wait<4, 1>(L + F_VI, lane);
      for (u32 it = 0; it < SPIN_MEM; ++it) {
        CBAR();
        s0 = ld128<AUX_SC1>(rws, so); s1 = ld128<AUX_SC1>(rws, so + 256);
        if (none((s0.y != tag) | (s0.w != tag) | (s1.y != tag) | (s1.w != tag))) break;
        flag_wait<1, 1>(L + F_PUB, lane);
      }
      float wgt[2];
#pragma unroll
      for (int bb = 0; bb < 2; ++bb) {
        const float m = asf(bb ? s1.x : s0.x), l = asf(bb ? s1.z : s0.z);
        float mg = fmaxf(m, dpp<ROR8>(m)); mg = fmaxf(mg, dpp<ROR4>(mg)); mg = mx32(mx16(mg));
        const float e = __builtin_amdgcn_exp2f((m - mg) * 1.4426950408889634f);
        float lg = e * l; lg += dpp<ROR8>(lg); lg += dpp<ROR4>(lg); lg = tr16(lg, lg); lg = tr32(lg, lg);
        wgt[bb] = e * __builtin_amdgcn_rcpf(lg);
      }
      LAS u32* ow = L + ((lane & 12) == 0 ? L_O + qd * 4 + (lane >> 4) : L_XN + lane);
      LAS u32* of = L + F_O;
      u32 setv = SET;
      PIN(wgt[0]); PIN(wgt[1]); PIN(ow); PIN(of); PIN(setv);
      if (GO) org += (u64)(long long)(int)__builtin_amdgcn_readfirstlane(L[L_SH]);
      PAD(8);
      flag_wait<1, 0>(L + F_PUB, lane);
      if (D1) __builtin_amdgcn_s_sleep(D1);
      if (TP1) pace<T_LAST + TP1>(org);
      poll2<SLP1>(rws, oo, O1_S, tag8, a0, a1);
      NV(2);
      f32x4 m0 = __builtin_bit_cast(f32x4, a0) * wgt[0], m1 = __builtin_bit_cast(f32x4, a1) * wgt[1];
      PIN(m0); PIN(m1);
      const float v0 = tr32(m0.x, m1.x), v2 = tr32(m0.z, m1.z), v1 = tr32(m0.y, m1.y), v3 = tr32(m0.w, m1.w);
      float e0 = tr16(v0, v2), e1 = tr16(v1, v3);
      e0 += dpp<ROR8>(e0); e1 += dpp<ROR8>(e1);
      e0 += dpp<ROR4>(e0); e1 += dpp<ROR4>(e1);
      *ow = pkbf(e0, e1);
      flag_set(of, setv);
      MK(4); NW(2);
    }
    if (GO != 0 && TP2 != 0 && kw != 1) { flag_wait<1, 4>(L + F_SH, lane); org += (u64)(long long)(int)__builtin_amdgcn_readfirstlane(L[L_SH]); }
    if (PRE && PRE < 3 && kw >= 2) {
      const int po = kw == 2 ? O1_OFF + (h * 32 + j) * O1_S : O2_OFF + (h * 32 + j) * O2_S;
      const u32 t0 = ld32<AUX_SC1>(rws, lane * PSTR < (kw == 2 ? 2048 : PRE == 2 ? 0 : 3584) ? po + lane * PSTR : OOB);
      asm volatile("" :: "v"(t0));
    }
    if (PT >= 2 && kw == 2) {
      u32 t0 = 0u;
      if (wg < 56) t0 = ld32<0>(mkrs(out, 2 * D * 2), lane == 0 ? 0 : OOB);
      if (PT >= 3) {
        t0 |= ld32<AUX_NT>(mkrs(wo, 4096 * D * 2), lane < 4 ? (ho * 512 + j * 16) * (D * 2) + (lane == 3 ? 14332 : lane * 4096) : OOB);
        t0 |= ld32<AUX_NT>(mkrs(wd, wg < 224 ? DF * D * 2 : 0), lane < 2 ? (64 * (wg % 28) + 8 * (wg / 28)) * (D * 2) + lane * 7164 : OOB);
      }
      asm volatile("" :: "v"(t0));
    }
    if (CT2 && kw == 3) {
      if (CT2 == 2) flag_wait<1, 4>(L + F_VI, lane);
      if (CT2 == 4) pace<T_LAST - 150>(org);
      u32 t0;
      if (CT2 >= 3) { const rsrc_t rc = mkrs((const char*)(pc0 & ~255ull), CODE_SPAN); t0 = ld32<0>(rc, lane * 128) | ld32<0>(rc, 8192 + lane * 128); }
      else t0 = ld32<0>(mkrs((const char*)(pc0 & ~255ull), CODE_SPAN), lane < 5 ? j * 640 + lane * 128 : OOB);
      asm volatile("" :: "v"(t0));
    }
    if (PRE >= 3 && kw >= 2) {
      u32 t0 = 0u;
      if (kw == 2) t0 = ld32<PREAUX>(rws, lane < 32 ? O1_OFF + (h * 32 + lane) * O1_S + j * 64 : OOB);
      else if (PRE == 4 && wg < 56) {
        const int po = O2_OFF + lane * O2_S + (wg / 28) * 1792 + (wg % 28) * 64;
#pragma unroll
        for (int i = 0; i < 4; ++i) t0 |= __builtin_amdgcn_raw_buffer_load_b32(rws, po, i * 64 * O2_S, PREAUX);
      }
      asm volatile("" :: "v"(t0));
    }
    tag_out = tag;
  } else {
    if (kw == 0 && lane < (L_END - L_WM) / 4) *(LAS u32x4*)(L + L_WM + 4 * lane) = u32x4{0u, 0u, 0u, 0u};
    LGKM0();
    __builtin_amdgcn_s_barrier();
    org = __builtin_amdgcn_s_memrealtime();
    __builtin_amdgcn_s_setprio(1);
    __builtin_amdgcn_sched_barrier(0);
    ROLE_ENTRY();
    if (GO) { flag_wait<1, 4>(L + F_SH, lane); org += (u64)(long long)(int)__builtin_amdgcn_readfirstlane(L[L_SH]); }
    STREAM_LOADS(vc, lane * 16 + kw * 1024, ((blk0 + BLK((u32)i)) & 511u) << 12, false);
    MV(4);
    flag_set(L + F_VI + kw);
    PAD(1);
    MK0();
    const int m = lane & 31, hf = lane >> 5;
    u32x4 msk;
#pragma unroll
    for (int r = 0; r < 4; ++r) msk[r] = (m < 16 && ((m & 7) >> 1) == r) ? ((m & 1) ? 0xffff0000u : 0x0000ffffu) : 0u;
    LAS f32x4* op = (LAS f32x4*)(LF + L_OP) + kw * 128 + 2 * m + hf;
    LAS f32x4* rp = (LAS f32x4*)(LF + L_OP) + lane;
    LAS u32* fp = L + F_V + kw;
    LAS u32* fr = L + F_V + (lane & 3);
    int o1o = O1_OFF + (h * 32 + j) * O1_S + lane * 16;
    PIN(op); PIN(rp); PIN(fp); PIN(fr); PIN(o1o);
    flag_wait<4, 1>(L + F_P, lane);
    ROLE_TAG();
    const LAS u32x4* pp = (const LAS u32x4*)(L + L_P + ((kw * 2 + hf) * 2 + ((m >> 3) & 1)) * 32);
    f32x16 acc = {};
#pragma unroll
    for (int g = 0; g < 8; ++g) {
      const u32x4 p4 = pp[g];
#pragma unroll
      for (int k = 0; k < 4; ++k) {
        const u32x4 a = msk & p4[k];
        acc = __builtin_amdgcn_mfma_f32_32x32x16_bf16(__builtin_bit_cast(bf16x8_t, a), __builtin_bit_cast(bf16x8_t, d[4 * g + k]), acc, 0, 0, 0);
      }
      __builtin_amdgcn_sched_barrier(0);
    }
    asm volatile("" :: "v"(code_touch));
    PAD(2);
    MV(6); NV(1);
    op[0] = f32x4{acc[0], acc[1], acc[2], acc[3]};
    op[64] = f32x4{acc[4], acc[5], acc[6], acc[7]};
    flag_set(fp);
    const u32 fl = *fr;
    CBAR();
    const f32x4 o0 = (rp[0] + rp[128]) + (rp[256] + rp[384]);
    const f32x4 o1 = (rp[64] + rp[192]) + (rp[320] + rp[448]);
    u32x4 t0 = tag4(o0.x, o0.y, o0.z, o0.w, tag8), t1 = tag4(o1.x, o1.y, o1.z, o1.w, tag8);
    PIN(t0); PIN(t1);
    if (none(fl != SET)) {
      st128(rws, o1o, t0);
      st128(rws, o1o + 1024, t1);
      flag_set(L + F_PUB);
      MK(3);
    }
    MW(4); MW(6); NW(1);
    tag_out = tag;
  }
  if (wg < 56 && (wg % 28) < ((ABL & 64) ? 16 : (ABL & 128) ? 8 : 28)) final_reduce(L, mkrs(ws, WS_BYTES), x, out, ws, wg, w, w - 4, lane, tag_out, org);
  return;
  }

  __builtin_amdgcn_s_setprio(3);
  ROLE_ENTRY();
  const bool mlp = wg < 224;
  const int cb2 = wg % 28, ke = wg / 28;
  const int rr = lane >> 3, c8 = lane & 7;
  const rsrc_t rx = mkrs((const u16*)x, 2 * D * 2);
  const u32x4 xa = ld128<0>(rx, lane < 56 ? lane * 16 : OOB), xb = ld128<0>(rx, lane < 56 ? D * 2 + lane * 16 : OOB);
  const u32 epoch = ld32<AUX_SC1>(rws, EP_OFF);
  const int row0 = kw < 2 ? 112 * (j >> 3) + 64 * kw : 56 * ke;
  const int nrow = kw == 0 ? 64 : kw == 1 ? 48 : mlp ? 56 : 0;
  u32x4 wv[8];
  {
    const rsrc_t rw = mkrs(kw < 2 ? wq : kw == 2 ? wg_ : wu, kw < 2 ? ((ABL & 32) ? 0 : D * 4096 * 2) : ((ABL & 8) ? 0 : D * DF * 2));
    const int rs = kw < 2 ? 8192 : DF * 2;
    const int vw0 = nrow ? (row0 + 2 * rr) * rs + (kw < 2 ? (hq * 512 + (j & 7) * 64) * 2 : cb2 * 128) + c8 * 16 : OOB;
    const int vw3 = 48 + 2 * rr < nrow ? vw0 : OOB;
#pragma unroll
    for (int i = 0; i < 8; ++i) wv[i] = ld128<AUX_NT>(rw, i < 6 ? vw0 : vw3, (16 * (i >> 1) + (i & 1)) * rs);
  }
  const int eo = lane < 8 ? QP_OFF + (hq * 32 + j) * QP_S + lane * 64 : lane < 24 ? GP_OFF + wg * GP_S + (lane - 8) * 64 : ST_OFF + (wg & 127) * 64;
  const u32 box_touch = ld32<AUX_SC1>(rws, (!(ABL & 16) && kw == 1 && lane < 25) ? eo : OOB);
  __builtin_amdgcn_sched_barrier(0);
  __builtin_amdgcn_s_barrier();
  org = __builtin_amdgcn_s_memrealtime();
  __builtin_amdgcn_sched_barrier(0);
  MK0();
  u32 page_touch = 0u;
  if (PT == 1 && kw == 3) {
    const char* pb = lane < 4 ? (const char*)wo + (size_t)(ho * 512 + j * 16) * (D * 2) : lane < 6 ? (const char*)wd + (size_t)(mlp ? 64 * cb2 + 8 * ke : 0) * (D * 2)
                   : lane == 6 ? (const char*)vc + (((size_t)reg * SK + (SK - W)) * DH * 2 + ((blk0 & 511u) << 12)) : lane == 7 ? (const char*)out : ws + O2_OFF + (h * 32 + j) * O2_S;
    const int po = lane < 4 ? (lane == 3 ? 14332 : lane * 4096) : lane == 5 ? 7164 : 0;
    if (lane < 9) page_touch = __builtin_nontemporal_load((const u32*)(pb + po));
  }
  __builtin_amdgcn_sched_barrier(0);

#pragma unroll
  for (int bb = 0; bb < 2; ++bb) {
    const u32x4 xv = bb ? xb : xa;
    float v[8];
#pragma unroll
    for (int k = 0; k < 4; ++k) { v[2 * k] = lo16(xv[k]); v[2 * k + 1] = hi16(xv[k]); }
    float s = 0.f;
#pragma unroll
    for (int k = 0; k < 8; ++k) s += v[k];
    const float mean = wave_sum(s) * (1.f / D);
    float q = 0.f;
#pragma unroll
    for (int k = 0; k < 8; ++k) { const float t = v[k] - mean; q += t * t; }
    q = lane < 56 ? q : 0.f;
    const float var = rbf(wave_sum(q) * (1.f / D));
    const float den = rbf(__builtin_amdgcn_sqrtf(rbf(var + 1e-5f))), rden = __builtin_amdgcn_rcpf(den), mb = rbf(mean);
#pragma unroll
    for (int k = 0; k < 8; ++k) { const float c = rbf(v[k] - mb), r = c * rden; v[k] = fmaf(fmaf(-r, den, c), rden, r); }
    u32x4 o;
#pragma unroll
    for (int k = 0; k < 4; ++k) o[k] = pkbf(v[2 * k], v[2 * k + 1]);
    if (lane < 56) *(LAS u32x4*)(L + L_XN + bb * 224 + lane * 4) = o;
    else if (bb == 1) *(LAS u32x4*)(L + L_XN + 224 + lane * 4) = u32x4{0u, 0u, 0u, 0u};
  }
  asm volatile("" :: "v"(code_touch));
  const u32 tag = __builtin_amdgcn_readfirstlane(epoch) + 1u, tag8 = tag & TMASK;
  if (kw == 0) L[L_TAG] = tag;
  if (GO && kw == 3 && wg < 8 && lane == 0) __builtin_amdgcn_raw_buffer_store_b64(u32x2{(u32)org, tag}, rws, OR_OFF + wg * 8, 0, AUX_SC1);
  MK(10);

  {
    float a0[8] = {}, a1[8] = {};
#pragma unroll
    for (int k = 0; k < 4; ++k) {
      const u32 x0 = L[L_XN + row0 / 2 + rr + 8 * k], x1 = L[L_XN + 224 + row0 / 2 + rr + 8 * k];
#pragma unroll
      for (int c = 0; c < 4; ++c) {
        const u32 pl = __builtin_amdgcn_perm(wv[2 * k + 1][c], wv[2 * k][c], 0x05040100u), ph = __builtin_amdgcn_perm(wv[2 * k + 1][c], wv[2 * k][c], 0x07060302u);
        a0[2 * c] = dot2(pl, x0, a0[2 * c]); a0[2 * c + 1] = dot2(ph, x0, a0[2 * c + 1]);
        a1[2 * c] = dot2(pl, x1, a1[2 * c]); a1[2 * c + 1] = dot2(ph, x1, a1[2 * c + 1]);
      }
    }
    asm volatile("" :: "v"(box_touch));
    MV(5);
    float t[8], u[4];
#pragma unroll
    for (int k = 0; k < 8; ++k) t[k] = tr32(a0[k], a1[k]);
#pragma unroll
    for (int k = 0; k < 4; ++k) { u[k] = tr16(t[k], t[k + 4]); u[k] += dpp<ROR8>(u[k]); }
    const int bb = lane >> 5, hf = (lane >> 4) & 1;
    LAS f32x4* qx = (LAS f32x4*)(LF + L_QX) + bb * 16 + c8 * 2 + hf;
    if (kw == 1) {
      if (!(lane & 8)) *qx = f32x4{u[0], u[1], u[2], u[3]};
      flag_set(L + F_QX);
    } else if (kw == 0) {
      flag_wait<1, 0>(L + F_QX, lane);
      const f32x4 o = *qx;
      if (!(ABL & 1) && !(lane & 8)) st128(rws, QP_OFF + (hq * 32 + j) * QP_S + bb * 256 + hf * 128 + c8 * 16, tag4(u[0] + o.x, u[1] + o.y, u[2] + o.z, u[3] + o.w, tag8));
    } else if (!(ABL & 2) && mlp) {
      LAS u32x4* gx = (LAS u32x4*)(L + L_GX) + (kw - 2) * 32;
      if (!(lane & 8)) gx[c8 * 4 + bb * 2 + hf] = tag4(u[0], u[1], u[2], u[3], tag8);
      CBAR();
      if (lane < 32) st128(rws, GP_OFF + wg * GP_S + (lane >> 2) * 128 + (kw - 2) * 64 + (lane & 3) * 16, gx[lane]);
    }
  }

  if (kw < 2) {
    __builtin_amdgcn_s_sleep(QDELAY);
    if (TQ) pace<TQ>(org);
    const int qo = QP_OFF + (h * 32 + 4 * kw + 8 * (lane >> 4)) * QP_S + b * 256 + (lane & 15) * 16;
    u32x4 a[4];
    for (u32 s = 0; s < SPIN_MEM; ++s) {
      CBAR();
      if (ABL & 1) { a[0] = a[1] = a[2] = a[3] = xa; break; }
#pragma unroll
      for (int i = 0; i < 4; ++i) a[i] = ld128<AUX_SC1>(rws, qo, i * QP_S);
      if (all_ok((untag4(a[0], tag8) | untag4(a[1], tag8)) | (untag4(a[2], tag8) | untag4(a[3], tag8)))) break;
    }
#pragma unroll
    for (int i = 0; i < 2; ++i) {
      const u32x4 a0 = a[2 * i], a1 = a[2 * i + 1];
      const float v0 = tr32(asf(a0.x), asf(a1.x)), v1 = tr32(asf(a0.y), asf(a1.y)), v2 = tr32(asf(a0.z), asf(a1.z)), v3 = tr32(asf(a0.w), asf(a1.w));
      L[L_Q + (4 * kw + 2 * i + (lane >> 5)) * 32 + 4 * (lane & 7) + 2 * ((lane >> 3) & 1) + ((lane >> 4) & 1)] = pkbf(tr16(v0, v2), tr16(v1, v3));
    }
    flag_set(L + F_Q + kw);
    MK(1);
  } else if (kw == 2) {
    u32 h0 = 0u, h1 = 0u;
    if (mlp) {
      const int go = GP_OFF + (cb2 + 28 * rr) * GP_S + ke * 128 + c8 * 16;
      u32x4 a;
      flag_wait<2, 1>(L + F_Q, lane);
      if (TG) pace<TG>(org);
      for (u32 s = 0; s < SPIN_MEM; ++s) {
        CBAR();
        if (ABL & 2) { a = xa; break; }
        a = ld128<AUX_SC1>(rws, go);
        if (all_ok(untag4(a, tag8))) break;
        __builtin_amdgcn_s_sleep(16);
      }
      float hv[4];
#pragma unroll
      for (int k = 0; k < 4; ++k) {
        float g = asf(a[k]);
        g += dpp<ROR8>(g); g = tr16(g, g); g = tr32(g, g);
        g = rbf(g);
        const float uu = dpp<ROR4>(g);
        const float z = 0.7978845608028654f * (g + 0.044715f * g * g * g);
        const float th = 1.f - 2.f * __builtin_amdgcn_rcpf(1.f + __builtin_amdgcn_exp2f(z * 2.8853900817779268f));
        hv[k] = rbf(0.5f * g * (1.f + th)) * uu;
      }
      h0 = pkbf(hv[0], hv[1]); h1 = pkbf(hv[2], hv[3]);
    }
    if (lane < 4) {
      *(LAS u32x2*)(L + L_H + (2 * (lane & 1)) * 4 + (lane >> 1) * 2) = u32x2{h0, SET};
      *(LAS u32x2*)(L + L_H + (2 * (lane & 1) + 1) * 4 + (lane >> 1) * 2) = u32x2{h1, SET};
    }
    CBAR();
  } else if (GO) {
    pace<300>(org);
    const u32x2 o = __builtin_amdgcn_raw_buffer_load_b64(rws, OR_OFF + (lane & 7) * 8, 0, AUX_SC1);
    const bool ok = o.y == tag;
    const float n = row_sum(ok ? 1.f : 0.f), dsum = row_sum(ok ? (float)(int)(o.x - (u32)org) : 0.f);
    const float sh = n > 0.f ? fminf(fmaxf(dsum * __builtin_amdgcn_rcpf(n) + GB, -80.f), 80.f) : 0.f;
    L[L_SH] = (u32)(int)sh;
    flag_set(L + F_SH);
  }

  asm volatile("" :: "v"(page_touch));
  const int rg = lane >> 4, cl = lane & 15;
  const int co = kw * 128 + cl * 8;
  u32x4 wa[4], wb[2];
  if (LATE == 0) flag_wait<2, 1>(L + F_Q, lane);
  if (LATE >= 1) flag_wait<4, 1>(L + F_VI, lane);
  if (LATE == 3) flag_wait<1, 0>(L + F_PUB, lane);
  if (GO) org += (u64)(long long)(int)__builtin_amdgcn_readfirstlane(L[L_SH]);
  if (LATE == 4) pace<PA + PV + (8 * PD + 8 * PDV) / 10 + LD>(org + kw * STG);
  if (LATE == 5) __builtin_amdgcn_s_sleep(LD);
  {
    const rsrc_t ro = mkrs(wo, (ABL & 4) ? 0 : 4096 * D * 2), rd = mkrs(wd, mlp && !(ABL & 4) ? DF * D * 2 : 0);
    const int vo = co < D ? (ho * 512 + j * 16 + 4 * rg) * (D * 2) + co * 2 : OOB;
    const int vd = co < D ? (64 * cb2 + 8 * ke + 2 * rg) * (D * 2) + co * 2 : OOB;
#pragma unroll
    for (int i = 0; i < 2; ++i) wb[i] = ld128<AUX_NT>(rd, vd, i * D * 2);
#pragma unroll
    for (int i = 0; i < 4; ++i) wa[i] = ld128<AUX_NT>(ro, vo, i * D * 2);
  }
  __builtin_amdgcn_sched_barrier(0);
  float a0[8], a1[8];
  u32 pa[8], pb[8];
  {
    u32x4 hh;
    for (u32 s = 0; s < SPIN_LDS; ++s) {
      CBAR();
      hh = *(LAS u32x4*)(L + L_H + rg * 4);
      if (none((hh.y != SET) | (hh.w != SET))) break;
      __builtin_amdgcn_s_sleep(1);
    }
#pragma unroll
    for (int k = 0; k < 4; ++k) {
      const u32 pl = __builtin_amdgcn_perm(wb[1][k], wb[0][k], 0x05040100u), ph = __builtin_amdgcn_perm(wb[1][k], wb[0][k], 0x07060302u);
      a0[2 * k] = dot2(pl, hh.x, 0.f); a0[2 * k + 1] = dot2(ph, hh.x, 0.f);
      a1[2 * k] = dot2(pl, hh.z, 0.f); a1[2 * k + 1] = dot2(ph, hh.z, 0.f);
    }
#pragma unroll
    for (int k = 0; k < 4; ++k) {
      pa[2 * k] = __builtin_amdgcn_perm(wa[1][k], wa[0][k], 0x05040100u); pa[2 * k + 1] = __builtin_amdgcn_perm(wa[1][k], wa[0][k], 0x07060302u);
      pb[2 * k] = __builtin_amdgcn_perm(wa[3][k], wa[2][k], 0x05040100u); pb[2 * k + 1] = __builtin_amdgcn_perm(wa[3][k], wa[2][k], 0x07060302u);
    }
  }
#pragma unroll
  for (int k = 0; k < 8; ++k) asm volatile("" : "+v"(a0[k]), "+v"(a1[k]), "+v"(pa[k]), "+v"(pb[k]));
  int o2o = !((ABL & 256) && j >= 16) && co < ((ABL & 64) ? 256 : (ABL & 128) ? 128 : D) ? O2_OFF + (h * 32 + j) * O2_S + (lane >> 5) * 1792 + kw * 512 + (((lane >> 4) & 1) * (kw == 3 ? 8 : 16) + cl) * 16 : OOB;
  LAS u32x4* orp = (LAS u32x4*)(L + L_O + rg * 4);
  LAS u32* ofp = L + F_O;
  PIN(o2o); PIN(orp); PIN(ofp);
  if (LATE == 0) flag_wait<4, 1>(L + F_VI, lane);
  {
    const u32x4 oo = data_wait(ofp, orp);
#pragma unroll
    for (int k = 0; k < 8; ++k) {
      a0[k] = dot2(pb[k], oo.y, dot2(pa[k], oo.x, a0[k]));
      a1[k] = dot2(pb[k], oo.w, dot2(pa[k], oo.z, a1[k]));
    }
    float t[8];
#pragma unroll
    for (int k = 0; k < 8; ++k) t[k] = tr32(a0[k], a1[k]);
    st128(rws, o2o, tag4(tr16(t[0], t[4]), tr16(t[1], t[5]), tr16(t[2], t[6]), tr16(t[3], t[7]), tag8));
  }
  flag_set(L + F_O2 + kw);
  NV(3); NW(3);
  MK(5);
}

extern "C" void kernel_launch(void* const* d_in, const int* in_sizes, int n_in, void* d_out, int out_size, void* d_ws, size_t ws_size,
                              hipStream_t stream) {
  fused_decode<<<256, 768, 0, stream>>>((const u16*)d_in[0], (const u16*)d_in[3], (const u16*)d_in[6], (const u16*)d_in[7], (const u16*)d_in[1],
                                        (char*)d_ws + WS_OFF, (const u16*)d_in[2], (const u16*)d_in[4], (const u16*)d_in[8], (u16*)d_out);
}
```

```cpp
#include <hip/hip_runtime.h>
#include <hip/hip_bf16.h>
#include <stdint.h>

typedef unsigned int u32;
typedef unsigned short u16;
typedef unsigned long long u64;
typedef u32 u32x2 __attribute__((ext_vector_type(2)));
typedef u32 u32x4 __attribute__((ext_vector_type(4)));
typedef float f32x4 __attribute__((ext_vector_type(4)));
typedef float f32x16 __attribute__((ext_vector_type(16)));
typedef __bf16 bf16x2_t __attribute__((ext_vector_type(2)));
typedef __bf16 bf16x8_t __attribute__((ext_vector_type(8)));
#define LAS __attribute__((address_space(3)))
typedef __amdgpu_buffer_rsrc_t rsrc_t;

#define STAMP 0
#define LATE 4
#define QDELAY 40
#define PRE 0
#define PSTR 64
#define ABL 0
#define SLP1 0
#define SLP2 0
#define PSTAG 5
#define DBL 0
#define PA 30
#define PD 620
#define PK0 2
#define D1 10
#define D2 9
#define PV 0
#define LD -45
#define PDV 650
#define STG 0
#define SR 3
#define SR2 0
#define GO 1
#define GB 5
#define TP1 0
#define TP2 0
#define PREAUX 16
#define STAUX 16
#define WIN 6
#define LAUX 16
#define FINE 4
#define FO 25
#define TQ 0
#define TG 0
#define WINV WIN
#define MVM 126
#define HS 0
#define QS 0
#define PADAT 0
#define PADN 2
#define PAD(k) do { if (PADAT == (k)) asm volatile(".fill %c0, 4, 0xBF800000" :: "i"(PADN)); } while (0)
#define CT2 0
#define PERM 0
#define PT 2
#define ROT 37
#define CODE_SPAN 15872
#define CTW 0
#define WS_OFF 0

namespace {
constexpr int D = 448, SK = 32768, W = 4096, DH = 256, DF = 1792;
constexpr int AUX_NT = 2, AUX_SC1 = 16;
constexpr int OOB = 1 << 30;
constexpr u32 TMASK = 0xfu;

constexpr int QP_S = 768, GP_S = 1280, O1_S = 2304, O2_S = 3840;
constexpr int EP_OFF = 0;
constexpr int OR_OFF = 2048;
constexpr int ST_OFF = 4096;
constexpr int QP_OFF = ST_OFF + 8192;
constexpr int GP_OFF = QP_OFF + 256 * QP_S;
constexpr int O1_OFF = GP_OFF + 256 * GP_S;
constexpr int O2_OFF = O1_OFF + 256 * O1_S;
constexpr int WS_BYTES = O2_OFF + 256 * O2_S;
constexpr int TS_OFF = 4 << 20;

constexpr int L_XN = 0;
constexpr int L_QX = L_XN + 480;
constexpr int L_GX = L_QX + 128;
constexpr int L_Q = L_GX + 256;
constexpr int L_P = L_Q + 256;
constexpr int L_OP = L_P + 512;
constexpr int L_WM = L_OP + 2048;
constexpr int L_WL = L_WM + 8;
constexpr int L_H = L_WL + 8;
constexpr int L_O = L_H + 16;
constexpr int F_O = L_O + 16;
constexpr int L_R = L_O + 32;
constexpr int F_P = L_R + 128;
constexpr int F_VI = F_P + 4;
constexpr int F_O2 = F_VI + 4;
constexpr int F_Q = F_O2 + 4;
constexpr int F_QX = F_Q + 2, F_PUB = F_QX + 1;
constexpr int F_V = F_PUB + 1;
constexpr int F_R = F_V + 4;
constexpr int L_TAG = F_R + 8;
constexpr int L_DG = L_TAG + 1;
constexpr int L_SH = L_TAG + 2, F_SH = L_TAG + 3;
constexpr int L_END = L_TAG + 4;
constexpr u32 SET = 1u;
static_assert((L_END - L_WM) % 4 == 0 && (L_END - L_WM) / 4 <= 64, "one 16-byte store per lane clears the flag area");

constexpr int T_LAST = PA + PV + (8 * PD + 7 * PDV) / 10;
constexpr u32 SPIN_LDS = 1u << 21, SPIN_MEM = 1u << 16;

__device__ __forceinline__ float asf(u32 v) { return __builtin_bit_cast(float, v); }
__device__ __forceinline__ u32 asu(float v) { return __builtin_bit_cast(u32, v); }
__device__ __forceinline__ float lo16(u32 v) { return asf(v << 16); }
__device__ __forceinline__ float hi16(u32 v) { return asf(v & 0xffff0000u); }
__device__ __forceinline__ u32 pkbf(float a, float b) {
  bf16x2_t r = {(__bf16)a, (__bf16)b};
  return __builtin_bit_cast(u32, r);
}
__device__ __forceinline__ float rbf(float a) { return (float)(__bf16)a; }
__device__ __forceinline__ float dot2(u32 a, u32 b, float c) {
  return __builtin_amdgcn_fdot2_f32_bf16(__builtin_bit_cast(bf16x2_t, a), __builtin_bit_cast(bf16x2_t, b), c, false);
}
template <int CTRL> __device__ __forceinline__ float dpp(float x) {
  return asf(__builtin_amdgcn_mov_dpp((int)asu(x), CTRL, 0xf, 0xf, true));
}
constexpr int XOR1 = 0xB1, XOR2 = 0x4E, ROR8 = 0x128, ROR4 = 0x124;
__device__ __forceinline__ float tr32(float a, float b) {
  auto r = __builtin_amdgcn_permlane32_swap(asu(a), asu(b), false, false);
  return asf(r[0]) + asf(r[1]);
}
__device__ __forceinline__ float tr16(float a, float b) {
  auto r = __builtin_amdgcn_permlane16_swap(asu(a), asu(b), false, false);
  return asf(r[0]) + asf(r[1]);
}
__device__ __forceinline__ float mx32(float a) {
  auto r = __builtin_amdgcn_permlane32_swap(asu(a), asu(a), false, false);
  return fmaxf(asf(r[0]), asf(r[1]));
}
__device__ __forceinline__ float mx16(float a) {
  auto r = __builtin_amdgcn_permlane16_swap(asu(a), asu(a), false, false);
  return fmaxf(asf(r[0]), asf(r[1]));
}
__device__ __forceinline__ float row_sum(float v) { v += dpp<ROR8>(v); v += dpp<ROR4>(v); v += dpp<XOR2>(v); v += dpp<XOR1>(v); return v; }
__device__ __forceinline__ float row_max(float v) {
  v = fmaxf(v, dpp<ROR8>(v)); v = fmaxf(v, dpp<ROR4>(v)); v = fmaxf(v, dpp<XOR2>(v)); v = fmaxf(v, dpp<XOR1>(v)); return v;
}
__device__ __forceinline__ float wave_sum(float v) { v = row_sum(v); v = tr16(v, v); return tr32(v, v); }

__device__ __forceinline__ rsrc_t mkrs(const void* p, u32 bytes) { return __builtin_amdgcn_make_buffer_rsrc((void*)p, 0, bytes, 0x00020000); }
template <int AUX> __device__ __forceinline__ u32x4 ld128(rsrc_t r, int voff, int soff = 0) { return __builtin_amdgcn_raw_buffer_load_b128(r, voff, soff, AUX); }
template <int AUX> __device__ __forceinline__ u32 ld32(rsrc_t r, int voff) { return __builtin_amdgcn_raw_buffer_load_b32(r, voff, 0, AUX); }
__device__ __forceinline__ void st128(rsrc_t r, int voff, u32x4 v) { __builtin_amdgcn_raw_buffer_store_b128(v, r, voff, 0, STAUX); }
__device__ __forceinline__ u32 tagw(float v, u32 tag4) { return (asu(v) & ~TMASK) | tag4; }
__device__ __forceinline__ u32x4 tag4(float a, float b, float c, float d, u32 t) { return u32x4{tagw(a, t), tagw(b, t), tagw(c, t), tagw(d, t)}; }
__device__ __forceinline__ u32 untag4(u32x4& v, u32 t) { v.x ^= t; v.y ^= t; v.z ^= t; v.w ^= t; return v.x | v.y | v.z | v.w; }
__device__ __forceinline__ bool none(bool bad) { return __builtin_amdgcn_ballot_w64(bad) == 0ull; }
__device__ __forceinline__ bool all_ok(u32 bad) { return none((bad & TMASK) != 0u); }

#define CBAR() asm volatile("" ::: "memory")
#define PIN(v) asm volatile("" : "+v"(v))
#define LGKM0() asm volatile("s_waitcnt lgkmcnt(0)" ::: "memory")
template <int N, int SLEEP> __device__ __forceinline__ void flag_wait(LAS u32* p, int lane) {
  u32 v, cnt;
  const u32 addr = (u32)(uintptr_t)(p + (lane & (N - 1)));
  asm volatile("s_mov_b32 %1, %4\n"
               "1:\n\t"
               "ds_read_b32 %0, %2\n\t"
               "s_waitcnt lgkmcnt(0)\n\t"
               "v_cmp_ne_u32 vcc, %3, %0\n\t"
               "s_cbranch_vccz 2f\n\t"
               "s_sleep %5\n\t"
               "s_sub_u32 %1, %1, 1\n\t"
               "s_cbranch_scc0 1b\n"
               "2:"
               : "=&v"(v), "=&s"(cnt) : "v"(addr), "i"(SET), "i"(SPIN_LDS), "i"(SLEEP) : "vcc", "scc", "memory");
}
template <int DUE> __device__ __forceinline__ void pace(u64 org) {
  asm volatile("1:\n\t"
               "s_memrealtime vcc\n\t"
               "s_waitcnt lgkmcnt(0)\n\t"
               "s_sub_u32 vcc_lo, vcc_lo, %0\n\t"
               "s_cmp_lt_i32 vcc_lo, %1\n\t"
               "s_cbranch_scc0 2f\n\t"
               "s_sleep 1\n\t"
               "s_branch 1b\n"
               "2:"
               :: "s"((u32)org), "i"(DUE), "s"(org) : "vcc", "scc", "memory");
}
__device__ __forceinline__ void flag_set(LAS u32* p, u32 set = SET) { CBAR(); *p = set; CBAR(); }

template <int SLP> __device__ __forceinline__ void poll2(rsrc_t rs, int voff, int soff, u32 t4, u32x4& r0, u32x4& r1) {
  u32 o0, o1, o2, o3, o4, o5, o6, o7, m, cnt;
  asm volatile("s_mov_b32 %9, %14\n"
               "1:\n\t"
               "buffer_load_dwordx4 v[144:147], %10, %11, 0 offen sc1\n\t"
               "buffer_load_dwordx4 v[148:151], %10, %11, %12 offen sc1\n\t"
               "s_sub_u32 %9, %9, 1\n\t"
               "s_waitcnt vmcnt(1)\n\t"
               "v_xor_b32 %0, %13, v144\n\tv_xor_b32 %1, %13, v145\n\tv_xor_b32 %2, %13, v146\n\tv_xor_b32 %3, %13, v147\n\t"
               "v_or3_b32 %8, %0, %1, %2\n\t"
               "s_waitcnt vmcnt(0)\n\t"
               "v_xor_b32 %4, %13, v148\n\tv_xor_b32 %5, %13, v149\n\tv_xor_b32 %6, %13, v150\n\tv_xor_b32 %7, %13, v151\n\t"
               "v_or3_b32 %8, %8, %3, %4\n\t"
               "v_or3_b32 %8, %8, %5, %6\n\t"
               "v_bitop3_b32 %8, %8, 15, %7 bitop3:0xc8\n\t"
               "v_cmp_ne_u32 vcc, 0, %8\n\t"
               "s_cbranch_vccz 2f\n\t"
               "s_sleep %15\n\t"
               "s_cbranch_scc0 1b\n"
               "2:"
               : "=&v"(o0), "=&v"(o1), "=&v"(o2), "=&v"(o3), "=&v"(o4), "=&v"(o5), "=&v"(o6), "=&v"(o7), "=&v"(m), "=&s"(cnt)
               : "v"(voff), "s"(rs), "s"(soff), "s"(t4), "i"(SPIN_MEM), "i"(SLP)
               : "vcc", "scc", "memory", "v144", "v145", "v146", "v147", "v148", "v149", "v150", "v151");
  r0 = u32x4{o0, o1, o2, o3};
  r1 = u32x4{o4, o5, o6, o7};
}
__device__ __forceinline__ u32x4 data_wait(LAS u32* flag, LAS u32x4* p) {
  u32x4 r;
  u32 v, cnt;
  asm volatile("s_mov_b32 %2, %5\n"
               "1:\n\t"
               "ds_read_b32 %1, %3\n\t"
               "ds_read_b128 %0, %4\n\t"
               "s_sub_u32 %2, %2, 1\n\t"
               "s_waitcnt lgkmcnt(0)\n\t"
               "v_cmp_ne_u32 vcc, 1, %1\n\t"
               "s_cbranch_vccz 2f\n\t"
               "s_cbranch_scc0 1b\n"
               "2:"
               : "=&v"(r), "=&v"(v), "=&s"(cnt) : "v"((u32)(uintptr_t)flag), "v"((u32)(uintptr_t)p), "i"(SPIN_LDS) : "vcc", "scc", "memory");
  return r;
}

#define MK(k)
#define MV(k)
#define MW(k)
#define NV(k)
#define NW(k)
#define NC(k, c)
#define MK0()

__device__ __forceinline__ void final_reduce(LAS u32* L, rsrc_t rws, const u16* x, u16* out, char* ws, int wg, int w, int grp, int lane, u32 tag, u64 org) {
  LAS float* const LF = (LAS float*)L;
  __builtin_amdgcn_s_setprio(3);
  PAD(6);
  const u32 tag8 = tag & TMASK;
  const int rb = wg / 28, cb = wg % 28, qd = lane & 3;
  const int pi = cb & 7, ph = cb < 24 ? pi >> 2 : pi >> 1, pg = cb < 24 ? pi & 3 : pi & 1;
  const int col = rb * D + 128 * (cb >> 3) + 8 * (4 * pg + ((lane >> 2) & 3)) + 4 * ph + (lane & 3);
  float xres = lo16(x[col]);
  int oo = O2_OFF + (grp * 32 + (lane >> 2)) * O2_S + rb * 1792 + cb * 64 + qd * 16;
  LAS float* wp = LF + ((lane & 12) == 0 ? L_R + (4 * qd + (lane >> 4)) * 8 + grp : L_XN + 128 + lane);
  LAS u32* fp = L + F_R + grp;
  LAS u32* fr = L + F_R + (lane & 7);
  LAS f32x4* rp = (LAS f32x4*)(LF + L_R + (lane & 15) * 8);
  int so = lane < 16 ? col * 2 : OOB;
  u32 tagv = SET;
  rsrc_t ro = mkrs(out, 2 * D * 2);
  asm volatile("" : "+s"(ro));
  PIN(xres); PIN(oo); PIN(wp); PIN(fp); PIN(fr); PIN(rp); PIN(so); PIN(tagv);
  flag_wait<1, 1>(L + F_PUB, lane);
  flag_wait<4, 0>(L + F_O2, lane);
  if (D2) __builtin_amdgcn_s_sleep(D2);
  if (TP2) pace<T_LAST + TP2>(org);
  u32x4 a0, a1;
  poll2<SLP2>(rws, oo, 16 * O2_S, tag8, a0, a1);
  MK(6);
  NV(4);
  float f = tr16(tr32(asf(a0.x) + asf(a1.x), asf(a0.z) + asf(a1.z)), tr32(asf(a0.y) + asf(a1.y), asf(a0.w) + asf(a1.w)));
  f += dpp<ROR8>(f); f += dpp<ROR4>(f);
  *wp = f;
  flag_set(fp, tagv);
  u32 fl = *fr;
  CBAR();
  const f32x4 r0 = rp[0], r1 = rp[1];
  const f32x4 rs4 = r0 + r1;
  const float tot = xres + ((rs4.x + rs4.z) + (rs4.y + rs4.w));
  u32 res = pkbf(tot, tot);
  PIN(res);
  if (none(fl != SET)) {
    __builtin_amdgcn_raw_buffer_store_b16((u16)res, ro, so, 0, 0);
    if (wg == 0 && lane == 0) __builtin_amdgcn_raw_buffer_store_b32(tag, rws, EP_OFF, 0, AUX_SC1);
    MK(7);
  }
  NW(4);
}

}

extern "C" __global__ void __launch_bounds__(768, 3)
fused_decode(const u16* __restrict__ x, const u16* __restrict__ wq, const u16* __restrict__ wg_, const u16* __restrict__ wu,
             const u16* __restrict__ kc, char* __restrict__ ws, const u16* __restrict__ vc, const u16* __restrict__ wo,
             const u16* __restrict__ wd, u16* __restrict__ out) {
  __shared__ __attribute__((aligned(16))) u32 smem[L_END];
  LAS u32* const L = (LAS u32*)smem;
  LAS float* const LF = (LAS float*)smem;
  const int wg = blockIdx.x, h = wg & 7, j = wg >> 3, b = j & 1, sp = j >> 1, reg = wg & 15;
  const int w = __builtin_amdgcn_readfirstlane(threadIdx.x >> 6), lane = threadIdx.x & 63, kw = w & 3;
  asm volatile("s_load_dword s94, %0, 0x0\n\ts_load_dword s95, %0, 0x40" :: "s"(__builtin_amdgcn_kernarg_segment_ptr()) : "s94", "s95");
  u64 pc0;
  asm volatile("s_getpc_b64 %0" : "=s"(pc0));
  const u32 code_touch = ld32<0>(mkrs((const char*)(pc0 & ~255ull), w == CTW ? CODE_SPAN : 0), lane < 5 ? j * 640 + lane * 128 : OOB);
  __builtin_amdgcn_sched_barrier(0);
#define ROLE_ENTRY() const rsrc_t rws = mkrs(ws, WS_BYTES)
#define ROLE_TAG() const u32 tag = __builtin_amdgcn_readfirstlane(L[L_TAG]), tag8 = tag & TMASK
  const u32 blk0 = sp * 32 + reg * ROT;
  const u32 srot = sp * SR + reg * SR2;
  const u32 gm = PERM ? 2 * ((sp + (PERM == 2 ? reg : 0)) & 3) + 1 : 1, gc = PERM ? (sp >> 2) + reg : 0;
#define BLK(slot) (PERM ? ((((((slot) >> 2) * gm + gc) & 7u) << 2) + ((slot) & 3u) + srot) & 31u : ((slot) + srot) & 31u)
  const int hq = (h + QS) & 7, ho = (h + HS) & 7;
  u64 org;
#define STREAM_LOADS(SRC, VOFF, SOFF, BAR) \
  const rsrc_t rkv = mkrs((SRC) + ((size_t)reg * SK + (SK - W)) * DH, W * DH * 2); \
  u32x4 d[32]; \
  if (BAR) { __builtin_amdgcn_sched_barrier(0); __builtin_amdgcn_s_barrier(); org = __builtin_amdgcn_s_memrealtime(); __builtin_amdgcn_sched_barrier(0); } \
  MV(2); \
  STREAM_GROUP(0, VOFF, SOFF, BAR) STREAM_GROUP(1, VOFF, SOFF, BAR) STREAM_GROUP(2, VOFF, SOFF, BAR) STREAM_GROUP(3, VOFF, SOFF, BAR) \
  STREAM_GROUP(4, VOFF, SOFF, BAR) STREAM_GROUP(5, VOFF, SOFF, BAR) STREAM_GROUP(6, VOFF, SOFF, BAR) STREAM_GROUP(7, VOFF, SOFF, BAR)
#define STREAM_GROUP(G, VOFF, SOFF, ISK) \
  if (PD > 0 && (!(ISK) || (G) >= PK0)) pace<PA + ((ISK) ? (G) * PD / 10 : PV + (8 * PD + (G) * PDV) / 10 - ((G) >= FINE ? FO : 0))>(org + kw * STG); \
  if (((ISK) ? WIN : WINV) > 0 && 4 * (G) > ((ISK) ? WIN : WINV)) asm volatile("s_waitcnt vmcnt(%0)" :: "i"((ISK) ? WIN : WINV) : "memory"); \
  _Pragma("unroll") for (int i = 4 * (G); i < 4 * (G) + 2; ++i) d[i] = ld128<AUX_NT>(rkv, (VOFF), (int)(SOFF)); \
  if (!(ISK) && (G) >= FINE) { __builtin_amdgcn_sched_barrier(0); pace<PA + PV + (8 * PD + (G) * PDV + PDV / 2) / 10 - FO>(org + kw * STG); } \
  _Pragma("unroll") for (int i = 4 * (G) + 2; i < 4 * (G) + 4; ++i) d[i] = ld128<AUX_NT>(rkv, (VOFF), (int)(SOFF)); \
  __builtin_amdgcn_sched_barrier(0);

  if (__builtin_expect(w >= 4, 0)) {
  u32 tag_out;
  if (w < 8) {
    __builtin_amdgcn_s_setprio(2);
    PAD(3);
    const int g = lane >> 4, key = (lane >> 1) & 7;
    const bool hb = lane & 1;
    const int kvo = key * 512 + (2 * g + (lane & 1)) * 16;
    STREAM_LOADS(kc, kvo + (i & 3) * 128, ((blk0 + BLK(kw + 4 * (i >> 2))) & 511u) << 12, true);
    MV(3);
    ROLE_ENTRY();
    MK0(); MW(2); MW(3);
    PAD(4);
    flag_wait<2, 1>(L + F_Q, lane);
    ROLE_TAG();
    u32x4 qa[4];
#pragma unroll
    for (int i = 0; i < 4; ++i) qa[i] = *(LAS u32x4*)(L + L_Q + (lane & 1) * 128 + i * 32 + (2 * g + ((lane >> 1) & 1)) * 4);
    f32x4 ea, eb;
#pragma unroll
    for (int n = 0; n < 4; ++n) {
      f32x4 ca = {}, cb = {};
#pragma unroll
      for (int i = 0; i < 4; ++i) {
        ca = __builtin_amdgcn_mfma_f32_16x16x32_bf16(__builtin_bit_cast(bf16x8_t, qa[i]), __builtin_bit_cast(bf16x8_t, d[8 * n + i]), ca, 0, 0, 0);
        cb = __builtin_amdgcn_mfma_f32_16x16x32_bf16(__builtin_bit_cast(bf16x8_t, qa[i]), __builtin_bit_cast(bf16x8_t, d[8 * n + 4 + i]), cb, 0, 0, 0);
      }
#pragma unroll
      for (int k = 0; k < 4; ++k) { ea[k] = (n == 0 || g == n) ? ca[k] : ea[k]; eb[k] = (n == 0 || g == n) ? cb[k] : eb[k]; }
    }
    asm volatile("" :: "v"(code_touch));
    float sc[2];
#pragma unroll
    for (int hd = 0; hd < 2; ++hd) sc[hd] = rbf((hb ? eb[2 + hd] : ea[hd]) + dpp<XOR1>(hb ? ea[2 + hd] : eb[hd])) * 0.0625f;
    const float wm0 = mx32(mx16(row_max(sc[0]))), wm1 = mx32(mx16(row_max(sc[1])));
    const u32 tag16 = SET;
    CBAR();
    if (lane < 2) L[L_WM + kw * 2 + lane] = (asu(lane ? wm1 : wm0) & 0xffff0000u) | tag16;
    u32x4 ma, mb;
    for (u32 s = 0; s < SPIN_LDS; ++s) {
      CBAR();
      ma = *(LAS u32x4*)(L + L_WM) ^ tag16; mb = *(LAS u32x4*)(L + L_WM + 4) ^ tag16;
      if (none((((ma.x | ma.y | ma.z | ma.w) | (mb.x | mb.y | mb.z | mb.w)) & 0xffffu) != 0u)) break;
    }
    const float M0 = fmaxf(fmaxf(asf(ma.x), asf(ma.z)), fmaxf(asf(mb.x), asf(mb.z)));
    const float M1 = fmaxf(fmaxf(asf(ma.y), asf(ma.w)), fmaxf(asf(mb.y), asf(mb.w)));
    const float p0 = rbf(__builtin_amdgcn_exp2f((sc[0] - M0) * 1.4426950408889634f));
    const float p1 = rbf(__builtin_amdgcn_exp2f((sc[1] - M1) * 1.4426950408889634f));
    LAS u32* pp = L + L_P + key * 64 + kw + 4 * (2 * g + (lane & 1));
    pp[0] = asu(p0) | (asu(p0) >> 16);
    pp[32] = asu(p1) | (asu(p1) >> 16);
    float ls = tr32(p0, p1);
    ls = row_sum(tr16(ls, ls));
    if ((lane & 31) == 0) LF[L_WL + kw * 2 + (lane >> 5)] = ls;
    flag_set(L + F_P + kw);
    PAD(5);
    MK(2);

    if (kw == 0) {
      flag_wait<4, 0>(L + F_P, lane);
      if (lane < 2) {
        const float Ls = (LF[L_WL + lane] + LF[L_WL + 2 + lane]) + (LF[L_WL + 4 + lane] + LF[L_WL + 6 + lane]);
        st128(rws, ST_OFF + (((h * 2 + lane) * 2 + b) * 16 + sp) * 16, u32x4{asu(lane ? M1 : M0), tag, asu(Ls), tag});
      }
    } else if (kw == 1) {
      __builtin_amdgcn_s_setprio(3);
      PAD(7);
      const int s = lane >> 2, qd = lane & 3;
      const int so = ST_OFF + ((ho * 2 + (j >> 4)) * 32 + s) * 16;
      const int oo = O1_OFF + (ho * 32 + 2 * s) * O1_S + j * 64 + qd * 16;
      u32x4 s0, s1, a0, a1;
      flag_wait<4, 1>(L + F_VI, lane);
      for (u32 it = 0; it < SPIN_MEM; ++it) {
        CBAR();
        s0 = ld128<AUX_SC1>(rws, so); s1 = ld128<AUX_SC1>(rws, so + 256);
        if (none((s0.y != tag) | (s0.w != tag) | (s1.y != tag) | (s1.w != tag))) break;
        flag_wait<1, 1>(L + F_PUB, lane);
      }
      float wgt[2];
#pragma unroll
      for (int bb = 0; bb < 2; ++bb) {
        const float m = asf(bb ? s1.x : s0.x), l = asf(bb ? s1.z : s0.z);
        float mg = fmaxf(m, dpp<ROR8>(m)); mg = fmaxf(mg, dpp<ROR4>(mg)); mg = mx32(mx16(mg));
        const float e = __builtin_amdgcn_exp2f((m - mg) * 1.4426950408889634f);
        float lg = e * l; lg += dpp<ROR8>(lg); lg += dpp<ROR4>(lg); lg = tr16(lg, lg); lg = tr32(lg, lg);
        wgt[bb] = e * __builtin_amdgcn_rcpf(lg);
      }
      LAS u32* ow = L + ((lane & 12) == 0 ? L_O + qd * 4 + (lane >> 4) : L_XN + lane);
      LAS u32* of = L + F_O;
      u32 setv = SET;
      PIN(wgt[0]); PIN(wgt[1]); PIN(ow); PIN(of); PIN(setv);
      if (GO) org += (u64)(long long)(int)__builtin_amdgcn_readfirstlane(L[L_SH]);
      PAD(8);
      flag_wait<1, 0>(L + F_PUB, lane);
      if (D1) __builtin_amdgcn_s_sleep(D1);
      if (TP1) pace<T_LAST + TP1>(org);
      poll2<SLP1>(rws, oo, O1_S, tag8, a0, a1);
      NV(2);
      f32x4 m0 = __builtin_bit_cast(f32x4, a0) * wgt[0], m1 = __builtin_bit_cast(f32x4, a1) * wgt[1];
      PIN(m0); PIN(m1);
      const float v0 = tr32(m0.x, m1.x), v2 = tr32(m0.z, m1.z), v1 = tr32(m0.y, m1.y), v3 = tr32(m0.w, m1.w);
      float e0 = tr16(v0, v2), e1 = tr16(v1, v3);
      e0 += dpp<ROR8>(e0); e1 += dpp<ROR8>(e1);
      e0 += dpp<ROR4>(e0); e1 += dpp<ROR4>(e1);
      *ow = pkbf(e0, e1);
      flag_set(of, setv);
      MK(4); NW(2);
    }
    if (GO != 0 && TP2 != 0 && kw != 1) { flag_wait<1, 4>(L + F_SH, lane); org += (u64)(long long)(int)__builtin_amdgcn_readfirstlane(L[L_SH]); }
    if (PRE && PRE < 3 && kw >= 2) {
      const int po = kw == 2 ? O1_OFF + (h * 32 + j) * O1_S : O2_OFF + (h * 32 + j) * O2_S;
      const u32 t0 = ld32<AUX_SC1>(rws, lane * PSTR < (kw == 2 ? 2048 : PRE == 2 ? 0 : 3584) ? po + lane * PSTR : OOB);
      asm volatile("" :: "v"(t0));
    }
    if ((PT == 2 || PT == 4) && kw == 2 && wg < 56) {
      const u32 t0 = ld32<0>(mkrs(out, 2 * D * 2), lane == 0 ? 0 : OOB);
      asm volatile("" :: "v"(t0));
    }
    if (CT2 && kw == 3) {
      if (CT2 == 2) flag_wait<1, 4>(L + F_VI, lane);
      if (CT2 == 4) pace<T_LAST - 150>(org);
      u32 t0;
      if (CT2 >= 3) { const rsrc_t rc = mkrs((const char*)(pc0 & ~255ull), CODE_SPAN); t0 = ld32<0>(rc, lane * 128) | ld32<0>(rc, 8192 + lane * 128); }
      else t0 = ld32<0>(mkrs((const char*)(pc0 & ~255ull), CODE_SPAN), lane < 5 ? j * 640 + lane * 128 : OOB);
      asm volatile("" :: "v"(t0));
    }
    if (PRE >= 3 && kw >= 2) {
      u32 t0 = 0u;
      if (kw == 2) t0 = ld32<PREAUX>(rws, lane < 32 ? O1_OFF + (h * 32 + lane) * O1_S + j * 64 : OOB);
      else if (PRE == 4 && wg < 56) {
        const int po = O2_OFF + lane * O2_S + (wg / 28) * 1792 + (wg % 28) * 64;
#pragma unroll
        for (int i = 0; i < 4; ++i) t0 |= __builtin_amdgcn_raw_buffer_load_b32(rws, po, i * 64 * O2_S, PREAUX);
      }
      asm volatile("" :: "v"(t0));
    }
    tag_out = tag;
  } else {
    if (kw == 0 && lane < (L_END - L_WM) / 4) *(LAS u32x4*)(L + L_WM + 4 * lane) = u32x4{0u, 0u, 0u, 0u};
    LGKM0();
    __builtin_amdgcn_s_barrier();
    org = __builtin_amdgcn_s_memrealtime();
    __builtin_amdgcn_s_setprio(1);
    __builtin_amdgcn_sched_barrier(0);
    ROLE_ENTRY();
    if (GO) { flag_wait<1, 4>(L + F_SH, lane); org += (u64)(long long)(int)__builtin_amdgcn_readfirstlane(L[L_SH]); }
    STREAM_LOADS(vc, lane * 16 + kw * 1024, ((blk0 + BLK((u32)i)) & 511u) << 12, false);
    MV(4);
    flag_set(L + F_VI + kw);
    PAD(1);
    MK0();
    const int m = lane & 31, hf = lane >> 5;
    u32x4 msk;
#pragma unroll
    for (int r = 0; r < 4; ++r) msk[r] = (m < 16 && ((m & 7) >> 1) == r) ? ((m & 1) ? 0xffff0000u : 0x0000ffffu) : 0u;
    LAS f32x4* op = (LAS f32x4*)(LF + L_OP) + kw * 128 + 2 * m + hf;
    LAS f32x4* rp = (LAS f32x4*)(LF + L_OP) + lane;
    LAS u32* fp = L + F_V + kw;
    LAS u32* fr = L + F_V + (lane & 3);
    int o1o = O1_OFF + (h * 32 + j) * O1_S + lane * 16;
    PIN(op); PIN(rp); PIN(fp); PIN(fr); PIN(o1o);
    flag_wait<4, 1>(L + F_P, lane);
    ROLE_TAG();
    const LAS u32x4* pp = (const LAS u32x4*)(L + L_P + ((kw * 2 + hf) * 2 + ((m >> 3) & 1)) * 32);
    f32x16 acc = {};
#pragma unroll
    for (int g = 0; g < 8; ++g) {
      const u32x4 p4 = pp[g];
#pragma unroll
      for (int k = 0; k < 4; ++k) {
        const u32x4 a = msk & p4[k];
        acc = __builtin_amdgcn_mfma_f32_32x32x16_bf16(__builtin_bit_cast(bf16x8_t, a), __builtin_bit_cast(bf16x8_t, d[4 * g + k]), acc, 0, 0, 0);
      }
      __builtin_amdgcn_sched_barrier(0);
    }
    asm volatile("" :: "v"(code_touch));
    PAD(2);
    MV(6); NV(1);
    op[0] = f32x4{acc[0], acc[1], acc[2], acc[3]};
    op[64] = f32x4{acc[4], acc[5], acc[6], acc[7]};
    flag_set(fp);
    const u32 fl = *fr;
    CBAR();
    const f32x4 o0 = (rp[0] + rp[128]) + (rp[256] + rp[384]);
    const f32x4 o1 = (rp[64] + rp[192]) + (rp[320] + rp[448]);
    u32x4 t0 = tag4(o0.x, o0.y, o0.z, o0.w, tag8), t1 = tag4(o1.x, o1.y, o1.z, o1.w, tag8);
    PIN(t0); PIN(t1);
    if (none(fl != SET)) {
      st128(rws, o1o, t0);
      st128(rws, o1o + 1024, t1);
      flag_set(L + F_PUB);
      MK(3);
    }
    MW(4); MW(6); NW(1);
    tag_out = tag;
  }
  if (wg < 56 && (wg % 28) < ((ABL & 64) ? 16 : (ABL & 128) ? 8 : 28)) final_reduce(L, mkrs(ws, WS_BYTES), x, out, ws, wg, w, w - 4, lane, tag_out, org);
  return;
  }

  __builtin_amdgcn_s_setprio(3);
  ROLE_ENTRY();
  const bool mlp = wg < 224;
  const int cb2 = wg % 28, ke = wg / 28;
  const int rr = lane >> 3, c8 = lane & 7;
  const rsrc_t rx = mkrs((const u16*)x, 2 * D * 2);
  const u32x4 xa = ld128<0>(rx, lane < 56 ? lane * 16 : OOB), xb = ld128<0>(rx, lane < 56 ? D * 2 + lane * 16 : OOB);
  const u32 epoch = ld32<AUX_SC1>(rws, EP_OFF);
  const int row0 = kw < 2 ? 112 * (j >> 3) + 64 * kw : 56 * ke;
  const int nrow = kw == 0 ? 64 : kw == 1 ? 48 : mlp ? 56 : 0;
  u32x4 wv[8];
  {
    const rsrc_t rw = mkrs(kw < 2 ? wq : kw == 2 ? wg_ : wu, kw < 2 ? ((ABL & 32) ? 0 : D * 4096 * 2) : ((ABL & 8) ? 0 : D * DF * 2));
    const int rs = kw < 2 ? 8192 : DF * 2;
    const int vw0 = nrow ? (row0 + 2 * rr) * rs + (kw < 2 ? (hq * 512 + (j & 7) * 64) * 2 : cb2 * 128) + c8 * 16 : OOB;
    const int vw3 = 48 + 2 * rr < nrow ? vw0 : OOB;
#pragma unroll
    for (int i = 0; i < 8; ++i) wv[i] = ld128<AUX_NT>(rw, i < 6 ? vw0 : vw3, (16 * (i >> 1) + (i & 1)) * rs);
  }
  const int eo = lane < 8 ? QP_OFF + (hq * 32 + j) * QP_S + lane * 64 : lane < 24 ? GP_OFF + wg * GP_S + (lane - 8) * 64 : ST_OFF + (wg & 127) * 64;
  const u32 box_touch = ld32<AUX_SC1>(rws, (!(ABL & 16) && kw == 1 && lane < 25) ? eo : OOB);
  __builtin_amdgcn_sched_barrier(0);
  __builtin_amdgcn_s_barrier();
  org = __builtin_amdgcn_s_memrealtime();
  __builtin_amdgcn_sched_barrier(0);
  MK0();
  const u32 page_touch = (PT == 4 || PT == 5) ? ld32<0>(mkrs(out, (kw == 2 && wg >= 248) ? 2 * D * 2 : 0), lane == 0 ? 0 : OOB) : 0u;
  __builtin_amdgcn_sched_barrier(0);

#pragma unroll
  for (int bb = 0; bb < 2; ++bb) {
    const u32x4 xv = bb ? xb : xa;
    float v[8];
#pragma unroll
    for (int k = 0; k < 4; ++k) { v[2 * k] = lo16(xv[k]); v[2 * k + 1] = hi16(xv[k]); }
    float s = 0.f;
#pragma unroll
    for (int k = 0; k < 8; ++k) s += v[k];
    const float mean = wave_sum(s) * (1.f / D);
    float q = 0.f;
#pragma unroll
    for (int k = 0; k < 8; ++k) { const float t = v[k] - mean; q += t * t; }
    q = lane < 56 ? q : 0.f;
    const float var = rbf(wave_sum(q) * (1.f / D));
    const float den = rbf(__builtin_amdgcn_sqrtf(rbf(var + 1e-5f))), rden = __builtin_amdgcn_rcpf(den), mb = rbf(mean);
#pragma unroll
    for (int k = 0; k < 8; ++k) { const float c = rbf(v[k] - mb), r = c * rden; v[k] = fmaf(fmaf(-r, den, c), rden, r); }
    u32x4 o;
#pragma unroll
    for (int k = 0; k < 4; ++k) o[k] = pkbf(v[2 * k], v[2 * k + 1]);
    if (lane < 56) *(LAS u32x4*)(L + L_XN + bb * 224 + lane * 4) = o;
    else if (bb == 1) *(LAS u32x4*)(L + L_XN + 224 + lane * 4) = u32x4{0u, 0u, 0u, 0u};
  }
  asm volatile("" :: "v"(code_touch));
  const u32 tag = __builtin_amdgcn_readfirstlane(epoch) + 1u, tag8 = tag & TMASK;
  if (kw == 0) L[L_TAG] = tag;
  if (GO && kw == 3 && wg < 8 && lane == 0) __builtin_amdgcn_raw_buffer_store_b64(u32x2{(u32)org, tag}, rws, OR_OFF + wg * 8, 0, AUX_SC1);
  MK(10);

  {
    float a0[8] = {}, a1[8] = {};
#pragma unroll
    for (int k = 0; k < 4; ++k) {
      const u32 x0 = L[L_XN + row0 / 2 + rr + 8 * k], x1 = L[L_XN + 224 + row0 / 2 + rr + 8 * k];
#pragma unroll
      for (int c = 0; c < 4; ++c) {
        const u32 pl = __builtin_amdgcn_perm(wv[2 * k + 1][c], wv[2 * k][c], 0x05040100u), ph = __builtin_amdgcn_perm(wv[2 * k + 1][c], wv[2 * k][c], 0x07060302u);
        a0[2 * c] = dot2(pl, x0, a0[2 * c]); a0[2 * c + 1] = dot2(ph, x0, a0[2 * c + 1]);
        a1[2 * c] = dot2(pl, x1, a1[2 * c]); a1[2 * c + 1] = dot2(ph, x1, a1[2 * c + 1]);
      }
    }
    asm volatile("" :: "v"(box_touch));
    MV(5);
    float t[8], u[4];
#pragma unroll
    for (int k = 0; k < 8; ++k) t[k] = tr32(a0[k], a1[k]);
#pragma unroll
    for (int k = 0; k < 4; ++k) { u[k] = tr16(t[k], t[k + 4]); u[k] += dpp<ROR8>(u[k]); }
    const int bb = lane >> 5, hf = (lane >> 4) & 1;
    LAS f32x4* qx = (LAS f32x4*)(LF + L_QX) + bb * 16 + c8 * 2 + hf;
    if (kw == 1) {
      if (!(lane & 8)) *qx = f32x4{u[0], u[1], u[2], u[3]};
      flag_set(L + F_QX);
    } else if (kw == 0) {
      flag_wait<1, 0>(L + F_QX, lane);
      const f32x4 o = *qx;
      if (!(ABL & 1) && !(lane & 8)) st128(rws, QP_OFF + (hq * 32 + j) * QP_S + bb * 256 + hf * 128 + c8 * 16, tag4(u[0] + o.x, u[1] + o.y, u[2] + o.z, u[3] + o.w, tag8));
    } else if (!(ABL & 2) && mlp) {
      LAS u32x4* gx = (LAS u32x4*)(L + L_GX) + (kw - 2) * 32;
      if (!(lane & 8)) gx[c8 * 4 + bb * 2 + hf] = tag4(u[0], u[1], u[2], u[3], tag8);
      CBAR();
      if (lane < 32) st128(rws, GP_OFF + wg * GP_S + (lane >> 2) * 128 + (kw - 2) * 64 + (lane & 3) * 16, gx[lane]);
    }
  }

  if (kw < 2) {
    __builtin_amdgcn_s_sleep(QDELAY);
    if (TQ) pace<TQ>(org);
    const int qo = QP_OFF + (h * 32 + 4 * kw + 8 * (lane >> 4)) * QP_S + b * 256 + (lane & 15) * 16;
    u32x4 a[4];
    for (u32 s = 0; s < SPIN_MEM; ++s) {
      CBAR();
      if (ABL & 1) { a[0] = a[1] = a[2] = a[3] = xa; break; }
#pragma unroll
      for (int i = 0; i < 4; ++i) a[i] = ld128<AUX_SC1>(rws, qo, i * QP_S);
      if (all_ok((untag4(a[0], tag8) | untag4(a[1], tag8)) | (untag4(a[2], tag8) | untag4(a[3], tag8)))) break;
    }
#pragma unroll
    for (int i = 0; i < 2; ++i) {
      const u32x4 a0 = a[2 * i], a1 = a[2 * i + 1];
      const float v0 = tr32(asf(a0.x), asf(a1.x)), v1 = tr32(asf(a0.y), asf(a1.y)), v2 = tr32(asf(a0.z), asf(a1.z)), v3 = tr32(asf(a0.w), asf(a1.w));
      L[L_Q + (4 * kw + 2 * i + (lane >> 5)) * 32 + 4 * (lane & 7) + 2 * ((lane >> 3) & 1) + ((lane >> 4) & 1)] = pkbf(tr16(v0, v2), tr16(v1, v3));
    }
    flag_set(L + F_Q + kw);
    MK(1);
  } else if (kw == 2) {
    u32 h0 = 0u, h1 = 0u;
    if (mlp) {
      const int go = GP_OFF + (cb2 + 28 * rr) * GP_S + ke * 128 + c8 * 16;
      u32x4 a;
      flag_wait<2, 1>(L + F_Q, lane);
      if (TG) pace<TG>(org);
      for (u32 s = 0; s < SPIN_MEM; ++s) {
        CBAR();
        if (ABL & 2) { a = xa; break; }
        a = ld128<AUX_SC1>(rws, go);
        if (all_ok(untag4(a, tag8))) break;
        __builtin_amdgcn_s_sleep(16);
      }
      float hv[4];
#pragma unroll
      for (int k = 0; k < 4; ++k) {
        float g = asf(a[k]);
        g += dpp<ROR8>(g); g = tr16(g, g); g = tr32(g, g);
        g = rbf(g);
        const float uu = dpp<ROR4>(g);
        const float z = 0.7978845608028654f * (g + 0.044715f * g * g * g);
        const float th = 1.f - 2.f * __builtin_amdgcn_rcpf(1.f + __builtin_amdgcn_exp2f(z * 2.8853900817779268f));
        hv[k] = rbf(0.5f * g * (1.f + th)) * uu;
      }
      h0 = pkbf(hv[0], hv[1]); h1 = pkbf(hv[2], hv[3]);
    }
    if (lane < 4) {
      *(LAS u32x2*)(L + L_H + (2 * (lane & 1)) * 4 + (lane >> 1) * 2) = u32x2{h0, SET};
      *(LAS u32x2*)(L + L_H + (2 * (lane & 1) + 1) * 4 + (lane >> 1) * 2) = u32x2{h1, SET};
    }
    CBAR();
  } else if (GO) {
    pace<300>(org);
    const u32x2 o = __builtin_amdgcn_raw_buffer_load_b64(rws, OR_OFF + (lane & 7) * 8, 0, AUX_SC1);
    const bool ok = o.y == tag;
    const float n = row_sum(ok ? 1.f : 0.f), dsum = row_sum(ok ? (float)(int)(o.x - (u32)org) : 0.f);
    const float sh = n > 0.f ? fminf(fmaxf(dsum * __builtin_amdgcn_rcpf(n) + GB, -80.f), 80.f) : 0.f;
    L[L_SH] = (u32)(int)sh;
    flag_set(L + F_SH);
  }

  asm volatile("" :: "v"(page_touch));
  const int rg = lane >> 4, cl = lane & 15;
  const int co = kw * 128 + cl * 8;
  u32x4 wa[4], wb[2];
  if (LATE == 0) flag_wait<2, 1>(L + F_Q, lane);
  if (LATE >= 1) flag_wait<4, 1>(L + F_VI, lane);
  if (LATE == 3) flag_wait<1, 0>(L + F_PUB, lane);
  if (GO) org += (u64)(long long)(int)__builtin_amdgcn_readfirstlane(L[L_SH]);
  if (LATE == 4) pace<PA + PV + (8 * PD + 8 * PDV) / 10 + LD>(org + kw * STG);
  if (LATE == 5) __builtin_amdgcn_s_sleep(LD);
  {
    const rsrc_t ro = mkrs(wo, (ABL & 4) ? 0 : 4096 * D * 2), rd = mkrs(wd, mlp && !(ABL & 4) ? DF * D * 2 : 0);
    const int vo = co < D ? (ho * 512 + j * 16 + 4 * rg) * (D * 2) + co * 2 : OOB;
    const int vd = co < D ? (64 * cb2 + 8 * ke + 2 * rg) * (D * 2) + co * 2 : OOB;
#pragma unroll
    for (int i = 0; i < 2; ++i) wb[i] = ld128<AUX_NT>(rd, vd, i * D * 2);
#pragma unroll
    for (int i = 0; i < 4; ++i) wa[i] = ld128<AUX_NT>(ro, vo, i * D * 2);
  }
  __builtin_amdgcn_sched_barrier(0);
  float a0[8], a1[8];
  u32 pa[8], pb[8];
  {
    u32x4 hh;
    for (u32 s = 0; s < SPIN_LDS; ++s) {
      CBAR();
      hh = *(LAS u32x4*)(L + L_H + rg * 4);
      if (none((hh.y != SET) | (hh.w != SET))) break;
      __builtin_amdgcn_s_sleep(1);
    }
#pragma unroll
    for (int k = 0; k < 4; ++k) {
      const u32 pl = __builtin_amdgcn_perm(wb[1][k], wb[0][k], 0x05040100u), ph = __builtin_amdgcn_perm(wb[1][k], wb[0][k], 0x07060302u);
      a0[2 * k] = dot2(pl, hh.x, 0.f); a0[2 * k + 1] = dot2(ph, hh.x, 0.f);
      a1[2 * k] = dot2(pl, hh.z, 0.f); a1[2 * k + 1] = dot2(ph, hh.z, 0.f);
    }
#pragma unroll
    for (int k = 0; k < 4; ++k) {
      pa[2 * k] = __builtin_amdgcn_perm(wa[1][k], wa[0][k], 0x05040100u); pa[2 * k + 1] = __builtin_amdgcn_perm(wa[1][k], wa[0][k], 0x07060302u);
      pb[2 * k] = __builtin_amdgcn_perm(wa[3][k], wa[2][k], 0x05040100u); pb[2 * k + 1] = __builtin_amdgcn_perm(wa[3][k], wa[2][k], 0x07060302u);
    }
  }
#pragma unroll
  for (int k = 0; k < 8; ++k) asm volatile("" : "+v"(a0[k]), "+v"(a1[k]), "+v"(pa[k]), "+v"(pb[k]));
  int o2o = !((ABL & 256) && j >= 16) && co < ((ABL & 64) ? 256 : (ABL & 128) ? 128 : D) ? O2_OFF + (h * 32 + j) * O2_S + (lane >> 5) * 1792 + kw * 512 + (((lane >> 4) & 1) * (kw == 3 ? 8 : 16) + cl) * 16 : OOB;
  LAS u32x4* orp = (LAS u32x4*)(L + L_O + rg * 4);
  LAS u32* ofp = L + F_O;
  PIN(o2o); PIN(orp); PIN(ofp);
  if (LATE == 0) flag_wait<4, 1>(L + F_VI, lane);
  {
    const u32x4 oo = data_wait(ofp, orp);
#pragma unroll
    for (int k = 0; k < 8; ++k) {
      a0[k] = dot2(pb[k], oo.y, dot2(pa[k], oo.x, a0[k]));
      a1[k] = dot2(pb[k], oo.w, dot2(pa[k], oo.z, a1[k]));
    }
    float t[8];
#pragma unroll
    for (int k = 0; k < 8; ++k) t[k] = tr32(a0[k], a1[k]);
    st128(rws, o2o, tag4(tr16(t[0], t[4]), tr16(t[1], t[5]), tr16(t[2], t[6]), tr16(t[3], t[7]), tag8));
  }
  flag_set(L + F_O2 + kw);
  NV(3); NW(3);
  MK(5);
}

extern "C" void kernel_launch(void* const* d_in, const int* in_sizes, int n_in, void* d_out, int out_size, void* d_ws, size_t ws_size,
                              hipStream_t stream) {
  fused_decode<<<256, 768, 0, stream>>>((const u16*)d_in[0], (const u16*)d_in[3], (const u16*)d_in[6], (const u16*)d_in[7], (const u16*)d_in[1],
                                        (char*)d_ws + WS_OFF, (const u16*)d_in[2], (const u16*)d_in[4], (const u16*)d_in[8], (u16*)d_out);
}
```

```cpp
#include <hip/hip_runtime.h>
#include <hip/hip_bf16.h>
#include <stdint.h>

typedef unsigned int u32;
typedef unsigned short u16;
typedef unsigned long long u64;
typedef u32 u32x2 __attribute__((ext_vector_type(2)));
typedef u32 u32x4 __attribute__((ext_vector_type(4)));
typedef float f32x4 __attribute__((ext_vector_type(4)));
typedef float f32x16 __attribute__((ext_vector_type(16)));
typedef __bf16 bf16x2_t __attribute__((ext_vector_type(2)));
typedef __bf16 bf16x8_t __attribute__((ext_vector_type(8)));
#define LAS __attribute__((address_space(3)))
typedef __amdgpu_buffer_rsrc_t rsrc_t;

#define STAMP 0
#define LATE 4
#define QDELAY 40
#define PRE 0
#define PSTR 64
#define ABL 0
#define SLP1 0
#define SLP2 0
#define PSTAG 5
#define DBL 0
#define PA 30
#define PD 620
#define PK0 2
#define D1 10
#define D2 9
#define PV 0
#define LD -45
#define PDV 650
#define STG 0
#define SR 3
#define SR2 0
#define GO 1
#define GB 5
#define TP1 0
#define TP2 0
#define PREAUX 16
#define STAUX 16
#define WIN 6
#define LAUX 16
#define FINE 4
#define FO 25
#define TQ 0
#define TG 0
#define WINV WIN
#define MVM 126
#define HS 0
#define QS 0
#define PADAT 0
#define PADN 2
#define PAD(k) do { if (PADAT == (k)) asm volatile(".fill %c0, 4, 0xBF800000" :: "i"(PADN)); } while (0)
#define CT2 0
#define PERM 0
#define PT 1
#define ROT 37
#define CODE_SPAN 15872
#define CTW 0
#define DIAG 9
#define AMP 8
#define BASE 0
#define WS_OFF 0

namespace {
constexpr int D = 448, SK = 32768, W = 4096, DH = 256, DF = 1792;
constexpr int AUX_NT = 2, AUX_SC1 = 16;
constexpr int OOB = 1 << 30;
constexpr u32 TMASK = 0xfu;

constexpr int QP_S = 768, GP_S = 1280, O1_S = 2304, O2_S = 3840;
constexpr int EP_OFF = 0;
constexpr int OR_OFF = 2048;
constexpr int ST_OFF = 4096;
constexpr int QP_OFF = ST_OFF + 8192;
constexpr int GP_OFF = QP_OFF + 256 * QP_S;
constexpr int O1_OFF = GP_OFF + 256 * GP_S;
constexpr int O2_OFF = O1_OFF + 256 * O1_S;
constexpr int WS_BYTES = O2_OFF + 256 * O2_S;
constexpr int TS_OFF = 4 << 20;

constexpr int L_XN = 0;
constexpr int L_QX = L_XN + 480;
constexpr int L_GX = L_QX + 128;
constexpr int L_Q = L_GX + 256;
constexpr int L_P = L_Q + 256;
constexpr int L_OP = L_P + 512;
constexpr int L_WM = L_OP + 2048;
constexpr int L_WL = L_WM + 8;
constexpr int L_H = L_WL + 8;
constexpr int L_O = L_H + 16;
constexpr int F_O = L_O + 16;
constexpr int L_R = L_O + 32;
constexpr int F_P = L_R + 128;
constexpr int F_VI = F_P + 4;
constexpr int F_O2 = F_VI + 4;
constexpr int F_Q = F_O2 + 4;
constexpr int F_QX = F_Q + 2, F_PUB = F_QX + 1;
constexpr int F_V = F_PUB + 1;
constexpr int F_R = F_V + 4;
constexpr int L_TAG = F_R + 8;
constexpr int L_DG = L_TAG + 1;
constexpr int L_SH = L_TAG + 2, F_SH = L_TAG + 3;
constexpr int L_END = L_TAG + 4;
constexpr u32 SET = 1u;
static_assert((L_END - L_WM) % 4 == 0 && (L_END - L_WM) / 4 <= 64, "one 16-byte store per lane clears the flag area");

constexpr int T_LAST = PA + PV + (8 * PD + 7 * PDV) / 10;
constexpr u32 SPIN_LDS = 1u << 21, SPIN_MEM = 1u << 16;

__device__ __forceinline__ float asf(u32 v) { return __builtin_bit_cast(float, v); }
__device__ __forceinline__ u32 asu(float v) { return __builtin_bit_cast(u32, v); }
__device__ __forceinline__ float lo16(u32 v) { return asf(v << 16); }
__device__ __forceinline__ float hi16(u32 v) { return asf(v & 0xffff0000u); }
__device__ __forceinline__ u32 pkbf(float a, float b) {
  bf16x2_t r = {(__bf16)a, (__bf16)b};
  return __builtin_bit_cast(u32, r);
}
__device__ __forceinline__ float rbf(float a) { return (float)(__bf16)a; }
__device__ __forceinline__ float dot2(u32 a, u32 b, float c) {
  return __builtin_amdgcn_fdot2_f32_bf16(__builtin_bit_cast(bf16x2_t, a), __builtin_bit_cast(bf16x2_t, b), c, false);
}
template <int CTRL> __device__ __forceinline__ float dpp(float x) {
  return asf(__builtin_amdgcn_mov_dpp((int)asu(x), CTRL, 0xf, 0xf, true));
}
constexpr int XOR1 = 0xB1, XOR2 = 0x4E, ROR8 = 0x128, ROR4 = 0x124;
__device__ __forceinline__ float tr32(float a, float b) {
  auto r = __builtin_amdgcn_permlane32_swap(asu(a), asu(b), false, false);
  return asf(r[0]) + asf(r[1]);
}
__device__ __forceinline__ float tr16(float a, float b) {
  auto r = __builtin_amdgcn_permlane16_swap(asu(a), asu(b), false, false);
  return asf(r[0]) + asf(r[1]);
}
__device__ __forceinline__ float mx32(float a) {
  auto r = __builtin_amdgcn_permlane32_swap(asu(a), asu(a), false, false);
  return fmaxf(asf(r[0]), asf(r[1]));
}
__device__ __forceinline__ float mx16(float a) {
  auto r = __builtin_amdgcn_permlane16_swap(asu(a), asu(a), false, false);
  return fmaxf(asf(r[0]), asf(r[1]));
}
__device__ __forceinline__ float row_sum(float v) { v += dpp<ROR8>(v); v += dpp<ROR4>(v); v += dpp<XOR2>(v); v += dpp<XOR1>(v); return v; }
__device__ __forceinline__ float row_max(float v) {
  v = fmaxf(v, dpp<ROR8>(v)); v = fmaxf(v, dpp<ROR4>(v)); v = fmaxf(v, dpp<XOR2>(v)); v = fmaxf(v, dpp<XOR1>(v)); return v;
}
__device__ __forceinline__ float wave_sum(float v) { v = row_sum(v); v = tr16(v, v); return tr32(v, v); }

__device__ __forceinline__ rsrc_t mkrs(const void* p, u32 bytes) { return __builtin_amdgcn_make_buffer_rsrc((void*)p, 0, bytes, 0x00020000); }
template <int AUX> __device__ __forceinline__ u32x4 ld128(rsrc_t r, int voff, int soff = 0) { return __builtin_amdgcn_raw_buffer_load_b128(r, voff, soff, AUX); }
template <int AUX> __device__ __forceinline__ u32 ld32(rsrc_t r, int voff) { return __builtin_amdgcn_raw_buffer_load_b32(r, voff, 0, AUX); }
__device__ __forceinline__ void st128(rsrc_t r, int voff, u32x4 v) { __builtin_amdgcn_raw_buffer_store_b128(v, r, voff, 0, STAUX); }
__device__ __forceinline__ u32 tagw(float v, u32 tag4) { return (asu(v) & ~TMASK) | tag4; }
__device__ __forceinline__ u32x4 tag4(float a, float b, float c, float d, u32 t) { return u32x4{tagw(a, t), tagw(b, t), tagw(c, t), tagw(d, t)}; }
__device__ __forceinline__ u32 untag4(u32x4& v, u32 t) { v.x ^= t; v.y ^= t; v.z ^= t; v.w ^= t; return v.x | v.y | v.z | v.w; }
__device__ __forceinline__ bool none(bool bad) { return __builtin_amdgcn_ballot_w64(bad) == 0ull; }
__device__ __forceinline__ bool all_ok(u32 bad) { return none((bad & TMASK) != 0u); }

#define CBAR() asm volatile("" ::: "memory")
#define PIN(v) asm volatile("" : "+v"(v))
#define LGKM0() asm volatile("s_waitcnt lgkmcnt(0)" ::: "memory")
template <int N, int SLEEP> __device__ __forceinline__ void flag_wait(LAS u32* p, int lane) {
  u32 v, cnt;
  const u32 addr = (u32)(uintptr_t)(p + (lane & (N - 1)));
  asm volatile("s_mov_b32 %1, %4\n"
               "1:\n\t"
               "ds_read_b32 %0, %2\n\t"
               "s_waitcnt lgkmcnt(0)\n\t"
               "v_cmp_ne_u32 vcc, %3, %0\n\t"
               "s_cbranch_vccz 2f\n\t"
               "s_sleep %5\n\t"
               "s_sub_u32 %1, %1, 1\n\t"
               "s_cbranch_scc0 1b\n"
               "2:"
               : "=&v"(v), "=&s"(cnt) : "v"(addr), "i"(SET), "i"(SPIN_LDS), "i"(SLEEP) : "vcc", "scc", "memory");
}
template <int DUE> __device__ __forceinline__ void pace(u64 org) {
  asm volatile("1:\n\t"
               "s_memrealtime vcc\n\t"
               "s_waitcnt lgkmcnt(0)\n\t"
               "s_sub_u32 vcc_lo, vcc_lo, %0\n\t"
               "s_cmp_lt_i32 vcc_lo, %1\n\t"
               "s_cbranch_scc0 2f\n\t"
               "s_sleep 1\n\t"
               "s_branch 1b\n"
               "2:"
               :: "s"((u32)org), "i"(DUE), "s"(org) : "vcc", "scc", "memory");
}
__device__ __forceinline__ void flag_set(LAS u32* p, u32 set = SET) { CBAR(); *p = set; CBAR(); }

template <int SLP> __device__ __forceinline__ void poll2(rsrc_t rs, int voff, int soff, u32 t4, u32x4& r0, u32x4& r1) {
  u32 o0, o1, o2, o3, o4, o5, o6, o7, m, cnt;
  asm volatile("s_mov_b32 %9, %14\n"
               "1:\n\t"
               "buffer_load_dwordx4 v[144:147], %10, %11, 0 offen sc1\n\t"
               "buffer_load_dwordx4 v[148:151], %10, %11, %12 offen sc1\n\t"
               "s_sub_u32 %9, %9, 1\n\t"
               "s_waitcnt vmcnt(1)\n\t"
               "v_xor_b32 %0, %13, v144\n\tv_xor_b32 %1, %13, v145\n\tv_xor_b32 %2, %13, v146\n\tv_xor_b32 %3, %13, v147\n\t"
               "v_or3_b32 %8, %0, %1, %2\n\t"
               "s_waitcnt vmcnt(0)\n\t"
               "v_xor_b32 %4, %13, v148\n\tv_xor_b32 %5, %13, v149\n\tv_xor_b32 %6, %13, v150\n\tv_xor_b32 %7, %13, v151\n\t"
               "v_or3_b32 %8, %8, %3, %4\n\t"
               "v_or3_b32 %8, %8, %5, %6\n\t"
               "v_bitop3_b32 %8, %8, 15, %7 bitop3:0xc8\n\t"
               "v_cmp_ne_u32 vcc, 0, %8\n\t"
               "s_cbranch_vccz 2f\n\t"
               "s_sleep %15\n\t"
               "s_cbranch_scc0 1b\n"
               "2:"
               : "=&v"(o0), "=&v"(o1), "=&v"(o2), "=&v"(o3), "=&v"(o4), "=&v"(o5), "=&v"(o6), "=&v"(o7), "=&v"(m), "=&s"(cnt)
               : "v"(voff), "s"(rs), "s"(soff), "s"(t4), "i"(SPIN_MEM), "i"(SLP)
               : "vcc", "scc", "memory", "v144", "v145", "v146", "v147", "v148", "v149", "v150", "v151");
  r0 = u32x4{o0, o1, o2, o3};
  r1 = u32x4{o4, o5, o6, o7};
}
__device__ __forceinline__ u32x4 data_wait(LAS u32* flag, LAS u32x4* p) {
  u32x4 r;
  u32 v, cnt;
  asm volatile("s_mov_b32 %2, %5\n"
               "1:\n\t"
               "ds_read_b32 %1, %3\n\t"
               "ds_read_b128 %0, %4\n\t"
               "s_sub_u32 %2, %2, 1\n\t"
               "s_waitcnt lgkmcnt(0)\n\t"
               "v_cmp_ne_u32 vcc, 1, %1\n\t"
               "s_cbranch_vccz 2f\n\t"
               "s_cbranch_scc0 1b\n"
               "2:"
               : "=&v"(r), "=&v"(v), "=&s"(cnt) : "v"((u32)(uintptr_t)flag), "v"((u32)(uintptr_t)p), "i"(SPIN_LDS) : "vcc", "scc", "memory");
  return r;
}

#define MK(k)
#define MV(k)
#define MW(k)
#define NV(k)
#define NW(k)
#define NC(k, c)
#define MK0()

__device__ __forceinline__ void final_reduce(LAS u32* L, rsrc_t rws, const u16* x, u16* out, char* ws, int wg, int w, int grp, int lane, u32 tag, u64 org, u64 tent) {
  LAS float* const LF = (LAS float*)L;
  __builtin_amdgcn_s_setprio(3);
  PAD(6);
  const u32 tag8 = tag & TMASK;
  const int rb = wg / 28, cb = wg % 28, qd = lane & 3;
  const int pi = cb & 7, ph = cb < 24 ? pi >> 2 : pi >> 1, pg = cb < 24 ? pi & 3 : pi & 1;
  const int col = rb * D + 128 * (cb >> 3) + 8 * (4 * pg + ((lane >> 2) & 3)) + 4 * ph + (lane & 3);
  float xres = lo16(x[col]);
  int oo = O2_OFF + (grp * 32 + (lane >> 2)) * O2_S + rb * 1792 + cb * 64 + qd * 16;
  LAS float* wp = LF + ((lane & 12) == 0 ? L_R + (4 * qd + (lane >> 4)) * 8 + grp : L_XN + 128 + lane);
  LAS u32* fp = L + F_R + grp;
  LAS u32* fr = L + F_R + (lane & 7);
  LAS f32x4* rp = (LAS f32x4*)(LF + L_R + (lane & 15) * 8);
  int so = lane < 16 ? col * 2 : OOB;
  u32 tagv = SET;
  rsrc_t ro = mkrs(out, 2 * D * 2);
  asm volatile("" : "+s"(ro));
  PIN(xres); PIN(oo); PIN(wp); PIN(fp); PIN(fr); PIN(rp); PIN(so); PIN(tagv);
  flag_wait<1, 1>(L + F_PUB, lane);
  flag_wait<4, 0>(L + F_O2, lane);
  if (D2) __builtin_amdgcn_s_sleep(D2);
  if (TP2) pace<T_LAST + TP2>(org);
  u32x4 a0, a1;
  poll2<SLP2>(rws, oo, 16 * O2_S, tag8, a0, a1);
  MK(6);
  NV(4);
  float f = tr16(tr32(asf(a0.x) + asf(a1.x), asf(a0.z) + asf(a1.z)), tr32(asf(a0.y) + asf(a1.y), asf(a0.w) + asf(a1.w)));
  f += dpp<ROR8>(f); f += dpp<ROR4>(f);
  *wp = f;
  flag_set(fp, tagv);
  u32 fl = *fr;
  CBAR();
  const f32x4 r0 = rp[0], r1 = rp[1];
  const f32x4 rs4 = r0 + r1;
  const float tot = xres + ((rs4.x + rs4.z) + (rs4.y + rs4.w));
  u32 res = pkbf(tot, tot);
  PIN(res);
  if (none(fl != SET)) {
    __builtin_amdgcn_raw_buffer_store_b16((u16)res, ro, so, 0, 0);
    if (wg == 0 && lane == 0) __builtin_amdgcn_raw_buffer_store_b32(tag, rws, EP_OFF, 0, AUX_SC1);
    if (wg == 0) {
      const u64 now = __builtin_amdgcn_s_memrealtime();
      u64 ev = DIAG == 0 ? tent : DIAG == 7 ? now : tent + __builtin_amdgcn_readfirstlane(L[L_DG]);
      const u64 until = tent + 3000 + AMP * (ev - tent - BASE);
      while (__builtin_amdgcn_s_memrealtime() < until) __builtin_amdgcn_s_sleep(1);
    }
    MK(7);
  }
  NW(4);
}

}

extern "C" __global__ void __launch_bounds__(768, 3)
fused_decode(const u16* __restrict__ x, const u16* __restrict__ wq, const u16* __restrict__ wg_, const u16* __restrict__ wu,
             const u16* __restrict__ kc, char* __restrict__ ws, const u16* __restrict__ vc, const u16* __restrict__ wo,
             const u16* __restrict__ wd, u16* __restrict__ out) {
  __shared__ __attribute__((aligned(16))) u32 smem[L_END];
  LAS u32* const L = (LAS u32*)smem;
  LAS float* const LF = (LAS float*)smem;
  const int wg = blockIdx.x, h = wg & 7, j = wg >> 3, b = j & 1, sp = j >> 1, reg = wg & 15;
  const int w = __builtin_amdgcn_readfirstlane(threadIdx.x >> 6), lane = threadIdx.x & 63, kw = w & 3;
  const u64 tent = __builtin_amdgcn_s_memrealtime();
#undef MK
#define MK(k) do { if (DIAG == (k)) { const u32 dt_ = (u32)(__builtin_amdgcn_s_memrealtime() - tent); if (lane == 0) (void)__hip_atomic_fetch_max(L + L_DG, dt_, __ATOMIC_RELAXED, __HIP_MEMORY_SCOPE_WORKGROUP); } } while (0)
  asm volatile("s_load_dword s94, %0, 0x0\n\ts_load_dword s95, %0, 0x40" :: "s"(__builtin_amdgcn_kernarg_segment_ptr()) : "s94", "s95");
  u64 pc0;
  asm volatile("s_getpc_b64 %0" : "=s"(pc0));
  const u32 code_touch = ld32<0>(mkrs((const char*)(pc0 & ~255ull), w == CTW ? CODE_SPAN : 0), lane < 5 ? j * 640 + lane * 128 : OOB);
  __builtin_amdgcn_sched_barrier(0);
#define ROLE_ENTRY() const rsrc_t rws = mkrs(ws, WS_BYTES)
#define ROLE_TAG() const u32 tag = __builtin_amdgcn_readfirstlane(L[L_TAG]), tag8 = tag & TMASK
  const u32 blk0 = sp * 32 + reg * ROT;
  const u32 srot = sp * SR + reg * SR2;
  const u32 gm = PERM ? 2 * ((sp + (PERM == 2 ? reg : 0)) & 3) + 1 : 1, gc = PERM ? (sp >> 2) + reg : 0;
#define BLK(slot) (PERM ? ((((((slot) >> 2) * gm + gc) & 7u) << 2) + ((slot) & 3u) + srot) & 31u : ((slot) + srot) & 31u)
  const int hq = (h + QS) & 7, ho = (h + HS) & 7;
  u64 org;
#define STREAM_LOADS(SRC, VOFF, SOFF, BAR) \
  const rsrc_t rkv = mkrs((SRC) + ((size_t)reg * SK + (SK - W)) * DH, W * DH * 2); \
  u32x4 d[32]; \
  if (BAR) { __builtin_amdgcn_sched_barrier(0); __builtin_amdgcn_s_barrier(); org = __builtin_amdgcn_s_memrealtime(); __builtin_amdgcn_sched_barrier(0); } \
  MV(2); \
  STREAM_GROUP(0, VOFF, SOFF, BAR) STREAM_GROUP(1, VOFF, SOFF, BAR) STREAM_GROUP(2, VOFF, SOFF, BAR) STREAM_GROUP(3, VOFF, SOFF, BAR) \
  STREAM_GROUP(4, VOFF, SOFF, BAR) STREAM_GROUP(5, VOFF, SOFF, BAR) STREAM_GROUP(6, VOFF, SOFF, BAR) STREAM_GROUP(7, VOFF, SOFF, BAR)
#define STREAM_GROUP(G, VOFF, SOFF, ISK) \
  if (PD > 0 && (!(ISK) || (G) >= PK0)) pace<PA + ((ISK) ? (G) * PD / 10 : PV + (8 * PD + (G) * PDV) / 10 - ((G) >= FINE ? FO : 0))>(org + kw * STG); \
  if (((ISK) ? WIN : WINV) > 0 && 4 * (G) > ((ISK) ? WIN : WINV)) asm volatile("s_waitcnt vmcnt(%0)" :: "i"((ISK) ? WIN : WINV) : "memory"); \
  _Pragma("unroll") for (int i = 4 * (G); i < 4 * (G) + 2; ++i) d[i] = ld128<AUX_NT>(rkv, (VOFF), (int)(SOFF)); \
  if (!(ISK) && (G) >= FINE) { __builtin_amdgcn_sched_barrier(0); pace<PA + PV + (8 * PD + (G) * PDV + PDV / 2) / 10 - FO>(org + kw * STG); } \
  _Pragma("unroll") for (int i = 4 * (G) + 2; i < 4 * (G) + 4; ++i) d[i] = ld128<AUX_NT>(rkv, (VOFF), (int)(SOFF)); \
  __builtin_amdgcn_sched_barrier(0);

  if (__builtin_expect(w >= 4, 0)) {
  u32 tag_out;
  if (w < 8) {
    __builtin_amdgcn_s_setprio(2);
    PAD(3);
    const int g = lane >> 4, key = (lane >> 1) & 7;
    const bool hb = lane & 1;
    const int kvo = key * 512 + (2 * g + (lane & 1)) * 16;
    STREAM_LOADS(kc, kvo + (i & 3) * 128, ((blk0 + BLK(kw + 4 * (i >> 2))) & 511u) << 12, true);
    MV(3);
    ROLE_ENTRY();
    MK0(); MW(2); MW(3);
    PAD(4);
    flag_wait<2, 1>(L + F_Q, lane);
    ROLE_TAG();
    u32x4 qa[4];
#pragma unroll
    for (int i = 0; i < 4; ++i) qa[i] = *(LAS u32x4*)(L + L_Q + (lane & 1) * 128 + i * 32 + (2 * g + ((lane >> 1) & 1)) * 4);
    f32x4 ea, eb;
#pragma unroll
    for (int n = 0; n < 4; ++n) {
      f32x4 ca = {}, cb = {};
#pragma unroll
      for (int i = 0; i < 4; ++i) {
        ca = __builtin_amdgcn_mfma_f32_16x16x32_bf16(__builtin_bit_cast(bf16x8_t, qa[i]), __builtin_bit_cast(bf16x8_t, d[8 * n + i]), ca, 0, 0, 0);
        cb = __builtin_amdgcn_mfma_f32_16x16x32_bf16(__builtin_bit_cast(bf16x8_t, qa[i]), __builtin_bit_cast(bf16x8_t, d[8 * n + 4 + i]), cb, 0, 0, 0);
      }
#pragma unroll
      for (int k = 0; k < 4; ++k) { ea[k] = (n == 0 || g == n) ? ca[k] : ea[k]; eb[k] = (n == 0 || g == n) ? cb[k] : eb[k]; }
    }
    asm volatile("" :: "v"(code_touch));
    float sc[2];
#pragma unroll
    for (int hd = 0; hd < 2; ++hd) sc[hd] = rbf((hb ? eb[2 + hd] : ea[hd]) + dpp<XOR1>(hb ? ea[2 + hd] : eb[hd])) * 0.0625f;
    const float wm0 = mx32(mx16(row_max(sc[0]))), wm1 = mx32(mx16(row_max(sc[1])));
    const u32 tag16 = SET;
    CBAR();
    if (lane < 2) L[L_WM + kw * 2 + lane] = (asu(lane ? wm1 : wm0) & 0xffff0000u) | tag16;
    u32x4 ma, mb;
    for (u32 s = 0; s < SPIN_LDS; ++s) {
      CBAR();
      ma = *(LAS u32x4*)(L + L_WM) ^ tag16; mb = *(LAS u32x4*)(L + L_WM + 4) ^ tag16;
      if (none((((ma.x | ma.y | ma.z | ma.w) | (mb.x | mb.y | mb.z | mb.w)) & 0xffffu) != 0u)) break;
    }
    const float M0 = fmaxf(fmaxf(asf(ma.x), asf(ma.z)), fmaxf(asf(mb.x), asf(mb.z)));
    const float M1 = fmaxf(fmaxf(asf(ma.y), asf(ma.w)), fmaxf(asf(mb.y), asf(mb.w)));
    const float p0 = rbf(__builtin_amdgcn_exp2f((sc[0] - M0) * 1.4426950408889634f));
    const float p1 = rbf(__builtin_amdgcn_exp2f((sc[1] - M1) * 1.4426950408889634f));
    LAS u32* pp = L + L_P + key * 64 + kw + 4 * (2 * g + (lane & 1));
    pp[0] = asu(p0) | (asu(p0) >> 16);
    pp[32] = asu(p1) | (asu(p1) >> 16);
    float ls = tr32(p0, p1);
    ls = row_sum(tr16(ls, ls));
    if ((lane & 31) == 0) LF[L_WL + kw * 2 + (lane >> 5)] = ls;
    flag_set(L + F_P + kw);
    PAD(5);
    MK(2);

    if (kw == 0) {
      flag_wait<4, 0>(L + F_P, lane);
      if (lane < 2) {
        const float Ls = (LF[L_WL + lane] + LF[L_WL + 2 + lane]) + (LF[L_WL + 4 + lane] + LF[L_WL + 6 + lane]);
        st128(rws, ST_OFF + (((h * 2 + lane) * 2 + b) * 16 + sp) * 16, u32x4{asu(lane ? M1 : M0), tag, asu(Ls), tag});
      }
    } else if (kw == 1) {
      __builtin_amdgcn_s_setprio(3);
      PAD(7);
      const int s = lane >> 2, qd = lane & 3;
      const int so = ST_OFF + ((ho * 2 + (j >> 4)) * 32 + s) * 16;
      const int oo = O1_OFF + (ho * 32 + 2 * s) * O1_S + j * 64 + qd * 16;
      u32x4 s0, s1, a0, a1;
      flag_wait<4, 1>(L + F_VI, lane);
      for (u32 it = 0; it < SPIN_MEM; ++it) {
        CBAR();
        s0 = ld128<AUX_SC1>(rws, so); s1 = ld128<AUX_SC1>(rws, so + 256);
        if (none((s0.y != tag) | (s0.w != tag) | (s1.y != tag) | (s1.w != tag))) break;
        flag_wait<1, 1>(L + F_PUB, lane);
      }
      float wgt[2];
#pragma unroll
      for (int bb = 0; bb < 2; ++bb) {
        const float m = asf(bb ? s1.x : s0.x), l = asf(bb ? s1.z : s0.z);
        float mg = fmaxf(m, dpp<ROR8>(m)); mg = fmaxf(mg, dpp<ROR4>(mg)); mg = mx32(mx16(mg));
        const float e = __builtin_amdgcn_exp2f((m - mg) * 1.4426950408889634f);
        float lg = e * l; lg += dpp<ROR8>(lg); lg += dpp<ROR4>(lg); lg = tr16(lg, lg); lg = tr32(lg, lg);
        wgt[bb] = e * __builtin_amdgcn_rcpf(lg);
      }
      LAS u32* ow = L + ((lane & 12) == 0 ? L_O + qd * 4 + (lane >> 4) : L_XN + lane);
      LAS u32* of = L + F_O;
      u32 setv = SET;
      PIN(wgt[0]); PIN(wgt[1]); PIN(ow); PIN(of); PIN(setv);
      if (GO) org += (u64)(long long)(int)__builtin_amdgcn_readfirstlane(L[L_SH]);
      PAD(8);
      flag_wait<1, 0>(L + F_PUB, lane);
      if (D1) __builtin_amdgcn_s_sleep(D1);
      if (TP1) pace<T_LAST + TP1>(org);
      poll2<SLP1>(rws, oo, O1_S, tag8, a0, a1);
      NV(2);
      f32x4 m0 = __builtin_bit_cast(f32x4, a0) * wgt[0], m1 = __builtin_bit_cast(f32x4, a1) * wgt[1];
      PIN(m0); PIN(m1);
      const float v0 = tr32(m0.x, m1.x), v2 = tr32(m0.z, m1.z), v1 = tr32(m0.y, m1.y), v3 = tr32(m0.w, m1.w);
      float e0 = tr16(v0, v2), e1 = tr16(v1, v3);
      e0 += dpp<ROR8>(e0); e1 += dpp<ROR8>(e1);
      e0 += dpp<ROR4>(e0); e1 += dpp<ROR4>(e1);
      *ow = pkbf(e0, e1);
      flag_set(of, setv);
      MK(4); NW(2);
    }
    if (GO != 0 && TP2 != 0 && kw != 1) { flag_wait<1, 4>(L + F_SH, lane); org += (u64)(long long)(int)__builtin_amdgcn_readfirstlane(L[L_SH]); }
    if (PRE && PRE < 3 && kw >= 2) {
      const int po = kw == 2 ? O1_OFF + (h * 32 + j) * O1_S : O2_OFF + (h * 32 + j) * O2_S;
      const u32 t0 = ld32<AUX_SC1>(rws, lane * PSTR < (kw == 2 ? 2048 : PRE == 2 ? 0 : 3584) ? po + lane * PSTR : OOB);
      asm volatile("" :: "v"(t0));
    }
    if (CT2 && kw == 3) {
      if (CT2 == 2) flag_wait<1, 4>(L + F_VI, lane);
      if (CT2 == 4) pace<T_LAST - 150>(org);
      u32 t0;
      if (CT2 >= 3) { const rsrc_t rc = mkrs((const char*)(pc0 & ~255ull), CODE_SPAN); t0 = ld32<0>(rc, lane * 128) | ld32<0>(rc, 8192 + lane * 128); }
      else t0 = ld32<0>(mkrs((const char*)(pc0 & ~255ull), CODE_SPAN), lane < 5 ? j * 640 + lane * 128 : OOB);
      asm volatile("" :: "v"(t0));
    }
    if (PRE >= 3 && kw >= 2) {
      u32 t0 = 0u;
      if (kw == 2) t0 = ld32<PREAUX>(rws, lane < 32 ? O1_OFF + (h * 32 + lane) * O1_S + j * 64 : OOB);
      else if (PRE == 4 && wg < 56) {
        const int po = O2_OFF + lane * O2_S + (wg / 28) * 1792 + (wg % 28) * 64;
#pragma unroll
        for (int i = 0; i < 4; ++i) t0 |= __builtin_amdgcn_raw_buffer_load_b32(rws, po, i * 64 * O2_S, PREAUX);
      }
      asm volatile("" :: "v"(t0));
    }
    tag_out = tag;
  } else {
    if (kw == 0 && lane < (L_END - L_WM) / 4) *(LAS u32x4*)(L + L_WM + 4 * lane) = u32x4{0u, 0u, 0u, 0u};
    LGKM0();
    __builtin_amdgcn_s_barrier();
    org = __builtin_amdgcn_s_memrealtime();
    __builtin_amdgcn_s_setprio(1);
    __builtin_amdgcn_sched_barrier(0);
    ROLE_ENTRY();
    if (GO) { flag_wait<1, 4>(L + F_SH, lane); org += (u64)(long long)(int)__builtin_amdgcn_readfirstlane(L[L_SH]); }
    STREAM_LOADS(vc, lane * 16 + kw * 1024, ((blk0 + BLK((u32)i)) & 511u) << 12, false);
    MV(4);
    flag_set(L + F_VI + kw);
    PAD(1);
    MK0();
    const int m = lane & 31, hf = lane >> 5;
    u32x4 msk;
#pragma unroll
    for (int r = 0; r < 4; ++r) msk[r] = (m < 16 && ((m & 7) >> 1) == r) ? ((m & 1) ? 0xffff0000u : 0x0000ffffu) : 0u;
    LAS f32x4* op = (LAS f32x4*)(LF + L_OP) + kw * 128 + 2 * m + hf;
    LAS f32x4* rp = (LAS f32x4*)(LF + L_OP) + lane;
    LAS u32* fp = L + F_V + kw;
    LAS u32* fr = L + F_V + (lane & 3);
    int o1o = O1_OFF + (h * 32 + j) * O1_S + lane * 16;
    PIN(op); PIN(rp); PIN(fp); PIN(fr); PIN(o1o);
    flag_wait<4, 1>(L + F_P, lane);
    ROLE_TAG();
    const LAS u32x4* pp = (const LAS u32x4*)(L + L_P + ((kw * 2 + hf) * 2 + ((m >> 3) & 1)) * 32);
    f32x16 acc = {};
#pragma unroll
    for (int g = 0; g < 8; ++g) {
      const u32x4 p4 = pp[g];
#pragma unroll
      for (int k = 0; k < 4; ++k) {
        const u32x4 a = msk & p4[k];
        acc = __builtin_amdgcn_mfma_f32_32x32x16_bf16(__builtin_bit_cast(bf16x8_t, a), __builtin_bit_cast(bf16x8_t, d[4 * g + k]), acc, 0, 0, 0);
      }
      __builtin_amdgcn_sched_barrier(0);
    }
    asm volatile("" :: "v"(code_touch));
    PAD(2);
    MV(6); NV(1);
    op[0] = f32x4{acc[0], acc[1], acc[2], acc[3]};
    op[64] = f32x4{acc[4], acc[5], acc[6], acc[7]};
    flag_set(fp);
    const u32 fl = *fr;
    CBAR();
    const f32x4 o0 = (rp[0] + rp[128]) + (rp[256] + rp[384]);
    const f32x4 o1 = (rp[64] + rp[192]) + (rp[320] + rp[448]);
    u32x4 t0 = tag4(o0.x, o0.y, o0.z, o0.w, tag8), t1 = tag4(o1.x, o1.y, o1.z, o1.w, tag8);
    PIN(t0); PIN(t1);
    if (none(fl != SET)) {
      st128(rws, o1o, t0);
      st128(rws, o1o + 1024, t1);
      flag_set(L + F_PUB);
      MK(3);
    }
    MW(4); MW(6); NW(1);
    tag_out = tag;
  }
  if (wg < 56 && (wg % 28) < ((ABL & 64) ? 16 : (ABL & 128) ? 8 : 28)) final_reduce(L, mkrs(ws, WS_BYTES), x, out, ws, wg, w, w - 4, lane, tag_out, org, tent);
  return;
  }

  __builtin_amdgcn_s_setprio(3);
  ROLE_ENTRY();
  const bool mlp = wg < 224;
  const int cb2 = wg % 28, ke = wg / 28;
  const int rr = lane >> 3, c8 = lane & 7;
  const rsrc_t rx = mkrs((const u16*)x, 2 * D * 2);
  const u32x4 xa = ld128<0>(rx, lane < 56 ? lane * 16 : OOB), xb = ld128<0>(rx, lane < 56 ? D * 2 + lane * 16 : OOB);
  const u32 epoch = ld32<AUX_SC1>(rws, EP_OFF);
  const int row0 = kw < 2 ? 112 * (j >> 3) + 64 * kw : 56 * ke;
  const int nrow = kw == 0 ? 64 : kw == 1 ? 48 : mlp ? 56 : 0;
  u32x4 wv[8];
  {
    const rsrc_t rw = mkrs(kw < 2 ? wq : kw == 2 ? wg_ : wu, kw < 2 ? ((ABL & 32) ? 0 : D * 4096 * 2) : ((ABL & 8) ? 0 : D * DF * 2));
    const int rs = kw < 2 ? 8192 : DF * 2;
    const int vw0 = nrow ? (row0 + 2 * rr) * rs + (kw < 2 ? (hq * 512 + (j & 7) * 64) * 2 : cb2 * 128) + c8 * 16 : OOB;
    const int vw3 = 48 + 2 * rr < nrow ? vw0 : OOB;
#pragma unroll
    for (int i = 0; i < 8; ++i) wv[i] = ld128<AUX_NT>(rw, i < 6 ? vw0 : vw3, (16 * (i >> 1) + (i & 1)) * rs);
  }
  const int eo = lane < 8 ? QP_OFF + (hq * 32 + j) * QP_S + lane * 64 : lane < 24 ? GP_OFF + wg * GP_S + (lane - 8) * 64 : ST_OFF + (wg & 127) * 64;
  const u32 box_touch = ld32<AUX_SC1>(rws, (!(ABL & 16) && kw == 1 && lane < 25) ? eo : OOB);
  __builtin_amdgcn_sched_barrier(0);
  __builtin_amdgcn_s_barrier();
  org = __builtin_amdgcn_s_memrealtime();
  __builtin_amdgcn_sched_barrier(0);
  MK0();
  MK(9);
  u32 page_touch = 0u;
  if (PT && kw == 3) {
    const char* pb = lane < 4 ? (const char*)wo + (size_t)(ho * 512 + j * 16) * (D * 2) : lane < 6 ? (const char*)wd + (size_t)(mlp ? 64 * cb2 + 8 * ke : 0) * (D * 2)
                   : lane == 6 ? (const char*)vc + (((size_t)reg * SK + (SK - W)) * DH * 2 + ((blk0 & 511u) << 12)) : lane == 7 ? (const char*)out : ws + O2_OFF + (h * 32 + j) * O2_S;
    const int po = lane < 4 ? (lane == 3 ? 14332 : lane * 4096) : lane == 5 ? 7164 : 0;
    if (lane < 9) page_touch = __builtin_nontemporal_load((const u32*)(pb + po));
  }
  __builtin_amdgcn_sched_barrier(0);

#pragma unroll
  for (int bb = 0; bb < 2; ++bb) {
    const u32x4 xv = bb ? xb : xa;
    float v[8];
#pragma unroll
    for (int k = 0; k < 4; ++k) { v[2 * k] = lo16(xv[k]); v[2 * k + 1] = hi16(xv[k]); }
    float s = 0.f;
#pragma unroll
    for (int k = 0; k < 8; ++k) s += v[k];
    const float mean = wave_sum(s) * (1.f / D);
    float q = 0.f;
#pragma unroll
    for (int k = 0; k < 8; ++k) { const float t = v[k] - mean; q += t * t; }
    q = lane < 56 ? q : 0.f;
    const float var = rbf(wave_sum(q) * (1.f / D));
    const float den = rbf(__builtin_amdgcn_sqrtf(rbf(var + 1e-5f))), rden = __builtin_amdgcn_rcpf(den), mb = rbf(mean);
#pragma unroll
    for (int k = 0; k < 8; ++k) { const float c = rbf(v[k] - mb), r = c * rden; v[k] = fmaf(fmaf(-r, den, c), rden, r); }
    u32x4 o;
#pragma unroll
    for (int k = 0; k < 4; ++k) o[k] = pkbf(v[2 * k], v[2 * k + 1]);
    if (lane < 56) *(LAS u32x4*)(L + L_XN + bb * 224 + lane * 4) = o;
    else if (bb == 1) *(LAS u32x4*)(L + L_XN + 224 + lane * 4) = u32x4{0u, 0u, 0u, 0u};
  }
  asm volatile("" :: "v"(code_touch));
  const u32 tag = __builtin_amdgcn_readfirstlane(epoch) + 1u, tag8 = tag & TMASK;
  if (kw == 0) L[L_TAG] = tag;
  if (GO && kw == 3 && wg < 8 && lane == 0) __builtin_amdgcn_raw_buffer_store_b64(u32x2{(u32)org, tag}, rws, OR_OFF + wg * 8, 0, AUX_SC1);
  MK(10);

  {
    float a0[8] = {}, a1[8] = {};
#pragma unroll
    for (int k = 0; k < 4; ++k) {
      const u32 x0 = L[L_XN + row0 / 2 + rr + 8 * k], x1 = L[L_XN + 224 + row0 / 2 + rr + 8 * k];
#pragma unroll
      for (int c = 0; c < 4; ++c) {
        const u32 pl = __builtin_amdgcn_perm(wv[2 * k + 1][c], wv[2 * k][c], 0x05040100u), ph = __builtin_amdgcn_perm(wv[2 * k + 1][c], wv[2 * k][c], 0x07060302u);
        a0[2 * c] = dot2(pl, x0, a0[2 * c]); a0[2 * c + 1] = dot2(ph, x0, a0[2 * c + 1]);
        a1[2 * c] = dot2(pl, x1, a1[2 * c]); a1[2 * c + 1] = dot2(ph, x1, a1[2 * c + 1]);
      }
    }
    asm volatile("" :: "v"(box_touch));
    MV(5);
    float t[8], u[4];
#pragma unroll
    for (int k = 0; k < 8; ++k) t[k] = tr32(a0[k], a1[k]);
#pragma unroll
    for (int k = 0; k < 4; ++k) { u[k] = tr16(t[k], t[k + 4]); u[k] += dpp<ROR8>(u[k]); }
    const int bb = lane >> 5, hf = (lane >> 4) & 1;
    LAS f32x4* qx = (LAS f32x4*)(LF + L_QX) + bb * 16 + c8 * 2 + hf;
    if (kw == 1) {
      if (!(lane & 8)) *qx = f32x4{u[0], u[1], u[2], u[3]};
      flag_set(L + F_QX);
    } else if (kw == 0) {
      flag_wait<1, 0>(L + F_QX, lane);
      const f32x4 o = *qx;
      if (!(ABL & 1) && !(lane & 8)) st128(rws, QP_OFF + (hq * 32 + j) * QP_S + bb * 256 + hf * 128 + c8 * 16, tag4(u[0] + o.x, u[1] + o.y, u[2] + o.z, u[3] + o.w, tag8));
    } else if (!(ABL & 2) && mlp) {
      LAS u32x4* gx = (LAS u32x4*)(L + L_GX) + (kw - 2) * 32;
      if (!(lane & 8)) gx[c8 * 4 + bb * 2 + hf] = tag4(u[0], u[1], u[2], u[3], tag8);
      CBAR();
      if (lane < 32) st128(rws, GP_OFF + wg * GP_S + (lane >> 2) * 128 + (kw - 2) * 64 + (lane & 3) * 16, gx[lane]);
    }
  }

  if (kw < 2) {
    __builtin_amdgcn_s_sleep(QDELAY);
    if (TQ) pace<TQ>(org);
    const int qo = QP_OFF + (h * 32 + 4 * kw + 8 * (lane >> 4)) * QP_S + b * 256 + (lane & 15) * 16;
    u32x4 a[4];
    for (u32 s = 0; s < SPIN_MEM; ++s) {
      CBAR();
      if (ABL & 1) { a[0] = a[1] = a[2] = a[3] = xa; break; }
#pragma unroll
      for (int i = 0; i < 4; ++i) a[i] = ld128<AUX_SC1>(rws, qo, i * QP_S);
      if (all_ok((untag4(a[0], tag8) | untag4(a[1], tag8)) | (untag4(a[2], tag8) | untag4(a[3], tag8)))) break;
    }
#pragma unroll
    for (int i = 0; i < 2; ++i) {
      const u32x4 a0 = a[2 * i], a1 = a[2 * i + 1];
      const float v0 = tr32(asf(a0.x), asf(a1.x)), v1 = tr32(asf(a0.y), asf(a1.y)), v2 = tr32(asf(a0.z), asf(a1.z)), v3 = tr32(asf(a0.w), asf(a1.w));
      L[L_Q + (4 * kw + 2 * i + (lane >> 5)) * 32 + 4 * (lane & 7) + 2 * ((lane >> 3) & 1) + ((lane >> 4) & 1)] = pkbf(tr16(v0, v2), tr16(v1, v3));
    }
    flag_set(L + F_Q + kw);
    MK(1);
  } else if (kw == 2) {
    u32 h0 = 0u, h1 = 0u;
    if (mlp) {
      const int go = GP_OFF + (cb2 + 28 * rr) * GP_S + ke * 128 + c8 * 16;
      u32x4 a;
      flag_wait<2, 1>(L + F_Q, lane);
      if (TG) pace<TG>(org);
      for (u32 s = 0; s < SPIN_MEM; ++s) {
        CBAR();
        if (ABL & 2) { a = xa; break; }
        a = ld128<AUX_SC1>(rws, go);
        if (all_ok(untag4(a, tag8))) break;
        __builtin_amdgcn_s_sleep(16);
      }
      float hv[4];
#pragma unroll
      for (int k = 0; k < 4; ++k) {
        float g = asf(a[k]);
        g += dpp<ROR8>(g); g = tr16(g, g); g = tr32(g, g);
        g = rbf(g);
        const float uu = dpp<ROR4>(g);
        const float z = 0.7978845608028654f * (g + 0.044715f * g * g * g);
        const float th = 1.f - 2.f * __builtin_amdgcn_rcpf(1.f + __builtin_amdgcn_exp2f(z * 2.8853900817779268f));
        hv[k] = rbf(0.5f * g * (1.f + th)) * uu;
      }
      h0 = pkbf(hv[0], hv[1]); h1 = pkbf(hv[2], hv[3]);
    }
    if (lane < 4) {
      *(LAS u32x2*)(L + L_H + (2 * (lane & 1)) * 4 + (lane >> 1) * 2) = u32x2{h0, SET};
      *(LAS u32x2*)(L + L_H + (2 * (lane & 1) + 1) * 4 + (lane >> 1) * 2) = u32x2{h1, SET};
    }
    CBAR();
  } else if (GO) {
    pace<300>(org);
    const u32x2 o = __builtin_amdgcn_raw_buffer_load_b64(rws, OR_OFF + (lane & 7) * 8, 0, AUX_SC1);
    const bool ok = o.y == tag;
    const float n = row_sum(ok ? 1.f : 0.f), dsum = row_sum(ok ? (float)(int)(o.x - (u32)org) : 0.f);
    const float sh = n > 0.f ? fminf(fmaxf(dsum * __builtin_amdgcn_rcpf(n) + GB, -80.f), 80.f) : 0.f;
    L[L_SH] = (u32)(int)sh;
    flag_set(L + F_SH);
  }

  asm volatile("" :: "v"(page_touch));
  const int rg = lane >> 4, cl = lane & 15;
  const int co = kw * 128 + cl * 8;
  u32x4 wa[4], wb[2];
  if (LATE == 0) flag_wait<2, 1>(L + F_Q, lane);
  if (LATE >= 1) flag_wait<4, 1>(L + F_VI, lane);
  if (LATE == 3) flag_wait<1, 0>(L + F_PUB, lane);
  if (GO) org += (u64)(long long)(int)__builtin_amdgcn_readfirstlane(L[L_SH]);
  if (LATE == 4) pace<PA + PV + (8 * PD + 8 * PDV) / 10 + LD>(org + kw * STG);
  if (LATE == 5) __builtin_amdgcn_s_sleep(LD);
  {
    const rsrc_t ro = mkrs(wo, (ABL & 4) ? 0 : 4096 * D * 2), rd = mkrs(wd, mlp && !(ABL & 4) ? DF * D * 2 : 0);
    const int vo = co < D ? (ho * 512 + j * 16 + 4 * rg) * (D * 2) + co * 2 : OOB;
    const int vd = co < D ? (64 * cb2 + 8 * ke + 2 * rg) * (D * 2) + co * 2 : OOB;
#pragma unroll
    for (int i = 0; i < 2; ++i) wb[i] = ld128<AUX_NT>(rd, vd, i * D * 2);
#pragma unroll
    for (int i = 0; i < 4; ++i) wa[i] = ld128<AUX_NT>(ro, vo, i * D * 2);
  }
  __builtin_amdgcn_sched_barrier(0);
  float a0[8], a1[8];
  u32 pa[8], pb[8];
  {
    u32x4 hh;
    for (u32 s = 0; s < SPIN_LDS; ++s) {
      CBAR();
      hh = *(LAS u32x4*)(L + L_H + rg * 4);
      if (none((hh.y != SET) | (hh.w != SET))) break;
      __builtin_amdgcn_s_sleep(1);
    }
#pragma unroll
    for (int k = 0; k < 4; ++k) {
      const u32 pl = __builtin_amdgcn_perm(wb[1][k], wb[0][k], 0x05040100u), ph = __builtin_amdgcn_perm(wb[1][k], wb[0][k], 0x07060302u);
      a0[2 * k] = dot2(pl, hh.x, 0.f); a0[2 * k + 1] = dot2(ph, hh.x, 0.f);
      a1[2 * k] = dot2(pl, hh.z, 0.f); a1[2 * k + 1] = dot2(ph, hh.z, 0.f);
    }
#pragma unroll
    for (int k = 0; k < 4; ++k) {
      pa[2 * k] = __builtin_amdgcn_perm(wa[1][k], wa[0][k], 0x05040100u); pa[2 * k + 1] = __builtin_amdgcn_perm(wa[1][k], wa[0][k], 0x07060302u);
      pb[2 * k] = __builtin_amdgcn_perm(wa[3][k], wa[2][k], 0x05040100u); pb[2 * k + 1] = __builtin_amdgcn_perm(wa[3][k], wa[2][k], 0x07060302u);
    }
  }
#pragma unroll
  for (int k = 0; k < 8; ++k) asm volatile("" : "+v"(a0[k]), "+v"(a1[k]), "+v"(pa[k]), "+v"(pb[k]));
  int o2o = !((ABL & 256) && j >= 16) && co < ((ABL & 64) ? 256 : (ABL & 128) ? 128 : D) ? O2_OFF + (h * 32 + j) * O2_S + (lane >> 5) * 1792 + kw * 512 + (((lane >> 4) & 1) * (kw == 3 ? 8 : 16) + cl) * 16 : OOB;
  LAS u32x4* orp = (LAS u32x4*)(L + L_O + rg * 4);
  LAS u32* ofp = L + F_O;
  PIN(o2o); PIN(orp); PIN(ofp);
  if (LATE == 0) flag_wait<4, 1>(L + F_VI, lane);
  {
    const u32x4 oo = data_wait(ofp, orp);
#pragma unroll
    for (int k = 0; k < 8; ++k) {
      a0[k] = dot2(pb[k], oo.y, dot2(pa[k], oo.x, a0[k]));
      a1[k] = dot2(pb[k], oo.w, dot2(pa[k], oo.z, a1[k]));
    }
    float t[8];
#pragma unroll
    for (int k = 0; k < 8; ++k) t[k] = tr32(a0[k], a1[k]);
    st128(rws, o2o, tag4(tr16(t[0], t[4]), tr16(t[1], t[5]), tr16(t[2], t[6]), tr16(t[3], t[7]), tag8));
  }
  flag_set(L + F_O2 + kw);
  NV(3); NW(3);
  MK(5);
}

extern "C" void kernel_launch(void* const* d_in, const int* in_sizes, int n_in, void* d_out, int out_size, void* d_ws, size_t ws_size,
                              hipStream_t stream) {
  fused_decode<<<256, 768, 0, stream>>>((const u16*)d_in[0], (const u16*)d_in[3], (const u16*)d_in[6], (const u16*)d_in[7], (const u16*)d_in[1],
                                        (char*)d_ws + WS_OFF, (const u16*)d_in[2], (const u16*)d_in[4], (const u16*)d_in[8], (u16*)d_out);
}
```

```cpp
#include <hip/hip_runtime.h>
#include <hip/hip_bf16.h>
#include <stdint.h>

typedef unsigned int u32;
typedef unsigned short u16;
typedef unsigned long long u64;
typedef u32 u32x2 __attribute__((ext_vector_type(2)));
typedef u32 u32x4 __attribute__((ext_vector_type(4)));
typedef float f32x4 __attribute__((ext_vector_type(4)));
typedef float f32x16 __attribute__((ext_vector_type(16)));
typedef __bf16 bf16x2_t __attribute__((ext_vector_type(2)));
typedef __bf16 bf16x8_t __attribute__((ext_vector_type(8)));
#define LAS __attribute__((address_space(3)))
typedef __amdgpu_buffer_rsrc_t rsrc_t;

#define STAMP 0
#define LATE 4
#define QDELAY 40
#define PRE 0
#define PSTR 64
#define ABL 0
#define SLP1 0
#define SLP2 0
#define PSTAG 5
#define DBL 0
#define PA 30
#define PD 620
#define PK0 2
#define D1 10
#define D2 9
#define PV 0
#define LD -45
#define PDV 650
#define STG 0
#define SR 3
#define SR2 0
#define GO 1
#define GB 5
#define TP1 0
#define TP2 0
#define PREAUX 16
#define STAUX 16
#define WIN 6
#define LAUX 16
#define FINE 4
#define FO 25
#define TQ 0
#define TG 0
#define WINV WIN
#define MVM 126
#define HS 0
#define QS 0
#define PADAT 0
#define PADN 2
#define PAD(k) do { if (PADAT == (k)) asm volatile(".fill %c0, 4, 0xBF800000" :: "i"(PADN)); } while (0)
#define CT2 0
#define PERM 0
#define PT 0
#define ROT 37
#define CODE_SPAN 15872
#define CTW 0
#define WS_OFF 0

namespace {
constexpr int D = 448, SK = 32768, W = 4096, DH = 256, DF = 1792;
constexpr int AUX_NT = 2, AUX_SC1 = 16;
constexpr int OOB = 1 << 30;
constexpr u32 TMASK = 0xfu;

constexpr int QP_S = 768, GP_S = 1280, O1_S = 2304, O2_S = 3840;
constexpr int EP_OFF = 0;
constexpr int OR_OFF = 2048;
constexpr int ST_OFF = 4096;
constexpr int QP_OFF = ST_OFF + 8192;
constexpr int GP_OFF = QP_OFF + 256 * QP_S;
constexpr int O1_OFF = GP_OFF + 256 * GP_S;
constexpr int O2_OFF = O1_OFF + 256 * O1_S;
constexpr int WS_BYTES = O2_OFF + 256 * O2_S;
constexpr int TS_OFF = 4 << 20;

constexpr int L_XN = 0;
constexpr int L_QX = L_XN + 480;
constexpr int L_GX = L_QX + 128;
constexpr int L_Q = L_GX + 256;
constexpr int L_P = L_Q + 256;
constexpr int L_OP = L_P + 512;
constexpr int L_WM = L_OP + 2048;
constexpr int L_WL = L_WM + 8;
constexpr int L_H = L_WL + 8;
constexpr int L_O = L_H + 16;
constexpr int F_O = L_O + 16;
constexpr int L_R = L_O + 32;
constexpr int F_P = L_R + 128;
constexpr int F_VI = F_P + 4;
constexpr int F_O2 = F_VI + 4;
constexpr int F_Q = F_O2 + 4;
constexpr int F_QX = F_Q + 2, F_PUB = F_QX + 1;
constexpr int F_V = F_PUB + 1;
constexpr int F_R = F_V + 4;
constexpr int L_TAG = F_R + 8;
constexpr int L_DG = L_TAG + 1;
constexpr int L_SH = L_TAG + 2, F_SH = L_TAG + 3;
constexpr int L_END = L_TAG + 4;
constexpr u32 SET = 1u;
static_assert((L_END - L_WM) % 4 == 0 && (L_END - L_WM) / 4 <= 64, "one 16-byte store per lane clears the flag area");

constexpr int T_LAST = PA + PV + (8 * PD + 7 * PDV) / 10;
constexpr u32 SPIN_LDS = 1u << 21, SPIN_MEM = 1u << 16;

__device__ __forceinline__ float asf(u32 v) { return __builtin_bit_cast(float, v); }
__device__ __forceinline__ u32 asu(float v) { return __builtin_bit_cast(u32, v); }
__device__ __forceinline__ float lo16(u32 v) { return asf(v << 16); }
__device__ __forceinline__ float hi16(u32 v) { return asf(v & 0xffff0000u); }
__device__ __forceinline__ u32 pkbf(float a, float b) {
  bf16x2_t r = {(__bf16)a, (__bf16)b};
  return __builtin_bit_cast(u32, r);
}
__device__ __forceinline__ float rbf(float a) { return (float)(__bf16)a; }
__device__ __forceinline__ float dot2(u32 a, u32 b, float c) {
  return __builtin_amdgcn_fdot2_f32_bf16(__builtin_bit_cast(bf16x2_t, a), __builtin_bit_cast(bf16x2_t, b), c, false);
}
template <int CTRL> __device__ __forceinline__ float dpp(float x) {
  return asf(__builtin_amdgcn_mov_dpp((int)asu(x), CTRL, 0xf, 0xf, true));
}
constexpr int XOR1 = 0xB1, XOR2 = 0x4E, ROR8 = 0x128, ROR4 = 0x124;
__device__ __forceinline__ float tr32(float a, float b) {
  auto r = __builtin_amdgcn_permlane32_swap(asu(a), asu(b), false, false);
  return asf(r[0]) + asf(r[1]);
}
__device__ __forceinline__ float tr16(float a, float b) {
  auto r = __builtin_amdgcn_permlane16_swap(asu(a), asu(b), false, false);
  return asf(r[0]) + asf(r[1]);
}
__device__ __forceinline__ float mx32(float a) {
  auto r = __builtin_amdgcn_permlane32_swap(asu(a), asu(a), false, false);
  return fmaxf(asf(r[0]), asf(r[1]));
}
__device__ __forceinline__ float mx16(float a) {
  auto r = __builtin_amdgcn_permlane16_swap(asu(a), asu(a), false, false);
  return fmaxf(asf(r[0]), asf(r[1]));
}
__device__ __forceinline__ float row_sum(float v) { v += dpp<ROR8>(v); v += dpp<ROR4>(v); v += dpp<XOR2>(v); v += dpp<XOR1>(v); return v; }
__device__ __forceinline__ float row_max(float v) {
  v = fmaxf(v, dpp<ROR8>(v)); v = fmaxf(v, dpp<ROR4>(v)); v = fmaxf(v, dpp<XOR2>(v)); v = fmaxf(v, dpp<XOR1>(v)); return v;
}
__device__ __forceinline__ float wave_sum(float v) { v = row_sum(v); v = tr16(v, v); return tr32(v, v); }

__device__ __forceinline__ rsrc_t mkrs(const void* p, u32 bytes) { return __builtin_amdgcn_make_buffer_rsrc((void*)p, 0, bytes, 0x00020000); }
template <int AUX> __device__ __forceinline__ u32x4 ld128(rsrc_t r, int voff, int soff = 0) { return __builtin_amdgcn_raw_buffer_load_b128(r, voff, soff, AUX); }
template <int AUX> __device__ __forceinline__ u32 ld32(rsrc_t r, int voff) { return __builtin_amdgcn_raw_buffer_load_b32(r, voff, 0, AUX); }
__device__ __forceinline__ void st128(rsrc_t r, int voff, u32x4 v) { __builtin_amdgcn_raw_buffer_store_b128(v, r, voff, 0, STAUX); }
__device__ __forceinline__ u32 tagw(float v, u32 tag4) { return (asu(v) & ~TMASK) | tag4; }
__device__ __forceinline__ u32x4 tag4(float a, float b, float c, float d, u32 t) { return u32x4{tagw(a, t), tagw(b, t), tagw(c, t), tagw(d, t)}; }
__device__ __forceinline__ u32 untag4(u32x4& v, u32 t) { v.x ^= t; v.y ^= t; v.z ^= t; v.w ^= t; return v.x | v.y | v.z | v.w; }
__device__ __forceinline__ bool none(bool bad) { return __builtin_amdgcn_ballot_w64(bad) == 0ull; }
__device__ __forceinline__ bool all_ok(u32 bad) { return none((bad & TMASK) != 0u); }

#define CBAR() asm volatile("" ::: "memory")
#define PIN(v) asm volatile("" : "+v"(v))
#define LGKM0() asm volatile("s_waitcnt lgkmcnt(0)" ::: "memory")
template <int N, int SLEEP> __device__ __forceinline__ void flag_wait(LAS u32* p, int lane) {
  u32 v, cnt;
  const u32 addr = (u32)(uintptr_t)(p + (lane & (N - 1)));
  asm volatile("s_mov_b32 %1, %4\n"
               "1:\n\t"
               "ds_read_b32 %0, %2\n\t"
               "s_waitcnt lgkmcnt(0)\n\t"
               "v_cmp_ne_u32 vcc, %3, %0\n\t"
               "s_cbranch_vccz 2f\n\t"
               "s_sleep %5\n\t"
               "s_sub_u32 %1, %1, 1\n\t"
               "s_cbranch_scc0 1b\n"
               "2:"
               : "=&v"(v), "=&s"(cnt) : "v"(addr), "i"(SET), "i"(SPIN_LDS), "i"(SLEEP) : "vcc", "scc", "memory");
}
template <int DUE> __device__ __forceinline__ void pace(u64 org) {
  asm volatile("1:\n\t"
               "s_memrealtime vcc\n\t"
               "s_waitcnt lgkmcnt(0)\n\t"
               "s_sub_u32 vcc_lo, vcc_lo, %0\n\t"
               "s_cmp_lt_i32 vcc_lo, %1\n\t"
               "s_cbranch_scc0 2f\n\t"
               "s_sleep 1\n\t"
               "s_branch 1b\n"
               "2:"
               :: "s"((u32)org), "i"(DUE), "s"(org) : "vcc", "scc", "memory");
}
__device__ __forceinline__ void flag_set(LAS u32* p, u32 set = SET) { CBAR(); *p = set; CBAR(); }

template <int SLP> __device__ __forceinline__ void poll2(rsrc_t rs, int voff, int soff, u32 t4, u32x4& r0, u32x4& r1) {
  u32 o0, o1, o2, o3, o4, o5, o6, o7, m, cnt;
  asm volatile("s_mov_b32 %9, %14\n"
               "1:\n\t"
               "buffer_load_dwordx4 v[144:147], %10, %11, 0 offen sc1\n\t"
               "buffer_load_dwordx4 v[148:151], %10, %11, %12 offen sc1\n\t"
               "s_sub_u32 %9, %9, 1\n\t"
               "s_waitcnt vmcnt(1)\n\t"
               "v_xor_b32 %0, %13, v144\n\tv_xor_b32 %1, %13, v145\n\tv_xor_b32 %2, %13, v146\n\tv_xor_b32 %3, %13, v147\n\t"
               "v_or3_b32 %8, %0, %1, %2\n\t"
               "s_waitcnt vmcnt(0)\n\t"
               "v_xor_b32 %4, %13, v148\n\tv_xor_b32 %5, %13, v149\n\tv_xor_b32 %6, %13, v150\n\tv_xor_b32 %7, %13, v151\n\t"
               "v_or3_b32 %8, %8, %3, %4\n\t"
               "v_or3_b32 %8, %8, %5, %6\n\t"
               "v_bitop3_b32 %8, %8, 15, %7 bitop3:0xc8\n\t"
               "v_cmp_ne_u32 vcc, 0, %8\n\t"
               "s_cbranch_vccz 2f\n\t"
               "s_sleep %15\n\t"
               "s_cbranch_scc0 1b\n"
               "2:"
               : "=&v"(o0), "=&v"(o1), "=&v"(o2), "=&v"(o3), "=&v"(o4), "=&v"(o5), "=&v"(o6), "=&v"(o7), "=&v"(m), "=&s"(cnt)
               : "v"(voff), "s"(rs), "s"(soff), "s"(t4), "i"(SPIN_MEM), "i"(SLP)
               : "vcc", "scc", "memory", "v144", "v145", "v146", "v147", "v148", "v149", "v150", "v151");
  r0 = u32x4{o0, o1, o2, o3};
  r1 = u32x4{o4, o5, o6, o7};
}
__device__ __forceinline__ u32x4 data_wait(LAS u32* flag, LAS u32x4* p) {
  u32x4 r;
  u32 v, cnt;
  asm volatile("s_mov_b32 %2, %5\n"
               "1:\n\t"
               "ds_read_b32 %1, %3\n\t"
               "ds_read_b128 %0, %4\n\t"
               "s_sub_u32 %2, %2, 1\n\t"
               "s_waitcnt lgkmcnt(0)\n\t"
               "v_cmp_ne_u32 vcc, 1, %1\n\t"
               "s_cbranch_vccz 2f\n\t"
               "s_cbranch_scc0 1b\n"
               "2:"
               : "=&v"(r), "=&v"(v), "=&s"(cnt) : "v"((u32)(uintptr_t)flag), "v"((u32)(uintptr_t)p), "i"(SPIN_LDS) : "vcc", "scc", "memory");
  return r;
}

#define MK(k)
#define MV(k)
#define MW(k)
#define NV(k)
#define NW(k)
#define NC(k, c)
#define MK0()

__device__ __forceinline__ void final_reduce(LAS u32* L, rsrc_t rws, const u16* x, u16* out, char* ws, int wg, int w, int grp, int lane, u32 tag, u64 org) {
  LAS float* const LF = (LAS float*)L;
  __builtin_amdgcn_s_setprio(3);
  PAD(6);
  const u32 tag8 = tag & TMASK;
  const int rb = wg / 28, cb = wg % 28, qd = lane & 3;
  const int pi = cb & 7, ph = cb < 24 ? pi >> 2 : pi >> 1, pg = cb < 24 ? pi & 3 : pi & 1;
  const int col = rb * D + 128 * (cb >> 3) + 8 * (4 * pg + ((lane >> 2) & 3)) + 4 * ph + (lane & 3);
  float xres = lo16(x[col]);
  int oo = O2_OFF + (grp * 32 + (lane >> 2)) * O2_S + rb * 1792 + cb * 64 + qd * 16;
  LAS float* wp = LF + ((lane & 12) == 0 ? L_R + (4 * qd + (lane >> 4)) * 8 + grp : L_XN + 128 + lane);
  LAS u32* fp = L + F_R + grp;
  LAS u32* fr = L + F_R + (lane & 7);
  LAS f32x4* rp = (LAS f32x4*)(LF + L_R + (lane & 15) * 8);
  int so = lane < 16 ? col * 2 : OOB;
  u32 tagv = SET;
  rsrc_t ro = mkrs(out, 2 * D * 2);
  asm volatile("" : "+s"(ro));
  PIN(xres); PIN(oo); PIN(wp); PIN(fp); PIN(fr); PIN(rp); PIN(so); PIN(tagv);
  flag_wait<1, 1>(L + F_PUB, lane);
  flag_wait<4, 0>(L + F_O2, lane);
  if (D2) __builtin_amdgcn_s_sleep(D2);
  if (TP2) pace<T_LAST + TP2>(org);
  u32x4 a0, a1;
  poll2<SLP2>(rws, oo, 16 * O2_S, tag8, a0, a1);
  MK(6);
  NV(4);
  float f = tr16(tr32(asf(a0.x) + asf(a1.x), asf(a0.z) + asf(a1.z)), tr32(asf(a0.y) + asf(a1.y), asf(a0.w) + asf(a1.w)));
  f += dpp<ROR8>(f); f += dpp<ROR4>(f);
  *wp = f;
  flag_set(fp, tagv);
  u32 fl = *fr;
  CBAR();
  const f32x4 r0 = rp[0], r1 = rp[1];
  const f32x4 rs4 = r0 + r1;
  const float tot = xres + ((rs4.x + rs4.z) + (rs4.y + rs4.w));
  u32 res = pkbf(tot, tot);
  PIN(res);
  if (none(fl != SET)) {
    __builtin_amdgcn_raw_buffer_store_b16((u16)res, ro, so, 0, 0);
    if (wg == 0 && lane == 0) __builtin_amdgcn_raw_buffer_store_b32(tag, rws, EP_OFF, 0, AUX_SC1);
    MK(7);
  }
  NW(4);
}

}

extern "C" __global__ void __launch_bounds__(768, 3)
fused_decode(const u16* __restrict__ x, const u16* __restrict__ wq, const u16* __restrict__ wg_, const u16* __restrict__ wu,
             const u16* __restrict__ kc, char* __restrict__ ws, const u16* __restrict__ vc, const u16* __restrict__ wo,
             const u16* __restrict__ wd, u16* __restrict__ out) {
  __shared__ __attribute__((aligned(16))) u32 smem[L_END];
  LAS u32* const L = (LAS u32*)smem;
  LAS float* const LF = (LAS float*)smem;
  const int wg = blockIdx.x, h = wg & 7, j = wg >> 3, b = j & 1, sp = j >> 1, reg = wg & 15;
  const int w = __builtin_amdgcn_readfirstlane(threadIdx.x >> 6), lane = threadIdx.x & 63, kw = w & 3;
  asm volatile("s_load_dword s94, %0, 0x0\n\ts_load_dword s95, %0, 0x40" :: "s"(__builtin_amdgcn_kernarg_segment_ptr()) : "s94", "s95");
  u64 pc0;
  asm volatile("s_getpc_b64 %0" : "=s"(pc0));
  const u32 code_touch = ld32<0>(mkrs((const char*)(pc0 & ~255ull), w == CTW ? CODE_SPAN : 0), lane < 5 ? j * 640 + lane * 128 : OOB);
  __builtin_amdgcn_sched_barrier(0);
#define ROLE_ENTRY() const rsrc_t rws = mkrs(ws, WS_BYTES)
#define ROLE_TAG() const u32 tag = __builtin_amdgcn_readfirstlane(L[L_TAG]), tag8 = tag & TMASK
  const u32 blk0 = sp * 32 + reg * ROT;
  const u32 srot = sp * SR + reg * SR2;
  const u32 gm = PERM ? 2 * ((sp + (PERM == 2 ? reg : 0)) & 3) + 1 : 1, gc = PERM ? (sp >> 2) + reg : 0;
#define BLK(slot) (PERM ? ((((((slot) >> 2) * gm + gc) & 7u) << 2) + ((slot) & 3u) + srot) & 31u : ((slot) + srot) & 31u)
  const int hq = (h + QS) & 7, ho = (h + HS) & 7;
  u64 org;
#define STREAM_LOADS(SRC, VOFF, SOFF, BAR) \
  const rsrc_t rkv = mkrs((SRC) + ((size_t)reg * SK + (SK - W)) * DH, W * DH * 2); \
  u32x4 d[32]; \
  if (BAR) { __builtin_amdgcn_sched_barrier(0); __builtin_amdgcn_s_barrier(); org = __builtin_amdgcn_s_memrealtime(); __builtin_amdgcn_sched_barrier(0); } \
  MV(2); \
  STREAM_GROUP(0, VOFF, SOFF, BAR) STREAM_GROUP(1, VOFF, SOFF, BAR) STREAM_GROUP(2, VOFF, SOFF, BAR) STREAM_GROUP(3, VOFF, SOFF, BAR) \
  STREAM_GROUP(4, VOFF, SOFF, BAR) STREAM_GROUP(5, VOFF, SOFF, BAR) STREAM_GROUP(6, VOFF, SOFF, BAR) STREAM_GROUP(7, VOFF, SOFF, BAR)
#define STREAM_GROUP(G, VOFF, SOFF, ISK) \
  if (PD > 0 && (!(ISK) || (G) >= PK0)) pace<PA + ((ISK) ? (G) * PD / 10 : PV + (8 * PD + (G) * PDV) / 10 - ((G) >= FINE ? FO : 0))>(org + kw * STG); \
  if (((ISK) ? WIN : WINV) > 0 && 4 * (G) > ((ISK) ? WIN : WINV)) asm volatile("s_waitcnt vmcnt(%0)" :: "i"((ISK) ? WIN : WINV) : "memory"); \
  _Pragma("unroll") for (int i = 4 * (G); i < 4 * (G) + 2; ++i) d[i] = ld128<AUX_NT>(rkv, (VOFF), (int)(SOFF)); \
  if (!(ISK) && (G) >= FINE) { __builtin_amdgcn_sched_barrier(0); pace<PA + PV + (8 * PD + (G) * PDV + PDV / 2) / 10 - FO>(org + kw * STG); } \
  _Pragma("unroll") for (int i = 4 * (G) + 2; i < 4 * (G) + 4; ++i) d[i] = ld128<AUX_NT>(rkv, (VOFF), (int)(SOFF)); \
  __builtin_amdgcn_sched_barrier(0);

  if (__builtin_expect(w >= 4, 0)) {
  u32 tag_out;
  if (w < 8) {
    __builtin_amdgcn_s_setprio(2);
    PAD(3);
    const int g = lane >> 4, key = (lane >> 1) & 7;
    const bool hb = lane & 1;
    const int kvo = key * 512 + (2 * g + (lane & 1)) * 16;
    STREAM_LOADS(kc, kvo + (i & 3) * 128, ((blk0 + BLK(kw + 4 * (i >> 2))) & 511u) << 12, true);
    MV(3);
    ROLE_ENTRY();
    MK0(); MW(2); MW(3);
    PAD(4);
    flag_wait<2, 1>(L + F_Q, lane);
    ROLE_TAG();
    u32x4 qa[4];
#pragma unroll
    for (int i = 0; i < 4; ++i) qa[i] = *(LAS u32x4*)(L + L_Q + (lane & 1) * 128 + i * 32 + (2 * g + ((lane >> 1) & 1)) * 4);
    f32x4 ea, eb;
#pragma unroll
    for (int n = 0; n < 4; ++n) {
      f32x4 ca = {}, cb = {};
#pragma unroll
      for (int i = 0; i < 4; ++i) {
        ca = __builtin_amdgcn_mfma_f32_16x16x32_bf16(__builtin_bit_cast(bf16x8_t, qa[i]), __builtin_bit_cast(bf16x8_t, d[8 * n + i]), ca, 0, 0, 0);
        cb = __builtin_amdgcn_mfma_f32_16x16x32_bf16(__builtin_bit_cast(bf16x8_t, qa[i]), __builtin_bit_cast(bf16x8_t, d[8 * n + 4 + i]), cb, 0, 0, 0);
      }
#pragma unroll
      for (int k = 0; k < 4; ++k) { ea[k] = (n == 0 || g == n) ? ca[k] : ea[k]; eb[k] = (n == 0 || g == n) ? cb[k] : eb[k]; }
    }
    asm volatile("" :: "v"(code_touch));
    float sc[2];
#pragma unroll
    for (int hd = 0; hd < 2; ++hd) sc[hd] = rbf((hb ? eb[2 + hd] : ea[hd]) + dpp<XOR1>(hb ? ea[2 + hd] : eb[hd])) * 0.0625f;
    const float wm0 = mx32(mx16(row_max(sc[0]))), wm1 = mx32(mx16(row_max(sc[1])));
    const u32 tag16 = SET;
    CBAR();
    if (lane < 2) L[L_WM + kw * 2 + lane] = (asu(lane ? wm1 : wm0) & 0xffff0000u) | tag16;
    u32x4 ma, mb;
    for (u32 s = 0; s < SPIN_LDS; ++s) {
      CBAR();
      ma = *(LAS u32x4*)(L + L_WM) ^ tag16; mb = *(LAS u32x4*)(L + L_WM + 4) ^ tag16;
      if (none((((ma.x | ma.y | ma.z | ma.w) | (mb.x | mb.y | mb.z | mb.w)) & 0xffffu) != 0u)) break;
    }
    const float M0 = fmaxf(fmaxf(asf(ma.x), asf(ma.z)), fmaxf(asf(mb.x), asf(mb.z)));
    const float M1 = fmaxf(fmaxf(asf(ma.y), asf(ma.w)), fmaxf(asf(mb.y), asf(mb.w)));
    const float p0 = rbf(__builtin_amdgcn_exp2f((sc[0] - M0) * 1.4426950408889634f));
    const float p1 = rbf(__builtin_amdgcn_exp2f((sc[1] - M1) * 1.4426950408889634f));
    LAS u32* pp = L + L_P + key * 64 + kw + 4 * (2 * g + (lane & 1));
    pp[0] = asu(p0) | (asu(p0) >> 16);
    pp[32] = asu(p1) | (asu(p1) >> 16);
    float ls = tr32(p0, p1);
    ls = row_sum(tr16(ls, ls));
    if ((lane & 31) == 0) LF[L_WL + kw * 2 + (lane >> 5)] = ls;
    flag_set(L + F_P + kw);
    PAD(5);
    MK(2);

    if (kw == 0) {
      flag_wait<4, 0>(L + F_P, lane);
      if (lane < 2) {
        const float Ls = (LF[L_WL + lane] + LF[L_WL + 2 + lane]) + (LF[L_WL + 4 + lane] + LF[L_WL + 6 + lane]);
        st128(rws, ST_OFF + (((h * 2 + lane) * 2 + b) * 16 + sp) * 16, u32x4{asu(lane ? M1 : M0), tag, asu(Ls), tag});
      }
    } else if (kw == 1) {
      __builtin_amdgcn_s_setprio(3);
      PAD(7);
      const int s = lane >> 2, qd = lane & 3;
      const int so = ST_OFF + ((ho * 2 + (j >> 4)) * 32 + s) * 16;
      const int oo = O1_OFF + (ho * 32 + 2 * s) * O1_S + j * 64 + qd * 16;
      u32x4 s0, s1, a0, a1;
      flag_wait<4, 1>(L + F_VI, lane);
      for (u32 it = 0; it < SPIN_MEM; ++it) {
        CBAR();
        s0 = ld128<AUX_SC1>(rws, so); s1 = ld128<AUX_SC1>(rws, so + 256);
        if (none((s0.y != tag) | (s0.w != tag) | (s1.y != tag) | (s1.w != tag))) break;
        flag_wait<1, 1>(L + F_PUB, lane);
      }
      float wgt[2];
#pragma unroll
      for (int bb = 0; bb < 2; ++bb) {
        const float m = asf(bb ? s1.x : s0.x), l = asf(bb ? s1.z : s0.z);
        float mg = fmaxf(m, dpp<ROR8>(m)); mg = fmaxf(mg, dpp<ROR4>(mg)); mg = mx32(mx16(mg));
        const float e = __builtin_amdgcn_exp2f((m - mg) * 1.4426950408889634f);
        float lg = e * l; lg += dpp<ROR8>(lg); lg += dpp<ROR4>(lg); lg = tr16(lg, lg); lg = tr32(lg, lg);
        wgt[bb] = e * __builtin_amdgcn_rcpf(lg);
      }
      LAS u32* ow = L + ((lane & 12) == 0 ? L_O + qd * 4 + (lane >> 4) : L_XN + lane);
      LAS u32* of = L + F_O;
      u32 setv = SET;
      PIN(wgt[0]); PIN(wgt[1]); PIN(ow); PIN(of); PIN(setv);
      if (GO) org += (u64)(long long)(int)__builtin_amdgcn_readfirstlane(L[L_SH]);
      PAD(8);
      flag_wait<1, 0>(L + F_PUB, lane);
      if (D1) __builtin_amdgcn_s_sleep(D1);
      if (TP1) pace<T_LAST + TP1>(org);
      poll2<SLP1>(rws, oo, O1_S, tag8, a0, a1);
      NV(2);
      f32x4 m0 = __builtin_bit_cast(f32x4, a0) * wgt[0], m1 = __builtin_bit_cast(f32x4, a1) * wgt[1];
      PIN(m0); PIN(m1);
      const float v0 = tr32(m0.x, m1.x), v2 = tr32(m0.z, m1.z), v1 = tr32(m0.y, m1.y), v3 = tr32(m0.w, m1.w);
      float e0 = tr16(v0, v2), e1 = tr16(v1, v3);
      e0 += dpp<ROR8>(e0); e1 += dpp<ROR8>(e1);
      e0 += dpp<ROR4>(e0); e1 += dpp<ROR4>(e1);
      *ow = pkbf(e0, e1);
      flag_set(of, setv);
      MK(4); NW(2);
    }
    if (GO != 0 && TP2 != 0 && kw != 1) { flag_wait<1, 4>(L + F_SH, lane); org += (u64)(long long)(int)__builtin_amdgcn_readfirstlane(L[L_SH]); }
    if (PRE && PRE < 3 && kw >= 2) {
      const int po = kw == 2 ? O1_OFF + (h * 32 + j) * O1_S : O2_OFF + (h * 32 + j) * O2_S;
      const u32 t0 = ld32<AUX_SC1>(rws, lane * PSTR < (kw == 2 ? 2048 : PRE == 2 ? 0 : 3584) ? po + lane * PSTR : OOB);
      asm volatile("" :: "v"(t0));
    }
    if ((PT == 2 || PT == 4) && kw == 2 && wg < 56) {
      const u32 t0 = ld32<0>(mkrs(out, 2 * D * 2), lane == 0 ? 0 : OOB);
      asm volatile("" :: "v"(t0));
    }
    if (CT2 && kw == 3) {
      if (CT2 == 2) flag_wait<1, 4>(L + F_VI, lane);
      if (CT2 == 4) pace<T_LAST - 150>(org);
      u32 t0;
      if (CT2 >= 3) { const rsrc_t rc = mkrs((const char*)(pc0 & ~255ull), CODE_SPAN); t0 = ld32<0>(rc, lane * 128) | ld32<0>(rc, 8192 + lane * 128); }
      else t0 = ld32<0>(mkrs((const char*)(pc0 & ~255ull), CODE_SPAN), lane < 5 ? j * 640 + lane * 128 : OOB);
      asm volatile("" :: "v"(t0));
    }
    if (PRE >= 3 && kw >= 2) {
      u32 t0 = 0u;
      if (kw == 2) t0 = ld32<PREAUX>(rws, lane < 32 ? O1_OFF + (h * 32 + lane) * O1_S + j * 64 : OOB);
      else if (PRE == 4 && wg < 56) {
        const int po = O2_OFF + lane * O2_S + (wg / 28) * 1792 + (wg % 28) * 64;
#pragma unroll
        for (int i = 0; i < 4; ++i) t0 |= __builtin_amdgcn_raw_buffer_load_b32(rws, po, i * 64 * O2_S, PREAUX);
      }
      asm volatile("" :: "v"(t0));
    }
    tag_out = tag;
  } else {
    if (kw == 0 && lane < (L_END - L_WM) / 4) *(LAS u32x4*)(L + L_WM + 4 * lane) = u32x4{0u, 0u, 0u, 0u};
    LGKM0();
    __builtin_amdgcn_s_barrier();
    org = __builtin_amdgcn_s_memrealtime();
    __builtin_amdgcn_s_setprio(1);
    __builtin_amdgcn_sched_barrier(0);
    ROLE_ENTRY();
    if (GO) { flag_wait<1, 4>(L + F_SH, lane); org += (u64)(long long)(int)__builtin_amdgcn_readfirstlane(L[L_SH]); }
    STREAM_LOADS(vc, lane * 16 + kw * 1024, ((blk0 + BLK((u32)i)) & 511u) << 12, false);
    MV(4);
    flag_set(L + F_VI + kw);
    PAD(1);
    MK0();
    const int m = lane & 31, hf = lane >> 5;
    u32x4 msk;
#pragma unroll
    for (int r = 0; r < 4; ++r) msk[r] = (m < 16 && ((m & 7) >> 1) == r) ? ((m & 1) ? 0xffff0000u : 0x0000ffffu) : 0u;
    LAS f32x4* op = (LAS f32x4*)(LF + L_OP) + kw * 128 + 2 * m + hf;
    LAS f32x4* rp = (LAS f32x4*)(LF + L_OP) + lane;
    LAS u32* fp = L + F_V + kw;
    LAS u32* fr = L + F_V + (lane & 3);
    int o1o = O1_OFF + (h * 32 + j) * O1_S + lane * 16;
    PIN(op); PIN(rp); PIN(fp); PIN(fr); PIN(o1o);
    flag_wait<4, 1>(L + F_P, lane);
    ROLE_TAG();
    const LAS u32x4* pp = (const LAS u32x4*)(L + L_P + ((kw * 2 + hf) * 2 + ((m >> 3) & 1)) * 32);
    f32x16 acc = {};
#pragma unroll
    for (int g = 0; g < 8; ++g) {
      const u32x4 p4 = pp[g];
#pragma unroll
      for (int k = 0; k < 4; ++k) {
        const u32x4 a = msk & p4[k];
        acc = __builtin_amdgcn_mfma_f32_32x32x16_bf16(__builtin_bit_cast(bf16x8_t, a), __builtin_bit_cast(bf16x8_t, d[4 * g + k]), acc, 0, 0, 0);
      }
      __builtin_amdgcn_sched_barrier(0);
    }
    asm volatile("" :: "v"(code_touch));
    PAD(2);
    MV(6); NV(1);
    op[0] = f32x4{acc[0], acc[1], acc[2], acc[3]};
    op[64] = f32x4{acc[4], acc[5], acc[6], acc[7]};
    flag_set(fp);
    const u32 fl = *fr;
    CBAR();
    const f32x4 o0 = (rp[0] + rp[128]) + (rp[256] + rp[384]);
    const f32x4 o1 = (rp[64] + rp[192]) + (rp[320] + rp[448]);
    u32x4 t0 = tag4(o0.x, o0.y, o0.z, o0.w, tag8), t1 = tag4(o1.x, o1.y, o1.z, o1.w, tag8);
    PIN(t0); PIN(t1);
    if (none(fl != SET)) {
      st128(rws, o1o, t0);
      st128(rws, o1o + 1024, t1);
      flag_set(L + F_PUB);
      MK(3);
    }
    MW(4); MW(6); NW(1);
    tag_out = tag;
  }
  if (wg < 56 && (wg % 28) < ((ABL & 64) ? 16 : (ABL & 128) ? 8 : 28)) final_reduce(L, mkrs(ws, WS_BYTES), x, out, ws, wg, w, w - 4, lane, tag_out, org);
  return;
  }

  __builtin_amdgcn_s_setprio(3);
  ROLE_ENTRY();
  const bool mlp = wg < 224;
  const int cb2 = wg % 28, ke = wg / 28;
  const int rr = lane >> 3, c8 = lane & 7;
  const rsrc_t rx = mkrs((const u16*)x, 2 * D * 2);
  const u32x4 xa = ld128<0>(rx, lane < 56 ? lane * 16 : OOB), xb = ld128<0>(rx, lane < 56 ? D * 2 + lane * 16 : OOB);
  const u32 epoch = ld32<AUX_SC1>(rws, EP_OFF);
  const int row0 = kw < 2 ? 112 * (j >> 3) + 64 * kw : 56 * ke;
  const int nrow = kw == 0 ? 64 : kw == 1 ? 48 : mlp ? 56 : 0;
  u32x4 wv[8];
  {
    const rsrc_t rw = mkrs(kw < 2 ? wq : kw == 2 ? wg_ : wu, kw < 2 ? ((ABL & 32) ? 0 : D * 4096 * 2) : ((ABL & 8) ? 0 : D * DF * 2));
    const int rs = kw < 2 ? 8192 : DF * 2;
    const int vw0 = nrow ? (row0 + 2 * rr) * rs + (kw < 2 ? (hq * 512 + (j & 7) * 64) * 2 : cb2 * 128) + c8 * 16 : OOB;
    const int vw3 = 48 + 2 * rr < nrow ? vw0 : OOB;
#pragma unroll
    for (int i = 0; i < 8; ++i) wv[i] = ld128<AUX_NT>(rw, i < 6 ? vw0 : vw3, (16 * (i >> 1) + (i & 1)) * rs);
  }
  const int eo = lane < 8 ? QP_OFF + (hq * 32 + j) * QP_S + lane * 64 : lane < 24 ? GP_OFF + wg * GP_S + (lane - 8) * 64 : ST_OFF + (wg & 127) * 64;
  const u32 box_touch = ld32<AUX_SC1>(rws, (!(ABL & 16) && kw == 1 && lane < 25) ? eo : OOB);
  __builtin_amdgcn_sched_barrier(0);
  __builtin_amdgcn_s_barrier();
  org = __builtin_amdgcn_s_memrealtime();
  __builtin_amdgcn_sched_barrier(0);
  MK0();
  const u32 page_touch = (PT == 4 || PT == 5) ? ld32<0>(mkrs(out, (kw == 2 && wg >= 248) ? 2 * D * 2 : 0), lane == 0 ? 0 : OOB) : 0u;
  __builtin_amdgcn_sched_barrier(0);

#pragma unroll
  for (int bb = 0; bb < 2; ++bb) {
    const u32x4 xv = bb ? xb : xa;
    float v[8];
#pragma unroll
    for (int k = 0; k < 4; ++k) { v[2 * k] = lo16(xv[k]); v[2 * k + 1] = hi16(xv[k]); }
    float s = 0.f;
#pragma unroll
    for (int k = 0; k < 8; ++k) s += v[k];
    const float mean = wave_sum(s) * (1.f / D);
    float q = 0.f;
#pragma unroll
    for (int k = 0; k < 8; ++k) { const float t = v[k] - mean; q += t * t; }
    q = lane < 56 ? q : 0.f;
    const float var = rbf(wave_sum(q) * (1.f / D));
    const float den = rbf(__builtin_amdgcn_sqrtf(rbf(var + 1e-5f))), rden = __builtin_amdgcn_rcpf(den), mb = rbf(mean);
#pragma unroll
    for (int k = 0; k < 8; ++k) { const float c = rbf(v[k] - mb), r = c * rden; v[k] = fmaf(fmaf(-r, den, c), rden, r); }
    u32x4 o;
#pragma unroll
    for (int k = 0; k < 4; ++k) o[k] = pkbf(v[2 * k], v[2 * k + 1]);
    if (lane < 56) *(LAS u32x4*)(L + L_XN + bb * 224 + lane * 4) = o;
    else if (bb == 1) *(LAS u32x4*)(L + L_XN + 224 + lane * 4) = u32x4{0u, 0u, 0u, 0u};
  }
  asm volatile("" :: "v"(code_touch));
  const u32 tag = __builtin_amdgcn_readfirstlane(epoch) + 1u, tag8 = tag & TMASK;
  if (kw == 0) L[L_TAG] = tag;
  if (GO && kw == 3 && wg < 8 && lane == 0) __builtin_amdgcn_raw_buffer_store_b64(u32x2{(u32)org, tag}, rws, OR_OFF + wg * 8, 0, AUX_SC1);
  MK(10);

  {
    float a0[8] = {}, a1[8] = {};
#pragma unroll
    for (int k = 0; k < 4; ++k) {
      const u32 x0 = L[L_XN + row0 / 2 + rr + 8 * k], x1 = L[L_XN + 224 + row0 / 2 + rr + 8 * k];
#pragma unroll
      for (int c = 0; c < 4; ++c) {
        const u32 pl = __builtin_amdgcn_perm(wv[2 * k + 1][c], wv[2 * k][c], 0x05040100u), ph = __builtin_amdgcn_perm(wv[2 * k + 1][c], wv[2 * k][c], 0x07060302u);
        a0[2 * c] = dot2(pl, x0, a0[2 * c]); a0[2 * c + 1] = dot2(ph, x0, a0[2 * c + 1]);
        a1[2 * c] = dot2(pl, x1, a1[2 * c]); a1[2 * c + 1] = dot2(ph, x1, a1[2 * c + 1]);
      }
    }
    asm volatile("" :: "v"(box_touch));
    MV(5);
    float t[8], u[4];
#pragma unroll
    for (int k = 0; k < 8; ++k) t[k] = tr32(a0[k], a1[k]);
#pragma unroll
    for (int k = 0; k < 4; ++k) { u[k] = tr16(t[k], t[k + 4]); u[k] += dpp<ROR8>(u[k]); }
    const int bb = lane >> 5, hf = (lane >> 4) & 1;
    LAS f32x4* qx = (LAS f32x4*)(LF + L_QX) + bb * 16 + c8 * 2 + hf;
    if (kw == 1) {
      if (!(lane & 8)) *qx = f32x4{u[0], u[1], u[2], u[3]};
      flag_set(L + F_QX);
    } else if (kw == 0) {
      flag_wait<1, 0>(L + F_QX, lane);
      const f32x4 o = *qx;
      if (!(ABL & 1) && !(lane & 8)) st128(rws, QP_OFF + (hq * 32 + j) * QP_S + bb * 256 + hf * 128 + c8 * 16, tag4(u[0] + o.x, u[1] + o.y, u[2] + o.z, u[3] + o.w, tag8));
    } else if (!(ABL & 2) && mlp) {
      LAS u32x4* gx = (LAS u32x4*)(L + L_GX) + (kw - 2) * 32;
      if (!(lane & 8)) gx[c8 * 4 + bb * 2 + hf] = tag4(u[0], u[1], u[2], u[3], tag8);
      CBAR();
      if (lane < 32) st128(rws, GP_OFF + wg * GP_S + (lane >> 2) * 128 + (kw - 2) * 64 + (lane & 3) * 16, gx[lane]);
    }
  }

  if (kw < 2) {
    __builtin_amdgcn_s_sleep(QDELAY);
    if (TQ) pace<TQ>(org);
    const int qo = QP_OFF + (h * 32 + 4 * kw + 8 * (lane >> 4)) * QP_S + b * 256 + (lane & 15) * 16;
    u32x4 a[4];
    for (u32 s = 0; s < SPIN_MEM; ++s) {
      CBAR();
      if (ABL & 1) { a[0] = a[1] = a[2] = a[3] = xa; break; }
#pragma unroll
      for (int i = 0; i < 4; ++i) a[i] = ld128<AUX_SC1>(rws, qo, i * QP_S);
      if (all_ok((untag4(a[0], tag8) | untag4(a[1], tag8)) | (untag4(a[2], tag8) | untag4(a[3], tag8)))) break;
    }
#pragma unroll
    for (int i = 0; i < 2; ++i) {
      const u32x4 a0 = a[2 * i], a1 = a[2 * i + 1];
      const float v0 = tr32(asf(a0.x), asf(a1.x)), v1 = tr32(asf(a0.y), asf(a1.y)), v2 = tr32(asf(a0.z), asf(a1.z)), v3 = tr32(asf(a0.w), asf(a1.w));
      L[L_Q + (4 * kw + 2 * i + (lane >> 5)) * 32 + 4 * (lane & 7) + 2 * ((lane >> 3) & 1) + ((lane >> 4) & 1)] = pkbf(tr16(v0, v2), tr16(v1, v3));
    }
    flag_set(L + F_Q + kw);
    MK(1);
  } else if (kw == 2) {
    u32 h0 = 0u, h1 = 0u;
    if (mlp) {
      const int go = GP_OFF + (cb2 + 28 * rr) * GP_S + ke * 128 + c8 * 16;
      u32x4 a;
      flag_wait<2, 1>(L + F_Q, lane);
      if (TG) pace<TG>(org);
      for (u32 s = 0; s < SPIN_MEM; ++s) {
        CBAR();
        if (ABL & 2) { a = xa; break; }
        a = ld128<AUX_SC1>(rws, go);
        if (all_ok(untag4(a, tag8))) break;
        __builtin_amdgcn_s_sleep(16);
      }
      float hv[4];
#pragma unroll
      for (int k = 0; k < 4; ++k) {
        float g = asf(a[k]);
        g += dpp<ROR8>(g); g = tr16(g, g); g = tr32(g, g);
        g = rbf(g);
        const float uu = dpp<ROR4>(g);
        const float z = 0.7978845608028654f * (g + 0.044715f * g * g * g);
        const float th = 1.f - 2.f * __builtin_amdgcn_rcpf(1.f + __builtin_amdgcn_exp2f(z * 2.8853900817779268f));
        hv[k] = rbf(0.5f * g * (1.f + th)) * uu;
      }
      h0 = pkbf(hv[0], hv[1]); h1 = pkbf(hv[2], hv[3]);
    }
    if (lane < 4) {
      *(LAS u32x2*)(L + L_H + (2 * (lane & 1)) * 4 + (lane >> 1) * 2) = u32x2{h0, SET};
      *(LAS u32x2*)(L + L_H + (2 * (lane & 1) + 1) * 4 + (lane >> 1) * 2) = u32x2{h1, SET};
    }
    CBAR();
  } else if (GO) {
    pace<300>(org);
    const u32x2 o = __builtin_amdgcn_raw_buffer_load_b64(rws, OR_OFF + (lane & 7) * 8, 0, AUX_SC1);
    const bool ok = o.y == tag;
    const float n = row_sum(ok ? 1.f : 0.f), dsum = row_sum(ok ? (float)(int)(o.x - (u32)org) : 0.f);
    const float sh = n > 0.f ? fminf(fmaxf(dsum * __builtin_amdgcn_rcpf(n) + GB, -80.f), 80.f) : 0.f;
    L[L_SH] = (u32)(int)sh;
    flag_set(L + F_SH);
  }

  asm volatile("" :: "v"(page_touch));
  const int rg = lane >> 4, cl = lane & 15;
  const int co = kw * 128 + cl * 8;
  u32x4 wa[4], wb[2];
  if (LATE == 0) flag_wait<2, 1>(L + F_Q, lane);
  if (LATE >= 1) flag_wait<4, 1>(L + F_VI, lane);
  if (LATE == 3) flag_wait<1, 0>(L + F_PUB, lane);
  if (GO) org += (u64)(long long)(int)__builtin_amdgcn_readfirstlane(L[L_SH]);
  if (LATE == 4) pace<PA + PV + (8 * PD + 8 * PDV) / 10 + LD>(org + kw * STG);
  if (LATE == 5) __builtin_amdgcn_s_sleep(LD);
  {
    const rsrc_t ro = mkrs(wo, (ABL & 4) ? 0 : 4096 * D * 2), rd = mkrs(wd, mlp && !(ABL & 4) ? DF * D * 2 : 0);
    const int vo = co < D ? (ho * 512 + j * 16 + 4 * rg) * (D * 2) + co * 2 : OOB;
    const int vd = co < D ? (64 * cb2 + 8 * ke + 2 * rg) * (D * 2) + co * 2 : OOB;
#pragma unroll
    for (int i = 0; i < 2; ++i) wb[i] = ld128<AUX_NT>(rd, vd, i * D * 2);
#pragma unroll
    for (int i = 0; i < 4; ++i) wa[i] = ld128<AUX_NT>(ro, vo, i * D * 2);
  }
  __builtin_amdgcn_sched_barrier(0);
  float a0[8], a1[8];
  u32 pa[8], pb[8];
  {
    u32x4 hh;
    for (u32 s = 0; s < SPIN_LDS; ++s) {
      CBAR();
      hh = *(LAS u32x4*)(L + L_H + rg * 4);
      if (none((hh.y != SET) | (hh.w != SET))) break;
      __builtin_amdgcn_s_sleep(1);
    }
#pragma unroll
    for (int k = 0; k < 4; ++k) {
      const u32 pl = __builtin_amdgcn_perm(wb[1][k], wb[0][k], 0x05040100u), ph = __builtin_amdgcn_perm(wb[1][k], wb[0][k], 0x07060302u);
      a0[2 * k] = dot2(pl, hh.x, 0.f); a0[2 * k + 1] = dot2(ph, hh.x, 0.f);
      a1[2 * k] = dot2(pl, hh.z, 0.f); a1[2 * k + 1] = dot2(ph, hh.z, 0.f);
    }
#pragma unroll
    for (int k = 0; k < 4; ++k) {
      pa[2 * k] = __builtin_amdgcn_perm(wa[1][k], wa[0][k], 0x05040100u); pa[2 * k + 1] = __builtin_amdgcn_perm(wa[1][k], wa[0][k], 0x07060302u);
      pb[2 * k] = __builtin_amdgcn_perm(wa[3][k], wa[2][k], 0x05040100u); pb[2 * k + 1] = __builtin_amdgcn_perm(wa[3][k], wa[2][k], 0x07060302u);
    }
  }
#pragma unroll
  for (int k = 0; k < 8; ++k) asm volatile("" : "+v"(a0[k]), "+v"(a1[k]), "+v"(pa[k]), "+v"(pb[k]));
  int o2o = !((ABL & 256) && j >= 16) && co < ((ABL & 64) ? 256 : (ABL & 128) ? 128 : D) ? O2_OFF + (h * 32 + j) * O2_S + (lane >> 5) * 1792 + kw * 512 + (((lane >> 4) & 1) * (kw == 3 ? 8 : 16) + cl) * 16 : OOB;
  LAS u32x4* orp = (LAS u32x4*)(L + L_O + rg * 4);
  LAS u32* ofp = L + F_O;
  PIN(o2o); PIN(orp); PIN(ofp);
  if (LATE == 0) flag_wait<4, 1>(L + F_VI, lane);
  {
    const u32x4 oo = data_wait(ofp, orp);
#pragma unroll
    for (int k = 0; k < 8; ++k) {
      a0[k] = dot2(pb[k], oo.y, dot2(pa[k], oo.x, a0[k]));
      a1[k] = dot2(pb[k], oo.w, dot2(pa[k], oo.z, a1[k]));
    }
    float t[8];
#pragma unroll
    for (int k = 0; k < 8; ++k) t[k] = tr32(a0[k], a1[k]);
    st128(rws, o2o, tag4(tr16(t[0], t[4]), tr16(t[1], t[5]), tr16(t[2], t[6]), tr16(t[3], t[7]), tag8));
  }
  flag_set(L + F_O2 + kw);
  NV(3); NW(3);
  MK(5);
}

extern "C" void kernel_launch(void* const* d_in, const int* in_sizes, int n_in, void* d_out, int out_size, void* d_ws, size_t ws_size,
                              hipStream_t stream) {
  fused_decode<<<256, 768, 0, stream>>>((const u16*)d_in[0], (const u16*)d_in[3], (const u16*)d_in[6], (const u16*)d_in[7], (const u16*)d_in[1],
                                        (char*)d_ws + WS_OFF, (const u16*)d_in[2], (const u16*)d_in[4], (const u16*)d_in[8], (u16*)d_out);
}
```

```cpp
#include <hip/hip_runtime.h>
#include <stdint.h>

typedef unsigned int u32;
typedef unsigned short u16;
typedef unsigned long long u64;
typedef u32 u32x2 __attribute__((ext_vector_type(2)));
typedef u32 u32x4 __attribute__((ext_vector_type(4)));
typedef float f32x4 __attribute__((ext_vector_type(4)));
typedef float f32x16 __attribute__((ext_vector_type(16)));
typedef __bf16 bf16x2_t __attribute__((ext_vector_type(2)));
typedef __bf16 bf16x8_t __attribute__((ext_vector_type(8)));
#define LAS __attribute__((address_space(3)))
typedef __amdgpu_buffer_rsrc_t rsrc_t;

namespace {
constexpr int D = 448, SK = 32768, W = 4096, DH = 256, DF = 1792;
constexpr int AUX_NT = 2, AUX_SC1 = 16;
constexpr int OOB = 1 << 30;
constexpr u32 TMASK = 0xfu;

constexpr int QP_S = 768, GP_S = 1280, O1_S = 2304, O2_S = 3840;
constexpr int EP_OFF = 0;
constexpr int OR_OFF = 2048;
constexpr int ST_OFF = 4096;
constexpr int QP_OFF = ST_OFF + 8192;
constexpr int GP_OFF = QP_OFF + 256 * QP_S;
constexpr int O1_OFF = GP_OFF + 256 * GP_S;
constexpr int O2_OFF = O1_OFF + 256 * O1_S;
constexpr int WS_BYTES = O2_OFF + 256 * O2_S;

constexpr int L_XN = 0;
constexpr int L_QX = L_XN + 480;
constexpr int L_GX = L_QX + 128;
constexpr int L_Q = L_GX + 256;
constexpr int L_P = L_Q + 256;
constexpr int L_OP = L_P + 512;
constexpr int L_WM = L_OP + 2048;
constexpr int L_WL = L_WM + 8;
constexpr int L_H = L_WL + 8;
constexpr int L_O = L_H + 16;
constexpr int F_O = L_O + 16;
constexpr int L_R = L_O + 32;
constexpr int F_P = L_R + 128;
constexpr int F_VI = F_P + 4;
constexpr int F_O2 = F_VI + 4;
constexpr int F_Q = F_O2 + 4;
constexpr int F_QX = F_Q + 2, F_PUB = F_QX + 1;
constexpr int F_V = F_PUB + 1;
constexpr int F_R = F_V + 4;
constexpr int L_TAG = F_R + 8;
constexpr int L_SH = L_TAG + 2, F_SH = L_TAG + 3;
constexpr int L_END = L_TAG + 4;
constexpr u32 SET = 1u;
static_assert((L_END - L_WM) % 4 == 0 && (L_END - L_WM) / 4 <= 64, "one 16-byte store per lane clears the flag area");

constexpr int T_K0 = 30, T_KSTEP10 = 620, T_VSTEP10 = 650;
constexpr int K_FREE = 2;
constexpr int V_FINE = 4, V_FINE_LEAD = 25;
constexpr int WINDOW = 6;
constexpr int T_ORG = 300, ORG_BIAS = 5;
constexpr int due_k(int g) { return T_K0 + g * T_KSTEP10 / 10; }
constexpr int due_v(int g, int half) { return T_K0 + (8 * T_KSTEP10 + g * T_VSTEP10 + half * T_VSTEP10 / 2) / 10 - (g >= V_FINE ? V_FINE_LEAD : 0); }
constexpr int Q_DELAY = 40, O1_DELAY = 10, O2_DELAY = 9;
constexpr int ROT = 37, SROT = 3;
constexpr int CODE_SPAN = 15360;
constexpr u32 SPIN_LDS = 1u << 21, SPIN_MEM = 1u << 16;

__device__ __forceinline__ float asf(u32 v) { return __builtin_bit_cast(float, v); }
__device__ __forceinline__ u32 asu(float v) { return __builtin_bit_cast(u32, v); }
__device__ __forceinline__ float lo16(u32 v) { return asf(v << 16); }
__device__ __forceinline__ float hi16(u32 v) { return asf(v & 0xffff0000u); }
__device__ __forceinline__ u32 pkbf(float a, float b) {
  bf16x2_t r = {(__bf16)a, (__bf16)b};
  return __builtin_bit_cast(u32, r);
}
__device__ __forceinline__ float rbf(float a) { return (float)(__bf16)a; }
__device__ __forceinline__ float dot2(u32 a, u32 b, float c) {
  return __builtin_amdgcn_fdot2_f32_bf16(__builtin_bit_cast(bf16x2_t, a), __builtin_bit_cast(bf16x2_t, b), c, false);
}
template <int CTRL> __device__ __forceinline__ float dpp(float x) {
  return asf(__builtin_amdgcn_mov_dpp((int)asu(x), CTRL, 0xf, 0xf, true));
}
constexpr int XOR1 = 0xB1, XOR2 = 0x4E, ROR8 = 0x128, ROR4 = 0x124;
__device__ __forceinline__ float tr32(float a, float b) {
  auto r = __builtin_amdgcn_permlane32_swap(asu(a), asu(b), false, false);
  return asf(r[0]) + asf(r[1]);
}
__device__ __forceinline__ float tr16(float a, float b) {
  auto r = __builtin_amdgcn_permlane16_swap(asu(a), asu(b), false, false);
  return asf(r[0]) + asf(r[1]);
}
__device__ __forceinline__ float mx32(float a) {
  auto r = __builtin_amdgcn_permlane32_swap(asu(a), asu(a), false, false);
  return fmaxf(asf(r[0]), asf(r[1]));
}
__device__ __forceinline__ float mx16(float a) {
  auto r = __builtin_amdgcn_permlane16_swap(asu(a), asu(a), false, false);
  return fmaxf(asf(r[0]), asf(r[1]));
}
__device__ __forceinline__ float row_sum(float v) { v += dpp<ROR8>(v); v += dpp<ROR4>(v); v += dpp<XOR2>(v); v += dpp<XOR1>(v); return v; }
__device__ __forceinline__ float row_max(float v) {
  v = fmaxf(v, dpp<ROR8>(v)); v = fmaxf(v, dpp<ROR4>(v)); v = fmaxf(v, dpp<XOR2>(v)); v = fmaxf(v, dpp<XOR1>(v)); return v;
}
__device__ __forceinline__ float wave_sum(float v) { v = row_sum(v); v = tr16(v, v); return tr32(v, v); }

__device__ __forceinline__ rsrc_t mkrs(const void* p, u32 bytes) { return __builtin_amdgcn_make_buffer_rsrc((void*)p, 0, bytes, 0x00020000); }
template <int AUX> __device__ __forceinline__ u32x4 ld128(rsrc_t r, int voff, int soff = 0) { return __builtin_amdgcn_raw_buffer_load_b128(r, voff, soff, AUX); }
template <int AUX> __device__ __forceinline__ u32 ld32(rsrc_t r, int voff) { return __builtin_amdgcn_raw_buffer_load_b32(r, voff, 0, AUX); }
__device__ __forceinline__ void st128(rsrc_t r, int voff, u32x4 v) { __builtin_amdgcn_raw_buffer_store_b128(v, r, voff, 0, AUX_SC1); }
__device__ __forceinline__ u32 tagw(float v, u32 tag4) { return (asu(v) & ~TMASK) | tag4; }
__device__ __forceinline__ u32x4 tag4(float a, float b, float c, float d, u32 t) { return u32x4{tagw(a, t), tagw(b, t), tagw(c, t), tagw(d, t)}; }
__device__ __forceinline__ u32 untag4(u32x4& v, u32 t) { v.x ^= t; v.y ^= t; v.z ^= t; v.w ^= t; return v.x | v.y | v.z | v.w; }
__device__ __forceinline__ bool none(bool bad) { return __builtin_amdgcn_ballot_w64(bad) == 0ull; }
__device__ __forceinline__ bool all_ok(u32 bad) { return none((bad & TMASK) != 0u); }

#define CBAR() asm volatile("" ::: "memory")
#define PIN(v) asm volatile("" : "+v"(v))
#define LGKM0() asm volatile("s_waitcnt lgkmcnt(0)" ::: "memory")
template <int N, int SLEEP> __device__ __forceinline__ void flag_wait(LAS u32* p, int lane) {
  u32 v, cnt;
  const u32 addr = (u32)(uintptr_t)(p + (lane & (N - 1)));
  asm volatile("s_mov_b32 %1, %4\n"
               "1:\n\t"
               "ds_read_b32 %0, %2\n\t"
               "s_waitcnt lgkmcnt(0)\n\t"
               "v_cmp_ne_u32 vcc, %3, %0\n\t"
               "s_cbranch_vccz 2f\n\t"
               "s_sleep %5\n\t"
               "s_sub_u32 %1, %1, 1\n\t"
               "s_cbranch_scc0 1b\n"
               "2:"
               : "=&v"(v), "=&s"(cnt) : "v"(addr), "i"(SET), "i"(SPIN_LDS), "i"(SLEEP) : "vcc", "scc", "memory");
}
template <int DUE> __device__ __forceinline__ void pace(u64 org) {
  asm volatile("1:\n\t"
               "s_memrealtime vcc\n\t"
               "s_waitcnt lgkmcnt(0)\n\t"
               "s_sub_u32 vcc_lo, vcc_lo, %0\n\t"
               "s_cmp_lt_i32 vcc_lo, %1\n\t"
               "s_cbranch_scc0 2f\n\t"
               "s_sleep 1\n\t"
               "s_branch 1b\n"
               "2:"
               :: "s"((u32)org), "i"(DUE), "s"(org) : "vcc", "scc", "memory");
}
__device__ __forceinline__ void flag_set(LAS u32* p, u32 set = SET) { CBAR(); *p = set; CBAR(); }

__device__ __forceinline__ void poll2(rsrc_t rs, int voff, int soff, u32 t4, u32x4& r0, u32x4& r1) {
  u32 o0, o1, o2, o3, o4, o5, o6, o7, m, cnt;
  asm volatile("s_mov_b32 %9, %14\n"
               "1:\n\t"
               "buffer_load_dwordx4 v[144:147], %10, %11, 0 offen sc1\n\t"
               "buffer_load_dwordx4 v[148:151], %10, %11, %12 offen sc1\n\t"
               "s_sub_u32 %9, %9, 1\n\t"
               "s_waitcnt vmcnt(1)\n\t"
               "v_xor_b32 %0, %13, v144\n\tv_xor_b32 %1, %13, v145\n\tv_xor_b32 %2, %13, v146\n\tv_xor_b32 %3, %13, v147\n\t"
               "v_or3_b32 %8, %0, %1, %2\n\t"
               "s_waitcnt vmcnt(0)\n\t"
               "v_xor_b32 %4, %13, v148\n\tv_xor_b32 %5, %13, v149\n\tv_xor_b32 %6, %13, v150\n\tv_xor_b32 %7, %13, v151\n\t"
               "v_or3_b32 %8, %8, %3, %4\n\t"
               "v_or3_b32 %8, %8, %5, %6\n\t"
               "v_bitop3_b32 %8, %8, 15, %7 bitop3:0xc8\n\t"
               "v_cmp_ne_u32 vcc, 0, %8\n\t"
               "s_cbranch_vccz 2f\n\t"
               "s_cbranch_scc0 1b\n"
               "2:"
               : "=&v"(o0), "=&v"(o1), "=&v"(o2), "=&v"(o3), "=&v"(o4), "=&v"(o5), "=&v"(o6), "=&v"(o7), "=&v"(m), "=&s"(cnt)
               : "v"(voff), "s"(rs), "s"(soff), "s"(t4), "i"(SPIN_MEM)
               : "vcc", "scc", "memory", "v144", "v145", "v146", "v147", "v148", "v149", "v150", "v151");
  r0 = u32x4{o0, o1, o2, o3};
  r1 = u32x4{o4, o5, o6, o7};
}
__device__ __forceinline__ u32x4 data_wait(LAS u32* flag, LAS u32x4* p) {
  u32x4 r;
  u32 v, cnt;
  asm volatile("s_mov_b32 %2, %5\n"
               "1:\n\t"
               "ds_read_b32 %1, %3\n\t"
               "ds_read_b128 %0, %4\n\t"
               "s_sub_u32 %2, %2, 1\n\t"
               "s_waitcnt lgkmcnt(0)\n\t"
               "v_cmp_ne_u32 vcc, 1, %1\n\t"
               "s_cbranch_vccz 2f\n\t"
               "s_cbranch_scc0 1b\n"
               "2:"
               : "=&v"(r), "=&v"(v), "=&s"(cnt) : "v"((u32)(uintptr_t)flag), "v"((u32)(uintptr_t)p), "i"(SPIN_LDS) : "vcc", "scc", "memory");
  return r;
}

__device__ __forceinline__ void final_reduce(LAS u32* L, rsrc_t rws, const u16* x, u16* out, int wg, int grp, int lane, u32 tag) {
  LAS float* const LF = (LAS float*)L;
  __builtin_amdgcn_s_setprio(3);
  const u32 tag8 = tag & TMASK;
  const int rb = wg / 28, cb = wg % 28, qd = lane & 3;
  const int pi = cb & 7, ph = cb < 24 ? pi >> 2 : pi >> 1, pg = cb < 24 ? pi & 3 : pi & 1;
  const int col = rb * D + 128 * (cb >> 3) + 8 * (4 * pg + ((lane >> 2) & 3)) + 4 * ph + (lane & 3);
  float xres = lo16(x[col]);
  int oo = O2_OFF + (grp * 32 + (lane >> 2)) * O2_S + rb * 1792 + cb * 64 + qd * 16;
  LAS float* wp = LF + ((lane & 12) == 0 ? L_R + (4 * qd + (lane >> 4)) * 8 + grp : L_XN + 128 + lane);
  LAS u32* fp = L + F_R + grp;
  LAS u32* fr = L + F_R + (lane & 7);
  LAS f32x4* rp = (LAS f32x4*)(LF + L_R + (lane & 15) * 8);
  int so = lane < 16 ? col * 2 : OOB;
  u32 setv = SET;
  rsrc_t ro = mkrs(out, 2 * D * 2);
  asm volatile("" : "+s"(ro));
  PIN(xres); PIN(oo); PIN(wp); PIN(fp); PIN(fr); PIN(rp); PIN(so); PIN(setv);
  flag_wait<1, 1>(L + F_PUB, lane);
  flag_wait<4, 0>(L + F_O2, lane);
  __builtin_amdgcn_s_sleep(O2_DELAY);
  u32x4 a0, a1;
  poll2(rws, oo, 16 * O2_S, tag8, a0, a1);
  float f = tr16(tr32(asf(a0.x) + asf(a1.x), asf(a0.z) + asf(a1.z)), tr32(asf(a0.y) + asf(a1.y), asf(a0.w) + asf(a1.w)));
  f += dpp<ROR8>(f); f += dpp<ROR4>(f);
  *wp = f;
  flag_set(fp, setv);
  u32 fl = *fr;
  CBAR();
  const f32x4 r0 = rp[0], r1 = rp[1];
  const f32x4 rs4 = r0 + r1;
  const float tot = xres + ((rs4.x + rs4.z) + (rs4.y + rs4.w));
  u32 res = pkbf(tot, tot);
  PIN(res);
  if (none(fl != SET)) {
    __builtin_amdgcn_raw_buffer_store_b16((u16)res, ro, so, 0, 0);
    if (wg == 0 && lane == 0) __builtin_amdgcn_raw_buffer_store_b32(tag, rws, EP_OFF, 0, AUX_SC1);
  }
}

}

extern "C" __global__ void __launch_bounds__(768, 3)
fused_decode(const u16* __restrict__ x, const u16* __restrict__ wq, const u16* __restrict__ wg_, const u16* __restrict__ wu,
             const u16* __restrict__ kc, char* __restrict__ ws, const u16* __restrict__ vc, const u16* __restrict__ wo,
             const u16* __restrict__ wd, u16* __restrict__ out) {
  __shared__ __attribute__((aligned(16))) u32 smem[L_END];
  LAS u32* const L = (LAS u32*)smem;
  LAS float* const LF = (LAS float*)smem;
  const int wg = blockIdx.x, h = wg & 7, j = wg >> 3, b = j & 1, sp = j >> 1, reg = wg & 15;
  const int w = __builtin_amdgcn_readfirstlane(threadIdx.x >> 6), lane = threadIdx.x & 63, kw = w & 3;
  asm volatile("s_load_dword s94, %0, 0x0\n\ts_load_dword s95, %0, 0x40" :: "s"(__builtin_amdgcn_kernarg_segment_ptr()) : "s94", "s95");
  u64 pc0;
  asm volatile("s_getpc_b64 %0" : "=s"(pc0));
  const u32 code_touch = ld32<0>(mkrs((const char*)(pc0 & ~255ull), w == 0 ? CODE_SPAN : 0), lane < 5 ? j * 640 + lane * 128 : OOB);
  __builtin_amdgcn_sched_barrier(0);
#define ROLE_ENTRY() const rsrc_t rws = mkrs(ws, WS_BYTES)
#define ROLE_TAG() const u32 tag = __builtin_amdgcn_readfirstlane(L[L_TAG]), tag8 = tag & TMASK
  const u32 blk0 = sp * 32 + reg * ROT;
  const u32 srot = sp * SROT;
#define BLOCK_OFF(slot) (((blk0 + (((slot) + srot) & 31u)) & 511u) << 12)
  u64 org;
#define STREAM_GROUP(G, VOFF, SLOT, ISK) \
  if (!(ISK) || (G) >= K_FREE) pace<(ISK) ? due_k(G) : due_v(G, 0)>(org); \
  if (4 * (G) > WINDOW) asm volatile("s_waitcnt vmcnt(%0)" :: "i"(WINDOW) : "memory"); \
  _Pragma("unroll") for (int i = 4 * (G); i < 4 * (G) + 2; ++i) d[i] = ld128<AUX_NT>(rkv, (VOFF), (int)BLOCK_OFF(SLOT)); \
  if (!(ISK) && (G) >= V_FINE) { __builtin_amdgcn_sched_barrier(0); pace<due_v(G, 1)>(org); } \
  _Pragma("unroll") for (int i = 4 * (G) + 2; i < 4 * (G) + 4; ++i) d[i] = ld128<AUX_NT>(rkv, (VOFF), (int)BLOCK_OFF(SLOT)); \
  __builtin_amdgcn_sched_barrier(0);
#define STREAM_LOADS(VOFF, SLOT, ISK) \
  STREAM_GROUP(0, VOFF, SLOT, ISK) STREAM_GROUP(1, VOFF, SLOT, ISK) STREAM_GROUP(2, VOFF, SLOT, ISK) STREAM_GROUP(3, VOFF, SLOT, ISK) \
  STREAM_GROUP(4, VOFF, SLOT, ISK) STREAM_GROUP(5, VOFF, SLOT, ISK) STREAM_GROUP(6, VOFF, SLOT, ISK) STREAM_GROUP(7, VOFF, SLOT, ISK)
#define WINDOW_RSRC(SRC) mkrs((SRC) + ((size_t)reg * SK + (SK - W)) * DH, W * DH * 2)

  if (__builtin_expect(w >= 4, 0)) {
  u32 tag_out;
  if (w < 8) {
    __builtin_amdgcn_s_setprio(2);
    const int g = lane >> 4, key = (lane >> 1) & 7;
    const bool hb = lane & 1;
    const int kvo = key * 512 + (2 * g + (lane & 1)) * 16;
    const rsrc_t rkv = WINDOW_RSRC(kc);
    u32x4 d[32];
    __builtin_amdgcn_sched_barrier(0);
    __builtin_amdgcn_s_barrier();
    org = __builtin_amdgcn_s_memrealtime();
    __builtin_amdgcn_sched_barrier(0);
    STREAM_LOADS(kvo + (i & 3) * 128, kw + 4 * (i >> 2), true)
    ROLE_ENTRY();
    flag_wait<2, 1>(L + F_Q, lane);
    ROLE_TAG();
    u32x4 qa[4];
#pragma unroll
    for (int i = 0; i < 4; ++i) qa[i] = *(LAS u32x4*)(L + L_Q + (lane & 1) * 128 + i * 32 + (2 * g + ((lane >> 1) & 1)) * 4);
    f32x4 ea, eb;
#pragma unroll
    for (int n = 0; n < 4; ++n) {
      f32x4 ca = {}, cb = {};
#pragma unroll
      for (int i = 0; i < 4; ++i) {
        ca = __builtin_amdgcn_mfma_f32_16x16x32_bf16(__builtin_bit_cast(bf16x8_t, qa[i]), __builtin_bit_cast(bf16x8_t, d[8 * n + i]), ca, 0, 0, 0);
        cb = __builtin_amdgcn_mfma_f32_16x16x32_bf16(__builtin_bit_cast(bf16x8_t, qa[i]), __builtin_bit_cast(bf16x8_t, d[8 * n + 4 + i]), cb, 0, 0, 0);
      }
#pragma unroll
      for (int k = 0; k < 4; ++k) { ea[k] = (n == 0 || g == n) ? ca[k] : ea[k]; eb[k] = (n == 0 || g == n) ? cb[k] : eb[k]; }
    }
    asm volatile("" :: "v"(code_touch));
    float sc[2];
#pragma unroll
    for (int hd = 0; hd < 2; ++hd) sc[hd] = rbf((hb ? eb[2 + hd] : ea[hd]) + dpp<XOR1>(hb ? ea[2 + hd] : eb[hd])) * 0.0625f;
    const float wm0 = mx32(mx16(row_max(sc[0]))), wm1 = mx32(mx16(row_max(sc[1])));
    CBAR();
    if (lane < 2) L[L_WM + kw * 2 + lane] = (asu(lane ? wm1 : wm0) & 0xffff0000u) | SET;
    u32x4 ma, mb;
    for (u32 s = 0; s < SPIN_LDS; ++s) {
      CBAR();
      ma = *(LAS u32x4*)(L + L_WM) ^ SET; mb = *(LAS u32x4*)(L + L_WM + 4) ^ SET;
      if (none((((ma.x | ma.y | ma.z | ma.w) | (mb.x | mb.y | mb.z | mb.w)) & 0xffffu) != 0u)) break;
    }
    const float M0 = fmaxf(fmaxf(asf(ma.x), asf(ma.z)), fmaxf(asf(mb.x), asf(mb.z)));
    const float M1 = fmaxf(fmaxf(asf(ma.y), asf(ma.w)), fmaxf(asf(mb.y), asf(mb.w)));
    const float p0 = rbf(__builtin_amdgcn_exp2f((sc[0] - M0) * 1.4426950408889634f));
    const float p1 = rbf(__builtin_amdgcn_exp2f((sc[1] - M1) * 1.4426950408889634f));
    LAS u32* pp = L + L_P + key * 64 + kw + 4 * (2 * g + (lane & 1));
    pp[0] = asu(p0) | (asu(p0) >> 16);
    pp[32] = asu(p1) | (asu(p1) >> 16);
    float ls = tr32(p0, p1);
    ls = row_sum(tr16(ls, ls));
    if ((lane & 31) == 0) LF[L_WL + kw * 2 + (lane >> 5)] = ls;
    flag_set(L + F_P + kw);

    if (kw == 0) {
      flag_wait<4, 0>(L + F_P, lane);
      if (lane < 2) {
        const float Ls = (LF[L_WL + lane] + LF[L_WL + 2 + lane]) + (LF[L_WL + 4 + lane] + LF[L_WL + 6 + lane]);
        st128(rws, ST_OFF + (((h * 2 + lane) * 2 + b) * 16 + sp) * 16, u32x4{asu(lane ? M1 : M0), tag, asu(Ls), tag});
      }
    } else if (kw == 1) {
      __builtin_amdgcn_s_setprio(3);
      const int s = lane >> 2, qd = lane & 3;
      const int so = ST_OFF + ((h * 2 + (j >> 4)) * 32 + s) * 16;
      const int oo = O1_OFF + (h * 32 + 2 * s) * O1_S + j * 64 + qd * 16;
      u32x4 s0, s1, a0, a1;
      flag_wait<4, 1>(L + F_VI, lane);
      for (u32 it = 0; it < SPIN_MEM; ++it) {
        CBAR();
        s0 = ld128<AUX_SC1>(rws, so); s1 = ld128<AUX_SC1>(rws, so + 256);
        if (none((s0.y != tag) | (s0.w != tag) | (s1.y != tag) | (s1.w != tag))) break;
        flag_wait<1, 1>(L + F_PUB, lane);
      }
      float wgt[2];
#pragma unroll
      for (int bb = 0; bb < 2; ++bb) {
        const float m = asf(bb ? s1.x : s0.x), l = asf(bb ? s1.z : s0.z);
        float mg = fmaxf(m, dpp<ROR8>(m)); mg = fmaxf(mg, dpp<ROR4>(mg)); mg = mx32(mx16(mg));
        const float e = __builtin_amdgcn_exp2f((m - mg) * 1.4426950408889634f);
        float lg = e * l; lg += dpp<ROR8>(lg); lg += dpp<ROR4>(lg); lg = tr16(lg, lg); lg = tr32(lg, lg);
        wgt[bb] = e * __builtin_amdgcn_rcpf(lg);
      }
      LAS u32* ow = L + ((lane & 12) == 0 ? L_O + qd * 4 + (lane >> 4) : L_XN + lane);
      LAS u32* of = L + F_O;
      u32 setv = SET;
      PIN(wgt[0]); PIN(wgt[1]); PIN(ow); PIN(of); PIN(setv);
      flag_wait<1, 0>(L + F_PUB, lane);
      __builtin_amdgcn_s_sleep(O1_DELAY);
      poll2(rws, oo, O1_S, tag8, a0, a1);
      f32x4 m0 = __builtin_bit_cast(f32x4, a0) * wgt[0], m1 = __builtin_bit_cast(f32x4, a1) * wgt[1];
      PIN(m0); PIN(m1);
      const float v0 = tr32(m0.x, m1.x), v2 = tr32(m0.z, m1.z), v1 = tr32(m0.y, m1.y), v3 = tr32(m0.w, m1.w);
      float e0 = tr16(v0, v2), e1 = tr16(v1, v3);
      e0 += dpp<ROR8>(e0); e1 += dpp<ROR8>(e1);
      e0 += dpp<ROR4>(e0); e1 += dpp<ROR4>(e1);
      *ow = pkbf(e0, e1);
      flag_set(of, setv);
    }
    tag_out = tag;
  } else {
    if (kw == 0 && lane < (L_END - L_WM) / 4) *(LAS u32x4*)(L + L_WM + 4 * lane) = u32x4{0u, 0u, 0u, 0u};
    LGKM0();
    __builtin_amdgcn_s_barrier();
    org = __builtin_amdgcn_s_memrealtime();
    __builtin_amdgcn_s_setprio(1);
    __builtin_amdgcn_sched_barrier(0);
    ROLE_ENTRY();
    flag_wait<1, 4>(L + F_SH, lane);
    org += (u64)(long long)(int)__builtin_amdgcn_readfirstlane(L[L_SH]);
    const rsrc_t rkv = WINDOW_RSRC(vc);
    u32x4 d[32];
    STREAM_LOADS(lane * 16 + kw * 1024, (u32)i, false)
    flag_set(L + F_VI + kw);
    const int m = lane & 31, hf = lane >> 5;
    u32x4 msk;
#pragma unroll
    for (int r = 0; r < 4; ++r) msk[r] = (m < 16 && ((m & 7) >> 1) == r) ? ((m & 1) ? 0xffff0000u : 0x0000ffffu) : 0u;
    LAS f32x4* op = (LAS f32x4*)(LF + L_OP) + kw * 128 + 2 * m + hf;
    LAS f32x4* rp = (LAS f32x4*)(LF + L_OP) + lane;
    LAS u32* fp = L + F_V + kw;
    LAS u32* fr = L + F_V + (lane & 3);
    int o1o = O1_OFF + (h * 32 + j) * O1_S + lane * 16;
    PIN(op); PIN(rp); PIN(fp); PIN(fr); PIN(o1o);
    flag_wait<4, 1>(L + F_P, lane);
    ROLE_TAG();
    const LAS u32x4* pp = (const LAS u32x4*)(L + L_P + ((kw * 2 + hf) * 2 + ((m >> 3) & 1)) * 32);
    f32x16 acc = {};
#pragma unroll
    for (int g = 0; g < 8; ++g) {
      const u32x4 p4 = pp[g];
#pragma unroll
      for (int k = 0; k < 4; ++k) {
        const u32x4 a = msk & p4[k];
        acc = __builtin_amdgcn_mfma_f32_32x32x16_bf16(__builtin_bit_cast(bf16x8_t, a), __builtin_bit_cast(bf16x8_t, d[4 * g + k]), acc, 0, 0, 0);
      }
      __builtin_amdgcn_sched_barrier(0);
    }
    asm volatile("" :: "v"(code_touch));
    op[0] = f32x4{acc[0], acc[1], acc[2], acc[3]};
    op[64] = f32x4{acc[4], acc[5], acc[6], acc[7]};
    flag_set(fp);
    const u32 fl = *fr;
    CBAR();
    const f32x4 o0 = (rp[0] + rp[128]) + (rp[256] + rp[384]);
    const f32x4 o1 = (rp[64] + rp[192]) + (rp[320] + rp[448]);
    u32x4 t0 = tag4(o0.x, o0.y, o0.z, o0.w, tag8), t1 = tag4(o1.x, o1.y, o1.z, o1.w, tag8);
    PIN(t0); PIN(t1);
    if (none(fl != SET)) {
      st128(rws, o1o, t0);
      st128(rws, o1o + 1024, t1);
      flag_set(L + F_PUB);
    }
    tag_out = tag;
  }
  if (wg < 56) final_reduce(L, mkrs(ws, WS_BYTES), x, out, wg, w - 4, lane, tag_out);
  return;
  }

  __builtin_amdgcn_s_setprio(3);
  ROLE_ENTRY();
  const bool mlp = wg < 224;
  const int cb2 = wg % 28, ke = wg / 28;
  const int rr = lane >> 3, c8 = lane & 7;
  const rsrc_t rx = mkrs((const u16*)x, 2 * D * 2);
  const u32x4 xa = ld128<0>(rx, lane < 56 ? lane * 16 : OOB), xb = ld128<0>(rx, lane < 56 ? D * 2 + lane * 16 : OOB);
  const u32 epoch = ld32<AUX_SC1>(rws, EP_OFF);
  const int row0 = kw < 2 ? 112 * (j >> 3) + 64 * kw : 56 * ke;
  const int nrow = kw == 0 ? 64 : kw == 1 ? 48 : mlp ? 56 : 0;
  u32x4 wv[8];
  {
    const rsrc_t rw = mkrs(kw < 2 ? wq : kw == 2 ? wg_ : wu, kw < 2 ? D * 4096 * 2 : D * DF * 2);
    const int rs = kw < 2 ? 8192 : DF * 2;
    const int vw0 = nrow ? (row0 + 2 * rr) * rs + (kw < 2 ? (h * 512 + (j & 7) * 64) * 2 : cb2 * 128) + c8 * 16 : OOB;
    const int vw3 = 48 + 2 * rr < nrow ? vw0 : OOB;
#pragma unroll
    for (int i = 0; i < 8; ++i) wv[i] = ld128<AUX_NT>(rw, i < 6 ? vw0 : vw3, (16 * (i >> 1) + (i & 1)) * rs);
  }
  const int eo = lane < 8 ? QP_OFF + (h * 32 + j) * QP_S + lane * 64 : lane < 24 ? GP_OFF + wg * GP_S + (lane - 8) * 64 : ST_OFF + (wg & 127) * 64;
  const u32 box_touch = ld32<AUX_SC1>(rws, (kw == 1 && lane < 25) ? eo : OOB);
  __builtin_amdgcn_sched_barrier(0);
  __builtin_amdgcn_s_barrier();
  org = __builtin_amdgcn_s_memrealtime();
  __builtin_amdgcn_sched_barrier(0);

#pragma unroll
  for (int bb = 0; bb < 2; ++bb) {
    const u32x4 xv = bb ? xb : xa;
    float v[8];
#pragma unroll
    for (int k = 0; k < 4; ++k) { v[2 * k] = lo16(xv[k]); v[2 * k + 1] = hi16(xv[k]); }
    float s = 0.f;
#pragma unroll
    for (int k = 0; k < 8; ++k) s += v[k];
    const float mean = wave_sum(s) * (1.f / D);
    float q = 0.f;
#pragma unroll
    for (int k = 0; k < 8; ++k) { const float t = v[k] - mean; q += t * t; }
    q = lane < 56 ? q : 0.f;
    const float var = rbf(wave_sum(q) * (1.f / D));
    const float den = rbf(__builtin_amdgcn_sqrtf(rbf(var + 1e-5f))), rden = __builtin_amdgcn_rcpf(den), mb = rbf(mean);
#pragma unroll
    for (int k = 0; k < 8; ++k) { const float c = rbf(v[k] - mb), r = c * rden; v[k] = fmaf(fmaf(-r, den, c), rden, r); }
    u32x4 o;
#pragma unroll
    for (int k = 0; k < 4; ++k) o[k] = pkbf(v[2 * k], v[2 * k + 1]);
    if (lane < 56) *(LAS u32x4*)(L + L_XN + bb * 224 + lane * 4) = o;
    else if (bb == 1) *(LAS u32x4*)(L + L_XN + 224 + lane * 4) = u32x4{0u, 0u, 0u, 0u};
  }
  asm volatile("" :: "v"(code_touch));
  const u32 tag = __builtin_amdgcn_readfirstlane(epoch) + 1u, tag8 = tag & TMASK;
  if (kw == 0) L[L_TAG] = tag;
  if (kw == 3 && wg < 8 && lane == 0) __builtin_amdgcn_raw_buffer_store_b64(u32x2{(u32)org, tag}, rws, OR_OFF + wg * 8, 0, AUX_SC1);

  {
    float a0[8] = {}, a1[8] = {};
#pragma unroll
    for (int k = 0; k < 4; ++k) {
      const u32 x0 = L[L_XN + row0 / 2 + rr + 8 * k], x1 = L[L_XN + 224 + row0 / 2 + rr + 8 * k];
#pragma unroll
      for (int c = 0; c < 4; ++c) {
        const u32 pl = __builtin_amdgcn_perm(wv[2 * k + 1][c], wv[2 * k][c], 0x05040100u), ph = __builtin_amdgcn_perm(wv[2 * k + 1][c], wv[2 * k][c], 0x07060302u);
        a0[2 * c] = dot2(pl, x0, a0[2 * c]); a0[2 * c + 1] = dot2(ph, x0, a0[2 * c + 1]);
        a1[2 * c] = dot2(pl, x1, a1[2 * c]); a1[2 * c + 1] = dot2(ph, x1, a1[2 * c + 1]);
      }
    }
    asm volatile("" :: "v"(box_touch));
    float t[8], u[4];
#pragma unroll
    for (int k = 0; k < 8; ++k) t[k] = tr32(a0[k], a1[k]);
#pragma unroll
    for (int k = 0; k < 4; ++k) { u[k] = tr16(t[k], t[k + 4]); u[k] += dpp<ROR8>(u[k]); }
    const int bb = lane >> 5, hf = (lane >> 4) & 1;
    LAS f32x4* qx = (LAS f32x4*)(LF + L_QX) + bb * 16 + c8 * 2 + hf;
    if (kw == 1) {
      if (!(lane & 8)) *qx = f32x4{u[0], u[1], u[2], u[3]};
      flag_set(L + F_QX);
    } else if (kw == 0) {
      flag_wait<1, 0>(L + F_QX, lane);
      const f32x4 o = *qx;
      if (!(lane & 8)) st128(rws, QP_OFF + (h * 32 + j) * QP_S + bb * 256 + hf * 128 + c8 * 16, tag4(u[0] + o.x, u[1] + o.y, u[2] + o.z, u[3] + o.w, tag8));
    } else if (mlp) {
      LAS u32x4* gx = (LAS u32x4*)(L + L_GX) + (kw - 2) * 32;
      if (!(lane & 8)) gx[c8 * 4 + bb * 2 + hf] = tag4(u[0], u[1], u[2], u[3], tag8);
      CBAR();
      if (lane < 32) st128(rws, GP_OFF + wg * GP_S + (lane >> 2) * 128 + (kw - 2) * 64 + (lane & 3) * 16, gx[lane]);
    }
  }

  if (kw < 2) {
    __builtin_amdgcn_s_sleep(Q_DELAY);
    const int qo = QP_OFF + (h * 32 + 4 * kw + 8 * (lane >> 4)) * QP_S + b * 256 + (lane & 15) * 16;
    u32x4 a[4];
    for (u32 s = 0; s < SPIN_MEM; ++s) {
      CBAR();
#pragma unroll
      for (int i = 0; i < 4; ++i) a[i] = ld128<AUX_SC1>(rws, qo, i * QP_S);
      if (all_ok((untag4(a[0], tag8) | untag4(a[1], tag8)) | (untag4(a[2], tag8) | untag4(a[3], tag8)))) break;
    }
#pragma unroll
    for (int i = 0; i < 2; ++i) {
      const u32x4 a0 = a[2 * i], a1 = a[2 * i + 1];
      const float v0 = tr32(asf(a0.x), asf(a1.x)), v1 = tr32(asf(a0.y), asf(a1.y)), v2 = tr32(asf(a0.z), asf(a1.z)), v3 = tr32(asf(a0.w), asf(a1.w));
      L[L_Q + (4 * kw + 2 * i + (lane >> 5)) * 32 + 4 * (lane & 7) + 2 * ((lane >> 3) & 1) + ((lane >> 4) & 1)] = pkbf(tr16(v0, v2), tr16(v1, v3));
    }
    flag_set(L + F_Q + kw);
  } else if (kw == 2) {
    u32 h0 = 0u, h1 = 0u;
    if (mlp) {
      const int go = GP_OFF + (cb2 + 28 * rr) * GP_S + ke * 128 + c8 * 16;
      u32x4 a;
      flag_wait<2, 1>(L + F_Q, lane);
      for (u32 s = 0; s < SPIN_MEM; ++s) {
        CBAR();
        a = ld128<AUX_SC1>(rws, go);
        if (all_ok(untag4(a, tag8))) break;
        __builtin_amdgcn_s_sleep(16);
      }
      float hv[4];
#pragma unroll
      for (int k = 0; k < 4; ++k) {
        float g = asf(a[k]);
        g += dpp<ROR8>(g); g = tr16(g, g); g = tr32(g, g);
        g = rbf(g);
        const float uu = dpp<ROR4>(g);
        const float z = 0.7978845608028654f * (g + 0.044715f * g * g * g);
        const float th = 1.f - 2.f * __builtin_amdgcn_rcpf(1.f + __builtin_amdgcn_exp2f(z * 2.8853900817779268f));
        hv[k] = rbf(0.5f * g * (1.f + th)) * uu;
      }
      h0 = pkbf(hv[0], hv[1]); h1 = pkbf(hv[2], hv[3]);
    }
    if (lane < 4) {
      *(LAS u32x2*)(L + L_H + (2 * (lane & 1)) * 4 + (lane >> 1) * 2) = u32x2{h0, SET};
      *(LAS u32x2*)(L + L_H + (2 * (lane & 1) + 1) * 4 + (lane >> 1) * 2) = u32x2{h1, SET};
    }
    CBAR();
  } else {
    pace<T_ORG>(org);
    const u32x2 o = __builtin_amdgcn_raw_buffer_load_b64(rws, OR_OFF + (lane & 7) * 8, 0, AUX_SC1);
    const bool ok = o.y == tag;
    const float n = row_sum(ok ? 1.f : 0.f), dsum = row_sum(ok ? (float)(int)(o.x - (u32)org) : 0.f);
    const float sh = n > 0.f ? fminf(fmaxf(dsum * __builtin_amdgcn_rcpf(n) + ORG_BIAS, -80.f), 80.f) : 0.f;
    L[L_SH] = (u32)(int)sh;
    flag_set(L + F_SH);
  }

  const int rg = lane >> 4, cl = lane & 15;
  const int co = kw * 128 + cl * 8;
  u32x4 wa[4], wb[2];
  flag_wait<4, 1>(L + F_VI, lane);
  {
    const rsrc_t ro = mkrs(wo, 4096 * D * 2), rd = mkrs(wd, mlp ? DF * D * 2 : 0);
    const int vo = co < D ? (h * 512 + j * 16 + 4 * rg) * (D * 2) + co * 2 : OOB;
    const int vd = co < D ? (64 * cb2 + 8 * ke + 2 * rg) * (D * 2) + co * 2 : OOB;
#pragma unroll
    for (int i = 0; i < 2; ++i) wb[i] = ld128<AUX_NT>(rd, vd, i * D * 2);
#pragma unroll
    for (int i = 0; i < 4; ++i) wa[i] = ld128<AUX_NT>(ro, vo, i * D * 2);
  }
  __builtin_amdgcn_sched_barrier(0);
  float a0[8], a1[8];
  u32 pa[8], pb[8];
  {
    u32x4 hh;
    for (u32 s = 0; s < SPIN_LDS; ++s) {
      CBAR();
      hh = *(LAS u32x4*)(L + L_H + rg * 4);
      if (none((hh.y != SET) | (hh.w != SET))) break;
      __builtin_amdgcn_s_sleep(1);
    }
#pragma unroll
    for (int k = 0; k < 4; ++k) {
      const u32 pl = __builtin_amdgcn_perm(wb[1][k], wb[0][k], 0x05040100u), ph = __builtin_amdgcn_perm(wb[1][k], wb[0][k], 0x07060302u);
      a0[2 * k] = dot2(pl, hh.x, 0.f); a0[2 * k + 1] = dot2(ph, hh.x, 0.f);
      a1[2 * k] = dot2(pl, hh.z, 0.f); a1[2 * k + 1] = dot2(ph, hh.z, 0.f);
    }
#pragma unroll
    for (int k = 0; k < 4; ++k) {
      pa[2 * k] = __builtin_amdgcn_perm(wa[1][k], wa[0][k], 0x05040100u); pa[2 * k + 1] = __builtin_amdgcn_perm(wa[1][k], wa[0][k], 0x07060302u);
      pb[2 * k] = __builtin_amdgcn_perm(wa[3][k], wa[2][k], 0x05040100u); pb[2 * k + 1] = __builtin_amdgcn_perm(wa[3][k], wa[2][k], 0x07060302u);
    }
  }
#pragma unroll
  for (int k = 0; k < 8; ++k) asm volatile("" : "+v"(a0[k]), "+v"(a1[k]), "+v"(pa[k]), "+v"(pb[k]));
  int o2o = co < D ? O2_OFF + (h * 32 + j) * O2_S + (lane >> 5) * 1792 + kw * 512 + (((lane >> 4) & 1) * (kw == 3 ? 8 : 16) + cl) * 16 : OOB;
  LAS u32x4* orp = (LAS u32x4*)(L + L_O + rg * 4);
  LAS u32* ofp = L + F_O;
  PIN(o2o); PIN(orp); PIN(ofp);
  {
    const u32x4 oo = data_wait(ofp, orp);
#pragma unroll
    for (int k = 0; k < 8; ++k) {
      a0[k] = dot2(pb[k], oo.y, dot2(pa[k], oo.x, a0[k]));
      a1[k] = dot2(pb[k], oo.w, dot2(pa[k], oo.z, a1[k]));
    }
    float t[8];
#pragma unroll
    for (int k = 0; k < 8; ++k) t[k] = tr32(a0[k], a1[k]);
    st128(rws, o2o, tag4(tr16(t[0], t[4]), tr16(t[1], t[5]), tr16(t[2], t[6]), tr16(t[3], t[7]), tag8));
  }
  flag_set(L + F_O2 + kw);
}

extern "C" void kernel_launch(void* const* d_in, const int* in_sizes, int n_in, void* d_out, int out_size, void* d_ws, size_t ws_size,
                              hipStream_t stream) {
  fused_decode<<<256, 768, 0, stream>>>((const u16*)d_in[0], (const u16*)d_in[3], (const u16*)d_in[6], (const u16*)d_in[7], (const u16*)d_in[1],
                                        (char*)d_ws, (const u16*)d_in[2], (const u16*)d_in[4], (const u16*)d_in[8], (u16*)d_out);
}
```

```cpp
#include <hip/hip_runtime.h>
#include <stdint.h>

typedef unsigned int u32;
typedef unsigned short u16;
typedef unsigned long long u64;
typedef u32 u32x2 __attribute__((ext_vector_type(2)));
typedef u32 u32x4 __attribute__((ext_vector_type(4)));
typedef float f32x4 __attribute__((ext_vector_type(4)));
typedef float f32x16 __attribute__((ext_vector_type(16)));
typedef __bf16 bf16x2_t __attribute__((ext_vector_type(2)));
typedef __bf16 bf16x8_t __attribute__((ext_vector_type(8)));
#define LAS __attribute__((address_space(3)))
typedef __amdgpu_buffer_rsrc_t rsrc_t;

namespace {
constexpr int D = 448, SK = 32768, W = 4096, DH = 256, DF = 1792;
constexpr int AUX_NT = 2, AUX_SC1 = 16;
constexpr int OOB = 1 << 30;
constexpr u32 TMASK = 0xfu;

constexpr int QP_S = 768, GP_S = 1280, O1_S = 2304, O2_S = 3840;
constexpr int EP_OFF = 0;
constexpr int OR_OFF = 2048;
constexpr int ST_OFF = 4096;
constexpr int QP_OFF = ST_OFF + 8192;
constexpr int GP_OFF = QP_OFF + 256 * QP_S;
constexpr int O1_OFF = GP_OFF + 256 * GP_S;
constexpr int O2_OFF = O1_OFF + 256 * O1_S;
constexpr int WS_BYTES = O2_OFF + 256 * O2_S;

constexpr int L_XN = 0;
constexpr int L_QX = L_XN + 480;
constexpr int L_GX = L_QX + 128;
constexpr int L_Q = L_GX + 256;
constexpr int L_P = L_Q + 256;
constexpr int L_OP = L_P + 512;
constexpr int L_WM = L_OP + 2048;
constexpr int L_WL = L_WM + 8;
constexpr int L_H = L_WL + 8;
constexpr int L_O = L_H + 16;
constexpr int F_O = L_O + 16;
constexpr int L_R = L_O + 32;
constexpr int F_P = L_R + 128;
constexpr int F_VI = F_P + 4;
constexpr int F_O2 = F_VI + 4;
constexpr int F_Q = F_O2 + 4;
constexpr int F_QX = F_Q + 2, F_PUB = F_QX + 1;
constexpr int F_V = F_PUB + 1;
constexpr int F_R = F_V + 4;
constexpr int L_TAG = F_R + 8;
constexpr int L_SH = L_TAG + 2, F_SH = L_TAG + 3;
constexpr int L_END = L_TAG + 4;
constexpr u32 SET = 1u;
static_assert((L_END - L_WM) % 4 == 0 && (L_END - L_WM) / 4 <= 64, "one 16-byte store per lane clears the flag area");

constexpr int T_K0 = 25, T_KSTEP10 = 615, T_VSTEP10 = 635;
constexpr int K_FREE = 2;
constexpr int V_FINE = 4, V_FINE_LEAD = 25;
constexpr int WINDOW = 6;
constexpr int T_ORG = 300, ORG_BIAS = 5;
constexpr int due_k(int g) { return T_K0 + g * T_KSTEP10 / 10; }
constexpr int due_v(int g, int half) { return T_K0 + (8 * T_KSTEP10 + g * T_VSTEP10 + half * T_VSTEP10 / 2) / 10 - (g >= V_FINE ? V_FINE_LEAD : 0); }
constexpr int Q_DELAY = 40, O1_DELAY = 8, O2_DELAY = 7;
constexpr int ROT = 70;
constexpr int CODE_SPAN = 15104;
constexpr u32 SPIN_LDS = 1u << 21, SPIN_MEM = 1u << 16;

__device__ __forceinline__ float asf(u32 v) { return __builtin_bit_cast(float, v); }
__device__ __forceinline__ u32 asu(float v) { return __builtin_bit_cast(u32, v); }
__device__ __forceinline__ float lo16(u32 v) { return asf(v << 16); }
__device__ __forceinline__ float hi16(u32 v) { return asf(v & 0xffff0000u); }
__device__ __forceinline__ u32 pkbf(float a, float b) {
  bf16x2_t r = {(__bf16)a, (__bf16)b};
  return __builtin_bit_cast(u32, r);
}
__device__ __forceinline__ float rbf(float a) { return (float)(__bf16)a; }
__device__ __forceinline__ float dot2(u32 a, u32 b, float c) {
  return __builtin_amdgcn_fdot2_f32_bf16(__builtin_bit_cast(bf16x2_t, a), __builtin_bit_cast(bf16x2_t, b), c, false);
}
template <int CTRL> __device__ __forceinline__ float dpp(float x) {
  return asf(__builtin_amdgcn_mov_dpp((int)asu(x), CTRL, 0xf, 0xf, true));
}
constexpr int XOR1 = 0xB1, XOR2 = 0x4E, ROR8 = 0x128, ROR4 = 0x124;
__device__ __forceinline__ float tr32(float a, float b) {
  auto r = __builtin_amdgcn_permlane32_swap(asu(a), asu(b), false, false);
  return asf(r[0]) + asf(r[1]);
}
__device__ __forceinline__ float tr16(float a, float b) {
  auto r = __builtin_amdgcn_permlane16_swap(asu(a), asu(b), false, false);
  return asf(r[0]) + asf(r[1]);
}
__device__ __forceinline__ float mx32(float a) {
  auto r = __builtin_amdgcn_permlane32_swap(asu(a), asu(a), false, false);
  return fmaxf(asf(r[0]), asf(r[1]));
}
__device__ __forceinline__ float mx16(float a) {
  auto r = __builtin_amdgcn_permlane16_swap(asu(a), asu(a), false, false);
  return fmaxf(asf(r[0]), asf(r[1]));
}
__device__ __forceinline__ float row_sum(float v) { v += dpp<ROR8>(v); v += dpp<ROR4>(v); v += dpp<XOR2>(v); v += dpp<XOR1>(v); return v; }
__device__ __forceinline__ float row_max(float v) {
  v = fmaxf(v, dpp<ROR8>(v)); v = fmaxf(v, dpp<ROR4>(v)); v = fmaxf(v, dpp<XOR2>(v)); v = fmaxf(v, dpp<XOR1>(v)); return v;
}
__device__ __forceinline__ float wave_sum(float v) { v = row_sum(v); v = tr16(v, v); return tr32(v, v); }

__device__ __forceinline__ rsrc_t mkrs(const void* p, u32 bytes) { return __builtin_amdgcn_make_buffer_rsrc((void*)p, 0, bytes, 0x00020000); }
template <int AUX> __device__ __forceinline__ u32x4 ld128(rsrc_t r, int voff, int soff = 0) { return __builtin_amdgcn_raw_buffer_load_b128(r, voff, soff, AUX); }
template <int AUX> __device__ __forceinline__ u32 ld32(rsrc_t r, int voff) { return __builtin_amdgcn_raw_buffer_load_b32(r, voff, 0, AUX); }
__device__ __forceinline__ void st128(rsrc_t r, int voff, u32x4 v) { __builtin_amdgcn_raw_buffer_store_b128(v, r, voff, 0, AUX_SC1); }
__device__ __forceinline__ u32 tagw(float v, u32 tag4) { return (asu(v) & ~TMASK) | tag4; }
__device__ __forceinline__ u32x4 tag4(float a, float b, float c, float d, u32 t) { return u32x4{tagw(a, t), tagw(b, t), tagw(c, t), tagw(d, t)}; }
__device__ __forceinline__ u32 untag4(u32x4& v, u32 t) { v.x ^= t; v.y ^= t; v.z ^= t; v.w ^= t; return v.x | v.y | v.z | v.w; }
__device__ __forceinline__ bool none(bool bad) { return __builtin_amdgcn_ballot_w64(bad) == 0ull; }
__device__ __forceinline__ bool all_ok(u32 bad) { return none((bad & TMASK) != 0u); }

#define CBAR() asm volatile("" ::: "memory")
#define PIN(v) asm volatile("" : "+v"(v))
#define LGKM0() asm volatile("s_waitcnt lgkmcnt(0)" ::: "memory")
template <int N, int SLEEP> __device__ __forceinline__ void flag_wait(LAS u32* p, int lane) {
  u32 v, cnt;
  const u32 addr = (u32)(uintptr_t)(p + (lane & (N - 1)));
  asm volatile("s_mov_b32 %1, %4\n"
               "1:\n\t"
               "ds_read_b32 %0, %2\n\t"
               "s_waitcnt lgkmcnt(0)\n\t"
               "v_cmp_ne_u32 vcc, %3, %0\n\t"
               "s_cbranch_vccz 2f\n\t"
               "s_sleep %5\n\t"
               "s_sub_u32 %1, %1, 1\n\t"
               "s_cbranch_scc0 1b\n"
               "2:"
               : "=&v"(v), "=&s"(cnt) : "v"(addr), "i"(SET), "i"(SPIN_LDS), "i"(SLEEP) : "vcc", "scc", "memory");
}
template <int DUE> __device__ __forceinline__ void pace(u64 org) {
  asm volatile("1:\n\t"
               "s_memrealtime vcc\n\t"
               "s_waitcnt lgkmcnt(0)\n\t"
               "s_sub_u32 vcc_lo, vcc_lo, %0\n\t"
               "s_cmp_lt_i32 vcc_lo, %1\n\t"
               "s_cbranch_scc0 2f\n\t"
               "s_sleep 1\n\t"
               "s_branch 1b\n"
               "2:"
               :: "s"((u32)org), "i"(DUE), "s"(org) : "vcc", "scc", "memory");
}
__device__ __forceinline__ void flag_set(LAS u32* p, u32 set = SET) { CBAR(); *p = set; CBAR(); }

__device__ __forceinline__ void poll2(rsrc_t rs, int voff, int soff, u32 t4, u32x4& r0, u32x4& r1) {
  u32 o0, o1, o2, o3, o4, o5, o6, o7, m, cnt;
  asm volatile("s_mov_b32 %9, %14\n"
               "1:\n\t"
               "buffer_load_dwordx4 v[144:147], %10, %11, 0 offen sc1\n\t"
               "buffer_load_dwordx4 v[148:151], %10, %11, %12 offen sc1\n\t"
               "s_sub_u32 %9, %9, 1\n\t"
               "s_waitcnt vmcnt(1)\n\t"
               "v_xor_b32 %0, %13, v144\n\tv_xor_b32 %1, %13, v145\n\tv_xor_b32 %2, %13, v146\n\tv_xor_b32 %3, %13, v147\n\t"
               "v_or3_b32 %8, %0, %1, %2\n\t"
               "s_waitcnt vmcnt(0)\n\t"
               "v_xor_b32 %4, %13, v148\n\tv_xor_b32 %5, %13, v149\n\tv_xor_b32 %6, %13, v150\n\tv_xor_b32 %7, %13, v151\n\t"
               "v_or3_b32 %8, %8, %3, %4\n\t"
               "v_or3_b32 %8, %8, %5, %6\n\t"
               "v_bitop3_b32 %8, %8, 15, %7 bitop3:0xc8\n\t"
               "v_cmp_ne_u32 vcc, 0, %8\n\t"
               "s_cbranch_vccz 2f\n\t"
               "s_cbranch_scc0 1b\n"
               "2:"
               : "=&v"(o0), "=&v"(o1), "=&v"(o2), "=&v"(o3), "=&v"(o4), "=&v"(o5), "=&v"(o6), "=&v"(o7), "=&v"(m), "=&s"(cnt)
               : "v"(voff), "s"(rs), "s"(soff), "s"(t4), "i"(SPIN_MEM)
               : "vcc", "scc", "memory", "v144", "v145", "v146", "v147", "v148", "v149", "v150", "v151");
  r0 = u32x4{o0, o1, o2, o3};
  r1 = u32x4{o4, o5, o6, o7};
}
__device__ __forceinline__ u32x4 data_wait(LAS u32* flag, LAS u32x4* p) {
  u32x4 r;
  u32 v, cnt;
  asm volatile("s_mov_b32 %2, %5\n"
               "1:\n\t"
               "ds_read_b32 %1, %3\n\t"
               "ds_read_b128 %0, %4\n\t"
               "s_sub_u32 %2, %2, 1\n\t"
               "s_waitcnt lgkmcnt(0)\n\t"
               "v_cmp_ne_u32 vcc, 1, %1\n\t"
               "s_cbranch_vccz 2f\n\t"
               "s_cbranch_scc0 1b\n"
               "2:"
               : "=&v"(r), "=&v"(v), "=&s"(cnt) : "v"((u32)(uintptr_t)flag), "v"((u32)(uintptr_t)p), "i"(SPIN_LDS) : "vcc", "scc", "memory");
  return r;
}

__device__ __forceinline__ void final_reduce(LAS u32* L, rsrc_t rws, const u16* x, u16* out, int rid, int grp, int lane, u32 tag) {
  LAS float* const LF = (LAS float*)L;
  __builtin_amdgcn_s_setprio(3);
  const u32 tag8 = tag & TMASK;
  const int rb = rid / 28, cb = rid % 28, qd = lane & 3;
  const int pi = cb & 7, ph = cb < 24 ? pi >> 2 : pi >> 1, pg = cb < 24 ? pi & 3 : pi & 1;
  const int col = rb * D + 128 * (cb >> 3) + 8 * (4 * pg + ((lane >> 2) & 3)) + 4 * ph + (lane & 3);
  float xres = lo16(x[col]);
  int oo = O2_OFF + (grp * 32 + (lane >> 2)) * O2_S + rb * 1792 + cb * 64 + qd * 16;
  LAS float* wp = LF + ((lane & 12) == 0 ? L_R + (4 * qd + (lane >> 4)) * 8 + grp : L_XN + 128 + lane);
  LAS u32* fp = L + F_R + grp;
  LAS u32* fr = L + F_R + (lane & 7);
  LAS f32x4* rp = (LAS f32x4*)(LF + L_R + (lane & 15) * 8);
  int so = lane < 16 ? col * 2 : OOB;
  u32 setv = SET;
  rsrc_t ro = mkrs(out, 2 * D * 2);
  asm volatile("" : "+s"(ro));
  PIN(xres); PIN(oo); PIN(wp); PIN(fp); PIN(fr); PIN(rp); PIN(so); PIN(setv);
  flag_wait<1, 1>(L + F_PUB, lane);
  flag_wait<4, 0>(L + F_O2, lane);
  __builtin_amdgcn_s_sleep(O2_DELAY);
  u32x4 a0, a1;
  poll2(rws, oo, 16 * O2_S, tag8, a0, a1);
  float f = tr16(tr32(asf(a0.x) + asf(a1.x), asf(a0.z) + asf(a1.z)), tr32(asf(a0.y) + asf(a1.y), asf(a0.w) + asf(a1.w)));
  f += dpp<ROR8>(f); f += dpp<ROR4>(f);
  *wp = f;
  flag_set(fp, setv);
  u32 fl = *fr;
  CBAR();
  const f32x4 r0 = rp[0], r1 = rp[1];
  const f32x4 rs4 = r0 + r1;
  const float tot = xres + ((rs4.x + rs4.z) + (rs4.y + rs4.w));
  u32 res = pkbf(tot, tot);
  PIN(res);
  if (none(fl != SET)) {
    __builtin_amdgcn_raw_buffer_store_b16((u16)res, ro, so, 0, 0);
    if (rid == 0 && lane == 0) __builtin_amdgcn_raw_buffer_store_b32(tag, rws, EP_OFF, 0, AUX_SC1);
  }
}

}

extern "C" __global__ void __launch_bounds__(768, 3)
fused_decode(const u16* __restrict__ x, const u16* __restrict__ wq, const u16* __restrict__ wg_, const u16* __restrict__ wu,
             const u16* __restrict__ kc, char* __restrict__ ws, const u16* __restrict__ vc, const u16* __restrict__ wo,
             const u16* __restrict__ wd, u16* __restrict__ out) {
  __shared__ __attribute__((aligned(16))) u32 smem[L_END];
  LAS u32* const L = (LAS u32*)smem;
  LAS float* const LF = (LAS float*)smem;
  const int wg = blockIdx.x, h = wg & 7, j = wg >> 3, b = j & 1, sp = j >> 1, reg = wg & 15;
  const int w = __builtin_amdgcn_readfirstlane(threadIdx.x >> 6), lane = threadIdx.x & 63, kw = w & 3;
  asm volatile("s_load_dword s94, %0, 0x0\n\ts_load_dword s95, %0, 0x40" :: "s"(__builtin_amdgcn_kernarg_segment_ptr()) : "s94", "s95");
  u64 pc0;
  asm volatile("s_getpc_b64 %0" : "=s"(pc0));
  const u32 code_touch = ld32<0>(mkrs((const char*)(pc0 & ~255ull), w == 0 ? CODE_SPAN : 0), lane < 5 ? j * 640 + lane * 128 : OOB);
  __builtin_amdgcn_sched_barrier(0);
#define ROLE_ENTRY() const rsrc_t rws = mkrs(ws, WS_BYTES)
#define ROLE_TAG() const u32 tag = __builtin_amdgcn_readfirstlane(L[L_TAG]), tag8 = tag & TMASK
  const u32 blk0 = sp * 4 + reg * ROT;
#define BLOCK_OFF(slot) (((blk0 + (((slot) >> 2) << 6) + ((slot) & 3u)) & 511u) << 12)
  const int hx = h ^ 4;
  u64 org;
#define STREAM_GROUP(G, VOFF, SLOT, ISK) \
  if (!(ISK) || (G) >= K_FREE) pace<(ISK) ? due_k(G) : due_v(G, 0)>(org); \
  if (4 * (G) > WINDOW) asm volatile("s_waitcnt vmcnt(%0)" :: "i"(WINDOW) : "memory"); \
  _Pragma("unroll") for (int i = 4 * (G); i < 4 * (G) + 2; ++i) d[i] = ld128<AUX_NT>(rkv, (VOFF), (int)BLOCK_OFF(SLOT)); \
  if (!(ISK) && (G) >= V_FINE) { __builtin_amdgcn_sched_barrier(0); pace<due_v(G, 1)>(org); } \
  _Pragma("unroll") for (int i = 4 * (G) + 2; i < 4 * (G) + 4; ++i) d[i] = ld128<AUX_NT>(rkv, (VOFF), (int)BLOCK_OFF(SLOT)); \
  __builtin_amdgcn_sched_barrier(0);
#define STREAM_LOADS(VOFF, SLOT, ISK) \
  STREAM_GROUP(0, VOFF, SLOT, ISK) STREAM_GROUP(1, VOFF, SLOT, ISK) STREAM_GROUP(2, VOFF, SLOT, ISK) STREAM_GROUP(3, VOFF, SLOT, ISK) \
  STREAM_GROUP(4, VOFF, SLOT, ISK) STREAM_GROUP(5, VOFF, SLOT, ISK) STREAM_GROUP(6, VOFF, SLOT, ISK) STREAM_GROUP(7, VOFF, SLOT, ISK)
#define WINDOW_RSRC(SRC) mkrs((SRC) + ((size_t)reg * SK + (SK - W)) * DH, W * DH * 2)

  if (__builtin_expect(w >= 4, 0)) {
  u32 tag_out;
  if (w < 8) {
    __builtin_amdgcn_s_setprio(2);
    const int g = lane >> 4, key = (lane >> 1) & 7;
    const bool hb = lane & 1;
    const int kvo = key * 512 + (2 * g + (lane & 1)) * 16;
    const rsrc_t rkv = WINDOW_RSRC(kc);
    u32x4 d[32];
    __builtin_amdgcn_sched_barrier(0);
    __builtin_amdgcn_s_barrier();
    org = __builtin_amdgcn_s_memrealtime();
    __builtin_amdgcn_sched_barrier(0);
    STREAM_LOADS(kvo + (i & 3) * 128, kw + 4 * (i >> 2), true)
    ROLE_ENTRY();
    flag_wait<2, 1>(L + F_Q, lane);
    ROLE_TAG();
    u32x4 qa[4];
#pragma unroll
    for (int i = 0; i < 4; ++i) qa[i] = *(LAS u32x4*)(L + L_Q + (lane & 1) * 128 + i * 32 + (2 * g + ((lane >> 1) & 1)) * 4);
    f32x4 ea, eb;
#pragma unroll
    for (int n = 0; n < 4; ++n) {
      f32x4 ca = {}, cb = {};
#pragma unroll
      for (int i = 0; i < 4; ++i) {
        ca = __builtin_amdgcn_mfma_f32_16x16x32_bf16(__builtin_bit_cast(bf16x8_t, qa[i]), __builtin_bit_cast(bf16x8_t, d[8 * n + i]), ca, 0, 0, 0);
        cb = __builtin_amdgcn_mfma_f32_16x16x32_bf16(__builtin_bit_cast(bf16x8_t, qa[i]), __builtin_bit_cast(bf16x8_t, d[8 * n + 4 + i]), cb, 0, 0, 0);
      }
#pragma unroll
      for (int k = 0; k < 4; ++k) { ea[k] = (n == 0 || g == n) ? ca[k] : ea[k]; eb[k] = (n == 0 || g == n) ? cb[k] : eb[k]; }
    }
    asm volatile("" :: "v"(code_touch));
    float sc[2];
#pragma unroll
    for (int hd = 0; hd < 2; ++hd) sc[hd] = rbf((hb ? eb[2 + hd] : ea[hd]) + dpp<XOR1>(hb ? ea[2 + hd] : eb[hd])) * 0.0625f;
    const float wm0 = mx32(mx16(row_max(sc[0]))), wm1 = mx32(mx16(row_max(sc[1])));
    CBAR();
    if (lane < 2) L[L_WM + kw * 2 + lane] = (asu(lane ? wm1 : wm0) & 0xffff0000u) | SET;
    u32x4 ma, mb;
    for (u32 s = 0; s < SPIN_LDS; ++s) {
      CBAR();
      ma = *(LAS u32x4*)(L + L_WM) ^ SET; mb = *(LAS u32x4*)(L + L_WM + 4) ^ SET;
      if (none((((ma.x | ma.y | ma.z | ma.w) | (mb.x | mb.y | mb.z | mb.w)) & 0xffffu) != 0u)) break;
    }
    const float M0 = fmaxf(fmaxf(asf(ma.x), asf(ma.z)), fmaxf(asf(mb.x), asf(mb.z)));
    const float M1 = fmaxf(fmaxf(asf(ma.y), asf(ma.w)), fmaxf(asf(mb.y), asf(mb.w)));
    const float p0 = rbf(__builtin_amdgcn_exp2f((sc[0] - M0) * 1.4426950408889634f));
    const float p1 = rbf(__builtin_amdgcn_exp2f((sc[1] - M1) * 1.4426950408889634f));
    LAS u32* pp = L + L_P + key * 64 + kw + 4 * (2 * g + (lane & 1));
    pp[0] = asu(p0) | (asu(p0) >> 16);
    pp[32] = asu(p1) | (asu(p1) >> 16);
    float ls = tr32(p0, p1);
    ls = row_sum(tr16(ls, ls));
    if ((lane & 31) == 0) LF[L_WL + kw * 2 + (lane >> 5)] = ls;
    flag_set(L + F_P + kw);

    if (kw == 0) {
      flag_wait<4, 0>(L + F_P, lane);
      if (lane < 2) {
        const float Ls = (LF[L_WL + lane] + LF[L_WL + 2 + lane]) + (LF[L_WL + 4 + lane] + LF[L_WL + 6 + lane]);
        st128(rws, ST_OFF + (((h * 2 + lane) * 2 + b) * 16 + sp) * 16, u32x4{asu(lane ? M1 : M0), tag, asu(Ls), tag});
      }
    } else if (kw == 1) {
      __builtin_amdgcn_s_setprio(3);
      const int s = lane >> 2, qd = lane & 3;
      const int so = ST_OFF + ((hx * 2 + (j >> 4)) * 32 + s) * 16;
      const int oo = O1_OFF + (hx * 32 + 2 * s) * O1_S + j * 64 + qd * 16;
      u32x4 s0, s1, a0, a1;
      flag_wait<4, 1>(L + F_VI, lane);
      for (u32 it = 0; it < SPIN_MEM; ++it) {
        CBAR();
        s0 = ld128<AUX_SC1>(rws, so); s1 = ld128<AUX_SC1>(rws, so + 256);
        if (none((s0.y != tag) | (s0.w != tag) | (s1.y != tag) | (s1.w != tag))) break;
        flag_wait<1, 1>(L + F_PUB, lane);
      }
      float wgt[2];
#pragma unroll
      for (int bb = 0; bb < 2; ++bb) {
        const float m = asf(bb ? s1.x : s0.x), l = asf(bb ? s1.z : s0.z);
        float mg = fmaxf(m, dpp<ROR8>(m)); mg = fmaxf(mg, dpp<ROR4>(mg)); mg = mx32(mx16(mg));
        const float e = __builtin_amdgcn_exp2f((m - mg) * 1.4426950408889634f);
        float lg = e * l; lg += dpp<ROR8>(lg); lg += dpp<ROR4>(lg); lg = tr16(lg, lg); lg = tr32(lg, lg);
        wgt[bb] = e * __builtin_amdgcn_rcpf(lg);
      }
      LAS u32* ow = L + ((lane & 12) == 0 ? L_O + qd * 4 + (lane >> 4) : L_XN + lane);
      LAS u32* of = L + F_O;
      u32 setv = SET;
      PIN(wgt[0]); PIN(wgt[1]); PIN(ow); PIN(of); PIN(setv);
      flag_wait<1, 0>(L + F_PUB, lane);
      __builtin_amdgcn_s_sleep(O1_DELAY);
      poll2(rws, oo, O1_S, tag8, a0, a1);
      f32x4 m0 = __builtin_bit_cast(f32x4, a0) * wgt[0], m1 = __builtin_bit_cast(f32x4, a1) * wgt[1];
      PIN(m0); PIN(m1);
      const float v0 = tr32(m0.x, m1.x), v2 = tr32(m0.z, m1.z), v1 = tr32(m0.y, m1.y), v3 = tr32(m0.w, m1.w);
      float e0 = tr16(v0, v2), e1 = tr16(v1, v3);
      e0 += dpp<ROR8>(e0); e1 += dpp<ROR8>(e1);
      e0 += dpp<ROR4>(e0); e1 += dpp<ROR4>(e1);
      *ow = pkbf(e0, e1);
      flag_set(of, setv);
    }
    tag_out = tag;
  } else {
    if (kw == 0 && lane < (L_END - L_WM) / 4) *(LAS u32x4*)(L + L_WM + 4 * lane) = u32x4{0u, 0u, 0u, 0u};
    LGKM0();
    __builtin_amdgcn_s_barrier();
    org = __builtin_amdgcn_s_memrealtime();
    __builtin_amdgcn_s_setprio(1);
    __builtin_amdgcn_sched_barrier(0);
    ROLE_ENTRY();
    flag_wait<1, 4>(L + F_SH, lane);
    org += (u64)(long long)(int)__builtin_amdgcn_readfirstlane(L[L_SH]);
    const rsrc_t rkv = WINDOW_RSRC(vc);
    u32x4 d[32];
    STREAM_LOADS(lane * 16 + kw * 1024, (u32)i, false)
    flag_set(L + F_VI + kw);
    const int m = lane & 31, hf = lane >> 5;
    u32x4 msk;
#pragma unroll
    for (int r = 0; r < 4; ++r) msk[r] = (m < 16 && ((m & 7) >> 1) == r) ? ((m & 1) ? 0xffff0000u : 0x0000ffffu) : 0u;
    LAS f32x4* op = (LAS f32x4*)(LF + L_OP) + kw * 128 + 2 * m + hf;
    LAS f32x4* rp = (LAS f32x4*)(LF + L_OP) + lane;
    LAS u32* fp = L + F_V + kw;
    LAS u32* fr = L + F_V + (lane & 3);
    int o1o = O1_OFF + (h * 32 + j) * O1_S + lane * 16;
    PIN(op); PIN(rp); PIN(fp); PIN(fr); PIN(o1o);
    flag_wait<4, 1>(L + F_P, lane);
    ROLE_TAG();
    const LAS u32x4* pp = (const LAS u32x4*)(L + L_P + ((kw * 2 + hf) * 2 + ((m >> 3) & 1)) * 32);
    f32x16 acc = {};
#pragma unroll
    for (int g = 0; g < 8; ++g) {
      const u32x4 p4 = pp[g];
#pragma unroll
      for (int k = 0; k < 4; ++k) {
        const u32x4 a = msk & p4[k];
        acc = __builtin_amdgcn_mfma_f32_32x32x16_bf16(__builtin_bit_cast(bf16x8_t, a), __builtin_bit_cast(bf16x8_t, d[4 * g + k]), acc, 0, 0, 0);
      }
      __builtin_amdgcn_sched_barrier(0);
    }
    asm volatile("" :: "v"(code_touch));
    op[0] = f32x4{acc[0], acc[1], acc[2], acc[3]};
    op[64] = f32x4{acc[4], acc[5], acc[6], acc[7]};
    flag_set(fp);
    const u32 fl = *fr;
    CBAR();
    const f32x4 o0 = (rp[0] + rp[128]) + (rp[256] + rp[384]);
    const f32x4 o1 = (rp[64] + rp[192]) + (rp[320] + rp[448]);
    u32x4 t0 = tag4(o0.x, o0.y, o0.z, o0.w, tag8), t1 = tag4(o1.x, o1.y, o1.z, o1.w, tag8);
    PIN(t0); PIN(t1);
    if (none(fl != SET)) {
      st128(rws, o1o, t0);
      st128(rws, o1o + 1024, t1);
      flag_set(L + F_PUB);
    }
    tag_out = tag;
  }
  if (!(h & 1) && j < 14) final_reduce(L, mkrs(ws, WS_BYTES), x, out, (h >> 1) * 14 + j, w - 4, lane, tag_out);
  return;
  }

  __builtin_amdgcn_s_setprio(3);
  ROLE_ENTRY();
  const bool mlp = wg < 224;
  const int cb2 = wg % 28, ke = wg / 28;
  const int rr = lane >> 3, c8 = lane & 7;
  const rsrc_t rx = mkrs((const u16*)x, 2 * D * 2);
  const u32x4 xa = ld128<0>(rx, lane < 56 ? lane * 16 : OOB), xb = ld128<0>(rx, lane < 56 ? D * 2 + lane * 16 : OOB);
  const u32 epoch = ld32<AUX_SC1>(rws, EP_OFF);
  const int row0 = kw < 2 ? 112 * (j >> 3) + 64 * kw : 56 * ke;
  const int nrow = kw == 0 ? 64 : kw == 1 ? 48 : mlp ? 56 : 0;
  u32x4 wv[8];
  {
    const rsrc_t rw = mkrs(kw < 2 ? wq : kw == 2 ? wg_ : wu, kw < 2 ? D * 4096 * 2 : D * DF * 2);
    const int rs = kw < 2 ? 8192 : DF * 2;
    const int vw0 = nrow ? (row0 + 2 * rr) * rs + (kw < 2 ? (hx * 512 + (j & 7) * 64) * 2 : cb2 * 128) + c8 * 16 : OOB;
    const int vw3 = 48 + 2 * rr < nrow ? vw0 : OOB;
#pragma unroll
    for (int i = 0; i < 8; ++i) wv[i] = ld128<AUX_NT>(rw, i < 6 ? vw0 : vw3, (16 * (i >> 1) + (i & 1)) * rs);
  }
  const int eo = lane < 8 ? QP_OFF + (hx * 32 + j) * QP_S + lane * 64 : lane < 24 ? GP_OFF + wg * GP_S + (lane - 8) * 64 : ST_OFF + (wg & 127) * 64;
  const u32 box_touch = ld32<AUX_SC1>(rws, (kw == 1 && lane < 25) ? eo : OOB);
  __builtin_amdgcn_sched_barrier(0);
  __builtin_amdgcn_s_barrier();
  org = __builtin_amdgcn_s_memrealtime();
  __builtin_amdgcn_sched_barrier(0);

#pragma unroll
  for (int bb = 0; bb < 2; ++bb) {
    const u32x4 xv = bb ? xb : xa;
    float v[8];
#pragma unroll
    for (int k = 0; k < 4; ++k) { v[2 * k] = lo16(xv[k]); v[2 * k + 1] = hi16(xv[k]); }
    float s = 0.f;
#pragma unroll
    for (int k = 0; k < 8; ++k) s += v[k];
    const float mean = wave_sum(s) * (1.f / D);
    float q = 0.f;
#pragma unroll
    for (int k = 0; k < 8; ++k) { const float t = v[k] - mean; q += t * t; }
    q = lane < 56 ? q : 0.f;
    const float var = rbf(wave_sum(q) * (1.f / D));
    const float den = rbf(__builtin_amdgcn_sqrtf(rbf(var + 1e-5f))), rden = __builtin_amdgcn_rcpf(den), mb = rbf(mean);
#pragma unroll
    for (int k = 0; k < 8; ++k) { const float c = rbf(v[k] - mb), r = c * rden; v[k] = fmaf(fmaf(-r, den, c), rden, r); }
    u32x4 o;
#pragma unroll
    for (int k = 0; k < 4; ++k) o[k] = pkbf(v[2 * k], v[2 * k + 1]);
    if (lane < 56) *(LAS u32x4*)(L + L_XN + bb * 224 + lane * 4) = o;
    else if (bb == 1) *(LAS u32x4*)(L + L_XN + 224 + lane * 4) = u32x4{0u, 0u, 0u, 0u};
  }
  asm volatile("" :: "v"(code_touch));
  const u32 tag = __builtin_amdgcn_readfirstlane(epoch) + 1u, tag8 = tag & TMASK;
  if (kw == 0) L[L_TAG] = tag;
  if (kw == 3 && wg < 8 && lane == 0) __builtin_amdgcn_raw_buffer_store_b64(u32x2{(u32)org, tag}, rws, OR_OFF + wg * 8, 0, AUX_SC1);

  {
    float a0[8] = {}, a1[8] = {};
#pragma unroll
    for (int k = 0; k < 4; ++k) {
      const u32 x0 = L[L_XN + row0 / 2 + rr + 8 * k], x1 = L[L_XN + 224 + row0 / 2 + rr + 8 * k];
#pragma unroll
      for (int c = 0; c < 4; ++c) {
        const u32 pl = __builtin_amdgcn_perm(wv[2 * k + 1][c], wv[2 * k][c], 0x05040100u), ph = __builtin_amdgcn_perm(wv[2 * k + 1][c], wv[2 * k][c], 0x07060302u);
        a0[2 * c] = dot2(pl, x0, a0[2 * c]); a0[2 * c + 1] = dot2(ph, x0, a0[2 * c + 1]);
        a1[2 * c] = dot2(pl, x1, a1[2 * c]); a1[2 * c + 1] = dot2(ph, x1, a1[2 * c + 1]);
      }
    }
    asm volatile("" :: "v"(box_touch));
    float t[8], u[4];
#pragma unroll
    for (int k = 0; k < 8; ++k) t[k] = tr32(a0[k], a1[k]);
#pragma unroll
    for (int k = 0; k < 4; ++k) { u[k] = tr16(t[k], t[k + 4]); u[k] += dpp<ROR8>(u[k]); }
    const int bb = lane >> 5, hf = (lane >> 4) & 1;
    LAS f32x4* qx = (LAS f32x4*)(LF + L_QX) + bb * 16 + c8 * 2 + hf;
    if (kw == 1) {
      if (!(lane & 8)) *qx = f32x4{u[0], u[1], u[2], u[3]};
      flag_set(L + F_QX);
    } else if (kw == 0) {
      flag_wait<1, 0>(L + F_QX, lane);
      const f32x4 o = *qx;
      if (!(lane & 8)) st128(rws, QP_OFF + (hx * 32 + j) * QP_S + bb * 256 + hf * 128 + c8 * 16, tag4(u[0] + o.x, u[1] + o.y, u[2] + o.z, u[3] + o.w, tag8));
    } else if (mlp) {
      LAS u32x4* gx = (LAS u32x4*)(L + L_GX) + (kw - 2) * 32;
      if (!(lane & 8)) gx[c8 * 4 + bb * 2 + hf] = tag4(u[0], u[1], u[2], u[3], tag8);
      CBAR();
      if (lane < 32) st128(rws, GP_OFF + wg * GP_S + (lane >> 2) * 128 + (kw - 2) * 64 + (lane & 3) * 16, gx[lane]);
    }
  }

  if (kw < 2) {
    __builtin_amdgcn_s_sleep(Q_DELAY);
    const int qo = QP_OFF + (h * 32 + 4 * kw + 8 * (lane >> 4)) * QP_S + b * 256 + (lane & 15) * 16;
    u32x4 a[4];
    for (u32 s = 0; s < SPIN_MEM; ++s) {
      CBAR();
#pragma unroll
      for (int i = 0; i < 4; ++i) a[i] = ld128<AUX_SC1>(rws, qo, i * QP_S);
      if (all_ok((untag4(a[0], tag8) | untag4(a[1], tag8)) | (untag4(a[2], tag8) | untag4(a[3], tag8)))) break;
    }
#pragma unroll
    for (int i = 0; i < 2; ++i) {
      const u32x4 a0 = a[2 * i], a1 = a[2 * i + 1];
      const float v0 = tr32(asf(a0.x), asf(a1.x)), v1 = tr32(asf(a0.y), asf(a1.y)), v2 = tr32(asf(a0.z), asf(a1.z)), v3 = tr32(asf(a0.w), asf(a1.w));
      L[L_Q + (4 * kw + 2 * i + (lane >> 5)) * 32 + 4 * (lane & 7) + 2 * ((lane >> 3) & 1) + ((lane >> 4) & 1)] = pkbf(tr16(v0, v2), tr16(v1, v3));
    }
    flag_set(L + F_Q + kw);
  } else if (kw == 2) {
    u32 h0 = 0u, h1 = 0u;
    if (mlp) {
      const int go = GP_OFF + (cb2 + 28 * rr) * GP_S + ke * 128 + c8 * 16;
      u32x4 a;
      flag_wait<2, 1>(L + F_Q, lane);
      for (u32 s = 0; s < SPIN_MEM; ++s) {
        CBAR();
        a = ld128<AUX_SC1>(rws, go);
        if (all_ok(untag4(a, tag8))) break;
        __builtin_amdgcn_s_sleep(16);
      }
      float hv[4];
#pragma unroll
      for (int k = 0; k < 4; ++k) {
        float g = asf(a[k]);
        g += dpp<ROR8>(g); g = tr16(g, g); g = tr32(g, g);
        g = rbf(g);
        const float uu = dpp<ROR4>(g);
        const float z = 0.7978845608028654f * (g + 0.044715f * g * g * g);
        const float th = 1.f - 2.f * __builtin_amdgcn_rcpf(1.f + __builtin_amdgcn_exp2f(z * 2.8853900817779268f));
        hv[k] = rbf(0.5f * g * (1.f + th)) * uu;
      }
      h0 = pkbf(hv[0], hv[1]); h1 = pkbf(hv[2], hv[3]);
    }
    if (lane < 4) {
      *(LAS u32x2*)(L + L_H + (2 * (lane & 1)) * 4 + (lane >> 1) * 2) = u32x2{h0, SET};
      *(LAS u32x2*)(L + L_H + (2 * (lane & 1) + 1) * 4 + (lane >> 1) * 2) = u32x2{h1, SET};
    }
    CBAR();
  } else {
    pace<T_ORG>(org);
    const u32x2 o = __builtin_amdgcn_raw_buffer_load_b64(rws, OR_OFF + (lane & 7) * 8, 0, AUX_SC1);
    const bool ok = o.y == tag;
    const float n = row_sum(ok ? 1.f : 0.f), dsum = row_sum(ok ? (float)(int)(o.x - (u32)org) : 0.f);
    const float sh = n > 0.f ? fminf(fmaxf(dsum * __builtin_amdgcn_rcpf(n) + ORG_BIAS, -80.f), 80.f) : 0.f;
    L[L_SH] = (u32)(int)sh;
    flag_set(L + F_SH);
  }

  const int rg = lane >> 4, cl = lane & 15;
  const int co = kw * 128 + cl * 8;
  u32x4 wa[4], wb[2];
  flag_wait<4, 1>(L + F_VI, lane);
  {
    const rsrc_t ro = mkrs(wo, 4096 * D * 2), rd = mkrs(wd, mlp ? DF * D * 2 : 0);
    const int vo = co < D ? (hx * 512 + j * 16 + 4 * rg) * (D * 2) + co * 2 : OOB;
    const int vd = co < D ? (64 * cb2 + 8 * ke + 2 * rg) * (D * 2) + co * 2 : OOB;
#pragma unroll
    for (int i = 0; i < 2; ++i) wb[i] = ld128<AUX_NT>(rd, vd, i * D * 2);
#pragma unroll
    for (int i = 0; i < 4; ++i) wa[i] = ld128<AUX_NT>(ro, vo, i * D * 2);
  }
  __builtin_amdgcn_sched_barrier(0);
  float a0[8], a1[8];
  u32 pa[8], pb[8];
  {
    u32x4 hh;
    for (u32 s = 0; s < SPIN_LDS; ++s) {
      CBAR();
      hh = *(LAS u32x4*)(L + L_H + rg * 4);
      if (none((hh.y != SET) | (hh.w != SET))) break;
      __builtin_amdgcn_s_sleep(1);
    }
#pragma unroll
    for (int k = 0; k < 4; ++k) {
      const u32 pl = __builtin_amdgcn_perm(wb[1][k], wb[0][k], 0x05040100u), ph = __builtin_amdgcn_perm(wb[1][k], wb[0][k], 0x07060302u);
      a0[2 * k] = dot2(pl, hh.x, 0.f); a0[2 * k + 1] = dot2(ph, hh.x, 0.f);
      a1[2 * k] = dot2(pl, hh.z, 0.f); a1[2 * k + 1] = dot2(ph, hh.z, 0.f);
    }
#pragma unroll
    for (int k = 0; k < 4; ++k) {
      pa[2 * k] = __builtin_amdgcn_perm(wa[1][k], wa[0][k], 0x05040100u); pa[2 * k + 1] = __builtin_amdgcn_perm(wa[1][k], wa[0][k], 0x07060302u);
      pb[2 * k] = __builtin_amdgcn_perm(wa[3][k], wa[2][k], 0x05040100u); pb[2 * k + 1] = __builtin_amdgcn_perm(wa[3][k], wa[2][k], 0x07060302u);
    }
  }
#pragma unroll
  for (int k = 0; k < 8; ++k) asm volatile("" : "+v"(a0[k]), "+v"(a1[k]), "+v"(pa[k]), "+v"(pb[k]));
  int o2o = co < D ? O2_OFF + (h * 32 + j) * O2_S + (lane >> 5) * 1792 + kw * 512 + (((lane >> 4) & 1) * (kw == 3 ? 8 : 16) + cl) * 16 : OOB;
  LAS u32x4* orp = (LAS u32x4*)(L + L_O + rg * 4);
  LAS u32* ofp = L + F_O;
  PIN(o2o); PIN(orp); PIN(ofp);
  {
    const u32x4 oo = data_wait(ofp, orp);
#pragma unroll
    for (int k = 0; k < 8; ++k) {
      a0[k] = dot2(pb[k], oo.y, dot2(pa[k], oo.x, a0[k]));
      a1[k] = dot2(pb[k], oo.w, dot2(pa[k], oo.z, a1[k]));
    }
    float t[8];
#pragma unroll
    for (int k = 0; k < 8; ++k) t[k] = tr32(a0[k], a1[k]);
    st128(rws, o2o, tag4(tr16(t[0], t[4]), tr16(t[1], t[5]), tr16(t[2], t[6]), tr16(t[3], t[7]), tag8));
  }
  flag_set(L + F_O2 + kw);
}

extern "C" void kernel_launch(void* const* d_in, const int* in_sizes, int n_in, void* d_out, int out_size, void* d_ws, size_t ws_size,
                              hipStream_t stream) {
  fused_decode<<<256, 768, 0, stream>>>((const u16*)d_in[0], (const u16*)d_in[3], (const u16*)d_in[6], (const u16*)d_in[7], (const u16*)d_in[1],
                                        (char*)d_ws, (const u16*)d_in[2], (const u16*)d_in[4], (const u16*)d_in[8], (u16*)d_out);
}
```

```cpp
#include <hip/hip_runtime.h>
#include <stdint.h>

typedef unsigned int u32;
typedef unsigned short u16;
typedef unsigned long long u64;
typedef u32 u32x2 __attribute__((ext_vector_type(2)));
typedef u32 u32x4 __attribute__((ext_vector_type(4)));
typedef float f32x4 __attribute__((ext_vector_type(4)));
typedef float f32x16 __attribute__((ext_vector_type(16)));
typedef __bf16 bf16x2_t __attribute__((ext_vector_type(2)));
typedef __bf16 bf16x8_t __attribute__((ext_vector_type(8)));
#define LAS __attribute__((address_space(3)))
typedef __amdgpu_buffer_rsrc_t rsrc_t;

namespace {
constexpr int D = 448, SK = 32768, W = 4096, DH = 256, DF = 1792;
constexpr int AUX_NT = 2, AUX_SC1 = 16;
constexpr int OOB = 1 << 30;
constexpr u32 TMASK = 0xfu;

constexpr int QP_S = 768, GP_S = 1280, O1_S = 2304, O2_S = 3840;
constexpr int EP_OFF = 0;
constexpr int OR_OFF = 2048;
constexpr int ST_OFF = 4096;
constexpr int QP_OFF = ST_OFF + 8192;
constexpr int GP_OFF = QP_OFF + 256 * QP_S;
constexpr int O1_OFF = GP_OFF + 256 * GP_S;
constexpr int O2_OFF = O1_OFF + 256 * O1_S;
constexpr int WS_BYTES = O2_OFF + 256 * O2_S;

constexpr int L_XN = 0;
constexpr int L_QX = L_XN + 480;
constexpr int L_GX = L_QX + 128;
constexpr int L_Q = L_GX + 256;
constexpr int L_P = L_Q + 256;
constexpr int L_OP = L_P + 512;
constexpr int L_WM = L_OP + 2048;
constexpr int L_WL = L_WM + 8;
constexpr int L_H = L_WL + 8;
constexpr int L_O = L_H + 16;
constexpr int F_O = L_O + 16;
constexpr int L_R = L_O + 32;
constexpr int F_P = L_R + 128;
constexpr int F_VI = F_P + 4;
constexpr int F_O2 = F_VI + 4;
constexpr int F_Q = F_O2 + 4;
constexpr int F_QX = F_Q + 2, F_PUB = F_QX + 1;
constexpr int F_V = F_PUB + 1;
constexpr int F_R = F_V + 4;
constexpr int L_TAG = F_R + 8;
constexpr int L_SH = L_TAG + 2, F_SH = L_TAG + 3;
constexpr int L_END = L_TAG + 4;
constexpr u32 SET = 1u;
static_assert((L_END - L_WM) % 4 == 0 && (L_END - L_WM) / 4 <= 64, "one 16-byte store per lane clears the flag area");

constexpr int T_K0 = 22, T_KSTEP10 = 615, T_VSTEP10 = 635;
constexpr int K_FREE = 2;
constexpr int V_FINE = 4, V_FINE_LEAD = 25;
constexpr int WINDOW = 6;
constexpr int T_ORG = 300, ORG_BIAS = 5;
constexpr int due_k(int g) { return T_K0 + g * T_KSTEP10 / 10; }
constexpr int due_v(int g, int half) { return T_K0 + (8 * T_KSTEP10 + g * T_VSTEP10 + half * T_VSTEP10 / 2) / 10 - (g >= V_FINE ? V_FINE_LEAD : 0); }
constexpr int T_LATE = due_v(8, 0);
constexpr int Q_DELAY = 40, O1_DELAY = 12, O2_DELAY = 7;
constexpr int ROT = 70;
constexpr int CODE_SPAN = 15104;
constexpr u32 SPIN_LDS = 1u << 21, SPIN_MEM = 1u << 16;

__device__ __forceinline__ float asf(u32 v) { return __builtin_bit_cast(float, v); }
__device__ __forceinline__ u32 asu(float v) { return __builtin_bit_cast(u32, v); }
__device__ __forceinline__ float lo16(u32 v) { return asf(v << 16); }
__device__ __forceinline__ float hi16(u32 v) { return asf(v & 0xffff0000u); }
__device__ __forceinline__ u32 pkbf(float a, float b) {
  bf16x2_t r = {(__bf16)a, (__bf16)b};
  return __builtin_bit_cast(u32, r);
}
__device__ __forceinline__ float rbf(float a) { return (float)(__bf16)a; }
__device__ __forceinline__ float dot2(u32 a, u32 b, float c) {
  return __builtin_amdgcn_fdot2_f32_bf16(__builtin_bit_cast(bf16x2_t, a), __builtin_bit_cast(bf16x2_t, b), c, false);
}
template <int CTRL> __device__ __forceinline__ float dpp(float x) {
  return asf(__builtin_amdgcn_mov_dpp((int)asu(x), CTRL, 0xf, 0xf, true));
}
constexpr int XOR1 = 0xB1, XOR2 = 0x4E, ROR8 = 0x128, ROR4 = 0x124;
__device__ __forceinline__ float tr32(float a, float b) {
  auto r = __builtin_amdgcn_permlane32_swap(asu(a), asu(b), false, false);
  return asf(r[0]) + asf(r[1]);
}
__device__ __forceinline__ float tr16(float a, float b) {
  auto r = __builtin_amdgcn_permlane16_swap(asu(a), asu(b), false, false);
  return asf(r[0]) + asf(r[1]);
}
__device__ __forceinline__ float mx32(float a) {
  auto r = __builtin_amdgcn_permlane32_swap(asu(a), asu(a), false, false);
  return fmaxf(asf(r[0]), asf(r[1]));
}
__device__ __forceinline__ float mx16(float a) {
  auto r = __builtin_amdgcn_permlane16_swap(asu(a), asu(a), false, false);
  return fmaxf(asf(r[0]), asf(r[1]));
}
__device__ __forceinline__ float row_sum(float v) { v += dpp<ROR8>(v); v += dpp<ROR4>(v); v += dpp<XOR2>(v); v += dpp<XOR1>(v); return v; }
__device__ __forceinline__ float row_max(float v) {
  v = fmaxf(v, dpp<ROR8>(v)); v = fmaxf(v, dpp<ROR4>(v)); v = fmaxf(v, dpp<XOR2>(v)); v = fmaxf(v, dpp<XOR1>(v)); return v;
}
__device__ __forceinline__ float wave_sum(float v) { v = row_sum(v); v = tr16(v, v); return tr32(v, v); }

__device__ __forceinline__ rsrc_t mkrs(const void* p, u32 bytes) { return __builtin_amdgcn_make_buffer_rsrc((void*)p, 0, bytes, 0x00020000); }
template <int AUX> __device__ __forceinline__ u32x4 ld128(rsrc_t r, int voff, int soff = 0) { return __builtin_amdgcn_raw_buffer_load_b128(r, voff, soff, AUX); }
template <int AUX> __device__ __forceinline__ u32 ld32(rsrc_t r, int voff) { return __builtin_amdgcn_raw_buffer_load_b32(r, voff, 0, AUX); }
__device__ __forceinline__ void st128(rsrc_t r, int voff, u32x4 v) { __builtin_amdgcn_raw_buffer_store_b128(v, r, voff, 0, AUX_SC1); }
__device__ __forceinline__ u32 tagw(float v, u32 tag4) { return (asu(v) & ~TMASK) | tag4; }
__device__ __forceinline__ u32x4 tag4(float a, float b, float c, float d, u32 t) { return u32x4{tagw(a, t), tagw(b, t), tagw(c, t), tagw(d, t)}; }
__device__ __forceinline__ u32 untag4(u32x4& v, u32 t) { v.x ^= t; v.y ^= t; v.z ^= t; v.w ^= t; return v.x | v.y | v.z | v.w; }
__device__ __forceinline__ bool none(bool bad) { return __builtin_amdgcn_ballot_w64(bad) == 0ull; }
__device__ __forceinline__ bool all_ok(u32 bad) { return none((bad & TMASK) != 0u); }

#define CBAR() asm volatile("" ::: "memory")
#define PIN(v) asm volatile("" : "+v"(v))
#define LGKM0() asm volatile("s_waitcnt lgkmcnt(0)" ::: "memory")
template <int N, int SLEEP> __device__ __forceinline__ void flag_wait(LAS u32* p, int lane) {
  u32 v, cnt;
  const u32 addr = (u32)(uintptr_t)(p + (lane & (N - 1)));
  asm volatile("s_mov_b32 %1, %4\n"
               "1:\n\t"
               "ds_read_b32 %0, %2\n\t"
               "s_waitcnt lgkmcnt(0)\n\t"
               "v_cmp_ne_u32 vcc, %3, %0\n\t"
               "s_cbranch_vccz 2f\n\t"
               "s_sleep %5\n\t"
               "s_sub_u32 %1, %1, 1\n\t"
               "s_cbranch_scc0 1b\n"
               "2:"
               : "=&v"(v), "=&s"(cnt) : "v"(addr), "i"(SET), "i"(SPIN_LDS), "i"(SLEEP) : "vcc", "scc", "memory");
}
template <int DUE> __device__ __forceinline__ void pace(u64 org) {
  asm volatile("1:\n\t"
               "s_memrealtime vcc\n\t"
               "s_waitcnt lgkmcnt(0)\n\t"
               "s_sub_u32 vcc_lo, vcc_lo, %0\n\t"
               "s_cmp_lt_i32 vcc_lo, %1\n\t"
               "s_cbranch_scc0 2f\n\t"
               "s_sleep 1\n\t"
               "s_branch 1b\n"
               "2:"
               :: "s"((u32)org), "i"(DUE), "s"(org) : "vcc", "scc", "memory");
}
__device__ __forceinline__ void flag_set(LAS u32* p, u32 set = SET) { CBAR(); *p = set; CBAR(); }

__device__ __forceinline__ void poll2(rsrc_t rs, int voff, int soff, u32 t4, u32x4& r0, u32x4& r1) {
  u32 o0, o1, o2, o3, o4, o5, o6, o7, m, cnt;
  asm volatile("s_mov_b32 %9, %14\n"
               "1:\n\t"
               "buffer_load_dwordx4 v[144:147], %10, %11, 0 offen sc1\n\t"
               "buffer_load_dwordx4 v[148:151], %10, %11, %12 offen sc1\n\t"
               "s_sub_u32 %9, %9, 1\n\t"
               "s_waitcnt vmcnt(1)\n\t"
               "v_xor_b32 %0, %13, v144\n\tv_xor_b32 %1, %13, v145\n\tv_xor_b32 %2, %13, v146\n\tv_xor_b32 %3, %13, v147\n\t"
               "v_or3_b32 %8, %0, %1, %2\n\t"
               "s_waitcnt vmcnt(0)\n\t"
               "v_xor_b32 %4, %13, v148\n\tv_xor_b32 %5, %13, v149\n\tv_xor_b32 %6, %13, v150\n\tv_xor_b32 %7, %13, v151\n\t"
               "v_or3_b32 %8, %8, %3, %4\n\t"
               "v_or3_b32 %8, %8, %5, %6\n\t"
               "v_bitop3_b32 %8, %8, 15, %7 bitop3:0xc8\n\t"
               "v_cmp_ne_u32 vcc, 0, %8\n\t"
               "s_cbranch_vccz 2f\n\t"
               "s_cbranch_scc0 1b\n"
               "2:"
               : "=&v"(o0), "=&v"(o1), "=&v"(o2), "=&v"(o3), "=&v"(o4), "=&v"(o5), "=&v"(o6), "=&v"(o7), "=&v"(m), "=&s"(cnt)
               : "v"(voff), "s"(rs), "s"(soff), "s"(t4), "i"(SPIN_MEM)
               : "vcc", "scc", "memory", "v144", "v145", "v146", "v147", "v148", "v149", "v150", "v151");
  r0 = u32x4{o0, o1, o2, o3};
  r1 = u32x4{o4, o5, o6, o7};
}
__device__ __forceinline__ u32x4 data_wait(LAS u32* flag, LAS u32x4* p) {
  u32x4 r;
  u32 v, cnt;
  asm volatile("s_mov_b32 %2, %5\n"
               "1:\n\t"
               "ds_read_b32 %1, %3\n\t"
               "ds_read_b128 %0, %4\n\t"
               "s_sub_u32 %2, %2, 1\n\t"
               "s_waitcnt lgkmcnt(0)\n\t"
               "v_cmp_ne_u32 vcc, 1, %1\n\t"
               "s_cbranch_vccz 2f\n\t"
               "s_cbranch_scc0 1b\n"
               "2:"
               : "=&v"(r), "=&v"(v), "=&s"(cnt) : "v"((u32)(uintptr_t)flag), "v"((u32)(uintptr_t)p), "i"(SPIN_LDS) : "vcc", "scc", "memory");
  return r;
}

__device__ __forceinline__ void final_reduce(LAS u32* L, rsrc_t rws, const u16* x, u16* out, int rid, int grp, int lane, u32 tag) {
  LAS float* const LF = (LAS float*)L;
  __builtin_amdgcn_s_setprio(3);
  const u32 tag8 = tag & TMASK;
  const int rb = rid / 28, cb = rid % 28, qd = lane & 3;
  const int pi = cb & 7, ph = cb < 24 ? pi >> 2 : pi >> 1, pg = cb < 24 ? pi & 3 : pi & 1;
  const int col = rb * D + 128 * (cb >> 3) + 8 * (4 * pg + ((lane >> 2) & 3)) + 4 * ph + (lane & 3);
  float xres = lo16(x[col]);
  int oo = O2_OFF + (grp * 32 + (lane >> 2)) * O2_S + rb * 1792 + cb * 64 + qd * 16;
  LAS float* wp = LF + ((lane & 12) == 0 ? L_R + (4 * qd + (lane >> 4)) * 8 + grp : L_XN + 128 + lane);
  LAS u32* fp = L + F_R + grp;
  LAS u32* fr = L + F_R + (lane & 7);
  LAS f32x4* rp = (LAS f32x4*)(LF + L_R + (lane & 15) * 8);
  int so = lane < 16 ? col * 2 : OOB;
  u32 setv = SET;
  rsrc_t ro = mkrs(out, 2 * D * 2);
  asm volatile("" : "+s"(ro));
  PIN(xres); PIN(oo); PIN(wp); PIN(fp); PIN(fr); PIN(rp); PIN(so); PIN(setv);
  flag_wait<1, 1>(L + F_PUB, lane);
  flag_wait<4, 0>(L + F_O2, lane);
  __builtin_amdgcn_s_sleep(O2_DELAY);
  u32x4 a0, a1;
  poll2(rws, oo, 16 * O2_S, tag8, a0, a1);
  float f = tr16(tr32(asf(a0.x) + asf(a1.x), asf(a0.z) + asf(a1.z)), tr32(asf(a0.y) + asf(a1.y), asf(a0.w) + asf(a1.w)));
  f += dpp<ROR8>(f); f += dpp<ROR4>(f);
  *wp = f;
  flag_set(fp, setv);
  u32 fl = *fr;
  CBAR();
  const f32x4 r0 = rp[0], r1 = rp[1];
  const f32x4 rs4 = r0 + r1;
  const float tot = xres + ((rs4.x + rs4.z) + (rs4.y + rs4.w));
  u32 res = pkbf(tot, tot);
  PIN(res);
  if (none(fl != SET)) {
    __builtin_amdgcn_raw_buffer_store_b16((u16)res, ro, so, 0, 0);
    if (rid == 0 && lane == 0) __builtin_amdgcn_raw_buffer_store_b32(tag, rws, EP_OFF, 0, AUX_SC1);
  }
}

}

extern "C" __global__ void __launch_bounds__(768, 3)
fused_decode(const u16* __restrict__ x, const u16* __restrict__ wq, const u16* __restrict__ wg_, const u16* __restrict__ wu,
             const u16* __restrict__ kc, char* __restrict__ ws, const u16* __restrict__ vc, const u16* __restrict__ wo,
             const u16* __restrict__ wd, u16* __restrict__ out) {
  __shared__ __attribute__((aligned(16))) u32 smem[L_END];
  LAS u32* const L = (LAS u32*)smem;
  LAS float* const LF = (LAS float*)smem;
  const int wg = blockIdx.x, h = wg & 7, j = wg >> 3, b = j & 1, sp = j >> 1, reg = wg & 15;
  const int w = __builtin_amdgcn_readfirstlane(threadIdx.x >> 6), lane = threadIdx.x & 63, kw = w & 3;
  asm volatile("s_load_dword s94, %0, 0x0\n\ts_load_dword s95, %0, 0x40" :: "s"(__builtin_amdgcn_kernarg_segment_ptr()) : "s94", "s95");
  u64 pc0;
  asm volatile("s_getpc_b64 %0" : "=s"(pc0));
  const u32 code_touch = ld32<0>(mkrs((const char*)(pc0 & ~255ull), w == 0 ? CODE_SPAN : 0), lane < 5 ? j * 640 + lane * 128 : OOB);
  __builtin_amdgcn_sched_barrier(0);
#define ROLE_ENTRY() const rsrc_t rws = mkrs(ws, WS_BYTES)
#define ROLE_TAG() const u32 tag = __builtin_amdgcn_readfirstlane(L[L_TAG]), tag8 = tag & TMASK
  const u32 blk0 = sp * 4 + reg * ROT;
#define BLOCK_OFF(slot) (((blk0 + (((slot) >> 2) << 6) + ((slot) & 3u)) & 511u) << 12)
  const int hx = h ^ 4;
  u64 org;
#define STREAM_GROUP(G, VOFF, SLOT, ISK) \
  if (!(ISK) || (G) >= K_FREE) pace<(ISK) ? due_k(G) : due_v(G, 0)>(org); \
  if (4 * (G) > WINDOW) asm volatile("s_waitcnt vmcnt(%0)" :: "i"(WINDOW) : "memory"); \
  _Pragma("unroll") for (int i = 4 * (G); i < 4 * (G) + 2; ++i) d[i] = ld128<AUX_NT>(rkv, (VOFF), (int)BLOCK_OFF(SLOT)); \
  if (!(ISK) && (G) >= V_FINE) { __builtin_amdgcn_sched_barrier(0); pace<due_v(G, 1)>(org); } \
  _Pragma("unroll") for (int i = 4 * (G) + 2; i < 4 * (G) + 4; ++i) d[i] = ld128<AUX_NT>(rkv, (VOFF), (int)BLOCK_OFF(SLOT)); \
  __builtin_amdgcn_sched_barrier(0);
#define STREAM_LOADS(VOFF, SLOT, ISK) \
  STREAM_GROUP(0, VOFF, SLOT, ISK) STREAM_GROUP(1, VOFF, SLOT, ISK) STREAM_GROUP(2, VOFF, SLOT, ISK) STREAM_GROUP(3, VOFF, SLOT, ISK) \
  STREAM_GROUP(4, VOFF, SLOT, ISK) STREAM_GROUP(5, VOFF, SLOT, ISK) STREAM_GROUP(6, VOFF, SLOT, ISK) STREAM_GROUP(7, VOFF, SLOT, ISK)
#define WINDOW_RSRC(SRC) mkrs((SRC) + ((size_t)reg * SK + (SK - W)) * DH, W * DH * 2)

  if (__builtin_expect(w >= 4, 0)) {
  u32 tag_out;
  if (w < 8) {
    __builtin_amdgcn_s_setprio(2);
    const int g = lane >> 4, key = (lane >> 1) & 7;
    const bool hb = lane & 1;
    const int kvo = key * 512 + (2 * g + (lane & 1)) * 16;
    const rsrc_t rkv = WINDOW_RSRC(kc);
    u32x4 d[32];
    __builtin_amdgcn_sched_barrier(0);
    __builtin_amdgcn_s_barrier();
    org = __builtin_amdgcn_s_memrealtime();
    __builtin_amdgcn_sched_barrier(0);
    STREAM_LOADS(kvo + (i & 3) * 128, kw + 4 * (i >> 2), true)
    ROLE_ENTRY();
    flag_wait<2, 1>(L + F_Q, lane);
    ROLE_TAG();
    u32x4 qa[4];
#pragma unroll
    for (int i = 0; i < 4; ++i) qa[i] = *(LAS u32x4*)(L + L_Q + (lane & 1) * 128 + i * 32 + (2 * g + ((lane >> 1) & 1)) * 4);
    f32x4 ea, eb;
#pragma unroll
    for (int n = 0; n < 4; ++n) {
      f32x4 ca = {}, cb = {};
#pragma unroll
      for (int i = 0; i < 4; ++i) {
        ca = __builtin_amdgcn_mfma_f32_16x16x32_bf16(__builtin_bit_cast(bf16x8_t, qa[i]), __builtin_bit_cast(bf16x8_t, d[8 * n + i]), ca, 0, 0, 0);
        cb = __builtin_amdgcn_mfma_f32_16x16x32_bf16(__builtin_bit_cast(bf16x8_t, qa[i]), __builtin_bit_cast(bf16x8_t, d[8 * n + 4 + i]), cb, 0, 0, 0);
      }
#pragma unroll
      for (int k = 0; k < 4; ++k) { ea[k] = (n == 0 || g == n) ? ca[k] : ea[k]; eb[k] = (n == 0 || g == n) ? cb[k] : eb[k]; }
    }
    asm volatile("" :: "v"(code_touch));
    float sc[2];
#pragma unroll
    for (int hd = 0; hd < 2; ++hd) sc[hd] = rbf((hb ? eb[2 + hd] : ea[hd]) + dpp<XOR1>(hb ? ea[2 + hd] : eb[hd])) * 0.0625f;
    const float wm0 = mx32(mx16(row_max(sc[0]))), wm1 = mx32(mx16(row_max(sc[1])));
    CBAR();
    if (lane < 2) L[L_WM + kw * 2 + lane] = (asu(lane ? wm1 : wm0) & 0xffff0000u) | SET;
    u32x4 ma, mb;
    for (u32 s = 0; s < SPIN_LDS; ++s) {
      CBAR();
      ma = *(LAS u32x4*)(L + L_WM) ^ SET; mb = *(LAS u32x4*)(L + L_WM + 4) ^ SET;
      if (none((((ma.x | ma.y | ma.z | ma.w) | (mb.x | mb.y | mb.z | mb.w)) & 0xffffu) != 0u)) break;
    }
    const float M0 = fmaxf(fmaxf(asf(ma.x), asf(ma.z)), fmaxf(asf(mb.x), asf(mb.z)));
    const float M1 = fmaxf(fmaxf(asf(ma.y), asf(ma.w)), fmaxf(asf(mb.y), asf(mb.w)));
    const float p0 = rbf(__builtin_amdgcn_exp2f((sc[0] - M0) * 1.4426950408889634f));
    const float p1 = rbf(__builtin_amdgcn_exp2f((sc[1] - M1) * 1.4426950408889634f));
    LAS u32* pp = L + L_P + key * 64 + kw + 4 * (2 * g + (lane & 1));
    pp[0] = asu(p0) | (asu(p0) >> 16);
    pp[32] = asu(p1) | (asu(p1) >> 16);
    float ls = tr32(p0, p1);
    ls = row_sum(tr16(ls, ls));
    if ((lane & 31) == 0) LF[L_WL + kw * 2 + (lane >> 5)] = ls;
    flag_set(L + F_P + kw);

    if (kw == 0) {
      flag_wait<4, 0>(L + F_P, lane);
      if (lane < 2) {
        const float Ls = (LF[L_WL + lane] + LF[L_WL + 2 + lane]) + (LF[L_WL + 4 + lane] + LF[L_WL + 6 + lane]);
        st128(rws, ST_OFF + (((h * 2 + lane) * 2 + b) * 16 + sp) * 16, u32x4{asu(lane ? M1 : M0), tag, asu(Ls), tag});
      }
    } else if (kw == 1) {
      __builtin_amdgcn_s_setprio(3);
      const int s = lane >> 2, qd = lane & 3;
      const int so = ST_OFF + ((hx * 2 + (j >> 4)) * 32 + s) * 16;
      const int oo = O1_OFF + (hx * 32 + 2 * s) * O1_S + j * 64 + qd * 16;
      u32x4 s0, s1, a0, a1;
      flag_wait<4, 1>(L + F_VI, lane);
      for (u32 it = 0; it < SPIN_MEM; ++it) {
        CBAR();
        s0 = ld128<AUX_SC1>(rws, so); s1 = ld128<AUX_SC1>(rws, so + 256);
        if (none((s0.y != tag) | (s0.w != tag) | (s1.y != tag) | (s1.w != tag))) break;
        flag_wait<1, 1>(L + F_PUB, lane);
      }
      float wgt[2];
#pragma unroll
      for (int bb = 0; bb < 2; ++bb) {
        const float m = asf(bb ? s1.x : s0.x), l = asf(bb ? s1.z : s0.z);
        float mg = fmaxf(m, dpp<ROR8>(m)); mg = fmaxf(mg, dpp<ROR4>(mg)); mg = mx32(mx16(mg));
        const float e = __builtin_amdgcn_exp2f((m - mg) * 1.4426950408889634f);
        float lg = e * l; lg += dpp<ROR8>(lg); lg += dpp<ROR4>(lg); lg = tr16(lg, lg); lg = tr32(lg, lg);
        wgt[bb] = e * __builtin_amdgcn_rcpf(lg);
      }
      LAS u32* ow = L + ((lane & 12) == 0 ? L_O + qd * 4 + (lane >> 4) : L_XN + lane);
      LAS u32* of = L + F_O;
      u32 setv = SET;
      PIN(wgt[0]); PIN(wgt[1]); PIN(ow); PIN(of); PIN(setv);
      flag_wait<1, 0>(L + F_PUB, lane);
      __builtin_amdgcn_s_sleep(O1_DELAY);
      poll2(rws, oo, O1_S, tag8, a0, a1);
      f32x4 m0 = __builtin_bit_cast(f32x4, a0) * wgt[0], m1 = __builtin_bit_cast(f32x4, a1) * wgt[1];
      PIN(m0); PIN(m1);
      const float v0 = tr32(m0.x, m1.x), v2 = tr32(m0.z, m1.z), v1 = tr32(m0.y, m1.y), v3 = tr32(m0.w, m1.w);
      float e0 = tr16(v0, v2), e1 = tr16(v1, v3);
      e0 += dpp<ROR8>(e0); e1 += dpp<ROR8>(e1);
      e0 += dpp<ROR4>(e0); e1 += dpp<ROR4>(e1);
      *ow = pkbf(e0, e1);
      flag_set(of, setv);
    }
    tag_out = tag;
  } else {
    if (kw == 0 && lane < (L_END - L_WM) / 4) *(LAS u32x4*)(L + L_WM + 4 * lane) = u32x4{0u, 0u, 0u, 0u};
    LGKM0();
    __builtin_amdgcn_s_barrier();
    org = __builtin_amdgcn_s_memrealtime();
    __builtin_amdgcn_s_setprio(1);
    __builtin_amdgcn_sched_barrier(0);
    ROLE_ENTRY();
    flag_wait<1, 4>(L + F_SH, lane);
    org += (u64)(long long)(int)__builtin_amdgcn_readfirstlane(L[L_SH]);
    const rsrc_t rkv = WINDOW_RSRC(vc);
    u32x4 d[32];
    STREAM_LOADS(lane * 16 + kw * 1024, (u32)i, false)
    flag_set(L + F_VI + kw);
    const int m = lane & 31, hf = lane >> 5;
    u32x4 msk;
#pragma unroll
    for (int r = 0; r < 4; ++r) msk[r] = (m < 16 && ((m & 7) >> 1) == r) ? ((m & 1) ? 0xffff0000u : 0x0000ffffu) : 0u;
    LAS f32x4* op = (LAS f32x4*)(LF + L_OP) + kw * 128 + 2 * m + hf;
    LAS f32x4* rp = (LAS f32x4*)(LF + L_OP) + lane;
    LAS u32* fp = L + F_V + kw;
    LAS u32* fr = L + F_V + (lane & 3);
    int o1o = O1_OFF + (h * 32 + j) * O1_S + lane * 16;
    PIN(op); PIN(rp); PIN(fp); PIN(fr); PIN(o1o);
    flag_wait<4, 1>(L + F_P, lane);
    ROLE_TAG();
    const LAS u32x4* pp = (const LAS u32x4*)(L + L_P + ((kw * 2 + hf) * 2 + ((m >> 3) & 1)) * 32);
    f32x16 acc = {};
#pragma unroll
    for (int g = 0; g < 8; ++g) {
      const u32x4 p4 = pp[g];
#pragma unroll
      for (int k = 0; k < 4; ++k) {
        const u32x4 a = msk & p4[k];
        acc = __builtin_amdgcn_mfma_f32_32x32x16_bf16(__builtin_bit_cast(bf16x8_t, a), __builtin_bit_cast(bf16x8_t, d[4 * g + k]), acc, 0, 0, 0);
      }
      __builtin_amdgcn_sched_barrier(0);
    }
    asm volatile("" :: "v"(code_touch));
    op[0] = f32x4{acc[0], acc[1], acc[2], acc[3]};
    op[64] = f32x4{acc[4], acc[5], acc[6], acc[7]};
    flag_set(fp);
    const u32 fl = *fr;
    CBAR();
    const f32x4 o0 = (rp[0] + rp[128]) + (rp[256] + rp[384]);
    const f32x4 o1 = (rp[64] + rp[192]) + (rp[320] + rp[448]);
    u32x4 t0 = tag4(o0.x, o0.y, o0.z, o0.w, tag8), t1 = tag4(o1.x, o1.y, o1.z, o1.w, tag8);
    PIN(t0); PIN(t1);
    if (none(fl != SET)) {
      st128(rws, o1o, t0);
      st128(rws, o1o + 1024, t1);
      flag_set(L + F_PUB);
    }
    tag_out = tag;
  }
  if (!(h & 1) && j < 14) final_reduce(L, mkrs(ws, WS_BYTES), x, out, (h >> 1) * 14 + j, w - 4, lane, tag_out);
  return;
  }

  __builtin_amdgcn_s_setprio(3);
  ROLE_ENTRY();
  const bool mlp = wg < 224;
  const int cb2 = wg % 28, ke = wg / 28;
  const int cbf = cb2 == 27 ? 0 : cb2 + 1;
  const int rr = lane >> 3, c8 = lane & 7;
  const rsrc_t rx = mkrs((const u16*)x, 2 * D * 2);
  const u32x4 xa = ld128<0>(rx, lane < 56 ? lane * 16 : OOB), xb = ld128<0>(rx, lane < 56 ? D * 2 + lane * 16 : OOB);
  const u32 epoch = ld32<AUX_SC1>(rws, EP_OFF);
  const int row0 = kw < 2 ? 112 * (j >> 3) + 64 * kw : 56 * ke;
  const int nrow = kw == 0 ? 64 : kw == 1 ? 48 : mlp ? 56 : 0;
  u32x4 wv[8];
  {
    const rsrc_t rw = mkrs(kw < 2 ? wq : kw == 2 ? wg_ : wu, kw < 2 ? D * 4096 * 2 : D * DF * 2);
    const int rs = kw < 2 ? 8192 : DF * 2;
    const int vw0 = nrow ? (row0 + 2 * rr) * rs + (kw < 2 ? (hx * 512 + (j & 7) * 64) * 2 : cb2 * 128) + c8 * 16 : OOB;
    const int vw3 = 48 + 2 * rr < nrow ? vw0 : OOB;
#pragma unroll
    for (int i = 0; i < 8; ++i) wv[i] = ld128<AUX_NT>(rw, i < 6 ? vw0 : vw3, (16 * (i >> 1) + (i & 1)) * rs);
  }
  const int eo = lane < 8 ? QP_OFF + (hx * 32 + j) * QP_S + lane * 64 : lane < 24 ? GP_OFF + wg * GP_S + (lane - 8) * 64 : ST_OFF + (wg & 127) * 64;
  const u32 box_touch = ld32<AUX_SC1>(rws, (kw == 1 && lane < 25) ? eo : OOB);
  __builtin_amdgcn_sched_barrier(0);
  __builtin_amdgcn_s_barrier();
  org = __builtin_amdgcn_s_memrealtime();
  __builtin_amdgcn_sched_barrier(0);

#pragma unroll
  for (int bb = 0; bb < 2; ++bb) {
    const u32x4 xv = bb ? xb : xa;
    float v[8];
#pragma unroll
    for (int k = 0; k < 4; ++k) { v[2 * k] = lo16(xv[k]); v[2 * k + 1] = hi16(xv[k]); }
    float s = 0.f;
#pragma unroll
    for (int k = 0; k < 8; ++k) s += v[k];
    const float mean = wave_sum(s) * (1.f / D);
    float q = 0.f;
#pragma unroll
    for (int k = 0; k < 8; ++k) { const float t = v[k] - mean; q += t * t; }
    q = lane < 56 ? q : 0.f;
    const float var = rbf(wave_sum(q) * (1.f / D));
    const float den = rbf(__builtin_amdgcn_sqrtf(rbf(var + 1e-5f))), rden = __builtin_amdgcn_rcpf(den), mb = rbf(mean);
#pragma unroll
    for (int k = 0; k < 8; ++k) { const float c = rbf(v[k] - mb), r = c * rden; v[k] = fmaf(fmaf(-r, den, c), rden, r); }
    u32x4 o;
#pragma unroll
    for (int k = 0; k < 4; ++k) o[k] = pkbf(v[2 * k], v[2 * k + 1]);
    if (lane < 56) *(LAS u32x4*)(L + L_XN + bb * 224 + lane * 4) = o;
    else if (bb == 1) *(LAS u32x4*)(L + L_XN + 224 + lane * 4) = u32x4{0u, 0u, 0u, 0u};
  }
  asm volatile("" :: "v"(code_touch));
  const u32 tag = __builtin_amdgcn_readfirstlane(epoch) + 1u, tag8 = tag & TMASK;
  if (kw == 0) L[L_TAG] = tag;
  if (kw == 3 && wg < 8 && lane == 0) __builtin_amdgcn_raw_buffer_store_b64(u32x2{(u32)org, tag}, rws, OR_OFF + wg * 8, 0, AUX_SC1);

  {
    float a0[8] = {}, a1[8] = {};
#pragma unroll
    for (int k = 0; k < 4; ++k) {
      const u32 x0 = L[L_XN + row0 / 2 + rr + 8 * k], x1 = L[L_XN + 224 + row0 / 2 + rr + 8 * k];
#pragma unroll
      for (int c = 0; c < 4; ++c) {
        const u32 pl = __builtin_amdgcn_perm(wv[2 * k + 1][c], wv[2 * k][c], 0x05040100u), ph = __builtin_amdgcn_perm(wv[2 * k + 1][c], wv[2 * k][c], 0x07060302u);
        a0[2 * c] = dot2(pl, x0, a0[2 * c]); a0[2 * c + 1] = dot2(ph, x0, a0[2 * c + 1]);
        a1[2 * c] = dot2(pl, x1, a1[2 * c]); a1[2 * c + 1] = dot2(ph, x1, a1[2 * c + 1]);
      }
    }
    asm volatile("" :: "v"(box_touch));
    float t[8], u[4];
#pragma unroll
    for (int k = 0; k < 8; ++k) t[k] = tr32(a0[k], a1[k]);
#pragma unroll
    for (int k = 0; k < 4; ++k) { u[k] = tr16(t[k], t[k + 4]); u[k] += dpp<ROR8>(u[k]); }
    const int bb = lane >> 5, hf = (lane >> 4) & 1;
    LAS f32x4* qx = (LAS f32x4*)(LF + L_QX) + bb * 16 + c8 * 2 + hf;
    if (kw == 1) {
      if (!(lane & 8)) *qx = f32x4{u[0], u[1], u[2], u[3]};
      flag_set(L + F_QX);
    } else if (kw == 0) {
      flag_wait<1, 0>(L + F_QX, lane);
      const f32x4 o = *qx;
      if (!(lane & 8)) st128(rws, QP_OFF + (hx * 32 + j) * QP_S + bb * 256 + hf * 128 + c8 * 16, tag4(u[0] + o.x, u[1] + o.y, u[2] + o.z, u[3] + o.w, tag8));
    } else if (mlp) {
      LAS u32x4* gx = (LAS u32x4*)(L + L_GX) + (kw - 2) * 32;
      if (!(lane & 8)) gx[c8 * 4 + bb * 2 + hf] = tag4(u[0], u[1], u[2], u[3], tag8);
      CBAR();
      if (lane < 32) st128(rws, GP_OFF + wg * GP_S + (lane >> 2) * 128 + (kw - 2) * 64 + (lane & 3) * 16, gx[lane]);
    }
  }

  if (kw < 2) {
    __builtin_amdgcn_s_sleep(Q_DELAY);
    const int qo = QP_OFF + (h * 32 + 4 * kw + 8 * (lane >> 4)) * QP_S + b * 256 + (lane & 15) * 16;
    u32x4 a[4];
    for (u32 s = 0; s < SPIN_MEM; ++s) {
      CBAR();
#pragma unroll
      for (int i = 0; i < 4; ++i) a[i] = ld128<AUX_SC1>(rws, qo, i * QP_S);
      if (all_ok((untag4(a[0], tag8) | untag4(a[1], tag8)) | (untag4(a[2], tag8) | untag4(a[3], tag8)))) break;
    }
#pragma unroll
    for (int i = 0; i < 2; ++i) {
      const u32x4 a0 = a[2 * i], a1 = a[2 * i + 1];
      const float v0 = tr32(asf(a0.x), asf(a1.x)), v1 = tr32(asf(a0.y), asf(a1.y)), v2 = tr32(asf(a0.z), asf(a1.z)), v3 = tr32(asf(a0.w), asf(a1.w));
      L[L_Q + (4 * kw + 2 * i + (lane >> 5)) * 32 + 4 * (lane & 7) + 2 * ((lane >> 3) & 1) + ((lane >> 4) & 1)] = pkbf(tr16(v0, v2), tr16(v1, v3));
    }
    flag_set(L + F_Q + kw);
  } else if (kw == 2) {
    u32 h0 = 0u, h1 = 0u;
    if (mlp) {
      const int go = GP_OFF + (cbf + 28 * rr) * GP_S + ke * 128 + c8 * 16;
      u32x4 a;
      flag_wait<2, 1>(L + F_Q, lane);
      for (u32 s = 0; s < SPIN_MEM; ++s) {
        CBAR();
        a = ld128<AUX_SC1>(rws, go);
        if (all_ok(untag4(a, tag8))) break;
        __builtin_amdgcn_s_sleep(16);
      }
      float hv[4];
#pragma unroll
      for (int k = 0; k < 4; ++k) {
        float g = asf(a[k]);
        g += dpp<ROR8>(g); g = tr16(g, g); g = tr32(g, g);
        g = rbf(g);
        const float uu = dpp<ROR4>(g);
        const float z = 0.7978845608028654f * (g + 0.044715f * g * g * g);
        const float th = 1.f - 2.f * __builtin_amdgcn_rcpf(1.f + __builtin_amdgcn_exp2f(z * 2.8853900817779268f));
        hv[k] = rbf(0.5f * g * (1.f + th)) * uu;
      }
      h0 = pkbf(hv[0], hv[1]); h1 = pkbf(hv[2], hv[3]);
    }
    if (lane < 4) {
      *(LAS u32x2*)(L + L_H + (2 * (lane & 1)) * 4 + (lane >> 1) * 2) = u32x2{h0, SET};
      *(LAS u32x2*)(L + L_H + (2 * (lane & 1) + 1) * 4 + (lane >> 1) * 2) = u32x2{h1, SET};
    }
    CBAR();
  } else {
    pace<T_ORG>(org);
    const u32x2 o = __builtin_amdgcn_raw_buffer_load_b64(rws, OR_OFF + (lane & 7) * 8, 0, AUX_SC1);
    const bool ok = o.y == tag;
    const float n = row_sum(ok ? 1.f : 0.f), dsum = row_sum(ok ? (float)(int)(o.x - (u32)org) : 0.f);
    const float sh = n > 0.f ? fminf(fmaxf(dsum * __builtin_amdgcn_rcpf(n) + ORG_BIAS, -80.f), 80.f) : 0.f;
    L[L_SH] = (u32)(int)sh;
    flag_set(L + F_SH);
  }

  const int rg = lane >> 4, cl = lane & 15;
  const int co = kw * 128 + cl * 8;
  u32x4 wa[4], wb[2];
  flag_wait<4, 1>(L + F_VI, lane);
  pace<T_LATE>(org + (u64)(long long)(int)__builtin_amdgcn_readfirstlane(L[L_SH]));
  {
    const rsrc_t ro = mkrs(wo, 4096 * D * 2), rd = mkrs(wd, mlp ? DF * D * 2 : 0);
    const int vo = co < D ? (hx * 512 + j * 16 + 4 * rg) * (D * 2) + co * 2 : OOB;
    const int vd = co < D ? (64 * cbf + 8 * ke + 2 * rg) * (D * 2) + co * 2 : OOB;
#pragma unroll
    for (int i = 0; i < 2; ++i) wb[i] = ld128<AUX_NT>(rd, vd, i * D * 2);
#pragma unroll
    for (int i = 0; i < 4; ++i) wa[i] = ld128<AUX_NT>(ro, vo, i * D * 2);
  }
  __builtin_amdgcn_sched_barrier(0);
  float a0[8], a1[8];
  u32 pa[8], pb[8];
  {
    u32x4 hh;
    for (u32 s = 0; s < SPIN_LDS; ++s) {
      CBAR();
      hh = *(LAS u32x4*)(L + L_H + rg * 4);
      if (none((hh.y != SET) | (hh.w != SET))) break;
      __builtin_amdgcn_s_sleep(1);
    }
#pragma unroll
    for (int k = 0; k < 4; ++k) {
      const u32 pl = __builtin_amdgcn_perm(wb[1][k], wb[0][k], 0x05040100u), ph = __builtin_amdgcn_perm(wb[1][k], wb[0][k], 0x07060302u);
      a0[2 * k] = dot2(pl, hh.x, 0.f); a0[2 * k + 1] = dot2(ph, hh.x, 0.f);
      a1[2 * k] = dot2(pl, hh.z, 0.f); a1[2 * k + 1] = dot2(ph, hh.z, 0.f);
    }
#pragma unroll
    for (int k = 0; k < 4; ++k) {
      pa[2 * k] = __builtin_amdgcn_perm(wa[1][k], wa[0][k], 0x05040100u); pa[2 * k + 1] = __builtin_amdgcn_perm(wa[1][k], wa[0][k], 0x07060302u);
      pb[2 * k] = __builtin_amdgcn_perm(wa[3][k], wa[2][k], 0x05040100u); pb[2 * k + 1] = __builtin_amdgcn_perm(wa[3][k], wa[2][k], 0x07060302u);
    }
  }
#pragma unroll
  for (int k = 0; k < 8; ++k) asm volatile("" : "+v"(a0[k]), "+v"(a1[k]), "+v"(pa[k]), "+v"(pb[k]));
  int o2o = co < D ? O2_OFF + (h * 32 + j) * O2_S + (lane >> 5) * 1792 + kw * 512 + (((lane >> 4) & 1) * (kw == 3 ? 8 : 16) + cl) * 16 : OOB;
  LAS u32x4* orp = (LAS u32x4*)(L + L_O + rg * 4);
  LAS u32* ofp = L + F_O;
  PIN(o2o); PIN(orp); PIN(ofp);
  {
    const u32x4 oo = data_wait(ofp, orp);
#pragma unroll
    for (int k = 0; k < 8; ++k) {
      a0[k] = dot2(pb[k], oo.y, dot2(pa[k], oo.x, a0[k]));
      a1[k] = dot2(pb[k], oo.w, dot2(pa[k], oo.z, a1[k]));
    }
    float t[8];
#pragma unroll
    for (int k = 0; k < 8; ++k) t[k] = tr32(a0[k], a1[k]);
    st128(rws, o2o, tag4(tr16(t[0], t[4]), tr16(t[1], t[5]), tr16(t[2], t[6]), tr16(t[3], t[7]), tag8));
  }
  flag_set(L + F_O2 + kw);
}

extern "C" void kernel_launch(void* const* d_in, const int* in_sizes, int n_in, void* d_out, int out_size, void* d_ws, size_t ws_size,
                              hipStream_t stream) {
  fused_decode<<<256, 768, 0, stream>>>((const u16*)d_in[0], (const u16*)d_in[3], (const u16*)d_in[6], (const u16*)d_in[7], (const u16*)d_in[1],
                                        (char*)d_ws, (const u16*)d_in[2], (const u16*)d_in[4], (const u16*)d_in[8], (u16*)d_out);
}
```

```cpp
#include <hip/hip_runtime.h>
#include <stdint.h>

typedef unsigned int u32;
typedef unsigned short u16;
typedef unsigned long long u64;
typedef u32 u32x2 __attribute__((ext_vector_type(2)));
typedef u32 u32x4 __attribute__((ext_vector_type(4)));
typedef float f32x4 __attribute__((ext_vector_type(4)));
typedef float f32x16 __attribute__((ext_vector_type(16)));
typedef __bf16 bf16x2_t __attribute__((ext_vector_type(2)));
typedef __bf16 bf16x8_t __attribute__((ext_vector_type(8)));
#define LAS __attribute__((address_space(3)))
typedef __amdgpu_buffer_rsrc_t rsrc_t;

namespace {
constexpr int D = 448, SK = 32768, W = 4096, DH = 256, DF = 1792;
constexpr int AUX_NT = 2, AUX_SC1 = 16;
constexpr int OOB = 1 << 30;
constexpr u32 TMASK = 0xfu;

constexpr int QP_S = 768, GP_S = 1280, O1_S = 2304, O2_S = 3840;
constexpr int EP_OFF = 0;
constexpr int OR_OFF = 2048;
constexpr int ST_OFF = 4096;
constexpr int QP_OFF = ST_OFF + 8192;
constexpr int GP_OFF = QP_OFF + 256 * QP_S;
constexpr int O1_OFF = GP_OFF + 256 * GP_S;
constexpr int O2_OFF = O1_OFF + 256 * O1_S;
constexpr int WS_BYTES = O2_OFF + 256 * O2_S;

constexpr int L_XN = 0;
constexpr int L_QX = L_XN + 480;
constexpr int L_GX = L_QX + 128;
constexpr int L_Q = L_GX + 256;
constexpr int L_P = L_Q + 256;
constexpr int L_OP = L_P + 512;
constexpr int L_WM = L_OP + 2048;
constexpr int L_WL = L_WM + 8;
constexpr int L_H = L_WL + 8;
constexpr int L_O = L_H + 16;
constexpr int F_O = L_O + 16;
constexpr int L_R = L_O + 32;
constexpr int F_P = L_R + 128;
constexpr int F_VI = F_P + 4;
constexpr int F_O2 = F_VI + 4;
constexpr int F_Q = F_O2 + 4;
constexpr int F_QX = F_Q + 2, F_PUB = F_QX + 1;
constexpr int F_V = F_PUB + 1;
constexpr int F_R = F_V + 4;
constexpr int L_TAG = F_R + 8;
constexpr int L_SH = L_TAG + 2, F_SH = L_TAG + 3;
constexpr int L_END = L_TAG + 4;
constexpr u32 SET = 1u;
static_assert((L_END - L_WM) % 4 == 0 && (L_END - L_WM) / 4 <= 64, "one 16-byte store per lane clears the flag area");

constexpr int T_K0 = 20, T_KSTEP10 = 610, T_VSTEP10 = 625;
constexpr int K_FREE = 2;
constexpr int V_FINE = 4, V_FINE_LEAD = 25;
constexpr int WINDOW = 6;
constexpr int T_ORG = 300, ORG_BIAS = 5;
constexpr int due_k(int g) { return T_K0 + g * T_KSTEP10 / 10; }
constexpr int due_v(int g, int half) { return T_K0 + (8 * T_KSTEP10 + g * T_VSTEP10 + half * T_VSTEP10 / 2) / 10 - (g >= V_FINE ? V_FINE_LEAD : 0); }
constexpr int T_LATE = due_v(8, 0);
constexpr int Q_DELAY = 40, O1_DELAY = 12, O2_DELAY = 7;
constexpr int ROT = 70;
constexpr int CODE_SPAN = 15360;
constexpr u32 SPIN_LDS = 1u << 21, SPIN_MEM = 1u << 16;

__device__ __forceinline__ float asf(u32 v) { return __builtin_bit_cast(float, v); }
__device__ __forceinline__ u32 asu(float v) { return __builtin_bit_cast(u32, v); }
__device__ __forceinline__ float lo16(u32 v) { return asf(v << 16); }
__device__ __forceinline__ float hi16(u32 v) { return asf(v & 0xffff0000u); }
__device__ __forceinline__ u32 pkbf(float a, float b) {
  bf16x2_t r = {(__bf16)a, (__bf16)b};
  return __builtin_bit_cast(u32, r);
}
__device__ __forceinline__ float rbf(float a) { return (float)(__bf16)a; }
__device__ __forceinline__ float dot2(u32 a, u32 b, float c) {
  return __builtin_amdgcn_fdot2_f32_bf16(__builtin_bit_cast(bf16x2_t, a), __builtin_bit_cast(bf16x2_t, b), c, false);
}
template <int CTRL> __device__ __forceinline__ float dpp(float x) {
  return asf(__builtin_amdgcn_mov_dpp((int)asu(x), CTRL, 0xf, 0xf, true));
}
constexpr int XOR1 = 0xB1, XOR2 = 0x4E, ROR8 = 0x128, ROR4 = 0x124;
__device__ __forceinline__ float tr32(float a, float b) {
  auto r = __builtin_amdgcn_permlane32_swap(asu(a), asu(b), false, false);
  return asf(r[0]) + asf(r[1]);
}
__device__ __forceinline__ float tr16(float a, float b) {
  auto r = __builtin_amdgcn_permlane16_swap(asu(a), asu(b), false, false);
  return asf(r[0]) + asf(r[1]);
}
__device__ __forceinline__ float mx32(float a) {
  auto r = __builtin_amdgcn_permlane32_swap(asu(a), asu(a), false, false);
  return fmaxf(asf(r[0]), asf(r[1]));
}
__device__ __forceinline__ float mx16(float a) {
  auto r = __builtin_amdgcn_permlane16_swap(asu(a), asu(a), false, false);
  return fmaxf(asf(r[0]), asf(r[1]));
}
__device__ __forceinline__ float row_sum(float v) { v += dpp<ROR8>(v); v += dpp<ROR4>(v); v += dpp<XOR2>(v); v += dpp<XOR1>(v); return v; }
__device__ __forceinline__ float row_max(float v) {
  v = fmaxf(v, dpp<ROR8>(v)); v = fmaxf(v, dpp<ROR4>(v)); v = fmaxf(v, dpp<XOR2>(v)); v = fmaxf(v, dpp<XOR1>(v)); return v;
}
__device__ __forceinline__ float wave_sum(float v) { v = row_sum(v); v = tr16(v, v); return tr32(v, v); }

__device__ __forceinline__ rsrc_t mkrs(const void* p, u32 bytes) { return __builtin_amdgcn_make_buffer_rsrc((void*)p, 0, bytes, 0x00020000); }
template <int AUX> __device__ __forceinline__ u32x4 ld128(rsrc_t r, int voff, int soff = 0) { return __builtin_amdgcn_raw_buffer_load_b128(r, voff, soff, AUX); }
template <int AUX> __device__ __forceinline__ u32 ld32(rsrc_t r, int voff) { return __builtin_amdgcn_raw_buffer_load_b32(r, voff, 0, AUX); }
__device__ __forceinline__ void st128(rsrc_t r, int voff, u32x4 v) { __builtin_amdgcn_raw_buffer_store_b128(v, r, voff, 0, AUX_SC1); }
__device__ __forceinline__ u32 tagw(float v, u32 tag4) { return (asu(v) & ~TMASK) | tag4; }
__device__ __forceinline__ u32x4 tag4(float a, float b, float c, float d, u32 t) { return u32x4{tagw(a, t), tagw(b, t), tagw(c, t), tagw(d, t)}; }
__device__ __forceinline__ u32 untag4(u32x4& v, u32 t) { v.x ^= t; v.y ^= t; v.z ^= t; v.w ^= t; return v.x | v.y | v.z | v.w; }
__device__ __forceinline__ bool none(bool bad) { return __builtin_amdgcn_ballot_w64(bad) == 0ull; }
__device__ __forceinline__ bool all_ok(u32 bad) { return none((bad & TMASK) != 0u); }

#define CBAR() asm volatile("" ::: "memory")
#define PIN(v) asm volatile("" : "+v"(v))
#define LGKM0() asm volatile("s_waitcnt lgkmcnt(0)" ::: "memory")
template <int N, int SLEEP> __device__ __forceinline__ void flag_wait(LAS u32* p, int lane) {
  u32 v, cnt;
  const u32 addr = (u32)(uintptr_t)(p + (lane & (N - 1)));
  asm volatile("s_mov_b32 %1, %4\n"
               "1:\n\t"
               "ds_read_b32 %0, %2\n\t"
               "s_waitcnt lgkmcnt(0)\n\t"
               "v_cmp_ne_u32 vcc, %3, %0\n\t"
               "s_cbranch_vccz 2f\n\t"
               "s_sleep %5\n\t"
               "s_sub_u32 %1, %1, 1\n\t"
               "s_cbranch_scc0 1b\n"
               "2:"
               : "=&v"(v), "=&s"(cnt) : "v"(addr), "i"(SET), "i"(SPIN_LDS), "i"(SLEEP) : "vcc", "scc", "memory");
}
template <int DUE> __device__ __forceinline__ void pace(u64 org) {
  asm volatile("1:\n\t"
               "s_memrealtime vcc\n\t"
               "s_waitcnt lgkmcnt(0)\n\t"
               "s_sub_u32 vcc_lo, vcc_lo, %0\n\t"
               "s_cmp_lt_i32 vcc_lo, %1\n\t"
               "s_cbranch_scc0 2f\n\t"
               "s_sleep 1\n\t"
               "s_branch 1b\n"
               "2:"
               :: "s"((u32)org), "i"(DUE), "s"(org) : "vcc", "scc", "memory");
}
__device__ __forceinline__ void flag_set(LAS u32* p, u32 set = SET) { CBAR(); *p = set; CBAR(); }

__device__ __forceinline__ void poll2(rsrc_t rs, int voff, int soff, u32 t4, u32x4& r0, u32x4& r1) {
  u32 o0, o1, o2, o3, o4, o5, o6, o7, m, cnt;
  asm volatile("s_mov_b32 %9, %14\n"
               "1:\n\t"
               "buffer_load_dwordx4 v[144:147], %10, %11, 0 offen sc1\n\t"
               "buffer_load_dwordx4 v[148:151], %10, %11, %12 offen sc1\n\t"
               "s_sub_u32 %9, %9, 1\n\t"
               "s_waitcnt vmcnt(1)\n\t"
               "v_xor_b32 %0, %13, v144\n\tv_xor_b32 %1, %13, v145\n\tv_xor_b32 %2, %13, v146\n\tv_xor_b32 %3, %13, v147\n\t"
               "v_or3_b32 %8, %0, %1, %2\n\t"
               "s_waitcnt vmcnt(0)\n\t"
               "v_xor_b32 %4, %13, v148\n\tv_xor_b32 %5, %13, v149\n\tv_xor_b32 %6, %13, v150\n\tv_xor_b32 %7, %13, v151\n\t"
               "v_or3_b32 %8, %8, %3, %4\n\t"
               "v_or3_b32 %8, %8, %5, %6\n\t"
               "v_bitop3_b32 %8, %8, 15, %7 bitop3:0xc8\n\t"
               "v_cmp_ne_u32 vcc, 0, %8\n\t"
               "s_cbranch_vccz 2f\n\t"
               "s_cbranch_scc0 1b\n"
               "2:"
               : "=&v"(o0), "=&v"(o1), "=&v"(o2), "=&v"(o3), "=&v"(o4), "=&v"(o5), "=&v"(o6), "=&v"(o7), "=&v"(m), "=&s"(cnt)
               : "v"(voff), "s"(rs), "s"(soff), "s"(t4), "i"(SPIN_MEM)
               : "vcc", "scc", "memory", "v144", "v145", "v146", "v147", "v148", "v149", "v150", "v151");
  r0 = u32x4{o0, o1, o2, o3};
  r1 = u32x4{o4, o5, o6, o7};
}
__device__ __forceinline__ u32x4 data_wait(LAS u32* flag, LAS u32x4* p) {
  u32x4 r;
  u32 v, cnt;
  asm volatile("s_mov_b32 %2, %5\n"
               "1:\n\t"
               "ds_read_b32 %1, %3\n\t"
               "ds_read_b128 %0, %4\n\t"
               "s_sub_u32 %2, %2, 1\n\t"
               "s_waitcnt lgkmcnt(0)\n\t"
               "v_cmp_ne_u32 vcc, 1, %1\n\t"
               "s_cbranch_vccz 2f\n\t"
               "s_cbranch_scc0 1b\n"
               "2:"
               : "=&v"(r), "=&v"(v), "=&s"(cnt) : "v"((u32)(uintptr_t)flag), "v"((u32)(uintptr_t)p), "i"(SPIN_LDS) : "vcc", "scc", "memory");
  return r;
}

__device__ __forceinline__ void final_reduce(LAS u32* L, rsrc_t rws, const u16* x, u16* out, int rid, int grp, int lane, u32 tag, u64 tent, u64 bar) {
  LAS float* const LF = (LAS float*)L;
  __builtin_amdgcn_s_setprio(3);
  const u32 tag8 = tag & TMASK;
  const int rb = rid / 28, cb = rid % 28, qd = lane & 3;
  const int pi = cb & 7, ph = cb < 24 ? pi >> 2 : pi >> 1, pg = cb < 24 ? pi & 3 : pi & 1;
  const int col = rb * D + 128 * (cb >> 3) + 8 * (4 * pg + ((lane >> 2) & 3)) + 4 * ph + (lane & 3);
  float xres = lo16(x[col]);
  int oo = O2_OFF + (grp * 32 + (lane >> 2)) * O2_S + rb * 1792 + cb * 64 + qd * 16;
  LAS float* wp = LF + ((lane & 12) == 0 ? L_R + (4 * qd + (lane >> 4)) * 8 + grp : L_XN + 128 + lane);
  LAS u32* fp = L + F_R + grp;
  LAS u32* fr = L + F_R + (lane & 7);
  LAS f32x4* rp = (LAS f32x4*)(LF + L_R + (lane & 15) * 8);
  int so = lane < 16 ? col * 2 : OOB;
  u32 setv = SET;
  rsrc_t ro = mkrs(out, 2 * D * 2);
  asm volatile("" : "+s"(ro));
  PIN(xres); PIN(oo); PIN(wp); PIN(fp); PIN(fr); PIN(rp); PIN(so); PIN(setv);
  flag_wait<1, 1>(L + F_PUB, lane);
  flag_wait<4, 0>(L + F_O2, lane);
  __builtin_amdgcn_s_sleep(O2_DELAY);
  u32x4 a0, a1;
  poll2(rws, oo, 16 * O2_S, tag8, a0, a1);
  float f = tr16(tr32(asf(a0.x) + asf(a1.x), asf(a0.z) + asf(a1.z)), tr32(asf(a0.y) + asf(a1.y), asf(a0.w) + asf(a1.w)));
  f += dpp<ROR8>(f); f += dpp<ROR4>(f);
  *wp = f;
  flag_set(fp, setv);
  u32 fl = *fr;
  CBAR();
  const f32x4 r0 = rp[0], r1 = rp[1];
  const f32x4 rs4 = r0 + r1;
  const float tot = xres + ((rs4.x + rs4.z) + (rs4.y + rs4.w));
  u32 res = pkbf(tot, tot);
  PIN(res);
  if (none(fl != SET)) {
    __builtin_amdgcn_raw_buffer_store_b16((u16)res, ro, so, 0, 0);
    if (rid == 0 && lane == 0) __builtin_amdgcn_raw_buffer_store_b32(tag, rws, EP_OFF, 0, AUX_SC1);
    if (rid == 0) {
      const u64 now = __builtin_amdgcn_s_memrealtime();
      const u64 until = tent + 3000 + 8 * (bar - tent);
      while (__builtin_amdgcn_s_memrealtime() < until) __builtin_amdgcn_s_sleep(1);
    }
  }
}

}

extern "C" __global__ void __launch_bounds__(768, 3)
fused_decode(const u16* __restrict__ x, const u16* __restrict__ wq, const u16* __restrict__ wg_, const u16* __restrict__ wu,
             const u16* __restrict__ kc, char* __restrict__ ws, const u16* __restrict__ vc, const u16* __restrict__ wo,
             const u16* __restrict__ wd, u16* __restrict__ out) {
  __shared__ __attribute__((aligned(16))) u32 smem[L_END];
  LAS u32* const L = (LAS u32*)smem;
  LAS float* const LF = (LAS float*)smem;
  const int wg = blockIdx.x, h = wg & 7, j = wg >> 3, b = j & 1, sp = j >> 1, reg = wg & 15;
  const int w = __builtin_amdgcn_readfirstlane(threadIdx.x >> 6), lane = threadIdx.x & 63, kw = w & 3;
  const u64 tent = __builtin_amdgcn_s_memrealtime();
  u64 bar;
  asm volatile("s_load_dword s94, %0, 0x0\n\ts_load_dword s95, %0, 0x40" :: "s"(__builtin_amdgcn_kernarg_segment_ptr()) : "s94", "s95");
  u64 pc0;
  asm volatile("s_getpc_b64 %0" : "=s"(pc0));
  const u32 code_touch = ld32<0>(mkrs((const char*)(pc0 & ~255ull), w == 0 ? CODE_SPAN : 0), lane < 5 ? j * 640 + lane * 128 : OOB);
  __builtin_amdgcn_sched_barrier(0);
#define ROLE_ENTRY() const rsrc_t rws = mkrs(ws, WS_BYTES)
#define ROLE_TAG() const u32 tag = __builtin_amdgcn_readfirstlane(L[L_TAG]), tag8 = tag & TMASK
  const u32 blk0 = sp * 4 + reg * ROT;
#define BLOCK_OFF(slot) (((blk0 + (((slot) >> 2) << 6) + ((slot) & 3u)) & 511u) << 12)
  const int hx = h ^ 4;
  u64 org;
#define STREAM_GROUP(G, VOFF, SLOT, ISK) \
  if (!(ISK) || (G) >= K_FREE) pace<(ISK) ? due_k(G) : due_v(G, 0)>(org); \
  if (4 * (G) > WINDOW) asm volatile("s_waitcnt vmcnt(%0)" :: "i"(WINDOW) : "memory"); \
  _Pragma("unroll") for (int i = 4 * (G); i < 4 * (G) + 2; ++i) d[i] = ld128<AUX_NT>(rkv, (VOFF), (int)BLOCK_OFF(SLOT)); \
  if (!(ISK) && (G) >= V_FINE) { __builtin_amdgcn_sched_barrier(0); pace<due_v(G, 1)>(org); } \
  _Pragma("unroll") for (int i = 4 * (G) + 2; i < 4 * (G) + 4; ++i) d[i] = ld128<AUX_NT>(rkv, (VOFF), (int)BLOCK_OFF(SLOT)); \
  __builtin_amdgcn_sched_barrier(0);
#define STREAM_LOADS(VOFF, SLOT, ISK) \
  STREAM_GROUP(0, VOFF, SLOT, ISK) STREAM_GROUP(1, VOFF, SLOT, ISK) STREAM_GROUP(2, VOFF, SLOT, ISK) STREAM_GROUP(3, VOFF, SLOT, ISK) \
  STREAM_GROUP(4, VOFF, SLOT, ISK) STREAM_GROUP(5, VOFF, SLOT, ISK) STREAM_GROUP(6, VOFF, SLOT, ISK) STREAM_GROUP(7, VOFF, SLOT, ISK)
#define WINDOW_RSRC(SRC) mkrs((SRC) + ((size_t)reg * SK + (SK - W)) * DH, W * DH * 2)

  if (__builtin_expect(w >= 4, 0)) {
  u32 tag_out;
  if (w < 8) {
    __builtin_amdgcn_s_setprio(2);
    const int g = lane >> 4, key = (lane >> 1) & 7;
    const bool hb = lane & 1;
    const int kvo = key * 512 + (2 * g + (lane & 1)) * 16;
    const rsrc_t rkv = WINDOW_RSRC(kc);
    u32x4 d[32];
    __builtin_amdgcn_sched_barrier(0);
    __builtin_amdgcn_s_barrier();
    org = __builtin_amdgcn_s_memrealtime(); bar = org;
    __builtin_amdgcn_sched_barrier(0);
    STREAM_LOADS(kvo + (i & 3) * 128, kw + 4 * (i >> 2), true)
    ROLE_ENTRY();
    flag_wait<2, 1>(L + F_Q, lane);
    ROLE_TAG();
    u32x4 qa[4];
#pragma unroll
    for (int i = 0; i < 4; ++i) qa[i] = *(LAS u32x4*)(L + L_Q + (lane & 1) * 128 + i * 32 + (2 * g + ((lane >> 1) & 1)) * 4);
    f32x4 ea, eb;
#pragma unroll
    for (int n = 0; n < 4; ++n) {
      f32x4 ca = {}, cb = {};
#pragma unroll
      for (int i = 0; i < 4; ++i) {
        ca = __builtin_amdgcn_mfma_f32_16x16x32_bf16(__builtin_bit_cast(bf16x8_t, qa[i]), __builtin_bit_cast(bf16x8_t, d[8 * n + i]), ca, 0, 0, 0);
        cb = __builtin_amdgcn_mfma_f32_16x16x32_bf16(__builtin_bit_cast(bf16x8_t, qa[i]), __builtin_bit_cast(bf16x8_t, d[8 * n + 4 + i]), cb, 0, 0, 0);
      }
#pragma unroll
      for (int k = 0; k < 4; ++k) { ea[k] = (n == 0 || g == n) ? ca[k] : ea[k]; eb[k] = (n == 0 || g == n) ? cb[k] : eb[k]; }
    }
    asm volatile("" :: "v"(code_touch));
    float sc[2];
#pragma unroll
    for (int hd = 0; hd < 2; ++hd) sc[hd] = rbf((hb ? eb[2 + hd] : ea[hd]) + dpp<XOR1>(hb ? ea[2 + hd] : eb[hd])) * 0.0625f;
    const float wm0 = mx32(mx16(row_max(sc[0]))), wm1 = mx32(mx16(row_max(sc[1])));
    CBAR();
    if (lane < 2) L[L_WM + kw * 2 + lane] = (asu(lane ? wm1 : wm0) & 0xffff0000u) | SET;
    u32x4 ma, mb;
    for (u32 s = 0; s < SPIN_LDS; ++s) {
      CBAR();
      ma = *(LAS u32x4*)(L + L_WM) ^ SET; mb = *(LAS u32x4*)(L + L_WM + 4) ^ SET;
      if (none((((ma.x | ma.y | ma.z | ma.w) | (mb.x | mb.y | mb.z | mb.w)) & 0xffffu) != 0u)) break;
    }
    const float M0 = fmaxf(fmaxf(asf(ma.x), asf(ma.z)), fmaxf(asf(mb.x), asf(mb.z)));
    const float M1 = fmaxf(fmaxf(asf(ma.y), asf(ma.w)), fmaxf(asf(mb.y), asf(mb.w)));
    const float p0 = rbf(__builtin_amdgcn_exp2f((sc[0] - M0) * 1.4426950408889634f));
    const float p1 = rbf(__builtin_amdgcn_exp2f((sc[1] - M1) * 1.4426950408889634f));
    LAS u32* pp = L + L_P + key * 64 + kw + 4 * (2 * g + (lane & 1));
    pp[0] = asu(p0) | (asu(p0) >> 16);
    pp[32] = asu(p1) | (asu(p1) >> 16);
    float ls = tr32(p0, p1);
    ls = row_sum(tr16(ls, ls));
    if ((lane & 31) == 0) LF[L_WL + kw * 2 + (lane >> 5)] = ls;
    flag_set(L + F_P + kw);

    if (kw == 0) {
      flag_wait<4, 0>(L + F_P, lane);
      if (lane < 2) {
        const float Ls = (LF[L_WL + lane] + LF[L_WL + 2 + lane]) + (LF[L_WL + 4 + lane] + LF[L_WL + 6 + lane]);
        st128(rws, ST_OFF + (((h * 2 + lane) * 2 + b) * 16 + sp) * 16, u32x4{asu(lane ? M1 : M0), tag, asu(Ls), tag});
      }
    } else if (kw == 1) {
      __builtin_amdgcn_s_setprio(3);
      const int s = lane >> 2, qd = lane & 3;
      const int so = ST_OFF + ((hx * 2 + (j >> 4)) * 32 + s) * 16;
      const int oo = O1_OFF + (hx * 32 + 2 * s) * O1_S + j * 64 + qd * 16;
      u32x4 s0, s1, a0, a1;
      flag_wait<4, 1>(L + F_VI, lane);
      for (u32 it = 0; it < SPIN_MEM; ++it) {
        CBAR();
        s0 = ld128<AUX_SC1>(rws, so); s1 = ld128<AUX_SC1>(rws, so + 256);
        if (none((s0.y != tag) | (s0.w != tag) | (s1.y != tag) | (s1.w != tag))) break;
        flag_wait<1, 1>(L + F_PUB, lane);
      }
      float wgt[2];
#pragma unroll
      for (int bb = 0; bb < 2; ++bb) {
        const float m = asf(bb ? s1.x : s0.x), l = asf(bb ? s1.z : s0.z);
        float mg = fmaxf(m, dpp<ROR8>(m)); mg = fmaxf(mg, dpp<ROR4>(mg)); mg = mx32(mx16(mg));
        const float e = __builtin_amdgcn_exp2f((m - mg) * 1.4426950408889634f);
        float lg = e * l; lg += dpp<ROR8>(lg); lg += dpp<ROR4>(lg); lg = tr16(lg, lg); lg = tr32(lg, lg);
        wgt[bb] = e * __builtin_amdgcn_rcpf(lg);
      }
      LAS u32* ow = L + ((lane & 12) == 0 ? L_O + qd * 4 + (lane >> 4) : L_XN + lane);
      LAS u32* of = L + F_O;
      u32 setv = SET;
      PIN(wgt[0]); PIN(wgt[1]); PIN(ow); PIN(of); PIN(setv);
      flag_wait<1, 0>(L + F_PUB, lane);
      __builtin_amdgcn_s_sleep(O1_DELAY);
      poll2(rws, oo, O1_S, tag8, a0, a1);
      f32x4 m0 = __builtin_bit_cast(f32x4, a0) * wgt[0], m1 = __builtin_bit_cast(f32x4, a1) * wgt[1];
      PIN(m0); PIN(m1);
      const float v0 = tr32(m0.x, m1.x), v2 = tr32(m0.z, m1.z), v1 = tr32(m0.y, m1.y), v3 = tr32(m0.w, m1.w);
      float e0 = tr16(v0, v2), e1 = tr16(v1, v3);
      e0 += dpp<ROR8>(e0); e1 += dpp<ROR8>(e1);
      e0 += dpp<ROR4>(e0); e1 += dpp<ROR4>(e1);
      *ow = pkbf(e0, e1);
      flag_set(of, setv);
    }
    tag_out = tag;
  } else {
    if (kw == 0 && lane < (L_END - L_WM) / 4) *(LAS u32x4*)(L + L_WM + 4 * lane) = u32x4{0u, 0u, 0u, 0u};
    LGKM0();
    __builtin_amdgcn_s_barrier();
    org = __builtin_amdgcn_s_memrealtime(); bar = org;
    __builtin_amdgcn_s_setprio(1);
    __builtin_amdgcn_sched_barrier(0);
    ROLE_ENTRY();
    flag_wait<1, 4>(L + F_SH, lane);
    org += (u64)(long long)(int)__builtin_amdgcn_readfirstlane(L[L_SH]);
    const rsrc_t rkv = WINDOW_RSRC(vc);
    u32x4 d[32];
    STREAM_LOADS(lane * 16 + kw * 1024, (u32)i, false)
    flag_set(L + F_VI + kw);
    const int m = lane & 31, hf = lane >> 5;
    u32x4 msk;
#pragma unroll
    for (int r = 0; r < 4; ++r) msk[r] = (m < 16 && ((m & 7) >> 1) == r) ? ((m & 1) ? 0xffff0000u : 0x0000ffffu) : 0u;
    LAS f32x4* op = (LAS f32x4*)(LF + L_OP) + kw * 128 + 2 * m + hf;
    LAS f32x4* rp = (LAS f32x4*)(LF + L_OP) + lane;
    LAS u32* fp = L + F_V + kw;
    LAS u32* fr = L + F_V + (lane & 3);
    int o1o = O1_OFF + (h * 32 + j) * O1_S + lane * 16;
    PIN(op); PIN(rp); PIN(fp); PIN(fr); PIN(o1o);
    flag_wait<4, 1>(L + F_P, lane);
    ROLE_TAG();
    const LAS u32x4* pp = (const LAS u32x4*)(L + L_P + ((kw * 2 + hf) * 2 + ((m >> 3) & 1)) * 32);
    f32x16 acc = {};
#pragma unroll
    for (int g = 0; g < 8; ++g) {
      const u32x4 p4 = pp[g];
#pragma unroll
      for (int k = 0; k < 4; ++k) {
        const u32x4 a = msk & p4[k];
        acc = __builtin_amdgcn_mfma_f32_32x32x16_bf16(__builtin_bit_cast(bf16x8_t, a), __builtin_bit_cast(bf16x8_t, d[4 * g + k]), acc, 0, 0, 0);
      }
      __builtin_amdgcn_sched_barrier(0);
    }
    asm volatile("" :: "v"(code_touch));
    op[0] = f32x4{acc[0], acc[1], acc[2], acc[3]};
    op[64] = f32x4{acc[4], acc[5], acc[6], acc[7]};
    flag_set(fp);
    const u32 fl = *fr;
    CBAR();
    const f32x4 o0 = (rp[0] + rp[128]) + (rp[256] + rp[384]);
    const f32x4 o1 = (rp[64] + rp[192]) + (rp[320] + rp[448]);
    u32x4 t0 = tag4(o0.x, o0.y, o0.z, o0.w, tag8), t1 = tag4(o1.x, o1.y, o1.z, o1.w, tag8);
    PIN(t0); PIN(t1);
    if (none(fl != SET)) {
      st128(rws, o1o, t0);
      st128(rws, o1o + 1024, t1);
      flag_set(L + F_PUB);
    }
    tag_out = tag;
  }
  if (!(h & 1) && j < 14) final_reduce(L, mkrs(ws, WS_BYTES), x, out, (h >> 1) * 14 + j, w - 4, lane, tag_out, tent, bar);
  return;
  }

  __builtin_amdgcn_s_setprio(3);
  ROLE_ENTRY();
  const bool mlp = wg < 224;
  const int cb2 = wg % 28, ke = wg / 28;
  const int cbf = cb2 == 27 ? 0 : cb2 + 1;
  const int rr = lane >> 3, c8 = lane & 7;
  const rsrc_t rx = mkrs((const u16*)x, 2 * D * 2);
  const u32x4 xa = ld128<0>(rx, lane < 56 ? lane * 16 : OOB), xb = ld128<0>(rx, lane < 56 ? D * 2 + lane * 16 : OOB);
  const u32 epoch = ld32<AUX_SC1>(rws, EP_OFF);
  const int row0 = kw < 2 ? 112 * (j >> 3) + 64 * kw : 56 * ke;
  const int nrow = kw == 0 ? 64 : kw == 1 ? 48 : mlp ? 56 : 0;
  u32x4 wv[8];
  {
    const rsrc_t rw = mkrs(kw < 2 ? wq : kw == 2 ? wg_ : wu, kw < 2 ? D * 4096 * 2 : D * DF * 2);
    const int rs = kw < 2 ? 8192 : DF * 2;
    const int vw0 = nrow ? (row0 + 2 * rr) * rs + (kw < 2 ? (hx * 512 + (j & 7) * 64) * 2 : cb2 * 128) + c8 * 16 : OOB;
    const int vw3 = 48 + 2 * rr < nrow ? vw0 : OOB;
#pragma unroll
    for (int i = 0; i < 8; ++i) wv[i] = ld128<AUX_NT>(rw, i < 6 ? vw0 : vw3, (16 * (i >> 1) + (i & 1)) * rs);
  }
  const int eo = lane < 8 ? QP_OFF + (hx * 32 + j) * QP_S + lane * 64 : lane < 24 ? GP_OFF + wg * GP_S + (lane - 8) * 64 : ST_OFF + (wg & 127) * 64;
  const u32 box_touch = ld32<AUX_SC1>(rws, (kw == 1 && lane < 25) ? eo : OOB);
  __builtin_amdgcn_sched_barrier(0);
  __builtin_amdgcn_s_barrier();
  org = __builtin_amdgcn_s_memrealtime();
  __builtin_amdgcn_sched_barrier(0);

#pragma unroll
  for (int bb = 0; bb < 2; ++bb) {
    const u32x4 xv = bb ? xb : xa;
    float v[8];
#pragma unroll
    for (int k = 0; k < 4; ++k) { v[2 * k] = lo16(xv[k]); v[2 * k + 1] = hi16(xv[k]); }
    float s = 0.f;
#pragma unroll
    for (int k = 0; k < 8; ++k) s += v[k];
    const float mean = wave_sum(s) * (1.f / D);
    float q = 0.f;
#pragma unroll
    for (int k = 0; k < 8; ++k) { const float t = v[k] - mean; q += t * t; }
    q = lane < 56 ? q : 0.f;
    const float var = rbf(wave_sum(q) * (1.f / D));
    const float den = rbf(__builtin_amdgcn_sqrtf(rbf(var + 1e-5f))), rden = __builtin_amdgcn_rcpf(den), mb = rbf(mean);
#pragma unroll
    for (int k = 0; k < 8; ++k) { const float c = rbf(v[k] - mb), r = c * rden; v[k] = fmaf(fmaf(-r, den, c), rden, r); }
    u32x4 o;
#pragma unroll
    for (int k = 0; k < 4; ++k) o[k] = pkbf(v[2 * k], v[2 * k + 1]);
    if (lane < 56) *(LAS u32x4*)(L + L_XN + bb * 224 + lane * 4) = o;
    else if (bb == 1) *(LAS u32x4*)(L + L_XN + 224 + lane * 4) = u32x4{0u, 0u, 0u, 0u};
  }
  asm volatile("" :: "v"(code_touch));
  const u32 tag = __builtin_amdgcn_readfirstlane(epoch) + 1u, tag8 = tag & TMASK;
  if (kw == 0) L[L_TAG] = tag;
  if (kw == 3 && wg < 8 && lane == 0) __builtin_amdgcn_raw_buffer_store_b64(u32x2{(u32)org, tag}, rws, OR_OFF + wg * 8, 0, AUX_SC1);

  {
    float a0[8] = {}, a1[8] = {};
#pragma unroll
    for (int k = 0; k < 4; ++k) {
      const u32 x0 = L[L_XN + row0 / 2 + rr + 8 * k], x1 = L[L_XN + 224 + row0 / 2 + rr + 8 * k];
#pragma unroll
      for (int c = 0; c < 4; ++c) {
        const u32 pl = __builtin_amdgcn_perm(wv[2 * k + 1][c], wv[2 * k][c], 0x05040100u), ph = __builtin_amdgcn_perm(wv[2 * k + 1][c], wv[2 * k][c], 0x07060302u);
        a0[2 * c] = dot2(pl, x0, a0[2 * c]); a0[2 * c + 1] = dot2(ph, x0, a0[2 * c + 1]);
        a1[2 * c] = dot2(pl, x1, a1[2 * c]); a1[2 * c + 1] = dot2(ph, x1, a1[2 * c + 1]);
      }
    }
    asm volatile("" :: "v"(box_touch));
    float t[8], u[4];
#pragma unroll
    for (int k = 0; k < 8; ++k) t[k] = tr32(a0[k], a1[k]);
#pragma unroll
    for (int k = 0; k < 4; ++k) { u[k] = tr16(t[k], t[k + 4]); u[k] += dpp<ROR8>(u[k]); }
    const int bb = lane >> 5, hf = (lane >> 4) & 1;
    LAS f32x4* qx = (LAS f32x4*)(LF + L_QX) + bb * 16 + c8 * 2 + hf;
    if (kw == 1) {
      if (!(lane & 8)) *qx = f32x4{u[0], u[1], u[2], u[3]};
      flag_set(L + F_QX);
    } else if (kw == 0) {
      flag_wait<1, 0>(L + F_QX, lane);
      const f32x4 o = *qx;
      if (!(lane & 8)) st128(rws, QP_OFF + (hx * 32 + j) * QP_S + bb * 256 + hf * 128 + c8 * 16, tag4(u[0] + o.x, u[1] + o.y, u[2] + o.z, u[3] + o.w, tag8));
    } else if (mlp) {
      LAS u32x4* gx = (LAS u32x4*)(L + L_GX) + (kw - 2) * 32;
      if (!(lane & 8)) gx[c8 * 4 + bb * 2 + hf] = tag4(u[0], u[1], u[2], u[3], tag8);
      CBAR();
      if (lane < 32) st128(rws, GP_OFF + wg * GP_S + (lane >> 2) * 128 + (kw - 2) * 64 + (lane & 3) * 16, gx[lane]);
    }
  }

  if (kw < 2) {
    __builtin_amdgcn_s_sleep(Q_DELAY);
    const int qo = QP_OFF + (h * 32 + 4 * kw + 8 * (lane >> 4)) * QP_S + b * 256 + (lane & 15) * 16;
    u32x4 a[4];
    for (u32 s = 0; s < SPIN_MEM; ++s) {
      CBAR();
#pragma unroll
      for (int i = 0; i < 4; ++i) a[i] = ld128<AUX_SC1>(rws, qo, i * QP_S);
      if (all_ok((untag4(a[0], tag8) | untag4(a[1], tag8)) | (untag4(a[2], tag8) | untag4(a[3], tag8)))) break;
    }
#pragma unroll
    for (int i = 0; i < 2; ++i) {
      const u32x4 a0 = a[2 * i], a1 = a[2 * i + 1];
      const float v0 = tr32(asf(a0.x), asf(a1.x)), v1 = tr32(asf(a0.y), asf(a1.y)), v2 = tr32(asf(a0.z), asf(a1.z)), v3 = tr32(asf(a0.w), asf(a1.w));
      L[L_Q + (4 * kw + 2 * i + (lane >> 5)) * 32 + 4 * (lane & 7) + 2 * ((lane >> 3) & 1) + ((lane >> 4) & 1)] = pkbf(tr16(v0, v2), tr16(v1, v3));
    }
    flag_set(L + F_Q + kw);
  } else if (kw == 2) {
    u32 h0 = 0u, h1 = 0u;
    if (mlp) {
      const int go = GP_OFF + (cbf + 28 * rr) * GP_S + ke * 128 + c8 * 16;
      u32x4 a;
      flag_wait<2, 1>(L + F_Q, lane);
      for (u32 s = 0; s < SPIN_MEM; ++s) {
        CBAR();
        a = ld128<AUX_SC1>(rws, go);
        if (all_ok(untag4(a, tag8))) break;
        __builtin_amdgcn_s_sleep(16);
      }
      float hv[4];
#pragma unroll
      for (int k = 0; k < 4; ++k) {
        float g = asf(a[k]);
        g += dpp<ROR8>(g); g = tr16(g, g); g = tr32(g, g);
        g = rbf(g);
        const float uu = dpp<ROR4>(g);
        const float z = 0.7978845608028654f * (g + 0.044715f * g * g * g);
        const float th = 1.f - 2.f * __builtin_amdgcn_rcpf(1.f + __builtin_amdgcn_exp2f(z * 2.8853900817779268f));
        hv[k] = rbf(0.5f * g * (1.f + th)) * uu;
      }
      h0 = pkbf(hv[0], hv[1]); h1 = pkbf(hv[2], hv[3]);
    }
    if (lane < 4) {
      *(LAS u32x2*)(L + L_H + (2 * (lane & 1)) * 4 + (lane >> 1) * 2) = u32x2{h0, SET};
      *(LAS u32x2*)(L + L_H + (2 * (lane & 1) + 1) * 4 + (lane >> 1) * 2) = u32x2{h1, SET};
    }
    CBAR();
  } else {
    pace<T_ORG>(org);
    const u32x2 o = __builtin_amdgcn_raw_buffer_load_b64(rws, OR_OFF + (lane & 7) * 8, 0, AUX_SC1);
    const bool ok = o.y == tag;
    const float n = row_sum(ok ? 1.f : 0.f), dsum = row_sum(ok ? (float)(int)(o.x - (u32)org) : 0.f);
    const float sh = n > 0.f ? fminf(fmaxf(dsum * __builtin_amdgcn_rcpf(n) + ORG_BIAS, -80.f), 80.f) : 0.f;
    L[L_SH] = (u32)(int)sh;
    flag_set(L + F_SH);
  }

  const int rg = lane >> 4, cl = lane & 15;
  const int co = kw * 128 + cl * 8;
  u32x4 wa[4], wb[2];
  flag_wait<4, 1>(L + F_VI, lane);
  pace<T_LATE>(org + (u64)(long long)(int)__builtin_amdgcn_readfirstlane(L[L_SH]));
  {
    const rsrc_t ro = mkrs(wo, 4096 * D * 2), rd = mkrs(wd, mlp ? DF * D * 2 : 0);
    const int vo = co < D ? (hx * 512 + j * 16 + 4 * rg) * (D * 2) + co * 2 : OOB;
    const int vd = co < D ? (64 * cbf + 8 * ke + 2 * rg) * (D * 2) + co * 2 : OOB;
#pragma unroll
    for (int i = 0; i < 2; ++i) wb[i] = ld128<AUX_NT>(rd, vd, i * D * 2);
#pragma unroll
    for (int i = 0; i < 4; ++i) wa[i] = ld128<AUX_NT>(ro, vo, i * D * 2);
  }
  __builtin_amdgcn_sched_barrier(0);
  float a0[8], a1[8];
  u32 pa[8], pb[8];
  {
    u32x4 hh;
    for (u32 s = 0; s < SPIN_LDS; ++s) {
      CBAR();
      hh = *(LAS u32x4*)(L + L_H + rg * 4);
      if (none((hh.y != SET) | (hh.w != SET))) break;
      __builtin_amdgcn_s_sleep(1);
    }
#pragma unroll
    for (int k = 0; k < 4; ++k) {
      const u32 pl = __builtin_amdgcn_perm(wb[1][k], wb[0][k], 0x05040100u), ph = __builtin_amdgcn_perm(wb[1][k], wb[0][k], 0x07060302u);
      a0[2 * k] = dot2(pl, hh.x, 0.f); a0[2 * k + 1] = dot2(ph, hh.x, 0.f);
      a1[2 * k] = dot2(pl, hh.z, 0.f); a1[2 * k + 1] = dot2(ph, hh.z, 0.f);
    }
#pragma unroll
    for (int k = 0; k < 4; ++k) {
      pa[2 * k] = __builtin_amdgcn_perm(wa[1][k], wa[0][k], 0x05040100u); pa[2 * k + 1] = __builtin_amdgcn_perm(wa[1][k], wa[0][k], 0x07060302u);
      pb[2 * k] = __builtin_amdgcn_perm(wa[3][k], wa[2][k], 0x05040100u); pb[2 * k + 1] = __builtin_amdgcn_perm(wa[3][k], wa[2][k], 0x07060302u);
    }
  }
#pragma unroll
  for (int k = 0; k < 8; ++k) asm volatile("" : "+v"(a0[k]), "+v"(a1[k]), "+v"(pa[k]), "+v"(pb[k]));
  int o2o = co < D ? O2_OFF + (h * 32 + j) * O2_S + (lane >> 5) * 1792 + kw * 512 + (((lane >> 4) & 1) * (kw == 3 ? 8 : 16) + cl) * 16 : OOB;
  LAS u32x4* orp = (LAS u32x4*)(L + L_O + rg * 4);
  LAS u32* ofp = L + F_O;
  PIN(o2o); PIN(orp); PIN(ofp);
  {
    const u32x4 oo = data_wait(ofp, orp);
#pragma unroll
    for (int k = 0; k < 8; ++k) {
      a0[k] = dot2(pb[k], oo.y, dot2(pa[k], oo.x, a0[k]));
      a1[k] = dot2(pb[k], oo.w, dot2(pa[k], oo.z, a1[k]));
    }
    float t[8];
#pragma unroll
    for (int k = 0; k < 8; ++k) t[k] = tr32(a0[k], a1[k]);
    st128(rws, o2o, tag4(tr16(t[0], t[4]), tr16(t[1], t[5]), tr16(t[2], t[6]), tr16(t[3], t[7]), tag8));
  }
  flag_set(L + F_O2 + kw);
}

extern "C" void kernel_launch(void* const* d_in, const int* in_sizes, int n_in, void* d_out, int out_size, void* d_ws, size_t ws_size,
                              hipStream_t stream) {
  fused_decode<<<256, 768, 0, stream>>>((const u16*)d_in[0], (const u16*)d_in[3], (const u16*)d_in[6], (const u16*)d_in[7], (const u16*)d_in[1],
                                        (char*)d_ws, (const u16*)d_in[2], (const u16*)d_in[4], (const u16*)d_in[8], (u16*)d_out);
}
```

```cpp
#include <hip/hip_runtime.h>
#include <stdint.h>

typedef unsigned int u32;
typedef unsigned short u16;
typedef unsigned long long u64;
typedef u32 u32x2 __attribute__((ext_vector_type(2)));
typedef u32 u32x4 __attribute__((ext_vector_type(4)));
typedef float f32x4 __attribute__((ext_vector_type(4)));
typedef float f32x16 __attribute__((ext_vector_type(16)));
typedef __bf16 bf16x2_t __attribute__((ext_vector_type(2)));
typedef __bf16 bf16x8_t __attribute__((ext_vector_type(8)));
#define LAS __attribute__((address_space(3)))
typedef __amdgpu_buffer_rsrc_t rsrc_t;

namespace {
constexpr int D = 448, SK = 32768, W = 4096, DH = 256, DF = 1792;
constexpr int AUX_NT = 2, AUX_SC1 = 16;
constexpr int OOB = 1 << 30;
constexpr u32 TMASK = 0xfu;

constexpr int QP_S = 768, GP_S = 1280, O1_S = 2304, O2_S = 3840;
constexpr int EP_OFF = 0;
constexpr int OR_OFF = 2048;
constexpr int ST_OFF = 4096;
constexpr int QP_OFF = ST_OFF + 8192;
constexpr int GP_OFF = QP_OFF + 256 * QP_S;
constexpr int O1_OFF = GP_OFF + 256 * GP_S;
constexpr int O2_OFF = O1_OFF + 256 * O1_S;
constexpr int WS_BYTES = O2_OFF + 256 * O2_S;

constexpr int L_XN = 0;
constexpr int L_QX = L_XN + 480;
constexpr int L_GX = L_QX + 128;
constexpr int L_Q = L_GX + 256;
constexpr int L_P = L_Q + 256;
constexpr int L_OP = L_P + 512;
constexpr int L_WM = L_OP + 2048;
constexpr int L_WL = L_WM + 8;
constexpr int L_H = L_WL + 8;
constexpr int L_O = L_H + 16;
constexpr int F_O = L_O + 16;
constexpr int L_R = L_O + 32;
constexpr int F_P = L_R + 128;
constexpr int F_VI = F_P + 4;
constexpr int F_O2 = F_VI + 4;
constexpr int F_Q = F_O2 + 4;
constexpr int F_QX = F_Q + 2, F_PUB = F_QX + 1;
constexpr int F_V = F_PUB + 1;
constexpr int F_R = F_V + 4;
constexpr int L_TAG = F_R + 8;
constexpr int L_SH = L_TAG + 2, F_SH = L_TAG + 3;
constexpr int L_END = L_TAG + 4;
constexpr u32 SET = 1u;
static_assert((L_END - L_WM) % 4 == 0 && (L_END - L_WM) / 4 <= 64, "one 16-byte store per lane clears the flag area");

constexpr int T_K0 = 25, T_KSTEP10 = 610, T_VSTEP10 = 625;
constexpr int K_FREE = 2;
constexpr int V_FINE = 4, V_FINE_LEAD = 25;
constexpr int WINDOW = 6;
constexpr int T_ORG = 300, ORG_BIAS = 5;
constexpr int due_k(int g) { return T_K0 + g * T_KSTEP10 / 10; }
constexpr int due_v(int g, int half) { return T_K0 + (8 * T_KSTEP10 + g * T_VSTEP10 + half * T_VSTEP10 / 2) / 10 - (g >= V_FINE ? V_FINE_LEAD : 0); }
constexpr int T_LATE = due_v(8, 0);
constexpr int Q_DELAY = 40, O1_DELAY = 12, O2_DELAY = 7;
constexpr int ROT = 70;
constexpr int CODE_SPAN = 15360;
constexpr u32 SPIN_LDS = 1u << 21, SPIN_MEM = 1u << 16;

__device__ __forceinline__ float asf(u32 v) { return __builtin_bit_cast(float, v); }
__device__ __forceinline__ u32 asu(float v) { return __builtin_bit_cast(u32, v); }
__device__ __forceinline__ float lo16(u32 v) { return asf(v << 16); }
__device__ __forceinline__ float hi16(u32 v) { return asf(v & 0xffff0000u); }
__device__ __forceinline__ u32 pkbf(float a, float b) {
  bf16x2_t r = {(__bf16)a, (__bf16)b};
  return __builtin_bit_cast(u32, r);
}
__device__ __forceinline__ float rbf(float a) { return (float)(__bf16)a; }
__device__ __forceinline__ float dot2(u32 a, u32 b, float c) {
  return __builtin_amdgcn_fdot2_f32_bf16(__builtin_bit_cast(bf16x2_t, a), __builtin_bit_cast(bf16x2_t, b), c, false);
}
template <int CTRL> __device__ __forceinline__ float dpp(float x) {
  return asf(__builtin_amdgcn_mov_dpp((int)asu(x), CTRL, 0xf, 0xf, true));
}
constexpr int XOR1 = 0xB1, XOR2 = 0x4E, ROR8 = 0x128, ROR4 = 0x124;
__device__ __forceinline__ float tr32(float a, float b) {
  auto r = __builtin_amdgcn_permlane32_swap(asu(a), asu(b), false, false);
  return asf(r[0]) + asf(r[1]);
}
__device__ __forceinline__ float tr16(float a, float b) {
  auto r = __builtin_amdgcn_permlane16_swap(asu(a), asu(b), false, false);
  return asf(r[0]) + asf(r[1]);
}
__device__ __forceinline__ float mx32(float a) {
  auto r = __builtin_amdgcn_permlane32_swap(asu(a), asu(a), false, false);
  return fmaxf(asf(r[0]), asf(r[1]));
}
__device__ __forceinline__ float mx16(float a) {
  auto r = __builtin_amdgcn_permlane16_swap(asu(a), asu(a), false, false);
  return fmaxf(asf(r[0]), asf(r[1]));
}
__device__ __forceinline__ float row_sum(float v) { v += dpp<ROR8>(v); v += dpp<ROR4>(v); v += dpp<XOR2>(v); v += dpp<XOR1>(v); return v; }
__device__ __forceinline__ float row_max(float v) {
  v = fmaxf(v, dpp<ROR8>(v)); v = fmaxf(v, dpp<ROR4>(v)); v = fmaxf(v, dpp<XOR2>(v)); v = fmaxf(v, dpp<XOR1>(v)); return v;
}
__device__ __forceinline__ float wave_sum(float v) { v = row_sum(v); v = tr16(v, v); return tr32(v, v); }

__device__ __forceinline__ rsrc_t mkrs(const void* p, u32 bytes) { return __builtin_amdgcn_make_buffer_rsrc((void*)p, 0, bytes, 0x00020000); }
template <int AUX> __device__ __forceinline__ u32x4 ld128(rsrc_t r, int voff, int soff = 0) { return __builtin_amdgcn_raw_buffer_load_b128(r, voff, soff, AUX); }
template <int AUX> __device__ __forceinline__ u32 ld32(rsrc_t r, int voff) { return __builtin_amdgcn_raw_buffer_load_b32(r, voff, 0, AUX); }
__device__ __forceinline__ void st128(rsrc_t r, int voff, u32x4 v) { __builtin_amdgcn_raw_buffer_store_b128(v, r, voff, 0, AUX_SC1); }
__device__ __forceinline__ u32 tagw(float v, u32 tag4) { return (asu(v) & ~TMASK) | tag4; }
__device__ __forceinline__ u32x4 tag4(float a, float b, float c, float d, u32 t) { return u32x4{tagw(a, t), tagw(b, t), tagw(c, t), tagw(d, t)}; }
__device__ __forceinline__ u32 untag4(u32x4& v, u32 t) { v.x ^= t; v.y ^= t; v.z ^= t; v.w ^= t; return v.x | v.y | v.z | v.w; }
__device__ __forceinline__ bool none(bool bad) { return __builtin_amdgcn_ballot_w64(bad) == 0ull; }
__device__ __forceinline__ bool all_ok(u32 bad) { return none((bad & TMASK) != 0u); }

#define CBAR() asm volatile("" ::: "memory")
#define PIN(v) asm volatile("" : "+v"(v))
#define LGKM0() asm volatile("s_waitcnt lgkmcnt(0)" ::: "memory")
template <int N, int SLEEP> __device__ __forceinline__ void flag_wait(LAS u32* p, int lane) {
  u32 v, cnt;
  const u32 addr = (u32)(uintptr_t)(p + (lane & (N - 1)));
  asm volatile("s_mov_b32 %1, %4\n"
               "1:\n\t"
               "ds_read_b32 %0, %2\n\t"
               "s_waitcnt lgkmcnt(0)\n\t"
               "v_cmp_ne_u32 vcc, %3, %0\n\t"
               "s_cbranch_vccz 2f\n\t"
               "s_sleep %5\n\t"
               "s_sub_u32 %1, %1, 1\n\t"
               "s_cbranch_scc0 1b\n"
               "2:"
               : "=&v"(v), "=&s"(cnt) : "v"(addr), "i"(SET), "i"(SPIN_LDS), "i"(SLEEP) : "vcc", "scc", "memory");
}
template <int DUE> __device__ __forceinline__ void pace(u64 org) {
  asm volatile("1:\n\t"
               "s_memrealtime vcc\n\t"
               "s_waitcnt lgkmcnt(0)\n\t"
               "s_sub_u32 vcc_lo, vcc_lo, %0\n\t"
               "s_cmp_lt_i32 vcc_lo, %1\n\t"
               "s_cbranch_scc0 2f\n\t"
               "s_sleep 1\n\t"
               "s_branch 1b\n"
               "2:"
               :: "s"((u32)org), "i"(DUE), "s"(org) : "vcc", "scc", "memory");
}
__device__ __forceinline__ void flag_set(LAS u32* p, u32 set = SET) { CBAR(); *p = set; CBAR(); }

__device__ __forceinline__ void poll2(rsrc_t rs, int voff, int soff, u32 t4, u32x4& r0, u32x4& r1) {
  u32 o0, o1, o2, o3, o4, o5, o6, o7, m, cnt;
  asm volatile("s_mov_b32 %9, %14\n"
               "1:\n\t"
               "buffer_load_dwordx4 v[144:147], %10, %11, 0 offen sc1\n\t"
               "buffer_load_dwordx4 v[148:151], %10, %11, %12 offen sc1\n\t"
               "s_sub_u32 %9, %9, 1\n\t"
               "s_waitcnt vmcnt(1)\n\t"
               "v_xor_b32 %0, %13, v144\n\tv_xor_b32 %1, %13, v145\n\tv_xor_b32 %2, %13, v146\n\tv_xor_b32 %3, %13, v147\n\t"
               "v_or3_b32 %8, %0, %1, %2\n\t"
               "s_waitcnt vmcnt(0)\n\t"
               "v_xor_b32 %4, %13, v148\n\tv_xor_b32 %5, %13, v149\n\tv_xor_b32 %6, %13, v150\n\tv_xor_b32 %7, %13, v151\n\t"
               "v_or3_b32 %8, %8, %3, %4\n\t"
               "v_or3_b32 %8, %8, %5, %6\n\t"
               "v_bitop3_b32 %8, %8, 15, %7 bitop3:0xc8\n\t"
               "v_cmp_ne_u32 vcc, 0, %8\n\t"
               "s_cbranch_vccz 2f\n\t"
               "s_cbranch_scc0 1b\n"
               "2:"
               : "=&v"(o0), "=&v"(o1), "=&v"(o2), "=&v"(o3), "=&v"(o4), "=&v"(o5), "=&v"(o6), "=&v"(o7), "=&v"(m), "=&s"(cnt)
               : "v"(voff), "s"(rs), "s"(soff), "s"(t4), "i"(SPIN_MEM)
               : "vcc", "scc", "memory", "v144", "v145", "v146", "v147", "v148", "v149", "v150", "v151");
  r0 = u32x4{o0, o1, o2, o3};
  r1 = u32x4{o4, o5, o6, o7};
}
__device__ __forceinline__ u32x4 data_wait(LAS u32* flag, LAS u32x4* p) {
  u32x4 r;
  u32 v, cnt;
  asm volatile("s_mov_b32 %2, %5\n"
               "1:\n\t"
               "ds_read_b32 %1, %3\n\t"
               "ds_read_b128 %0, %4\n\t"
               "s_sub_u32 %2, %2, 1\n\t"
               "s_waitcnt lgkmcnt(0)\n\t"
               "v_cmp_ne_u32 vcc, 1, %1\n\t"
               "s_cbranch_vccz 2f\n\t"
               "s_cbranch_scc0 1b\n"
               "2:"
               : "=&v"(r), "=&v"(v), "=&s"(cnt) : "v"((u32)(uintptr_t)flag), "v"((u32)(uintptr_t)p), "i"(SPIN_LDS) : "vcc", "scc", "memory");
  return r;
}

__device__ __forceinline__ void final_reduce(LAS u32* L, rsrc_t rws, const u16* x, u16* out, int rid, int grp, int lane, u32 tag, u64 tent, u64 bar) {
  LAS float* const LF = (LAS float*)L;
  __builtin_amdgcn_s_setprio(3);
  const u32 tagp = tag & TMASK;
  const int rb = rid / 28, cb = rid % 28, qd = lane & 3;
  const int pi = cb & 7, ph = cb < 24 ? pi >> 2 : pi >> 1, pg = cb < 24 ? pi & 3 : pi & 1;
  const int col = rb * D + 128 * (cb >> 3) + 8 * (4 * pg + ((lane >> 2) & 3)) + 4 * ph + (lane & 3);
  float xres = lo16(x[col]);
  int oo = O2_OFF + (grp * 32 + (lane >> 2)) * O2_S + rb * 1792 + cb * 64 + qd * 16;
  LAS float* wp = LF + ((lane & 12) == 0 ? L_R + (4 * qd + (lane >> 4)) * 8 + grp : L_XN + 128 + lane);
  LAS u32* fp = L + F_R + grp;
  LAS u32* fr = L + F_R + (lane & 7);
  LAS f32x4* rp = (LAS f32x4*)(LF + L_R + (lane & 15) * 8);
  int so = lane < 16 ? col * 2 : OOB;
  u32 setv = SET;
  rsrc_t ro = mkrs(out, 2 * D * 2);
  asm volatile("" : "+s"(ro));
  PIN(xres); PIN(oo); PIN(wp); PIN(fp); PIN(fr); PIN(rp); PIN(so); PIN(setv);
  flag_wait<1, 1>(L + F_PUB, lane);
  flag_wait<4, 0>(L + F_O2, lane);
  __builtin_amdgcn_s_sleep(O2_DELAY);
  u32x4 a0, a1;
  poll2(rws, oo, 16 * O2_S, tagp, a0, a1);
  float f = tr16(tr32(asf(a0.x) + asf(a1.x), asf(a0.z) + asf(a1.z)), tr32(asf(a0.y) + asf(a1.y), asf(a0.w) + asf(a1.w)));
  f += dpp<ROR8>(f); f += dpp<ROR4>(f);
  *wp = f;
  flag_set(fp, setv);
  u32 fl = *fr;
  CBAR();
  const f32x4 r0 = rp[0], r1 = rp[1];
  const f32x4 rs4 = r0 + r1;
  const float tot = xres + ((rs4.x + rs4.z) + (rs4.y + rs4.w));
  u32 res = pkbf(tot, tot);
  PIN(res);
  if (none(fl != SET)) {
    __builtin_amdgcn_raw_buffer_store_b16((u16)res, ro, so, 0, 0);
    if (rid == 0 && lane == 0) __builtin_amdgcn_raw_buffer_store_b32(tag, rws, EP_OFF, 0, AUX_SC1);
    if (rid == 0) {
      const u64 now = __builtin_amdgcn_s_memrealtime();
      const u64 until = tent + 3000 + 8 * (bar - tent);
      while (__builtin_amdgcn_s_memrealtime() < until) __builtin_amdgcn_s_sleep(1);
    }
  }
}

}

extern "C" __global__ void __launch_bounds__(768, 3)
fused_decode(const u16* __restrict__ x, const u16* __restrict__ wq, const u16* __restrict__ wg_, const u16* __restrict__ wu,
             const u16* __restrict__ kc, char* __restrict__ ws, const u16* __restrict__ vc, const u16* __restrict__ wo,
             const u16* __restrict__ wd, u16* __restrict__ out) {
  __shared__ __attribute__((aligned(16))) u32 smem[L_END];
  LAS u32* const L = (LAS u32*)smem;
  LAS float* const LF = (LAS float*)smem;
  const int wg = blockIdx.x, h = wg & 7, j = wg >> 3, b = j & 1, sp = j >> 1, reg = wg & 15;
  const int w = __builtin_amdgcn_readfirstlane(threadIdx.x >> 6), lane = threadIdx.x & 63, kw = w & 3;
  const u64 tent = __builtin_amdgcn_s_memrealtime();
  u64 bar;
  asm volatile("s_load_dword s94, %0, 0x0\n\ts_load_dword s95, %0, 0x40" :: "s"(__builtin_amdgcn_kernarg_segment_ptr()) : "s94", "s95");
  u64 pc0;
  asm volatile("s_getpc_b64 %0" : "=s"(pc0));
  const u32 code_touch = ld32<0>(mkrs((const char*)(pc0 & ~255ull), w == 0 ? CODE_SPAN : 0), lane < 5 ? j * 640 + lane * 128 : OOB);
  __builtin_amdgcn_sched_barrier(0);
#define ROLE_ENTRY() const rsrc_t rws = mkrs(ws, WS_BYTES)
#define ROLE_TAG() const u32 tag = __builtin_amdgcn_readfirstlane(L[L_TAG]), tagp = tag & TMASK
  const u32 blk0 = sp * 4 + reg * ROT;
#define BLOCK_OFF(slot) (((blk0 + (((slot) >> 2) << 6) + ((slot) & 3u)) & 511u) << 12)
  const int hx = h ^ 4;
  u64 org;
#define STREAM_GROUP(G, VOFF, SLOT, ISK) \
  if (!(ISK) || (G) >= K_FREE) pace<(ISK) ? due_k(G) : due_v(G, 0)>(org); \
  if (4 * (G) > WINDOW) asm volatile("s_waitcnt vmcnt(%0)" :: "i"(WINDOW) : "memory"); \
  _Pragma("unroll") for (int i = 4 * (G); i < 4 * (G) + 2; ++i) d[i] = ld128<AUX_NT>(rkv, (VOFF), (int)BLOCK_OFF(SLOT)); \
  if (!(ISK) && (G) >= V_FINE) { __builtin_amdgcn_sched_barrier(0); pace<due_v(G, 1)>(org); } \
  _Pragma("unroll") for (int i = 4 * (G) + 2; i < 4 * (G) + 4; ++i) d[i] = ld128<AUX_NT>(rkv, (VOFF), (int)BLOCK_OFF(SLOT)); \
  __builtin_amdgcn_sched_barrier(0);
#define STREAM_LOADS(VOFF, SLOT, ISK) \
  STREAM_GROUP(0, VOFF, SLOT, ISK) STREAM_GROUP(1, VOFF, SLOT, ISK) STREAM_GROUP(2, VOFF, SLOT, ISK) STREAM_GROUP(3, VOFF, SLOT, ISK) \
  STREAM_GROUP(4, VOFF, SLOT, ISK) STREAM_GROUP(5, VOFF, SLOT, ISK) STREAM_GROUP(6, VOFF, SLOT, ISK) STREAM_GROUP(7, VOFF, SLOT, ISK)
#define WINDOW_RSRC(SRC) mkrs((SRC) + ((size_t)reg * SK + (SK - W)) * DH, W * DH * 2)

  if (__builtin_expect(w >= 4, 0)) {
    u32 tag_out;
    if (w < 8) {
      __builtin_amdgcn_s_setprio(2);
      const int g = lane >> 4, key = (lane >> 1) & 7;
      const bool hb = lane & 1;
      const int kvo = key * 512 + (2 * g + (lane & 1)) * 16;
      const rsrc_t rkv = WINDOW_RSRC(kc);
      u32x4 d[32];
      __builtin_amdgcn_sched_barrier(0);
      __builtin_amdgcn_s_barrier();
      org = __builtin_amdgcn_s_memrealtime(); bar = org;
      __builtin_amdgcn_sched_barrier(0);
      STREAM_LOADS(kvo + (i & 3) * 128, kw + 4 * (i >> 2), true)
      ROLE_ENTRY();
      flag_wait<2, 1>(L + F_Q, lane);
      ROLE_TAG();
      u32x4 qa[4];
#pragma unroll
      for (int i = 0; i < 4; ++i) qa[i] = *(LAS u32x4*)(L + L_Q + (lane & 1) * 128 + i * 32 + (2 * g + ((lane >> 1) & 1)) * 4);
      f32x4 ea, eb;
#pragma unroll
      for (int n = 0; n < 4; ++n) {
        f32x4 ca = {}, cb = {};
#pragma unroll
        for (int i = 0; i < 4; ++i) {
          ca = __builtin_amdgcn_mfma_f32_16x16x32_bf16(__builtin_bit_cast(bf16x8_t, qa[i]), __builtin_bit_cast(bf16x8_t, d[8 * n + i]), ca, 0, 0, 0);
          cb = __builtin_amdgcn_mfma_f32_16x16x32_bf16(__builtin_bit_cast(bf16x8_t, qa[i]), __builtin_bit_cast(bf16x8_t, d[8 * n + 4 + i]), cb, 0, 0, 0);
        }
#pragma unroll
        for (int k = 0; k < 4; ++k) { ea[k] = (n == 0 || g == n) ? ca[k] : ea[k]; eb[k] = (n == 0 || g == n) ? cb[k] : eb[k]; }
      }
      asm volatile("" :: "v"(code_touch));
      float sc[2];
#pragma unroll
      for (int hd = 0; hd < 2; ++hd) sc[hd] = rbf((hb ? eb[2 + hd] : ea[hd]) + dpp<XOR1>(hb ? ea[2 + hd] : eb[hd])) * 0.0625f;
      const float wm0 = mx32(mx16(row_max(sc[0]))), wm1 = mx32(mx16(row_max(sc[1])));
      CBAR();
      if (lane < 2) L[L_WM + kw * 2 + lane] = (asu(lane ? wm1 : wm0) & 0xffff0000u) | SET;
      u32x4 ma, mb;
      for (u32 s = 0; s < SPIN_LDS; ++s) {
        CBAR();
        ma = *(LAS u32x4*)(L + L_WM) ^ SET; mb = *(LAS u32x4*)(L + L_WM + 4) ^ SET;
        if (none((((ma.x | ma.y | ma.z | ma.w) | (mb.x | mb.y | mb.z | mb.w)) & 0xffffu) != 0u)) break;
      }
      const float M0 = fmaxf(fmaxf(asf(ma.x), asf(ma.z)), fmaxf(asf(mb.x), asf(mb.z)));
      const float M1 = fmaxf(fmaxf(asf(ma.y), asf(ma.w)), fmaxf(asf(mb.y), asf(mb.w)));
      const float p0 = rbf(__builtin_amdgcn_exp2f((sc[0] - M0) * 1.4426950408889634f));
      const float p1 = rbf(__builtin_amdgcn_exp2f((sc[1] - M1) * 1.4426950408889634f));
      LAS u32* pp = L + L_P + key * 64 + kw + 4 * (2 * g + (lane & 1));
      pp[0] = asu(p0) | (asu(p0) >> 16);
      pp[32] = asu(p1) | (asu(p1) >> 16);
      float ls = tr32(p0, p1);
      ls = row_sum(tr16(ls, ls));
      if ((lane & 31) == 0) LF[L_WL + kw * 2 + (lane >> 5)] = ls;
      flag_set(L + F_P + kw);

      if (kw == 0) {
        flag_wait<4, 0>(L + F_P, lane);
        if (lane < 2) {
          const float Ls = (LF[L_WL + lane] + LF[L_WL + 2 + lane]) + (LF[L_WL + 4 + lane] + LF[L_WL + 6 + lane]);
          st128(rws, ST_OFF + (((h * 2 + lane) * 2 + b) * 16 + sp) * 16, u32x4{asu(lane ? M1 : M0), tag, asu(Ls), tag});
        }
      } else if (kw == 1) {
        __builtin_amdgcn_s_setprio(3);
        const int s = lane >> 2, qd = lane & 3;
        const int so = ST_OFF + ((hx * 2 + (j >> 4)) * 32 + s) * 16;
        const int oo = O1_OFF + (hx * 32 + 2 * s) * O1_S + j * 64 + qd * 16;
        u32x4 s0, s1, a0, a1;
        flag_wait<4, 1>(L + F_VI, lane);
        for (u32 it = 0; it < SPIN_MEM; ++it) {
          CBAR();
          s0 = ld128<AUX_SC1>(rws, so); s1 = ld128<AUX_SC1>(rws, so + 256);
          if (none((s0.y != tag) | (s0.w != tag) | (s1.y != tag) | (s1.w != tag))) break;
          flag_wait<1, 1>(L + F_PUB, lane);
        }
        float wgt[2];
#pragma unroll
        for (int bb = 0; bb < 2; ++bb) {
          const float m = asf(bb ? s1.x : s0.x), l = asf(bb ? s1.z : s0.z);
          float mg = fmaxf(m, dpp<ROR8>(m)); mg = fmaxf(mg, dpp<ROR4>(mg)); mg = mx32(mx16(mg));
          const float e = __builtin_amdgcn_exp2f((m - mg) * 1.4426950408889634f);
          float lg = e * l; lg += dpp<ROR8>(lg); lg += dpp<ROR4>(lg); lg = tr16(lg, lg); lg = tr32(lg, lg);
          wgt[bb] = e * __builtin_amdgcn_rcpf(lg);
        }
        LAS u32* ow = L + ((lane & 12) == 0 ? L_O + qd * 4 + (lane >> 4) : L_XN + lane);
        LAS u32* of = L + F_O;
        u32 setv = SET;
        PIN(wgt[0]); PIN(wgt[1]); PIN(ow); PIN(of); PIN(setv);
        flag_wait<1, 0>(L + F_PUB, lane);
        __builtin_amdgcn_s_sleep(O1_DELAY);
        poll2(rws, oo, O1_S, tagp, a0, a1);
        f32x4 m0 = __builtin_bit_cast(f32x4, a0) * wgt[0], m1 = __builtin_bit_cast(f32x4, a1) * wgt[1];
        PIN(m0); PIN(m1);
        const float v0 = tr32(m0.x, m1.x), v2 = tr32(m0.z, m1.z), v1 = tr32(m0.y, m1.y), v3 = tr32(m0.w, m1.w);
        float e0 = tr16(v0, v2), e1 = tr16(v1, v3);
        e0 += dpp<ROR8>(e0); e1 += dpp<ROR8>(e1);
        e0 += dpp<ROR4>(e0); e1 += dpp<ROR4>(e1);
        *ow = pkbf(e0, e1);
        flag_set(of, setv);
      }
      tag_out = tag;
    } else {
      if (kw == 0 && lane < (L_END - L_WM) / 4) *(LAS u32x4*)(L + L_WM + 4 * lane) = u32x4{0u, 0u, 0u, 0u};
      LGKM0();
      __builtin_amdgcn_s_barrier();
      org = __builtin_amdgcn_s_memrealtime(); bar = org;
      __builtin_amdgcn_s_setprio(1);
      __builtin_amdgcn_sched_barrier(0);
      ROLE_ENTRY();
      flag_wait<1, 4>(L + F_SH, lane);
      org += (u64)(long long)(int)__builtin_amdgcn_readfirstlane(L[L_SH]);
      const rsrc_t rkv = WINDOW_RSRC(vc);
      u32x4 d[32];
      STREAM_LOADS(lane * 16 + kw * 1024, (u32)i, false)
      flag_set(L + F_VI + kw);
      const int m = lane & 31, hf = lane >> 5;
      u32x4 msk;
#pragma unroll
      for (int r = 0; r < 4; ++r) msk[r] = (m < 16 && ((m & 7) >> 1) == r) ? ((m & 1) ? 0xffff0000u : 0x0000ffffu) : 0u;
      LAS f32x4* op = (LAS f32x4*)(LF + L_OP) + kw * 128 + 2 * m + hf;
      LAS f32x4* rp = (LAS f32x4*)(LF + L_OP) + lane;
      LAS u32* fp = L + F_V + kw;
      LAS u32* fr = L + F_V + (lane & 3);
      int o1o = O1_OFF + (h * 32 + j) * O1_S + lane * 16;
      PIN(op); PIN(rp); PIN(fp); PIN(fr); PIN(o1o);
      flag_wait<4, 1>(L + F_P, lane);
      ROLE_TAG();
      const LAS u32x4* pp = (const LAS u32x4*)(L + L_P + ((kw * 2 + hf) * 2 + ((m >> 3) & 1)) * 32);
      f32x16 acc = {};
#pragma unroll
      for (int g = 0; g < 8; ++g) {
        const u32x4 p4 = pp[g];
#pragma unroll
        for (int k = 0; k < 4; ++k) {
          const u32x4 a = msk & p4[k];
          acc = __builtin_amdgcn_mfma_f32_32x32x16_bf16(__builtin_bit_cast(bf16x8_t, a), __builtin_bit_cast(bf16x8_t, d[4 * g + k]), acc, 0, 0, 0);
        }
        __builtin_amdgcn_sched_barrier(0);
      }
      asm volatile("" :: "v"(code_touch));
      op[0] = f32x4{acc[0], acc[1], acc[2], acc[3]};
      op[64] = f32x4{acc[4], acc[5], acc[6], acc[7]};
      flag_set(fp);
      const u32 fl = *fr;
      CBAR();
      const f32x4 o0 = (rp[0] + rp[128]) + (rp[256] + rp[384]);
      const f32x4 o1 = (rp[64] + rp[192]) + (rp[320] + rp[448]);
      u32x4 t0 = tag4(o0.x, o0.y, o0.z, o0.w, tagp), t1 = tag4(o1.x, o1.y, o1.z, o1.w, tagp);
      PIN(t0); PIN(t1);
      if (none(fl != SET)) {
        st128(rws, o1o, t0);
        st128(rws, o1o + 1024, t1);
        flag_set(L + F_PUB);
      }
      tag_out = tag;
    }
    if (!(h & 1) && j < 14) final_reduce(L, mkrs(ws, WS_BYTES), x, out, (h >> 1) * 14 + j, w - 4, lane, tag_out, tent, bar);
    return;
  }

  __builtin_amdgcn_s_setprio(3);
  const bool mlp = wg < 224;
  const int ke = (wg * 2341) >> 16, cb2 = wg - 28 * ke;
  const int rr = lane >> 3, c8 = lane & 7;
  const bool isq = kw < 2;
  int rowq = 112 * (j >> 3) + 64 * kw, rowm = 56 * ke, colq = (hx * 512 + (j & 7) * 64) * 2, colm = cb2 * 128;
  asm volatile("" : "+s"(rowq), "+s"(rowm), "+s"(colq), "+s"(colm));
  const int row0 = isq ? rowq : rowm;
  const int nrow = (isq | mlp) ? (0x38383040 >> (8 * kw)) & 0xff : 0;
  int rs = isq ? 8192 : DF * 2;
  int vw0 = (row0 + 2 * rr) * rs + (isq ? colq : colm) + c8 * 16;
  vw0 = nrow ? vw0 : OOB;
  int vw3 = 48 + 2 * rr < nrow ? vw0 : OOB;
  int xo = lane < 56 ? lane * 16 : OOB;
  int mat = kw;
  asm volatile("" : "+v"(vw0), "+v"(vw3), "+v"(xo), "+s"(rs), "+s"(mat));
  __builtin_amdgcn_sched_barrier(0);
  ROLE_ENTRY();
  const rsrc_t rx = mkrs((const u16*)x, 2 * D * 2);
  const u32x4 xa = ld128<0>(rx, xo), xb = ld128<0>(rx, xo + D * 2);
  const u32 epoch = ld32<AUX_SC1>(rws, EP_OFF);
  __builtin_amdgcn_sched_barrier(0);
  u32x4 wv[8];
  {
    const rsrc_t rw = mkrs(mat < 2 ? wq : mat == 2 ? wg_ : wu, mat < 2 ? D * 4096 * 2 : D * DF * 2);
#pragma unroll
    for (int i = 0; i < 8; ++i) wv[i] = ld128<AUX_NT>(rw, i < 6 ? vw0 : vw3, (16 * (i >> 1) + (i & 1)) * rs);
  }
  __builtin_amdgcn_sched_barrier(0);
  __builtin_amdgcn_s_barrier();
  org = __builtin_amdgcn_s_memrealtime();
  __builtin_amdgcn_sched_barrier(0);
  const int cbf = cb2 == 27 ? 0 : cb2 + 1;
  const int e0 = QP_OFF + (hx * 32 + j) * QP_S, e1 = GP_OFF + wg * GP_S - 512, e2 = ST_OFF + (wg & 127) * 64 - 24 * 64;
  const u32 box_touch = ld32<AUX_SC1>(rws, (kw == 1 && lane < 25) ? (lane < 8 ? e0 : lane < 24 ? e1 : e2) + lane * 64 : OOB);
  __builtin_amdgcn_sched_barrier(0);

#pragma unroll
  for (int bb = 0; bb < 2; ++bb) {
    const u32x4 xv = bb ? xb : xa;
    float v[8];
#pragma unroll
    for (int k = 0; k < 4; ++k) { v[2 * k] = lo16(xv[k]); v[2 * k + 1] = hi16(xv[k]); }
    float s = 0.f;
#pragma unroll
    for (int k = 0; k < 8; ++k) s += v[k];
    const float mean = wave_sum(s) * (1.f / D);
    float q = 0.f;
#pragma unroll
    for (int k = 0; k < 8; ++k) { const float t = v[k] - mean; q += t * t; }
    q = lane < 56 ? q : 0.f;
    const float var = rbf(wave_sum(q) * (1.f / D));
    const float den = rbf(__builtin_amdgcn_sqrtf(rbf(var + 1e-5f))), rden = __builtin_amdgcn_rcpf(den), mb = rbf(mean);
#pragma unroll
    for (int k = 0; k < 8; ++k) { const float c = rbf(v[k] - mb), r = c * rden; v[k] = fmaf(fmaf(-r, den, c), rden, r); }
    u32x4 o;
#pragma unroll
    for (int k = 0; k < 4; ++k) o[k] = pkbf(v[2 * k], v[2 * k + 1]);
    if (lane < 56) *(LAS u32x4*)(L + L_XN + bb * 224 + lane * 4) = o;
    else if (bb == 1) *(LAS u32x4*)(L + L_XN + 224 + lane * 4) = u32x4{0u, 0u, 0u, 0u};
  }
  asm volatile("" :: "v"(code_touch));
  const u32 tag = __builtin_amdgcn_readfirstlane(epoch) + 1u, tagp = tag & TMASK;
  if (kw == 0) L[L_TAG] = tag;
  if (kw == 3 && wg < 8 && lane == 0) __builtin_amdgcn_raw_buffer_store_b64(u32x2{(u32)org, tag}, rws, OR_OFF + wg * 8, 0, AUX_SC1);

  {
    float a0[8] = {}, a1[8] = {};
#pragma unroll
    for (int k = 0; k < 4; ++k) {
      const u32 x0 = L[L_XN + row0 / 2 + rr + 8 * k], x1 = L[L_XN + 224 + row0 / 2 + rr + 8 * k];
#pragma unroll
      for (int c = 0; c < 4; ++c) {
        const u32 pl = __builtin_amdgcn_perm(wv[2 * k + 1][c], wv[2 * k][c], 0x05040100u), ph = __builtin_amdgcn_perm(wv[2 * k + 1][c], wv[2 * k][c], 0x07060302u);
        a0[2 * c] = dot2(pl, x0, a0[2 * c]); a0[2 * c + 1] = dot2(ph, x0, a0[2 * c + 1]);
        a1[2 * c] = dot2(pl, x1, a1[2 * c]); a1[2 * c + 1] = dot2(ph, x1, a1[2 * c + 1]);
      }
    }
    asm volatile("" :: "v"(box_touch));
    float t[8], u[4];
#pragma unroll
    for (int k = 0; k < 8; ++k) t[k] = tr32(a0[k], a1[k]);
#pragma unroll
    for (int k = 0; k < 4; ++k) { u[k] = tr16(t[k], t[k + 4]); u[k] += dpp<ROR8>(u[k]); }
    const int bb = lane >> 5, hf = (lane >> 4) & 1;
    LAS f32x4* qx = (LAS f32x4*)(LF + L_QX) + bb * 16 + c8 * 2 + hf;
    if (kw == 1) {
      if (!(lane & 8)) *qx = f32x4{u[0], u[1], u[2], u[3]};
      flag_set(L + F_QX);
    } else if (kw == 0) {
      flag_wait<1, 0>(L + F_QX, lane);
      const f32x4 o = *qx;
      if (!(lane & 8)) st128(rws, QP_OFF + (hx * 32 + j) * QP_S + bb * 256 + hf * 128 + c8 * 16, tag4(u[0] + o.x, u[1] + o.y, u[2] + o.z, u[3] + o.w, tagp));
    } else if (mlp) {
      LAS u32x4* gx = (LAS u32x4*)(L + L_GX) + (kw - 2) * 32;
      if (!(lane & 8)) gx[c8 * 4 + bb * 2 + hf] = tag4(u[0], u[1], u[2], u[3], tagp);
      CBAR();
      if (lane < 32) st128(rws, GP_OFF + wg * GP_S + (lane >> 2) * 128 + (kw - 2) * 64 + (lane & 3) * 16, gx[lane]);
    }
  }

  if (kw < 2) {
    __builtin_amdgcn_s_sleep(Q_DELAY);
    const int qo = QP_OFF + (h * 32 + 4 * kw + 8 * (lane >> 4)) * QP_S + b * 256 + (lane & 15) * 16;
    u32x4 a[4];
    for (u32 s = 0; s < SPIN_MEM; ++s) {
      CBAR();
#pragma unroll
      for (int i = 0; i < 4; ++i) a[i] = ld128<AUX_SC1>(rws, qo, i * QP_S);
      if (all_ok((untag4(a[0], tagp) | untag4(a[1], tagp)) | (untag4(a[2], tagp) | untag4(a[3], tagp)))) break;
    }
#pragma unroll
    for (int i = 0; i < 2; ++i) {
      const u32x4 a0 = a[2 * i], a1 = a[2 * i + 1];
      const float v0 = tr32(asf(a0.x), asf(a1.x)), v1 = tr32(asf(a0.y), asf(a1.y)), v2 = tr32(asf(a0.z), asf(a1.z)), v3 = tr32(asf(a0.w), asf(a1.w));
      L[L_Q + (4 * kw + 2 * i + (lane >> 5)) * 32 + 4 * (lane & 7) + 2 * ((lane >> 3) & 1) + ((lane >> 4) & 1)] = pkbf(tr16(v0, v2), tr16(v1, v3));
    }
    flag_set(L + F_Q + kw);
  } else if (kw == 2) {
    u32 h0 = 0u, h1 = 0u;
    if (mlp) {
      const int go = GP_OFF + (cbf + 28 * rr) * GP_S + ke * 128 + c8 * 16;
      u32x4 a;
      flag_wait<2, 1>(L + F_Q, lane);
      for (u32 s = 0; s < SPIN_MEM; ++s) {
        CBAR();
        a = ld128<AUX_SC1>(rws, go);
        if (all_ok(untag4(a, tagp))) break;
        __builtin_amdgcn_s_sleep(16);
      }
      float hv[4];
#pragma unroll
      for (int k = 0; k < 4; ++k) {
        float g = asf(a[k]);
        g += dpp<ROR8>(g); g = tr16(g, g); g = tr32(g, g);
        g = rbf(g);
        const float uu = dpp<ROR4>(g);
        const float z = 0.7978845608028654f * (g + 0.044715f * g * g * g);
        const float th = 1.f - 2.f * __builtin_amdgcn_rcpf(1.f + __builtin_amdgcn_exp2f(z * 2.8853900817779268f));
        hv[k] = rbf(0.5f * g * (1.f + th)) * uu;
      }
      h0 = pkbf(hv[0], hv[1]); h1 = pkbf(hv[2], hv[3]);
    }
    if (lane < 4) {
      *(LAS u32x2*)(L + L_H + (2 * (lane & 1)) * 4 + (lane >> 1) * 2) = u32x2{h0, SET};
      *(LAS u32x2*)(L + L_H + (2 * (lane & 1) + 1) * 4 + (lane >> 1) * 2) = u32x2{h1, SET};
    }
    CBAR();
  } else {
    pace<T_ORG>(org);
    const u32x2 o = __builtin_amdgcn_raw_buffer_load_b64(rws, OR_OFF + (lane & 7) * 8, 0, AUX_SC1);
    const bool ok = o.y == tag;
    const float n = row_sum(ok ? 1.f : 0.f), dsum = row_sum(ok ? (float)(int)(o.x - (u32)org) : 0.f);
    const float sh = n > 0.f ? fminf(fmaxf(dsum * __builtin_amdgcn_rcpf(n) + ORG_BIAS, -80.f), 80.f) : 0.f;
    L[L_SH] = (u32)(int)sh;
    flag_set(L + F_SH);
  }

  const int rg = lane >> 4, cl = lane & 15;
  const int co = kw * 128 + cl * 8;
  u32x4 wa[4], wb[2];
  flag_wait<4, 1>(L + F_VI, lane);
  pace<T_LATE>(org + (u64)(long long)(int)__builtin_amdgcn_readfirstlane(L[L_SH]));
  {
    const rsrc_t ro = mkrs(wo, 4096 * D * 2), rd = mkrs(wd, mlp ? DF * D * 2 : 0);
    const int vo = co < D ? (hx * 512 + j * 16 + 4 * rg) * (D * 2) + co * 2 : OOB;
    const int vd = co < D ? (64 * cbf + 8 * ke + 2 * rg) * (D * 2) + co * 2 : OOB;
#pragma unroll
    for (int i = 0; i < 2; ++i) wb[i] = ld128<AUX_NT>(rd, vd, i * D * 2);
#pragma unroll
    for (int i = 0; i < 4; ++i) wa[i] = ld128<AUX_NT>(ro, vo, i * D * 2);
  }
  __builtin_amdgcn_sched_barrier(0);
  float a0[8], a1[8];
  u32 pa[8], pb[8];
  {
    u32x4 hh;
    for (u32 s = 0; s < SPIN_LDS; ++s) {
      CBAR();
      hh = *(LAS u32x4*)(L + L_H + rg * 4);
      if (none((hh.y != SET) | (hh.w != SET))) break;
      __builtin_amdgcn_s_sleep(1);
    }
#pragma unroll
    for (int k = 0; k < 4; ++k) {
      const u32 pl = __builtin_amdgcn_perm(wb[1][k], wb[0][k], 0x05040100u), ph = __builtin_amdgcn_perm(wb[1][k], wb[0][k], 0x07060302u);
      a0[2 * k] = dot2(pl, hh.x, 0.f); a0[2 * k + 1] = dot2(ph, hh.x, 0.f);
      a1[2 * k] = dot2(pl, hh.z, 0.f); a1[2 * k + 1] = dot2(ph, hh.z, 0.f);
    }
#pragma unroll
    for (int k = 0; k < 4; ++k) {
      pa[2 * k] = __builtin_amdgcn_perm(wa[1][k], wa[0][k], 0x05040100u); pa[2 * k + 1] = __builtin_amdgcn_perm(wa[1][k], wa[0][k], 0x07060302u);
      pb[2 * k] = __builtin_amdgcn_perm(wa[3][k], wa[2][k], 0x05040100u); pb[2 * k + 1] = __builtin_amdgcn_perm(wa[3][k], wa[2][k], 0x07060302u);
    }
  }
#pragma unroll
  for (int k = 0; k < 8; ++k) asm volatile("" : "+v"(a0[k]), "+v"(a1[k]), "+v"(pa[k]), "+v"(pb[k]));
  int o2o = co < D ? O2_OFF + (h * 32 + j) * O2_S + (lane >> 5) * 1792 + kw * 512 + (((lane >> 4) & 1) * (kw == 3 ? 8 : 16) + cl) * 16 : OOB;
  LAS u32x4* orp = (LAS u32x4*)(L + L_O + rg * 4);
  LAS u32* ofp = L + F_O;
  PIN(o2o); PIN(orp); PIN(ofp);
  {
    const u32x4 oo = data_wait(ofp, orp);
#pragma unroll
    for (int k = 0; k < 8; ++k) {
      a0[k] = dot2(pb[k], oo.y, dot2(pa[k], oo.x, a0[k]));
      a1[k] = dot2(pb[k], oo.w, dot2(pa[k], oo.z, a1[k]));
    }
    float t[8];
#pragma unroll
    for (int k = 0; k < 8; ++k) t[k] = tr32(a0[k], a1[k]);
    st128(rws, o2o, tag4(tr16(t[0], t[4]), tr16(t[1], t[5]), tr16(t[2], t[6]), tr16(t[3], t[7]), tagp));
  }
  flag_set(L + F_O2 + kw);
}

extern "C" void kernel_launch(void* const* d_in, const int* in_sizes, int n_in, void* d_out, int out_size, void* d_ws, size_t ws_size,
                              hipStream_t stream) {
  fused_decode<<<256, 768, 0, stream>>>((const u16*)d_in[0], (const u16*)d_in[3], (const u16*)d_in[6], (const u16*)d_in[7], (const u16*)d_in[1],
                                        (char*)d_ws, (const u16*)d_in[2], (const u16*)d_in[4], (const u16*)d_in[8], (u16*)d_out);
}
```

```cpp
#include <hip/hip_runtime.h>
#include <stdint.h>

typedef unsigned int u32;
typedef unsigned short u16;
typedef unsigned long long u64;
typedef u32 u32x2 __attribute__((ext_vector_type(2)));
typedef u32 u32x4 __attribute__((ext_vector_type(4)));
typedef float f32x4 __attribute__((ext_vector_type(4)));
typedef float f32x16 __attribute__((ext_vector_type(16)));
typedef __bf16 bf16x2_t __attribute__((ext_vector_type(2)));
typedef __bf16 bf16x8_t __attribute__((ext_vector_type(8)));
#define LAS __attribute__((address_space(3)))
typedef __amdgpu_buffer_rsrc_t rsrc_t;

namespace {
constexpr int D = 448, SK = 32768, W = 4096, DH = 256, DF = 1792;
constexpr int AUX_NT = 2, AUX_SC1 = 16;
constexpr int OOB = 1 << 30;
constexpr u32 TMASK = 0xfu;

constexpr int QP_S = 768, GP_S = 1280, O1_S = 2304, O2_S = 3840;
constexpr int EP_OFF = 0;
constexpr int OR_OFF = 2048;
constexpr int ST_OFF = 4096;
constexpr int QP_OFF = ST_OFF + 8192;
constexpr int GP_OFF = QP_OFF + 256 * QP_S;
constexpr int O1_OFF = GP_OFF + 256 * GP_S;
constexpr int O2_OFF = O1_OFF + 256 * O1_S;
constexpr int WS_BYTES = O2_OFF + 256 * O2_S;

constexpr int L_XN = 0;
constexpr int L_QX = L_XN + 480;
constexpr int L_GX = L_QX + 128;
constexpr int L_Q = L_GX + 256;
constexpr int L_P = L_Q + 256;
constexpr int L_OP = L_P + 512;
constexpr int L_WM = L_OP + 2048;
constexpr int L_WL = L_WM + 8;
constexpr int L_H = L_WL + 8;
constexpr int L_O = L_H + 16;
constexpr int F_O = L_O + 16;
constexpr int L_R = L_O + 32;
constexpr int F_P = L_R + 128;
constexpr int F_VI = F_P + 4;
constexpr int F_O2 = F_VI + 4;
constexpr int F_Q = F_O2 + 4;
constexpr int F_QX = F_Q + 2, F_PUB = F_QX + 1;
constexpr int F_V = F_PUB + 1;
constexpr int F_R = F_V + 4;
constexpr int L_TAG = F_R + 8;
constexpr int L_SH = L_TAG + 2, F_SH = L_TAG + 3;
constexpr int L_END = L_TAG + 4;
constexpr u32 SET = 1u;
static_assert((L_END - L_WM) % 4 == 0 && (L_END - L_WM) / 4 <= 64, "one 16-byte store per lane clears the flag area");

constexpr int T_K0 = 25, T_KSTEP10 = 610, T_VSTEP10 = 625;
constexpr int K_FREE = 2;
constexpr int V_FINE = 4, V_FINE_LEAD = 25;
constexpr int WINDOW = 6;
constexpr int T_ORG = 300, ORG_BIAS = 5;
constexpr int due_k(int g) { return T_K0 + g * T_KSTEP10 / 10; }
constexpr int due_v(int g, int half) { return T_K0 + (8 * T_KSTEP10 + g * T_VSTEP10 + half * T_VSTEP10 / 2) / 10 - (g >= V_FINE ? V_FINE_LEAD : 0); }
constexpr int T_LATE = due_v(8, 0);
constexpr int Q_DELAY = 40, O1_DELAY = 12, O2_DELAY = 8;
constexpr int ROT = 70;
constexpr int CODE_SPAN = 15104;
constexpr u32 SPIN_LDS = 1u << 21, SPIN_MEM = 1u << 16;

__device__ __forceinline__ float asf(u32 v) { return __builtin_bit_cast(float, v); }
__device__ __forceinline__ u32 asu(float v) { return __builtin_bit_cast(u32, v); }
__device__ __forceinline__ float lo16(u32 v) { return asf(v << 16); }
__device__ __forceinline__ float hi16(u32 v) { return asf(v & 0xffff0000u); }
__device__ __forceinline__ u32 pkbf(float a, float b) {
  bf16x2_t r = {(__bf16)a, (__bf16)b};
  return __builtin_bit_cast(u32, r);
}
__device__ __forceinline__ float rbf(float a) { return (float)(__bf16)a; }
__device__ __forceinline__ float dot2(u32 a, u32 b, float c) {
  return __builtin_amdgcn_fdot2_f32_bf16(__builtin_bit_cast(bf16x2_t, a), __builtin_bit_cast(bf16x2_t, b), c, false);
}
template <int CTRL> __device__ __forceinline__ float dpp(float x) {
  return asf(__builtin_amdgcn_mov_dpp((int)asu(x), CTRL, 0xf, 0xf, true));
}
constexpr int XOR1 = 0xB1, XOR2 = 0x4E, ROR8 = 0x128, ROR4 = 0x124;
__device__ __forceinline__ float tr32(float a, float b) {
  auto r = __builtin_amdgcn_permlane32_swap(asu(a), asu(b), false, false);
  return asf(r[0]) + asf(r[1]);
}
__device__ __forceinline__ float tr16(float a, float b) {
  auto r = __builtin_amdgcn_permlane16_swap(asu(a), asu(b), false, false);
  return asf(r[0]) + asf(r[1]);
}
__device__ __forceinline__ float mx32(float a) {
  auto r = __builtin_amdgcn_permlane32_swap(asu(a), asu(a), false, false);
  return fmaxf(asf(r[0]), asf(r[1]));
}
__device__ __forceinline__ float mx16(float a) {
  auto r = __builtin_amdgcn_permlane16_swap(asu(a), asu(a), false, false);
  return fmaxf(asf(r[0]), asf(r[1]));
}
__device__ __forceinline__ float row_sum(float v) { v += dpp<ROR8>(v); v += dpp<ROR4>(v); v += dpp<XOR2>(v); v += dpp<XOR1>(v); return v; }
__device__ __forceinline__ float row_max(float v) {
  v = fmaxf(v, dpp<ROR8>(v)); v = fmaxf(v, dpp<ROR4>(v)); v = fmaxf(v, dpp<XOR2>(v)); v = fmaxf(v, dpp<XOR1>(v)); return v;
}
__device__ __forceinline__ float wave_sum(float v) { v = row_sum(v); v = tr16(v, v); return tr32(v, v); }

__device__ __forceinline__ rsrc_t mkrs(const void* p, u32 bytes) { return __builtin_amdgcn_make_buffer_rsrc((void*)p, 0, bytes, 0x00020000); }
template <int AUX> __device__ __forceinline__ u32x4 ld128(rsrc_t r, int voff, int soff = 0) { return __builtin_amdgcn_raw_buffer_load_b128(r, voff, soff, AUX); }
template <int AUX> __device__ __forceinline__ u32 ld32(rsrc_t r, int voff) { return __builtin_amdgcn_raw_buffer_load_b32(r, voff, 0, AUX); }
__device__ __forceinline__ void st128(rsrc_t r, int voff, u32x4 v) { __builtin_amdgcn_raw_buffer_store_b128(v, r, voff, 0, AUX_SC1); }
__device__ __forceinline__ u32 tagw(float v, u32 tag4) { return (asu(v) & ~TMASK) | tag4; }
__device__ __forceinline__ u32x4 tag4(float a, float b, float c, float d, u32 t) { return u32x4{tagw(a, t), tagw(b, t), tagw(c, t), tagw(d, t)}; }
__device__ __forceinline__ u32 untag4(u32x4& v, u32 t) { v.x ^= t; v.y ^= t; v.z ^= t; v.w ^= t; return v.x | v.y | v.z | v.w; }
__device__ __forceinline__ bool none(bool bad) { return __builtin_amdgcn_ballot_w64(bad) == 0ull; }
__device__ __forceinline__ bool all_ok(u32 bad) { return none((bad & TMASK) != 0u); }

#define CBAR() asm volatile("" ::: "memory")
#define PIN(v) asm volatile("" : "+v"(v))
#define LGKM0() asm volatile("s_waitcnt lgkmcnt(0)" ::: "memory")
template <int N, int SLEEP> __device__ __forceinline__ void flag_wait(LAS u32* p, int lane) {
  u32 v, cnt;
  const u32 addr = (u32)(uintptr_t)(p + (lane & (N - 1)));
  asm volatile("s_mov_b32 %1, %4\n"
               "1:\n\t"
               "ds_read_b32 %0, %2\n\t"
               "s_waitcnt lgkmcnt(0)\n\t"
               "v_cmp_ne_u32 vcc, %3, %0\n\t"
               "s_cbranch_vccz 2f\n\t"
               "s_sleep %5\n\t"
               "s_sub_u32 %1, %1, 1\n\t"
               "s_cbranch_scc0 1b\n"
               "2:"
               : "=&v"(v), "=&s"(cnt) : "v"(addr), "i"(SET), "i"(SPIN_LDS), "i"(SLEEP) : "vcc", "scc", "memory");
}
template <int DUE> __device__ __forceinline__ void pace(u64 org) {
  asm volatile("1:\n\t"
               "s_memrealtime vcc\n\t"
               "s_waitcnt lgkmcnt(0)\n\t"
               "s_sub_u32 vcc_lo, vcc_lo, %0\n\t"
               "s_cmp_lt_i32 vcc_lo, %1\n\t"
               "s_cbranch_scc0 2f\n\t"
               "s_sleep 1\n\t"
               "s_branch 1b\n"
               "2:"
               :: "s"((u32)org), "i"(DUE), "s"(org) : "vcc", "scc", "memory");
}
__device__ __forceinline__ void flag_set(LAS u32* p, u32 set = SET) { CBAR(); *p = set; CBAR(); }

__device__ __forceinline__ void poll2(rsrc_t rs, int voff, int soff, u32 t4, u32x4& r0, u32x4& r1) {
  u32 o0, o1, o2, o3, o4, o5, o6, o7, m, cnt;
  asm volatile("s_mov_b32 %9, %14\n"
               "1:\n\t"
               "buffer_load_dwordx4 v[144:147], %10, %11, 0 offen sc1\n\t"
               "buffer_load_dwordx4 v[148:151], %10, %11, %12 offen sc1\n\t"
               "s_sub_u32 %9, %9, 1\n\t"
               "s_waitcnt vmcnt(1)\n\t"
               "v_xor_b32 %0, %13, v144\n\tv_xor_b32 %1, %13, v145\n\tv_xor_b32 %2, %13, v146\n\tv_xor_b32 %3, %13, v147\n\t"
               "v_or3_b32 %8, %0, %1, %2\n\t"
               "s_waitcnt vmcnt(0)\n\t"
               "v_xor_b32 %4, %13, v148\n\tv_xor_b32 %5, %13, v149\n\tv_xor_b32 %6, %13, v150\n\tv_xor_b32 %7, %13, v151\n\t"
               "v_or3_b32 %8, %8, %3, %4\n\t"
               "v_or3_b32 %8, %8, %5, %6\n\t"
               "v_bitop3_b32 %8, %8, 15, %7 bitop3:0xc8\n\t"
               "v_cmp_ne_u32 vcc, 0, %8\n\t"
               "s_cbranch_vccz 2f\n\t"
               "s_cbranch_scc0 1b\n"
               "2:"
               : "=&v"(o0), "=&v"(o1), "=&v"(o2), "=&v"(o3), "=&v"(o4), "=&v"(o5), "=&v"(o6), "=&v"(o7), "=&v"(m), "=&s"(cnt)
               : "v"(voff), "s"(rs), "s"(soff), "s"(t4), "i"(SPIN_MEM)
               : "vcc", "scc", "memory", "v144", "v145", "v146", "v147", "v148", "v149", "v150", "v151");
  r0 = u32x4{o0, o1, o2, o3};
  r1 = u32x4{o4, o5, o6, o7};
}
__device__ __forceinline__ u32x4 data_wait(LAS u32* flag, LAS u32x4* p) {
  u32x4 r;
  u32 v, cnt;
  asm volatile("s_mov_b32 %2, %5\n"
               "1:\n\t"
               "ds_read_b32 %1, %3\n\t"
               "ds_read_b128 %0, %4\n\t"
               "s_sub_u32 %2, %2, 1\n\t"
               "s_waitcnt lgkmcnt(0)\n\t"
               "v_cmp_ne_u32 vcc, 1, %1\n\t"
               "s_cbranch_vccz 2f\n\t"
               "s_cbranch_scc0 1b\n"
               "2:"
               : "=&v"(r), "=&v"(v), "=&s"(cnt) : "v"((u32)(uintptr_t)flag), "v"((u32)(uintptr_t)p), "i"(SPIN_LDS) : "vcc", "scc", "memory");
  return r;
}

__device__ __forceinline__ void final_reduce(LAS u32* L, rsrc_t rws, const u16* x, u16* out, int rid, int grp, int lane, u32 tag) {
  LAS float* const LF = (LAS float*)L;
  __builtin_amdgcn_s_setprio(3);
  const u32 tagp = tag & TMASK;
  const int rb = rid / 28, cb = rid % 28, qd = lane & 3;
  const int pi = cb & 7, ph = cb < 24 ? pi >> 2 : pi >> 1, pg = cb < 24 ? pi & 3 : pi & 1;
  const int col = rb * D + 128 * (cb >> 3) + 8 * (4 * pg + ((lane >> 2) & 3)) + 4 * ph + (lane & 3);
  float xres = lo16(x[col]);
  int oo = O2_OFF + (grp * 32 + (lane >> 2)) * O2_S + rb * 1792 + cb * 64 + qd * 16;
  LAS float* wp = LF + ((lane & 12) == 0 ? L_R + (4 * qd + (lane >> 4)) * 8 + grp : L_XN + 128 + lane);
  LAS u32* fp = L + F_R + grp;
  LAS u32* fr = L + F_R + (lane & 7);
  LAS f32x4* rp = (LAS f32x4*)(LF + L_R + (lane & 15) * 8);
  int so = lane < 16 ? col * 2 : OOB;
  u32 setv = SET;
  rsrc_t ro = mkrs(out, 2 * D * 2);
  asm volatile("" : "+s"(ro));
  PIN(xres); PIN(oo); PIN(wp); PIN(fp); PIN(fr); PIN(rp); PIN(so); PIN(setv);
  flag_wait<1, 1>(L + F_PUB, lane);
  flag_wait<4, 0>(L + F_O2, lane);
  __builtin_amdgcn_s_sleep(O2_DELAY);
  u32x4 a0, a1;
  poll2(rws, oo, 16 * O2_S, tagp, a0, a1);
  float f = tr16(tr32(asf(a0.x) + asf(a1.x), asf(a0.z) + asf(a1.z)), tr32(asf(a0.y) + asf(a1.y), asf(a0.w) + asf(a1.w)));
  f += dpp<ROR8>(f); f += dpp<ROR4>(f);
  *wp = f;
  flag_set(fp, setv);
  u32 fl = *fr;
  CBAR();
  const f32x4 r0 = rp[0], r1 = rp[1];
  const f32x4 rs4 = r0 + r1;
  const float tot = xres + ((rs4.x + rs4.z) + (rs4.y + rs4.w));
  u32 res = pkbf(tot, tot);
  PIN(res);
  if (none(fl != SET)) {
    __builtin_amdgcn_raw_buffer_store_b16((u16)res, ro, so, 0, 0);
    if (rid == 0 && lane == 0) __builtin_amdgcn_raw_buffer_store_b32(tag, rws, EP_OFF, 0, AUX_SC1);
  }
}

}

extern "C" __global__ void __launch_bounds__(768, 3)
fused_decode(const u16* __restrict__ x, const u16* __restrict__ wq, const u16* __restrict__ wg_, const u16* __restrict__ wu,
             const u16* __restrict__ kc, char* __restrict__ ws, const u16* __restrict__ vc, const u16* __restrict__ wo,
             const u16* __restrict__ wd, u16* __restrict__ out) {
  __shared__ __attribute__((aligned(16))) u32 smem[L_END];
  LAS u32* const L = (LAS u32*)smem;
  LAS float* const LF = (LAS float*)smem;
  const int wg = blockIdx.x, h = wg & 7, j = wg >> 3, b = j & 1, sp = j >> 1, reg = wg & 15;
  const int w = __builtin_amdgcn_readfirstlane(threadIdx.x >> 6), lane = threadIdx.x & 63, kw = w & 3;
  asm volatile("s_load_dword s94, %0, 0x0\n\ts_load_dword s95, %0, 0x40" :: "s"(__builtin_amdgcn_kernarg_segment_ptr()) : "s94", "s95");
  u64 pc0;
  asm volatile("s_getpc_b64 %0" : "=s"(pc0));
  const u32 code_touch = ld32<0>(mkrs((const char*)(pc0 & ~255ull), w == 0 ? CODE_SPAN : 0), lane < 5 ? j * 640 + lane * 128 : OOB);
  __builtin_amdgcn_sched_barrier(0);
#define ROLE_ENTRY() const rsrc_t rws = mkrs(ws, WS_BYTES)
#define ROLE_TAG() const u32 tag = __builtin_amdgcn_readfirstlane(L[L_TAG]), tagp = tag & TMASK
  const u32 blk0 = sp * 4 + reg * ROT;
#define BLOCK_OFF(slot) (((blk0 + (((slot) >> 2) << 6) + ((slot) & 3u)) & 511u) << 12)
  const int hx = h ^ 4;
  u64 org;
#define STREAM_GROUP(G, VOFF, SLOT, ISK) \
  if (!(ISK) || (G) >= K_FREE) pace<(ISK) ? due_k(G) : due_v(G, 0)>(org); \
  if (4 * (G) > WINDOW) asm volatile("s_waitcnt vmcnt(%0)" :: "i"(WINDOW) : "memory"); \
  _Pragma("unroll") for (int i = 4 * (G); i < 4 * (G) + 2; ++i) d[i] = ld128<AUX_NT>(rkv, (VOFF), (int)BLOCK_OFF(SLOT)); \
  if (!(ISK) && (G) >= V_FINE) { __builtin_amdgcn_sched_barrier(0); pace<due_v(G, 1)>(org); } \
  _Pragma("unroll") for (int i = 4 * (G) + 2; i < 4 * (G) + 4; ++i) d[i] = ld128<AUX_NT>(rkv, (VOFF), (int)BLOCK_OFF(SLOT)); \
  __builtin_amdgcn_sched_barrier(0);
#define STREAM_LOADS(VOFF, SLOT, ISK) \
  STREAM_GROUP(0, VOFF, SLOT, ISK) STREAM_GROUP(1, VOFF, SLOT, ISK) STREAM_GROUP(2, VOFF, SLOT, ISK) STREAM_GROUP(3, VOFF, SLOT, ISK) \
  STREAM_GROUP(4, VOFF, SLOT, ISK) STREAM_GROUP(5, VOFF, SLOT, ISK) STREAM_GROUP(6, VOFF, SLOT, ISK) STREAM_GROUP(7, VOFF, SLOT, ISK)
#define WINDOW_RSRC(SRC) mkrs((SRC) + ((size_t)reg * SK + (SK - W)) * DH, W * DH * 2)

  if (__builtin_expect(w >= 4, 0)) {
    u32 tag_out;
    if (w < 8) {
      __builtin_amdgcn_s_setprio(2);
      const int g = lane >> 4, key = (lane >> 1) & 7;
      const bool hb = lane & 1;
      const int kvo = key * 512 + (2 * g + (lane & 1)) * 16;
      const rsrc_t rkv = WINDOW_RSRC(kc);
      u32x4 d[32];
      __builtin_amdgcn_sched_barrier(0);
      __builtin_amdgcn_s_barrier();
      org = __builtin_amdgcn_s_memrealtime();
      __builtin_amdgcn_sched_barrier(0);
      STREAM_LOADS(kvo + (i & 3) * 128, kw + 4 * (i >> 2), true)
      ROLE_ENTRY();
      flag_wait<2, 1>(L + F_Q, lane);
      ROLE_TAG();
      u32x4 qa[4];
#pragma unroll
      for (int i = 0; i < 4; ++i) qa[i] = *(LAS u32x4*)(L + L_Q + (lane & 1) * 128 + i * 32 + (2 * g + ((lane >> 1) & 1)) * 4);
      f32x4 ea, eb;
#pragma unroll
      for (int n = 0; n < 4; ++n) {
        f32x4 ca = {}, cb = {};
#pragma unroll
        for (int i = 0; i < 4; ++i) {
          ca = __builtin_amdgcn_mfma_f32_16x16x32_bf16(__builtin_bit_cast(bf16x8_t, qa[i]), __builtin_bit_cast(bf16x8_t, d[8 * n + i]), ca, 0, 0, 0);
          cb = __builtin_amdgcn_mfma_f32_16x16x32_bf16(__builtin_bit_cast(bf16x8_t, qa[i]), __builtin_bit_cast(bf16x8_t, d[8 * n + 4 + i]), cb, 0, 0, 0);
        }
#pragma unroll
        for (int k = 0; k < 4; ++k) { ea[k] = (n == 0 || g == n) ? ca[k] : ea[k]; eb[k] = (n == 0 || g == n) ? cb[k] : eb[k]; }
      }
      asm volatile("" :: "v"(code_touch));
      float sc[2];
#pragma unroll
      for (int hd = 0; hd < 2; ++hd) sc[hd] = rbf((hb ? eb[2 + hd] : ea[hd]) + dpp<XOR1>(hb ? ea[2 + hd] : eb[hd])) * 0.0625f;
      const float wm0 = mx32(mx16(row_max(sc[0]))), wm1 = mx32(mx16(row_max(sc[1])));
      CBAR();
      if (lane < 2) L[L_WM + kw * 2 + lane] = (asu(lane ? wm1 : wm0) & 0xffff0000u) | SET;
      u32x4 ma, mb;
      for (u32 s = 0; s < SPIN_LDS; ++s) {
        CBAR();
        ma = *(LAS u32x4*)(L + L_WM) ^ SET; mb = *(LAS u32x4*)(L + L_WM + 4) ^ SET;
        if (none((((ma.x | ma.y | ma.z | ma.w) | (mb.x | mb.y | mb.z | mb.w)) & 0xffffu) != 0u)) break;
      }
      const float M0 = fmaxf(fmaxf(asf(ma.x), asf(ma.z)), fmaxf(asf(mb.x), asf(mb.z)));
      const float M1 = fmaxf(fmaxf(asf(ma.y), asf(ma.w)), fmaxf(asf(mb.y), asf(mb.w)));
      const float p0 = rbf(__builtin_amdgcn_exp2f((sc[0] - M0) * 1.4426950408889634f));
      const float p1 = rbf(__builtin_amdgcn_exp2f((sc[1] - M1) * 1.4426950408889634f));
      LAS u32* pp = L + L_P + key * 64 + kw + 4 * (2 * g + (lane & 1));
      pp[0] = asu(p0) | (asu(p0) >> 16);
      pp[32] = asu(p1) | (asu(p1) >> 16);
      float ls = tr32(p0, p1);
      ls = row_sum(tr16(ls, ls));
      if ((lane & 31) == 0) LF[L_WL + kw * 2 + (lane >> 5)] = ls;
      flag_set(L + F_P + kw);

      if (kw == 0) {
        flag_wait<4, 0>(L + F_P, lane);
        if (lane < 2) {
          const float Ls = (LF[L_WL + lane] + LF[L_WL + 2 + lane]) + (LF[L_WL + 4 + lane] + LF[L_WL + 6 + lane]);
          st128(rws, ST_OFF + (((h * 2 + lane) * 2 + b) * 16 + sp) * 16, u32x4{asu(lane ? M1 : M0), tag, asu(Ls), tag});
        }
      } else if (kw == 1) {
        __builtin_amdgcn_s_setprio(3);
        const int s = lane >> 2, qd = lane & 3;
        const int so = ST_OFF + ((hx * 2 + (j >> 4)) * 32 + s) * 16;
        const int oo = O1_OFF + (hx * 32 + 2 * s) * O1_S + j * 64 + qd * 16;
        u32x4 s0, s1, a0, a1;
        flag_wait<4, 1>(L + F_VI, lane);
        for (u32 it = 0; it < SPIN_MEM; ++it) {
          CBAR();
          s0 = ld128<AUX_SC1>(rws, so); s1 = ld128<AUX_SC1>(rws, so + 256);
          if (none((s0.y != tag) | (s0.w != tag) | (s1.y != tag) | (s1.w != tag))) break;
          flag_wait<1, 1>(L + F_PUB, lane);
        }
        float wgt[2];
#pragma unroll
        for (int bb = 0; bb < 2; ++bb) {
          const float m = asf(bb ? s1.x : s0.x), l = asf(bb ? s1.z : s0.z);
          float mg = fmaxf(m, dpp<ROR8>(m)); mg = fmaxf(mg, dpp<ROR4>(mg)); mg = mx32(mx16(mg));
          const float e = __builtin_amdgcn_exp2f((m - mg) * 1.4426950408889634f);
          float lg = e * l; lg += dpp<ROR8>(lg); lg += dpp<ROR4>(lg); lg = tr16(lg, lg); lg = tr32(lg, lg);
          wgt[bb] = e * __builtin_amdgcn_rcpf(lg);
        }
        LAS u32* ow = L + ((lane & 12) == 0 ? L_O + qd * 4 + (lane >> 4) : L_XN + lane);
        LAS u32* of = L + F_O;
        u32 setv = SET;
        PIN(wgt[0]); PIN(wgt[1]); PIN(ow); PIN(of); PIN(setv);
        flag_wait<1, 0>(L + F_PUB, lane);
        __builtin_amdgcn_s_sleep(O1_DELAY);
        poll2(rws, oo, O1_S, tagp, a0, a1);
        f32x4 m0 = __builtin_bit_cast(f32x4, a0) * wgt[0], m1 = __builtin_bit_cast(f32x4, a1) * wgt[1];
        PIN(m0); PIN(m1);
        const float v0 = tr32(m0.x, m1.x), v2 = tr32(m0.z, m1.z), v1 = tr32(m0.y, m1.y), v3 = tr32(m0.w, m1.w);
        float e0 = tr16(v0, v2), e1 = tr16(v1, v3);
        e0 += dpp<ROR8>(e0); e1 += dpp<ROR8>(e1);
        e0 += dpp<ROR4>(e0); e1 += dpp<ROR4>(e1);
        *ow = pkbf(e0, e1);
        flag_set(of, setv);
      }
      tag_out = tag;
    } else {
      if (kw == 0 && lane < (L_END - L_WM) / 4) *(LAS u32x4*)(L + L_WM + 4 * lane) = u32x4{0u, 0u, 0u, 0u};
      LGKM0();
      __builtin_amdgcn_s_barrier();
      org = __builtin_amdgcn_s_memrealtime();
      __builtin_amdgcn_s_setprio(1);
      __builtin_amdgcn_sched_barrier(0);
      ROLE_ENTRY();
      flag_wait<1, 4>(L + F_SH, lane);
      org += (u64)(long long)(int)__builtin_amdgcn_readfirstlane(L[L_SH]);
      const rsrc_t rkv = WINDOW_RSRC(vc);
      u32x4 d[32];
      STREAM_LOADS(lane * 16 + kw * 1024, (u32)i, false)
      flag_set(L + F_VI + kw);
      const int m = lane & 31, hf = lane >> 5;
      u32x4 msk;
#pragma unroll
      for (int r = 0; r < 4; ++r) msk[r] = (m < 16 && ((m & 7) >> 1) == r) ? ((m & 1) ? 0xffff0000u : 0x0000ffffu) : 0u;
      LAS f32x4* op = (LAS f32x4*)(LF + L_OP) + kw * 128 + 2 * m + hf;
      LAS f32x4* rp = (LAS f32x4*)(LF + L_OP) + lane;
      LAS u32* fp = L + F_V + kw;
      LAS u32* fr = L + F_V + (lane & 3);
      int o1o = O1_OFF + (h * 32 + j) * O1_S + lane * 16;
      PIN(op); PIN(rp); PIN(fp); PIN(fr); PIN(o1o);
      flag_wait<4, 1>(L + F_P, lane);
      ROLE_TAG();
      const LAS u32x4* pp = (const LAS u32x4*)(L + L_P + ((kw * 2 + hf) * 2 + ((m >> 3) & 1)) * 32);
      f32x16 acc = {};
#pragma unroll
      for (int g = 0; g < 8; ++g) {
        const u32x4 p4 = pp[g];
#pragma unroll
        for (int k = 0; k < 4; ++k) {
          const u32x4 a = msk & p4[k];
          acc = __builtin_amdgcn_mfma_f32_32x32x16_bf16(__builtin_bit_cast(bf16x8_t, a), __builtin_bit_cast(bf16x8_t, d[4 * g + k]), acc, 0, 0, 0);
        }
        __builtin_amdgcn_sched_barrier(0);
      }
      asm volatile("" :: "v"(code_touch));
      op[0] = f32x4{acc[0], acc[1], acc[2], acc[3]};
      op[64] = f32x4{acc[4], acc[5], acc[6], acc[7]};
      flag_set(fp);
      const u32 fl = *fr;
      CBAR();
      const f32x4 o0 = (rp[0] + rp[128]) + (rp[256] + rp[384]);
      const f32x4 o1 = (rp[64] + rp[192]) + (rp[320] + rp[448]);
      u32x4 t0 = tag4(o0.x, o0.y, o0.z, o0.w, tagp), t1 = tag4(o1.x, o1.y, o1.z, o1.w, tagp);
      PIN(t0); PIN(t1);
      if (none(fl != SET)) {
        st128(rws, o1o, t0);
        st128(rws, o1o + 1024, t1);
        flag_set(L + F_PUB);
      }
      tag_out = tag;
    }
    if (!(h & 1) && j < 14) final_reduce(L, mkrs(ws, WS_BYTES), x, out, (h >> 1) * 14 + j, w - 4, lane, tag_out);
    return;
  }

  __builtin_amdgcn_s_setprio(3);
  const bool mlp = wg < 224;
  const int ke = (wg * 2341) >> 16, cb2 = wg - 28 * ke;
  const int rr = lane >> 3, c8 = lane & 7;
  const bool isq = kw < 2;
  int rowq = 112 * (j >> 3) + 64 * kw, rowm = 56 * ke, colq = (hx * 512 + (j & 7) * 64) * 2, colm = cb2 * 128;
  asm volatile("" : "+s"(rowq), "+s"(rowm), "+s"(colq), "+s"(colm));
  const int row0 = isq ? rowq : rowm;
  const int nrow = (isq | mlp) ? (0x38383040 >> (8 * kw)) & 0xff : 0;
  int rs = isq ? 8192 : DF * 2;
  int vw0 = (row0 + 2 * rr) * rs + (isq ? colq : colm) + c8 * 16;
  vw0 = nrow ? vw0 : OOB;
  int vw3 = 48 + 2 * rr < nrow ? vw0 : OOB;
  int xo = lane < 56 ? lane * 16 : OOB;
  int mat = kw;
  asm volatile("" : "+v"(vw0), "+v"(vw3), "+v"(xo), "+s"(rs), "+s"(mat));
  __builtin_amdgcn_sched_barrier(0);
  ROLE_ENTRY();
  const rsrc_t rx = mkrs((const u16*)x, 2 * D * 2);
  const u32x4 xa = ld128<0>(rx, xo), xb = ld128<0>(rx, xo + D * 2);
  const u32 epoch = ld32<AUX_SC1>(rws, EP_OFF);
  __builtin_amdgcn_sched_barrier(0);
  u32x4 wv[8];
  {
    const rsrc_t rw = mkrs(mat < 2 ? wq : mat == 2 ? wg_ : wu, mat < 2 ? D * 4096 * 2 : D * DF * 2);
#pragma unroll
    for (int i = 0; i < 8; ++i) wv[i] = ld128<AUX_NT>(rw, i < 6 ? vw0 : vw3, (16 * (i >> 1) + (i & 1)) * rs);
  }
  __builtin_amdgcn_sched_barrier(0);
  __builtin_amdgcn_s_barrier();
  org = __builtin_amdgcn_s_memrealtime();
  __builtin_amdgcn_sched_barrier(0);
  const int cbf = cb2 == 27 ? 0 : cb2 + 1;
  const int e0 = QP_OFF + (hx * 32 + j) * QP_S, e1 = GP_OFF + wg * GP_S - 512, e2 = ST_OFF + (wg & 127) * 64 - 24 * 64;
  const u32 box_touch = ld32<AUX_SC1>(rws, (kw == 1 && lane < 25) ? (lane < 8 ? e0 : lane < 24 ? e1 : e2) + lane * 64 : OOB);
  __builtin_amdgcn_sched_barrier(0);

#pragma unroll
  for (int bb = 0; bb < 2; ++bb) {
    const u32x4 xv = bb ? xb : xa;
    float v[8];
#pragma unroll
    for (int k = 0; k < 4; ++k) { v[2 * k] = lo16(xv[k]); v[2 * k + 1] = hi16(xv[k]); }
    float s = 0.f;
#pragma unroll
    for (int k = 0; k < 8; ++k) s += v[k];
    const float mean = wave_sum(s) * (1.f / D);
    float q = 0.f;
#pragma unroll
    for (int k = 0; k < 8; ++k) { const float t = v[k] - mean; q += t * t; }
    q = lane < 56 ? q : 0.f;
    const float var = rbf(wave_sum(q) * (1.f / D));
    const float den = rbf(__builtin_amdgcn_sqrtf(rbf(var + 1e-5f))), rden = __builtin_amdgcn_rcpf(den), mb = rbf(mean);
#pragma unroll
    for (int k = 0; k < 8; ++k) { const float c = rbf(v[k] - mb), r = c * rden; v[k] = fmaf(fmaf(-r, den, c), rden, r); }
    u32x4 o;
#pragma unroll
    for (int k = 0; k < 4; ++k) o[k] = pkbf(v[2 * k], v[2 * k + 1]);
    if (lane < 56) *(LAS u32x4*)(L + L_XN + bb * 224 + lane * 4) = o;
    else if (bb == 1) *(LAS u32x4*)(L + L_XN + 224 + lane * 4) = u32x4{0u, 0u, 0u, 0u};
  }
  asm volatile("" :: "v"(code_touch));
  const u32 tag = __builtin_amdgcn_readfirstlane(epoch) + 1u, tagp = tag & TMASK;
  if (kw == 0) L[L_TAG] = tag;
  if (kw == 3 && wg < 8 && lane == 0) __builtin_amdgcn_raw_buffer_store_b64(u32x2{(u32)org, tag}, rws, OR_OFF + wg * 8, 0, AUX_SC1);

  {
    float a0[8] = {}, a1[8] = {};
#pragma unroll
    for (int k = 0; k < 4; ++k) {
      const u32 x0 = L[L_XN + row0 / 2 + rr + 8 * k], x1 = L[L_XN + 224 + row0 / 2 + rr + 8 * k];
#pragma unroll
      for (int c = 0; c < 4; ++c) {
        const u32 pl = __builtin_amdgcn_perm(wv[2 * k + 1][c], wv[2 * k][c], 0x05040100u), ph = __builtin_amdgcn_perm(wv[2 * k + 1][c], wv[2 * k][c], 0x07060302u);
        a0[2 * c] = dot2(pl, x0, a0[2 * c]); a0[2 * c + 1] = dot2(ph, x0, a0[2 * c + 1]);
        a1[2 * c] = dot2(pl, x1, a1[2 * c]); a1[2 * c + 1] = dot2(ph, x1, a1[2 * c + 1]);
      }
    }
    asm volatile("" :: "v"(box_touch));
    float t[8], u[4];
#pragma unroll
    for (int k = 0; k < 8; ++k) t[k] = tr32(a0[k], a1[k]);
#pragma unroll
    for (int k = 0; k < 4; ++k) { u[k] = tr16(t[k], t[k + 4]); u[k] += dpp<ROR8>(u[k]); }
    const int bb = lane >> 5, hf = (lane >> 4) & 1;
    LAS f32x4* qx = (LAS f32x4*)(LF + L_QX) + bb * 16 + c8 * 2 + hf;
    if (kw == 1) {
      if (!(lane & 8)) *qx = f32x4{u[0], u[1], u[2], u[3]};
      flag_set(L + F_QX);
    } else if (kw == 0) {
      flag_wait<1, 0>(L + F_QX, lane);
      const f32x4 o = *qx;
      if (!(lane & 8)) st128(rws, QP_OFF + (hx * 32 + j) * QP_S + bb * 256 + hf * 128 + c8 * 16, tag4(u[0] + o.x, u[1] + o.y, u[2] + o.z, u[3] + o.w, tagp));
    } else if (mlp) {
      LAS u32x4* gx = (LAS u32x4*)(L + L_GX) + (kw - 2) * 32;
      if (!(lane & 8)) gx[c8 * 4 + bb * 2 + hf] = tag4(u[0], u[1], u[2], u[3], tagp);
      CBAR();
      if (lane < 32) st128(rws, GP_OFF + wg * GP_S + (lane >> 2) * 128 + (kw - 2) * 64 + (lane & 3) * 16, gx[lane]);
    }
  }

  if (kw < 2) {
    __builtin_amdgcn_s_sleep(Q_DELAY);
    const int qo = QP_OFF + (h * 32 + 4 * kw + 8 * (lane >> 4)) * QP_S + b * 256 + (lane & 15) * 16;
    u32x4 a[4];
    for (u32 s = 0; s < SPIN_MEM; ++s) {
      CBAR();
#pragma unroll
      for (int i = 0; i < 4; ++i) a[i] = ld128<AUX_SC1>(rws, qo, i * QP_S);
      if (all_ok((untag4(a[0], tagp) | untag4(a[1], tagp)) | (untag4(a[2], tagp) | untag4(a[3], tagp)))) break;
    }
#pragma unroll
    for (int i = 0; i < 2; ++i) {
      const u32x4 a0 = a[2 * i], a1 = a[2 * i + 1];
      const float v0 = tr32(asf(a0.x), asf(a1.x)), v1 = tr32(asf(a0.y), asf(a1.y)), v2 = tr32(asf(a0.z), asf(a1.z)), v3 = tr32(asf(a0.w), asf(a1.w));
      L[L_Q + (4 * kw + 2 * i + (lane >> 5)) * 32 + 4 * (lane & 7) + 2 * ((lane >> 3) & 1) + ((lane >> 4) & 1)] = pkbf(tr16(v0, v2), tr16(v1, v3));
    }
    flag_set(L + F_Q + kw);
  } else if (kw == 2) {
    u32 h0 = 0u, h1 = 0u;
    if (mlp) {
      const int go = GP_OFF + (cbf + 28 * rr) * GP_S + ke * 128 + c8 * 16;
      u32x4 a;
      flag_wait<2, 1>(L + F_Q, lane);
      for (u32 s = 0; s < SPIN_MEM; ++s) {
        CBAR();
        a = ld128<AUX_SC1>(rws, go);
        if (all_ok(untag4(a, tagp))) break;
        __builtin_amdgcn_s_sleep(16);
      }
      float hv[4];
#pragma unroll
      for (int k = 0; k < 4; ++k) {
        float g = asf(a[k]);
        g += dpp<ROR8>(g); g = tr16(g, g); g = tr32(g, g);
        g = rbf(g);
        const float uu = dpp<ROR4>(g);
        const float z = 0.7978845608028654f * (g + 0.044715f * g * g * g);
        const float th = 1.f - 2.f * __builtin_amdgcn_rcpf(1.f + __builtin_amdgcn_exp2f(z * 2.8853900817779268f));
        hv[k] = rbf(0.5f * g * (1.f + th)) * uu;
      }
      h0 = pkbf(hv[0], hv[1]); h1 = pkbf(hv[2], hv[3]);
    }
    if (lane < 4) {
      *(LAS u32x2*)(L + L_H + (2 * (lane & 1)) * 4 + (lane >> 1) * 2) = u32x2{h0, SET};
      *(LAS u32x2*)(L + L_H + (2 * (lane & 1) + 1) * 4 + (lane >> 1) * 2) = u32x2{h1, SET};
    }
    CBAR();
  } else {
    pace<T_ORG>(org);
    const u32x2 o = __builtin_amdgcn_raw_buffer_load_b64(rws, OR_OFF + (lane & 7) * 8, 0, AUX_SC1);
    const bool ok = o.y == tag;
    const float n = row_sum(ok ? 1.f : 0.f), dsum = row_sum(ok ? (float)(int)(o.x - (u32)org) : 0.f);
    const float sh = n > 0.f ? fminf(fmaxf(dsum * __builtin_amdgcn_rcpf(n) + ORG_BIAS, -80.f), 80.f) : 0.f;
    L[L_SH] = (u32)(int)sh;
    flag_set(L + F_SH);
  }

  const int rg = lane >> 4, cl = lane & 15;
  const int co = kw * 128 + cl * 8;
  u32x4 wa[4], wb[2];
  flag_wait<4, 1>(L + F_VI, lane);
  pace<T_LATE>(org + (u64)(long long)(int)__builtin_amdgcn_readfirstlane(L[L_SH]));
  {
    const rsrc_t ro = mkrs(wo, 4096 * D * 2), rd = mkrs(wd, mlp ? DF * D * 2 : 0);
    const int vo = co < D ? (hx * 512 + j * 16 + 4 * rg) * (D * 2) + co * 2 : OOB;
    const int vd = co < D ? (64 * cbf + 8 * ke + 2 * rg) * (D * 2) + co * 2 : OOB;
#pragma unroll
    for (int i = 0; i < 2; ++i) wb[i] = ld128<AUX_NT>(rd, vd, i * D * 2);
#pragma unroll
    for (int i = 0; i < 4; ++i) wa[i] = ld128<AUX_NT>(ro, vo, i * D * 2);
  }
  __builtin_amdgcn_sched_barrier(0);
  float a0[8], a1[8];
  u32 pa[8], pb[8];
  {
    u32x4 hh;
    for (u32 s = 0; s < SPIN_LDS; ++s) {
      CBAR();
      hh = *(LAS u32x4*)(L + L_H + rg * 4);
      if (none((hh.y != SET) | (hh.w != SET))) break;
      __builtin_amdgcn_s_sleep(1);
    }
#pragma unroll
    for (int k = 0; k < 4; ++k) {
      const u32 pl = __builtin_amdgcn_perm(wb[1][k], wb[0][k], 0x05040100u), ph = __builtin_amdgcn_perm(wb[1][k], wb[0][k], 0x07060302u);
      a0[2 * k] = dot2(pl, hh.x, 0.f); a0[2 * k + 1] = dot2(ph, hh.x, 0.f);
      a1[2 * k] = dot2(pl, hh.z, 0.f); a1[2 * k + 1] = dot2(ph, hh.z, 0.f);
    }
#pragma unroll
    for (int k = 0; k < 4; ++k) {
      pa[2 * k] = __builtin_amdgcn_perm(wa[1][k], wa[0][k], 0x05040100u); pa[2 * k + 1] = __builtin_amdgcn_perm(wa[1][k], wa[0][k], 0x07060302u);
      pb[2 * k] = __builtin_amdgcn_perm(wa[3][k], wa[2][k], 0x05040100u); pb[2 * k + 1] = __builtin_amdgcn_perm(wa[3][k], wa[2][k], 0x07060302u);
    }
  }
#pragma unroll
  for (int k = 0; k < 8; ++k) asm volatile("" : "+v"(a0[k]), "+v"(a1[k]), "+v"(pa[k]), "+v"(pb[k]));
  int o2o = co < D ? O2_OFF + (h * 32 + j) * O2_S + (lane >> 5) * 1792 + kw * 512 + (((lane >> 4) & 1) * (kw == 3 ? 8 : 16) + cl) * 16 : OOB;
  LAS u32x4* orp = (LAS u32x4*)(L + L_O + rg * 4);
  LAS u32* ofp = L + F_O;
  PIN(o2o); PIN(orp); PIN(ofp);
  {
    const u32x4 oo = data_wait(ofp, orp);
#pragma unroll
    for (int k = 0; k < 8; ++k) {
      a0[k] = dot2(pb[k], oo.y, dot2(pa[k], oo.x, a0[k]));
      a1[k] = dot2(pb[k], oo.w, dot2(pa[k], oo.z, a1[k]));
    }
    float t[8];
#pragma unroll
    for (int k = 0; k < 8; ++k) t[k] = tr32(a0[k], a1[k]);
    st128(rws, o2o, tag4(tr16(t[0], t[4]), tr16(t[1], t[5]), tr16(t[2], t[6]), tr16(t[3], t[7]), tagp));
  }
  flag_set(L + F_O2 + kw);
}

extern "C" void kernel_launch(void* const* d_in, const int* in_sizes, int n_in, void* d_out, int out_size, void* d_ws, size_t ws_size,
                              hipStream_t stream) {
  fused_decode<<<256, 768, 0, stream>>>((const u16*)d_in[0], (const u16*)d_in[3], (const u16*)d_in[6], (const u16*)d_in[7], (const u16*)d_in[1],
                                        (char*)d_ws, (const u16*)d_in[2], (const u16*)d_in[4], (const u16*)d_in[8], (u16*)d_out);
}
```

```cpp
#include <hip/hip_runtime.h>
#include <stdint.h>

typedef unsigned int u32;
typedef unsigned short u16;
typedef unsigned long long u64;
typedef u32 u32x2 __attribute__((ext_vector_type(2)));
typedef u32 u32x4 __attribute__((ext_vector_type(4)));
typedef float f32x4 __attribute__((ext_vector_type(4)));
typedef float f32x16 __attribute__((ext_vector_type(16)));
typedef __bf16 bf16x2_t __attribute__((ext_vector_type(2)));
typedef __bf16 bf16x8_t __attribute__((ext_vector_type(8)));
#define LAS __attribute__((address_space(3)))
typedef __amdgpu_buffer_rsrc_t rsrc_t;

namespace {
constexpr int D = 448, SK = 32768, W = 4096, DH = 256, DF = 1792;
constexpr int AUX_NT = 2, AUX_SC1 = 16;
constexpr int OOB = 1 << 30;
constexpr u32 TMASK = 0xfu;

constexpr int QP_S = 768, GP_S = 1280, O1_S = 2304, O2_S = 3840;
constexpr int EP_OFF = 0;
constexpr int OR_OFF = 2048;
constexpr int ST_OFF = 4096;
constexpr int QP_OFF = ST_OFF + 8192;
constexpr int GP_OFF = QP_OFF + 256 * QP_S;
constexpr int O1_OFF = GP_OFF + 256 * GP_S;
constexpr int O2_OFF = O1_OFF + 256 * O1_S;
constexpr int WS_BYTES = O2_OFF + 256 * O2_S;

constexpr int L_XN = 0;
constexpr int L_QX = L_XN + 480;
constexpr int L_GX = L_QX + 128;
constexpr int L_Q = L_GX + 256;
constexpr int L_P = L_Q + 256;
constexpr int L_OP = L_P + 512;
constexpr int L_WM = L_OP + 2048;
constexpr int L_WL = L_WM + 8;
constexpr int L_H = L_WL + 8;
constexpr int L_O = L_H + 16;
constexpr int F_O = L_O + 16;
constexpr int L_R = L_O + 32;
constexpr int F_P = L_R + 128;
constexpr int F_VI = F_P + 4;
constexpr int F_O2 = F_VI + 4;
constexpr int F_Q = F_O2 + 4;
constexpr int F_QX = F_Q + 2, F_PUB = F_QX + 1;
constexpr int C_V = F_PUB + 1, C_R = C_V + 1;
constexpr int L_TAG = C_V + 4;
constexpr int L_SH = L_TAG + 2, F_SH = L_TAG + 3;
constexpr int L_END = L_TAG + 4;
constexpr u32 SET = 1u;
static_assert((L_END - L_WM) % 4 == 0 && (L_END - L_WM) / 4 <= 64, "one 16-byte store per lane clears the flag area");

constexpr int T_K0 = 25, T_KSTEP10 = 610, T_VSTEP10 = 625;
constexpr int K_FREE = 2;
constexpr int V_FINE = 4, V_FINE_LEAD = 25;
constexpr int WINDOW = 6;
constexpr int T_ORG = 300, ORG_BIAS = 5;
constexpr int due_k(int g) { return T_K0 + g * T_KSTEP10 / 10; }
constexpr int due_v(int g, int half) { return T_K0 + (8 * T_KSTEP10 + g * T_VSTEP10 + half * T_VSTEP10 / 2) / 10 - (g >= V_FINE ? V_FINE_LEAD : 0); }
constexpr int T_LATE = due_v(8, 0);
constexpr int Q_DELAY = 40, O1_DELAY = 11, O2_DELAY = 8;
constexpr int ROT = 70;
constexpr int CODE_SPAN = 15360;
constexpr u32 SPIN_LDS = 1u << 21, SPIN_MEM = 1u << 16;

__device__ __forceinline__ float asf(u32 v) { return __builtin_bit_cast(float, v); }
__device__ __forceinline__ u32 asu(float v) { return __builtin_bit_cast(u32, v); }
__device__ __forceinline__ float lo16(u32 v) { return asf(v << 16); }
__device__ __forceinline__ float hi16(u32 v) { return asf(v & 0xffff0000u); }
__device__ __forceinline__ u32 pkbf(float a, float b) {
  bf16x2_t r = {(__bf16)a, (__bf16)b};
  return __builtin_bit_cast(u32, r);
}
__device__ __forceinline__ float rbf(float a) { return (float)(__bf16)a; }
__device__ __forceinline__ float dot2(u32 a, u32 b, float c) {
  return __builtin_amdgcn_fdot2_f32_bf16(__builtin_bit_cast(bf16x2_t, a), __builtin_bit_cast(bf16x2_t, b), c, false);
}
template <int CTRL> __device__ __forceinline__ float dpp(float x) {
  return asf(__builtin_amdgcn_mov_dpp((int)asu(x), CTRL, 0xf, 0xf, true));
}
constexpr int XOR1 = 0xB1, XOR2 = 0x4E, ROR8 = 0x128, ROR4 = 0x124;
__device__ __forceinline__ float tr32(float a, float b) {
  auto r = __builtin_amdgcn_permlane32_swap(asu(a), asu(b), false, false);
  return asf(r[0]) + asf(r[1]);
}
__device__ __forceinline__ float tr16(float a, float b) {
  auto r = __builtin_amdgcn_permlane16_swap(asu(a), asu(b), false, false);
  return asf(r[0]) + asf(r[1]);
}
__device__ __forceinline__ float mx32(float a) {
  auto r = __builtin_amdgcn_permlane32_swap(asu(a), asu(a), false, false);
  return fmaxf(asf(r[0]), asf(r[1]));
}
__device__ __forceinline__ float mx16(float a) {
  auto r = __builtin_amdgcn_permlane16_swap(asu(a), asu(a), false, false);
  return fmaxf(asf(r[0]), asf(r[1]));
}
__device__ __forceinline__ float row_sum(float v) { v += dpp<ROR8>(v); v += dpp<ROR4>(v); v += dpp<XOR2>(v); v += dpp<XOR1>(v); return v; }
__device__ __forceinline__ float row_max(float v) {
  v = fmaxf(v, dpp<ROR8>(v)); v = fmaxf(v, dpp<ROR4>(v)); v = fmaxf(v, dpp<XOR2>(v)); v = fmaxf(v, dpp<XOR1>(v)); return v;
}
__device__ __forceinline__ float wave_sum(float v) { v = row_sum(v); v = tr16(v, v); return tr32(v, v); }

__device__ __forceinline__ rsrc_t mkrs(const void* p, u32 bytes) { return __builtin_amdgcn_make_buffer_rsrc((void*)p, 0, bytes, 0x00020000); }
template <int AUX> __device__ __forceinline__ u32x4 ld128(rsrc_t r, int voff, int soff = 0) { return __builtin_amdgcn_raw_buffer_load_b128(r, voff, soff, AUX); }
template <int AUX> __device__ __forceinline__ u32 ld32(rsrc_t r, int voff) { return __builtin_amdgcn_raw_buffer_load_b32(r, voff, 0, AUX); }
__device__ __forceinline__ void st128(rsrc_t r, int voff, u32x4 v) { __builtin_amdgcn_raw_buffer_store_b128(v, r, voff, 0, AUX_SC1); }
__device__ __forceinline__ u32 tagw(float v, u32 tag4) { return (asu(v) & ~TMASK) | tag4; }
__device__ __forceinline__ u32x4 tag4(float a, float b, float c, float d, u32 t) { return u32x4{tagw(a, t), tagw(b, t), tagw(c, t), tagw(d, t)}; }
__device__ __forceinline__ u32 untag4(u32x4& v, u32 t) { v.x ^= t; v.y ^= t; v.z ^= t; v.w ^= t; return v.x | v.y | v.z | v.w; }
__device__ __forceinline__ bool none(bool bad) { return __builtin_amdgcn_ballot_w64(bad) == 0ull; }
__device__ __forceinline__ bool all_ok(u32 bad) { return none((bad & TMASK) != 0u); }

#define CBAR() asm volatile("" ::: "memory")
#define PIN(v) asm volatile("" : "+v"(v))
#define LGKM0() asm volatile("s_waitcnt lgkmcnt(0)" ::: "memory")
template <int N, int SLEEP> __device__ __forceinline__ void flag_wait(LAS u32* p, int lane) {
  u32 v, cnt;
  const u32 addr = (u32)(uintptr_t)(p + (lane & (N - 1)));
  asm volatile("s_mov_b32 %1, %4\n"
               "1:\n\t"
               "ds_read_b32 %0, %2\n\t"
               "s_waitcnt lgkmcnt(0)\n\t"
               "v_cmp_ne_u32 vcc, %3, %0\n\t"
               "s_cbranch_vccz 2f\n\t"
               "s_sleep %5\n\t"
               "s_sub_u32 %1, %1, 1\n\t"
               "s_cbranch_scc0 1b\n"
               "2:"
               : "=&v"(v), "=&s"(cnt) : "v"(addr), "i"(SET), "i"(SPIN_LDS), "i"(SLEEP) : "vcc", "scc", "memory");
}
template <int DUE> __device__ __forceinline__ void pace(u64 org) {
  asm volatile("1:\n\t"
               "s_memrealtime vcc\n\t"
               "s_waitcnt lgkmcnt(0)\n\t"
               "s_sub_u32 vcc_lo, vcc_lo, %0\n\t"
               "s_cmp_lt_i32 vcc_lo, %1\n\t"
               "s_cbranch_scc0 2f\n\t"
               "s_sleep 1\n\t"
               "s_branch 1b\n"
               "2:"
               :: "s"((u32)org), "i"(DUE), "s"(org) : "vcc", "scc", "memory");
}
__device__ __forceinline__ void flag_set(LAS u32* p, u32 set = SET) { CBAR(); *p = set; CBAR(); }

__device__ __forceinline__ u32 ticket_issue(LAS u32* p, u32 one) {
  u32 t;
  asm volatile("s_mov_b64 exec, 1\n\tds_add_rtn_u32 %0, %1, %2\n\ts_mov_b64 exec, -1" : "=&v"(t) : "v"((u32)(uintptr_t)p), "v"(one) : "memory");
  return t;
}
__device__ __forceinline__ u32 ticket_value(u32 t) {
  asm volatile("s_waitcnt lgkmcnt(0)" : "+v"(t) :: "memory");
  return __builtin_amdgcn_readfirstlane(t);
}

__device__ __forceinline__ void poll2(rsrc_t rs, int voff, int soff, u32 t4, u32x4& r0, u32x4& r1) {
  u32 o0, o1, o2, o3, o4, o5, o6, o7, m, cnt;
  asm volatile("s_mov_b32 %9, %14\n"
               "1:\n\t"
               "buffer_load_dwordx4 v[144:147], %10, %11, 0 offen sc1\n\t"
               "buffer_load_dwordx4 v[148:151], %10, %11, %12 offen sc1\n\t"
               "s_sub_u32 %9, %9, 1\n\t"
               "s_waitcnt vmcnt(1)\n\t"
               "v_xor_b32 %0, %13, v144\n\tv_xor_b32 %1, %13, v145\n\tv_xor_b32 %2, %13, v146\n\tv_xor_b32 %3, %13, v147\n\t"
               "v_or3_b32 %8, %0, %1, %2\n\t"
               "s_waitcnt vmcnt(0)\n\t"
               "v_xor_b32 %4, %13, v148\n\tv_xor_b32 %5, %13, v149\n\tv_xor_b32 %6, %13, v150\n\tv_xor_b32 %7, %13, v151\n\t"
               "v_or3_b32 %8, %8, %3, %4\n\t"
               "v_or3_b32 %8, %8, %5, %6\n\t"
               "v_bitop3_b32 %8, %8, 15, %7 bitop3:0xc8\n\t"
               "v_cmp_ne_u32 vcc, 0, %8\n\t"
               "s_cbranch_vccz 2f\n\t"
               "s_cbranch_scc0 1b\n"
               "2:"
               : "=&v"(o0), "=&v"(o1), "=&v"(o2), "=&v"(o3), "=&v"(o4), "=&v"(o5), "=&v"(o6), "=&v"(o7), "=&v"(m), "=&s"(cnt)
               : "v"(voff), "s"(rs), "s"(soff), "s"(t4), "i"(SPIN_MEM)
               : "vcc", "scc", "memory", "v144", "v145", "v146", "v147", "v148", "v149", "v150", "v151");
  r0 = u32x4{o0, o1, o2, o3};
  r1 = u32x4{o4, o5, o6, o7};
}
__device__ __forceinline__ u32x4 data_wait(LAS u32* flag, LAS u32x4* p) {
  u32x4 r;
  u32 v, cnt;
  asm volatile("s_mov_b32 %2, %5\n"
               "1:\n\t"
               "ds_read_b32 %1, %3\n\t"
               "ds_read_b128 %0, %4\n\t"
               "s_sub_u32 %2, %2, 1\n\t"
               "s_waitcnt lgkmcnt(0)\n\t"
               "v_cmp_ne_u32 vcc, 1, %1\n\t"
               "s_cbranch_vccz 2f\n\t"
               "s_cbranch_scc0 1b\n"
               "2:"
               : "=&v"(r), "=&v"(v), "=&s"(cnt) : "v"((u32)(uintptr_t)flag), "v"((u32)(uintptr_t)p), "i"(SPIN_LDS) : "vcc", "scc", "memory");
  return r;
}

__device__ __forceinline__ void final_reduce(LAS u32* L, rsrc_t rws, const u16* x, u16* out, int rid, int grp, int lane, u32 tag, u64 tent, u64 bar) {
  LAS float* const LF = (LAS float*)L;
  __builtin_amdgcn_s_setprio(3);
  const u32 tagp = tag & TMASK;
  const int rb = rid / 28, cb = rid % 28, qd = lane & 3;
  const int pi = cb & 7, ph = cb < 24 ? pi >> 2 : pi >> 1, pg = cb < 24 ? pi & 3 : pi & 1;
  const int col = rb * D + 128 * (cb >> 3) + 8 * (4 * pg + ((lane >> 2) & 3)) + 4 * ph + (lane & 3);
  float xres = lo16(x[col]);
  int oo = O2_OFF + (grp * 32 + (lane >> 2)) * O2_S + rb * 1792 + cb * 64 + qd * 16;
  LAS float* wp = LF + ((lane & 12) == 0 ? L_R + (4 * qd + (lane >> 4)) * 8 + grp : L_XN + 128 + lane);
  LAS u32* cp = L + C_R;
  LAS f32x4* rp = (LAS f32x4*)(LF + L_R + (lane & 15) * 8);
  int so = lane < 16 ? col * 2 : OOB;
  u32 setv = SET;
  rsrc_t ro = mkrs(out, 2 * D * 2);
  asm volatile("" : "+s"(ro));
  PIN(xres); PIN(oo); PIN(wp); PIN(cp); PIN(rp); PIN(so); PIN(setv);
  flag_wait<1, 1>(L + F_PUB, lane);
  flag_wait<4, 0>(L + F_O2, lane);
  __builtin_amdgcn_s_sleep(O2_DELAY);
  u32x4 a0, a1;
  poll2(rws, oo, 16 * O2_S, tagp, a0, a1);
  float f = tr16(tr32(asf(a0.x) + asf(a1.x), asf(a0.z) + asf(a1.z)), tr32(asf(a0.y) + asf(a1.y), asf(a0.w) + asf(a1.w)));
  f += dpp<ROR8>(f); f += dpp<ROR4>(f);
  *wp = f;
  const u32 tk = ticket_issue(cp, setv);
  const f32x4 r0 = rp[0], r1 = rp[1];
  const f32x4 rs4 = r0 + r1;
  const float tot = xres + ((rs4.x + rs4.z) + (rs4.y + rs4.w));
  u32 res = pkbf(tot, tot);
  PIN(res);
  if (ticket_value(tk) == 7) {
    __builtin_amdgcn_raw_buffer_store_b16((u16)res, ro, so, 0, 0);
    if (rid == 0 && lane == 0) __builtin_amdgcn_raw_buffer_store_b32(tag, rws, EP_OFF, 0, AUX_SC1);
    if (rid == 0) {
      const u64 now = __builtin_amdgcn_s_memrealtime();
      const u64 until = tent + 3000 + 8 * (now - tent - 1200);
      while (__builtin_amdgcn_s_memrealtime() < until) __builtin_amdgcn_s_sleep(1);
    }
  }
}

}

extern "C" __global__ void __launch_bounds__(768, 3)
fused_decode(const u16* __restrict__ x, const u16* __restrict__ wq, const u16* __restrict__ wg_, const u16* __restrict__ wu,
             const u16* __restrict__ kc, char* __restrict__ ws, const u16* __restrict__ vc, const u16* __restrict__ wo,
             const u16* __restrict__ wd, u16* __restrict__ out) {
  __shared__ __attribute__((aligned(16))) u32 smem[L_END];
  LAS u32* const L = (LAS u32*)smem;
  LAS float* const LF = (LAS float*)smem;
  const int wg = blockIdx.x, h = wg & 7, j = wg >> 3, b = j & 1, sp = j >> 1, reg = wg & 15;
  const int w = __builtin_amdgcn_readfirstlane(threadIdx.x >> 6), lane = threadIdx.x & 63, kw = w & 3;
  const u64 tent = __builtin_amdgcn_s_memrealtime();
  u64 bar;
  asm volatile("s_load_dword s94, %0, 0x0\n\ts_load_dword s95, %0, 0x40" :: "s"(__builtin_amdgcn_kernarg_segment_ptr()) : "s94", "s95");
  u64 pc0;
  asm volatile("s_getpc_b64 %0" : "=s"(pc0));
  const u32 code_touch = ld32<0>(mkrs((const char*)(pc0 & ~255ull), w == 0 ? CODE_SPAN : 0), lane < 5 ? j * 640 + lane * 128 : OOB);
  __builtin_amdgcn_sched_barrier(0);
#define ROLE_ENTRY() const rsrc_t rws = mkrs(ws, WS_BYTES)
#define ROLE_TAG() const u32 tag = __builtin_amdgcn_readfirstlane(L[L_TAG]), tagp = tag & TMASK
  const u32 blk0 = sp * 4 + reg * ROT;
#define BLOCK_OFF(slot) (((blk0 + (((slot) >> 2) << 6) + ((slot) & 3u)) & 511u) << 12)
  const int hx = h ^ 4;
  u64 org;
#define STREAM_GROUP(G, VOFF, SLOT, ISK) \
  if (!(ISK) || (G) >= K_FREE) pace<(ISK) ? due_k(G) : due_v(G, 0)>(org); \
  if (4 * (G) > WINDOW) asm volatile("s_waitcnt vmcnt(%0)" :: "i"(WINDOW) : "memory"); \
  _Pragma("unroll") for (int i = 4 * (G); i < 4 * (G) + 2; ++i) d[i] = ld128<AUX_NT>(rkv, (VOFF), (int)BLOCK_OFF(SLOT)); \
  if (!(ISK) && (G) >= V_FINE) { __builtin_amdgcn_sched_barrier(0); pace<due_v(G, 1)>(org); } \
  _Pragma("unroll") for (int i = 4 * (G) + 2; i < 4 * (G) + 4; ++i) d[i] = ld128<AUX_NT>(rkv, (VOFF), (int)BLOCK_OFF(SLOT)); \
  __builtin_amdgcn_sched_barrier(0);
#define STREAM_LOADS(VOFF, SLOT, ISK) \
  STREAM_GROUP(0, VOFF, SLOT, ISK) STREAM_GROUP(1, VOFF, SLOT, ISK) STREAM_GROUP(2, VOFF, SLOT, ISK) STREAM_GROUP(3, VOFF, SLOT, ISK) \
  STREAM_GROUP(4, VOFF, SLOT, ISK) STREAM_GROUP(5, VOFF, SLOT, ISK) STREAM_GROUP(6, VOFF, SLOT, ISK) STREAM_GROUP(7, VOFF, SLOT, ISK)
#define WINDOW_RSRC(SRC) mkrs((SRC) + ((size_t)reg * SK + (SK - W)) * DH, W * DH * 2)

  if (__builtin_expect(w >= 4, 0)) {
    u32 tag_out;
    if (w < 8) {
      __builtin_amdgcn_s_setprio(2);
      const int g = lane >> 4, key = (lane >> 1) & 7;
      const bool hb = lane & 1;
      const int kvo = key * 512 + (2 * g + (lane & 1)) * 16;
      const rsrc_t rkv = WINDOW_RSRC(kc);
      u32x4 d[32];
      __builtin_amdgcn_sched_barrier(0);
      __builtin_amdgcn_s_barrier();
      org = __builtin_amdgcn_s_memrealtime(); bar = org;
      __builtin_amdgcn_sched_barrier(0);
      STREAM_LOADS(kvo + (i & 3) * 128, kw + 4 * (i >> 2), true)
      ROLE_ENTRY();
      flag_wait<2, 1>(L + F_Q, lane);
      ROLE_TAG();
      u32x4 qa[4];
#pragma unroll
      for (int i = 0; i < 4; ++i) qa[i] = *(LAS u32x4*)(L + L_Q + (lane & 1) * 128 + i * 32 + (2 * g + ((lane >> 1) & 1)) * 4);
      f32x4 ea, eb;
#pragma unroll
      for (int n = 0; n < 4; ++n) {
        f32x4 ca = {}, cb = {};
#pragma unroll
        for (int i = 0; i < 4; ++i) {
          ca = __builtin_amdgcn_mfma_f32_16x16x32_bf16(__builtin_bit_cast(bf16x8_t, qa[i]), __builtin_bit_cast(bf16x8_t, d[8 * n + i]), ca, 0, 0, 0);
          cb = __builtin_amdgcn_mfma_f32_16x16x32_bf16(__builtin_bit_cast(bf16x8_t, qa[i]), __builtin_bit_cast(bf16x8_t, d[8 * n + 4 + i]), cb, 0, 0, 0);
        }
#pragma unroll
        for (int k = 0; k < 4; ++k) { ea[k] = (n == 0 || g == n) ? ca[k] : ea[k]; eb[k] = (n == 0 || g == n) ? cb[k] : eb[k]; }
      }
      asm volatile("" :: "v"(code_touch));
      float sc[2];
#pragma unroll
      for (int hd = 0; hd < 2; ++hd) sc[hd] = rbf((hb ? eb[2 + hd] : ea[hd]) + dpp<XOR1>(hb ? ea[2 + hd] : eb[hd])) * 0.0625f;
      const float wm0 = mx32(mx16(row_max(sc[0]))), wm1 = mx32(mx16(row_max(sc[1])));
      CBAR();
      if (lane < 2) L[L_WM + kw * 2 + lane] = (asu(lane ? wm1 : wm0) & 0xffff0000u) | SET;
      u32x4 ma, mb;
      for (u32 s = 0; s < SPIN_LDS; ++s) {
        CBAR();
        ma = *(LAS u32x4*)(L + L_WM) ^ SET; mb = *(LAS u32x4*)(L + L_WM + 4) ^ SET;
        if (none((((ma.x | ma.y | ma.z | ma.w) | (mb.x | mb.y | mb.z | mb.w)) & 0xffffu) != 0u)) break;
      }
      const float M0 = fmaxf(fmaxf(asf(ma.x), asf(ma.z)), fmaxf(asf(mb.x), asf(mb.z)));
      const float M1 = fmaxf(fmaxf(asf(ma.y), asf(ma.w)), fmaxf(asf(mb.y), asf(mb.w)));
      const float p0 = rbf(__builtin_amdgcn_exp2f((sc[0] - M0) * 1.4426950408889634f));
      const float p1 = rbf(__builtin_amdgcn_exp2f((sc[1] - M1) * 1.4426950408889634f));
      LAS u32* pp = L + L_P + key * 64 + kw + 4 * (2 * g + (lane & 1));
      pp[0] = asu(p0) | (asu(p0) >> 16);
      pp[32] = asu(p1) | (asu(p1) >> 16);
      float ls = tr32(p0, p1);
      ls = row_sum(tr16(ls, ls));
      if ((lane & 31) == 0) LF[L_WL + kw * 2 + (lane >> 5)] = ls;
      flag_set(L + F_P + kw);

      if (kw == 0) {
        flag_wait<4, 0>(L + F_P, lane);
        if (lane < 2) {
          const float Ls = (LF[L_WL + lane] + LF[L_WL + 2 + lane]) + (LF[L_WL + 4 + lane] + LF[L_WL + 6 + lane]);
          st128(rws, ST_OFF + (((h * 2 + lane) * 2 + b) * 16 + sp) * 16, u32x4{asu(lane ? M1 : M0), tag, asu(Ls), tag});
        }
      } else if (kw == 1) {
        __builtin_amdgcn_s_setprio(3);
        const int s = lane >> 2, qd = lane & 3;
        const int so = ST_OFF + ((hx * 2 + (j >> 4)) * 32 + s) * 16;
        const int oo = O1_OFF + (hx * 32 + 2 * s) * O1_S + j * 64 + qd * 16;
        u32x4 s0, s1, a0, a1;
        flag_wait<4, 1>(L + F_VI, lane);
        for (u32 it = 0; it < SPIN_MEM; ++it) {
          CBAR();
          s0 = ld128<AUX_SC1>(rws, so); s1 = ld128<AUX_SC1>(rws, so + 256);
          if (none((s0.y != tag) | (s0.w != tag) | (s1.y != tag) | (s1.w != tag))) break;
          flag_wait<1, 1>(L + F_PUB, lane);
        }
        float wgt[2];
#pragma unroll
        for (int bb = 0; bb < 2; ++bb) {
          const float m = asf(bb ? s1.x : s0.x), l = asf(bb ? s1.z : s0.z);
          float mg = fmaxf(m, dpp<ROR8>(m)); mg = fmaxf(mg, dpp<ROR4>(mg)); mg = mx32(mx16(mg));
          const float e = __builtin_amdgcn_exp2f((m - mg) * 1.4426950408889634f);
          float lg = e * l; lg += dpp<ROR8>(lg); lg += dpp<ROR4>(lg); lg = tr16(lg, lg); lg = tr32(lg, lg);
          wgt[bb] = e * __builtin_amdgcn_rcpf(lg);
        }
        LAS u32* ow = L + ((lane & 12) == 0 ? L_O + qd * 4 + (lane >> 4) : L_XN + lane);
        LAS u32* of = L + F_O;
        u32 setv = SET;
        PIN(wgt[0]); PIN(wgt[1]); PIN(ow); PIN(of); PIN(setv);
        flag_wait<1, 0>(L + F_PUB, lane);
        __builtin_amdgcn_s_sleep(O1_DELAY);
        poll2(rws, oo, O1_S, tagp, a0, a1);
        f32x4 m0 = __builtin_bit_cast(f32x4, a0) * wgt[0], m1 = __builtin_bit_cast(f32x4, a1) * wgt[1];
        PIN(m0); PIN(m1);
        const float v0 = tr32(m0.x, m1.x), v2 = tr32(m0.z, m1.z), v1 = tr32(m0.y, m1.y), v3 = tr32(m0.w, m1.w);
        float e0 = tr16(v0, v2), e1 = tr16(v1, v3);
        e0 += dpp<ROR8>(e0); e1 += dpp<ROR8>(e1);
        e0 += dpp<ROR4>(e0); e1 += dpp<ROR4>(e1);
        *ow = pkbf(e0, e1);
        flag_set(of, setv);
      }
      tag_out = tag;
    } else {
      if (kw == 0 && lane < (L_END - L_WM) / 4) *(LAS u32x4*)(L + L_WM + 4 * lane) = u32x4{0u, 0u, 0u, 0u};
      LGKM0();
      __builtin_amdgcn_s_barrier();
      org = __builtin_amdgcn_s_memrealtime(); bar = org;
      __builtin_amdgcn_s_setprio(1);
      __builtin_amdgcn_sched_barrier(0);
      ROLE_ENTRY();
      flag_wait<1, 4>(L + F_SH, lane);
      org += (u64)(long long)(int)__builtin_amdgcn_readfirstlane(L[L_SH]);
      const rsrc_t rkv = WINDOW_RSRC(vc);
      u32x4 d[32];
      STREAM_LOADS(lane * 16 + kw * 1024, (u32)i, false)
      flag_set(L + F_VI + kw);
      const int m = lane & 31, hf = lane >> 5;
      u32x4 msk;
#pragma unroll
      for (int r = 0; r < 4; ++r) msk[r] = (m < 16 && ((m & 7) >> 1) == r) ? ((m & 1) ? 0xffff0000u : 0x0000ffffu) : 0u;
      LAS f32x4* op = (LAS f32x4*)(LF + L_OP) + kw * 128 + 2 * m + hf;
      LAS f32x4* rp = (LAS f32x4*)(LF + L_OP) + lane;
      LAS u32* cp = L + C_V;
      u32 one = SET;
      int o1o = O1_OFF + (h * 32 + j) * O1_S + lane * 16;
      PIN(op); PIN(rp); PIN(cp); PIN(one); PIN(o1o);
      flag_wait<4, 1>(L + F_P, lane);
      ROLE_TAG();
      const LAS u32x4* pp = (const LAS u32x4*)(L + L_P + ((kw * 2 + hf) * 2 + ((m >> 3) & 1)) * 32);
      f32x16 acc = {};
#pragma unroll
      for (int g = 0; g < 8; ++g) {
        const u32x4 p4 = pp[g];
#pragma unroll
        for (int k = 0; k < 4; ++k) {
          const u32x4 a = msk & p4[k];
          acc = __builtin_amdgcn_mfma_f32_32x32x16_bf16(__builtin_bit_cast(bf16x8_t, a), __builtin_bit_cast(bf16x8_t, d[4 * g + k]), acc, 0, 0, 0);
        }
        __builtin_amdgcn_sched_barrier(0);
      }
      asm volatile("" :: "v"(code_touch));
      op[0] = f32x4{acc[0], acc[1], acc[2], acc[3]};
      op[64] = f32x4{acc[4], acc[5], acc[6], acc[7]};
      const u32 tk = ticket_issue(cp, one);
      const f32x4 o0 = (rp[0] + rp[128]) + (rp[256] + rp[384]);
      const f32x4 o1 = (rp[64] + rp[192]) + (rp[320] + rp[448]);
      u32x4 t0 = tag4(o0.x, o0.y, o0.z, o0.w, tagp), t1 = tag4(o1.x, o1.y, o1.z, o1.w, tagp);
      PIN(t0); PIN(t1);
      if (ticket_value(tk) == 3) {
        st128(rws, o1o, t0);
        st128(rws, o1o + 1024, t1);
        flag_set(L + F_PUB);
      }
      tag_out = tag;
    }
    if (!(h & 1) && j < 14) final_reduce(L, mkrs(ws, WS_BYTES), x, out, (h >> 1) * 14 + j, w - 4, lane, tag_out, tent, bar);
    return;
  }

  __builtin_amdgcn_s_setprio(3);
  const bool mlp = wg < 224;
  const int ke = (wg * 2341) >> 16, cb2 = wg - 28 * ke;
  const int rr = lane >> 3, c8 = lane & 7;
  const bool isq = kw < 2;
  int rowq = 112 * (j >> 3) + 64 * kw, rowm = 56 * ke, colq = (hx * 512 + (j & 7) * 64) * 2, colm = cb2 * 128;
  asm volatile("" : "+s"(rowq), "+s"(rowm), "+s"(colq), "+s"(colm));
  const int row0 = isq ? rowq : rowm;
  const int nrow = (isq | mlp) ? (0x38383040 >> (8 * kw)) & 0xff : 0;
  int rs = isq ? 8192 : DF * 2;
  int vw0 = (row0 + 2 * rr) * rs + (isq ? colq : colm) + c8 * 16;
  vw0 = nrow ? vw0 : OOB;
  int vw3 = 48 + 2 * rr < nrow ? vw0 : OOB;
  int xo = lane < 56 ? lane * 16 : OOB;
  int mat = kw;
  asm volatile("" : "+v"(vw0), "+v"(vw3), "+v"(xo), "+s"(rs), "+s"(mat));
  __builtin_amdgcn_sched_barrier(0);
  ROLE_ENTRY();
  const rsrc_t rx = mkrs((const u16*)x, 2 * D * 2);
  const u32x4 xa = ld128<0>(rx, xo), xb = ld128<0>(rx, xo + D * 2);
  const u32 epoch = ld32<AUX_SC1>(rws, EP_OFF);
  __builtin_amdgcn_sched_barrier(0);
  u32x4 wv[8];
  {
    const rsrc_t rw = mkrs(mat < 2 ? wq : mat == 2 ? wg_ : wu, mat < 2 ? D * 4096 * 2 : D * DF * 2);
#pragma unroll
    for (int i = 0; i < 8; ++i) wv[i] = ld128<AUX_NT>(rw, i < 6 ? vw0 : vw3, (16 * (i >> 1) + (i & 1)) * rs);
  }
  __builtin_amdgcn_sched_barrier(0);
  __builtin_amdgcn_s_barrier();
  org = __builtin_amdgcn_s_memrealtime();
  __builtin_amdgcn_sched_barrier(0);
  const int cbf = cb2 == 27 ? 0 : cb2 + 1;
  const int e0 = QP_OFF + (hx * 32 + j) * QP_S, e1 = GP_OFF + wg * GP_S - 512, e2 = ST_OFF + (wg & 127) * 64 - 24 * 64;
  const u32 box_touch = ld32<AUX_SC1>(rws, (kw == 1 && lane < 25) ? (lane < 8 ? e0 : lane < 24 ? e1 : e2) + lane * 64 : OOB);
  __builtin_amdgcn_sched_barrier(0);

#pragma unroll
  for (int bb = 0; bb < 2; ++bb) {
    const u32x4 xv = bb ? xb : xa;
    float v[8];
#pragma unroll
    for (int k = 0; k < 4; ++k) { v[2 * k] = lo16(xv[k]); v[2 * k + 1] = hi16(xv[k]); }
    float s = 0.f;
#pragma unroll
    for (int k = 0; k < 8; ++k) s += v[k];
    const float mean = wave_sum(s) * (1.f / D);
    float q = 0.f;
#pragma unroll
    for (int k = 0; k < 8; ++k) { const float t = v[k] - mean; q += t * t; }
    q = lane < 56 ? q : 0.f;
    const float var = rbf(wave_sum(q) * (1.f / D));
    const float den = rbf(__builtin_amdgcn_sqrtf(rbf(var + 1e-5f))), rden = __builtin_amdgcn_rcpf(den), mb = rbf(mean);
#pragma unroll
    for (int k = 0; k < 8; ++k) { const float c = rbf(v[k] - mb), r = c * rden; v[k] = fmaf(fmaf(-r, den, c), rden, r); }
    u32x4 o;
#pragma unroll
    for (int k = 0; k < 4; ++k) o[k] = pkbf(v[2 * k], v[2 * k + 1]);
    if (lane < 56) *(LAS u32x4*)(L + L_XN + bb * 224 + lane * 4) = o;
    else if (bb == 1) *(LAS u32x4*)(L + L_XN + 224 + lane * 4) = u32x4{0u, 0u, 0u, 0u};
  }
  asm volatile("" :: "v"(code_touch));
  const u32 tag = __builtin_amdgcn_readfirstlane(epoch) + 1u, tagp = tag & TMASK;
  if (kw == 0) L[L_TAG] = tag;
  if (kw == 3 && wg < 8 && lane == 0) __builtin_amdgcn_raw_buffer_store_b64(u32x2{(u32)org, tag}, rws, OR_OFF + wg * 8, 0, AUX_SC1);

  {
    float a0[8] = {}, a1[8] = {};
#pragma unroll
    for (int k = 0; k < 4; ++k) {
      const u32 x0 = L[L_XN + row0 / 2 + rr + 8 * k], x1 = L[L_XN + 224 + row0 / 2 + rr + 8 * k];
#pragma unroll
      for (int c = 0; c < 4; ++c) {
        const u32 pl = __builtin_amdgcn_perm(wv[2 * k + 1][c], wv[2 * k][c], 0x05040100u), ph = __builtin_amdgcn_perm(wv[2 * k + 1][c], wv[2 * k][c], 0x07060302u);
        a0[2 * c] = dot2(pl, x0, a0[2 * c]); a0[2 * c + 1] = dot2(ph, x0, a0[2 * c + 1]);
        a1[2 * c] = dot2(pl, x1, a1[2 * c]); a1[2 * c + 1] = dot2(ph, x1, a1[2 * c + 1]);
      }
    }
    asm volatile("" :: "v"(box_touch));
    float t[8], u[4];
#pragma unroll
    for (int k = 0; k < 8; ++k) t[k] = tr32(a0[k], a1[k]);
#pragma unroll
    for (int k = 0; k < 4; ++k) { u[k] = tr16(t[k], t[k + 4]); u[k] += dpp<ROR8>(u[k]); }
    const int bb = lane >> 5, hf = (lane >> 4) & 1;
    LAS f32x4* qx = (LAS f32x4*)(LF + L_QX) + bb * 16 + c8 * 2 + hf;
    if (kw == 1) {
      if (!(lane & 8)) *qx = f32x4{u[0], u[1], u[2], u[3]};
      flag_set(L + F_QX);
    } else if (kw == 0) {
      flag_wait<1, 0>(L + F_QX, lane);
      const f32x4 o = *qx;
      if (!(lane & 8)) st128(rws, QP_OFF + (hx * 32 + j) * QP_S + bb * 256 + hf * 128 + c8 * 16, tag4(u[0] + o.x, u[1] + o.y, u[2] + o.z, u[3] + o.w, tagp));
    } else if (mlp) {
      LAS u32x4* gx = (LAS u32x4*)(L + L_GX) + (kw - 2) * 32;
      if (!(lane & 8)) gx[c8 * 4 + bb * 2 + hf] = tag4(u[0], u[1], u[2], u[3], tagp);
      CBAR();
      if (lane < 32) st128(rws, GP_OFF + wg * GP_S + (lane >> 2) * 128 + (kw - 2) * 64 + (lane & 3) * 16, gx[lane]);
    }
  }

  if (kw < 2) {
    __builtin_amdgcn_s_sleep(Q_DELAY);
    const int qo = QP_OFF + (h * 32 + 4 * kw + 8 * (lane >> 4)) * QP_S + b * 256 + (lane & 15) * 16;
    u32x4 a[4];
    for (u32 s = 0; s < SPIN_MEM; ++s) {
      CBAR();
#pragma unroll
      for (int i = 0; i < 4; ++i) a[i] = ld128<AUX_SC1>(rws, qo, i * QP_S);
      if (all_ok((untag4(a[0], tagp) | untag4(a[1], tagp)) | (untag4(a[2], tagp) | untag4(a[3], tagp)))) break;
    }
#pragma unroll
    for (int i = 0; i < 2; ++i) {
      const u32x4 a0 = a[2 * i], a1 = a[2 * i + 1];
      const float v0 = tr32(asf(a0.x), asf(a1.x)), v1 = tr32(asf(a0.y), asf(a1.y)), v2 = tr32(asf(a0.z), asf(a1.z)), v3 = tr32(asf(a0.w), asf(a1.w));
      L[L_Q + (4 * kw + 2 * i + (lane >> 5)) * 32 + 4 * (lane & 7) + 2 * ((lane >> 3) & 1) + ((lane >> 4) & 1)] = pkbf(tr16(v0, v2), tr16(v1, v3));
    }
    flag_set(L + F_Q + kw);
  } else if (kw == 2) {
    u32 h0 = 0u, h1 = 0u;
    if (mlp) {
      const int go = GP_OFF + (cbf + 28 * rr) * GP_S + ke * 128 + c8 * 16;
      u32x4 a;
      flag_wait<2, 1>(L + F_Q, lane);
      for (u32 s = 0; s < SPIN_MEM; ++s) {
        CBAR();
        a = ld128<AUX_SC1>(rws, go);
        if (all_ok(untag4(a, tagp))) break;
        __builtin_amdgcn_s_sleep(16);
      }
      float hv[4];
#pragma unroll
      for (int k = 0; k < 4; ++k) {
        float g = asf(a[k]);
        g += dpp<ROR8>(g); g = tr16(g, g); g = tr32(g, g);
        g = rbf(g);
        const float uu = dpp<ROR4>(g);
        const float z = 0.7978845608028654f * (g + 0.044715f * g * g * g);
        const float th = 1.f - 2.f * __builtin_amdgcn_rcpf(1.f + __builtin_amdgcn_exp2f(z * 2.8853900817779268f));
        hv[k] = rbf(0.5f * g * (1.f + th)) * uu;
      }
      h0 = pkbf(hv[0], hv[1]); h1 = pkbf(hv[2], hv[3]);
    }
    if (lane < 4) {
      *(LAS u32x2*)(L + L_H + (2 * (lane & 1)) * 4 + (lane >> 1) * 2) = u32x2{h0, SET};
      *(LAS u32x2*)(L + L_H + (2 * (lane & 1) + 1) * 4 + (lane >> 1) * 2) = u32x2{h1, SET};
    }
    CBAR();
  } else {
    pace<T_ORG>(org);
    const u32x2 o = __builtin_amdgcn_raw_buffer_load_b64(rws, OR_OFF + (lane & 7) * 8, 0, AUX_SC1);
    const bool ok = o.y == tag;
    const float n = row_sum(ok ? 1.f : 0.f), dsum = row_sum(ok ? (float)(int)(o.x - (u32)org) : 0.f);
    const float sh = n > 0.f ? fminf(fmaxf(dsum * __builtin_amdgcn_rcpf(n) + ORG_BIAS, -80.f), 80.f) : 0.f;
    L[L_SH] = (u32)(int)sh;
    flag_set(L + F_SH);
  }

  const int rg = lane >> 4, cl = lane & 15;
  const int co = kw * 128 + cl * 8;
  u32x4 wa[4], wb[2];
  flag_wait<4, 1>(L + F_VI, lane);
  pace<T_LATE>(org + (u64)(long long)(int)__builtin_amdgcn_readfirstlane(L[L_SH]));
  {
    const rsrc_t ro = mkrs(wo, 4096 * D * 2), rd = mkrs(wd, mlp ? DF * D * 2 : 0);
    const int vo = co < D ? (hx * 512 + j * 16 + 4 * rg) * (D * 2) + co * 2 : OOB;
    const int vd = co < D ? (64 * cbf + 8 * ke + 2 * rg) * (D * 2) + co * 2 : OOB;
#pragma unroll
    for (int i = 0; i < 2; ++i) wb[i] = ld128<AUX_NT>(rd, vd, i * D * 2);
#pragma unroll
    for (int i = 0; i < 4; ++i) wa[i] = ld128<AUX_NT>(ro, vo, i * D * 2);
  }
  __builtin_amdgcn_sched_barrier(0);
  float a0[8], a1[8];
  u32 pa[8], pb[8];
  {
    u32x4 hh;
    for (u32 s = 0; s < SPIN_LDS; ++s) {
      CBAR();
      hh = *(LAS u32x4*)(L + L_H + rg * 4);
      if (none((hh.y != SET) | (hh.w != SET))) break;
      __builtin_amdgcn_s_sleep(1);
    }
#pragma unroll
    for (int k = 0; k < 4; ++k) {
      const u32 pl = __builtin_amdgcn_perm(wb[1][k], wb[0][k], 0x05040100u), ph = __builtin_amdgcn_perm(wb[1][k], wb[0][k], 0x07060302u);
      a0[2 * k] = dot2(pl, hh.x, 0.f); a0[2 * k + 1] = dot2(ph, hh.x, 0.f);
      a1[2 * k] = dot2(pl, hh.z, 0.f); a1[2 * k + 1] = dot2(ph, hh.z, 0.f);
    }
#pragma unroll
    for (int k = 0; k < 4; ++k) {
      pa[2 * k] = __builtin_amdgcn_perm(wa[1][k], wa[0][k], 0x05040100u); pa[2 * k + 1] = __builtin_amdgcn_perm(wa[1][k], wa[0][k], 0x07060302u);
      pb[2 * k] = __builtin_amdgcn_perm(wa[3][k], wa[2][k], 0x05040100u); pb[2 * k + 1] = __builtin_amdgcn_perm(wa[3][k], wa[2][k], 0x07060302u);
    }
  }
#pragma unroll
  for (int k = 0; k < 8; ++k) asm volatile("" : "+v"(a0[k]), "+v"(a1[k]), "+v"(pa[k]), "+v"(pb[k]));
  int o2o = co < D ? O2_OFF + (h * 32 + j) * O2_S + (lane >> 5) * 1792 + kw * 512 + (((lane >> 4) & 1) * (kw == 3 ? 8 : 16) + cl) * 16 : OOB;
  LAS u32x4* orp = (LAS u32x4*)(L + L_O + rg * 4);
  LAS u32* ofp = L + F_O;
  PIN(o2o); PIN(orp); PIN(ofp);
  {
    const u32x4 oo = data_wait(ofp, orp);
#pragma unroll
    for (int k = 0; k < 8; ++k) {
      a0[k] = dot2(pb[k], oo.y, dot2(pa[k], oo.x, a0[k]));
      a1[k] = dot2(pb[k], oo.w, dot2(pa[k], oo.z, a1[k]));
    }
    float t[8];
#pragma unroll
    for (int k = 0; k < 8; ++k) t[k] = tr32(a0[k], a1[k]);
    st128(rws, o2o, tag4(tr16(t[0], t[4]), tr16(t[1], t[5]), tr16(t[2], t[6]), tr16(t[3], t[7]), tagp));
  }
  flag_set(L + F_O2 + kw);
}

extern "C" void kernel_launch(void* const* d_in, const int* in_sizes, int n_in, void* d_out, int out_size, void* d_ws, size_t ws_size,
                              hipStream_t stream) {
  fused_decode<<<256, 768, 0, stream>>>((const u16*)d_in[0], (const u16*)d_in[3], (const u16*)d_in[6], (const u16*)d_in[7], (const u16*)d_in[1],
                                        (char*)d_ws, (const u16*)d_in[2], (const u16*)d_in[4], (const u16*)d_in[8], (u16*)d_out);
}
```

```cpp
#include <hip/hip_runtime.h>
#include <stdint.h>

typedef unsigned int u32;
typedef unsigned short u16;
typedef unsigned long long u64;
typedef u32 u32x2 __attribute__((ext_vector_type(2)));
typedef u32 u32x4 __attribute__((ext_vector_type(4)));
typedef float f32x4 __attribute__((ext_vector_type(4)));
typedef float f32x16 __attribute__((ext_vector_type(16)));
typedef __bf16 bf16x2_t __attribute__((ext_vector_type(2)));
typedef __bf16 bf16x8_t __attribute__((ext_vector_type(8)));
#define LAS __attribute__((address_space(3)))
typedef __amdgpu_buffer_rsrc_t rsrc_t;

namespace {
constexpr int D = 448, SK = 32768, W = 4096, DH = 256, DF = 1792;
constexpr int AUX_NT = 2, AUX_SC1 = 16;
constexpr int OOB = 1 << 30;
constexpr u32 TMASK = 0xfu;

constexpr int QP_S = 768, GP_S = 1280, O1_S = 2304, O2_S = 3840;
constexpr int EP_OFF = 0;
constexpr int OR_OFF = 2048;
constexpr int ST_OFF = 4096;
constexpr int QP_OFF = ST_OFF + 8192;
constexpr int GP_OFF = QP_OFF + 256 * QP_S;
constexpr int O1_OFF = GP_OFF + 256 * GP_S;
constexpr int O2_OFF = O1_OFF + 256 * O1_S;
constexpr int WS_BYTES = O2_OFF + 256 * O2_S;

constexpr int L_XN = 0;
constexpr int L_QX = L_XN + 480;
constexpr int L_GX = L_QX + 128;
constexpr int L_Q = L_GX + 256;
constexpr int L_P = L_Q + 256;
constexpr int L_OP = L_P + 512;
constexpr int L_WM = L_OP + 2048;
constexpr int L_WL = L_WM + 8;
constexpr int L_H = L_WL + 8;
constexpr int L_O = L_H + 16;
constexpr int F_O = L_O + 16;
constexpr int L_R = L_O + 32;
constexpr int F_P = L_R + 128;
constexpr int F_VI = F_P + 4;
constexpr int F_O2 = F_VI + 4;
constexpr int F_Q = F_O2 + 4;
constexpr int F_QX = F_Q + 2, F_PUB = F_QX + 1;
constexpr int C_V = F_PUB + 1, C_R = C_V + 1;
constexpr int L_TAG = C_V + 4;
constexpr int L_SH = L_TAG + 2, F_SH = L_TAG + 3;
constexpr int L_END = L_TAG + 4;
constexpr u32 SET = 1u;
static_assert((L_END - L_WM) % 4 == 0 && (L_END - L_WM) / 4 <= 64, "one 16-byte store per lane clears the flag area");

constexpr int T_K0 = 25, T_KSTEP10 = 610, T_VSTEP10 = 625;
constexpr int K_FREE = 2;
constexpr int V_FINE = 4, V_FINE_LEAD = 25;
constexpr int WINDOW = 6;
constexpr int T_ORG = 300, ORG_BIAS = 5;
constexpr int due_k(int g) { return T_K0 + g * T_KSTEP10 / 10; }
constexpr int due_v(int g, int half) { return T_K0 + (8 * T_KSTEP10 + g * T_VSTEP10 + half * T_VSTEP10 / 2) / 10 - (g >= V_FINE ? V_FINE_LEAD : 0); }
constexpr int T_LATE = due_v(8, 0);
constexpr int Q_DELAY = 40, O1_DELAY = 11, O2_DELAY = 8;
constexpr int ROT = 70;
constexpr int CODE_SPAN = 15104;
constexpr u32 SPIN_LDS = 1u << 21, SPIN_MEM = 1u << 16;

__device__ __forceinline__ float asf(u32 v) { return __builtin_bit_cast(float, v); }
__device__ __forceinline__ u32 asu(float v) { return __builtin_bit_cast(u32, v); }
__device__ __forceinline__ float lo16(u32 v) { return asf(v << 16); }
__device__ __forceinline__ float hi16(u32 v) { return asf(v & 0xffff0000u); }
__device__ __forceinline__ u32 pkbf(float a, float b) {
  bf16x2_t r = {(__bf16)a, (__bf16)b};
  return __builtin_bit_cast(u32, r);
}
__device__ __forceinline__ float rbf(float a) { return (float)(__bf16)a; }
__device__ __forceinline__ float dot2(u32 a, u32 b, float c) {
  return __builtin_amdgcn_fdot2_f32_bf16(__builtin_bit_cast(bf16x2_t, a), __builtin_bit_cast(bf16x2_t, b), c, false);
}
template <int CTRL> __device__ __forceinline__ float dpp(float x) {
  return asf(__builtin_amdgcn_mov_dpp((int)asu(x), CTRL, 0xf, 0xf, true));
}
constexpr int XOR1 = 0xB1, XOR2 = 0x4E, ROR8 = 0x128, ROR4 = 0x124;
__device__ __forceinline__ float tr32(float a, float b) {
  auto r = __builtin_amdgcn_permlane32_swap(asu(a), asu(b), false, false);
  return asf(r[0]) + asf(r[1]);
}
__device__ __forceinline__ float tr16(float a, float b) {
  auto r = __builtin_amdgcn_permlane16_swap(asu(a), asu(b), false, false);
  return asf(r[0]) + asf(r[1]);
}
__device__ __forceinline__ float mx32(float a) {
  auto r = __builtin_amdgcn_permlane32_swap(asu(a), asu(a), false, false);
  return fmaxf(asf(r[0]), asf(r[1]));
}
__device__ __forceinline__ float mx16(float a) {
  auto r = __builtin_amdgcn_permlane16_swap(asu(a), asu(a), false, false);
  return fmaxf(asf(r[0]), asf(r[1]));
}
__device__ __forceinline__ float row_sum(float v) { v += dpp<ROR8>(v); v += dpp<ROR4>(v); v += dpp<XOR2>(v); v += dpp<XOR1>(v); return v; }
__device__ __forceinline__ float row_max(float v) {
  v = fmaxf(v, dpp<ROR8>(v)); v = fmaxf(v, dpp<ROR4>(v)); v = fmaxf(v, dpp<XOR2>(v)); v = fmaxf(v, dpp<XOR1>(v)); return v;
}
__device__ __forceinline__ float wave_sum(float v) { v = row_sum(v); v = tr16(v, v); return tr32(v, v); }

__device__ __forceinline__ rsrc_t mkrs(const void* p, u32 bytes) { return __builtin_amdgcn_make_buffer_rsrc((void*)p, 0, bytes, 0x00020000); }
template <int AUX> __device__ __forceinline__ u32x4 ld128(rsrc_t r, int voff, int soff = 0) { return __builtin_amdgcn_raw_buffer_load_b128(r, voff, soff, AUX); }
template <int AUX> __device__ __forceinline__ u32 ld32(rsrc_t r, int voff) { return __builtin_amdgcn_raw_buffer_load_b32(r, voff, 0, AUX); }
__device__ __forceinline__ void st128(rsrc_t r, int voff, u32x4 v) { __builtin_amdgcn_raw_buffer_store_b128(v, r, voff, 0, AUX_SC1); }
__device__ __forceinline__ u32 tagw(float v, u32 tag4) { return (asu(v) & ~TMASK) | tag4; }
__device__ __forceinline__ u32x4 tag4(float a, float b, float c, float d, u32 t) { return u32x4{tagw(a, t), tagw(b, t), tagw(c, t), tagw(d, t)}; }
__device__ __forceinline__ u32 untag4(u32x4& v, u32 t) { v.x ^= t; v.y ^= t; v.z ^= t; v.w ^= t; return v.x | v.y | v.z | v.w; }
__device__ __forceinline__ bool none(bool bad) { return __builtin_amdgcn_ballot_w64(bad) == 0ull; }
__device__ __forceinline__ bool all_ok(u32 bad) { return none((bad & TMASK) != 0u); }

#define CBAR() asm volatile("" ::: "memory")
#define PIN(v) asm volatile("" : "+v"(v))
#define LGKM0() asm volatile("s_waitcnt lgkmcnt(0)" ::: "memory")
template <int N, int SLEEP> __device__ __forceinline__ void flag_wait(LAS u32* p, int lane) {
  u32 v, cnt;
  const u32 addr = (u32)(uintptr_t)(p + (lane & (N - 1)));
  asm volatile("s_mov_b32 %1, %4\n"
               "1:\n\t"
               "ds_read_b32 %0, %2\n\t"
               "s_waitcnt lgkmcnt(0)\n\t"
               "v_cmp_ne_u32 vcc, %3, %0\n\t"
               "s_cbranch_vccz 2f\n\t"
               "s_sleep %5\n\t"
               "s_sub_u32 %1, %1, 1\n\t"
               "s_cbranch_scc0 1b\n"
               "2:"
               : "=&v"(v), "=&s"(cnt) : "v"(addr), "i"(SET), "i"(SPIN_LDS), "i"(SLEEP) : "vcc", "scc", "memory");
}
template <int DUE> __device__ __forceinline__ void pace(u64 org) {
  asm volatile("1:\n\t"
               "s_memrealtime vcc\n\t"
               "s_waitcnt lgkmcnt(0)\n\t"
               "s_sub_u32 vcc_lo, vcc_lo, %0\n\t"
               "s_cmp_lt_i32 vcc_lo, %1\n\t"
               "s_cbranch_scc0 2f\n\t"
               "s_sleep 1\n\t"
               "s_branch 1b\n"
               "2:"
               :: "s"((u32)org), "i"(DUE), "s"(org) : "vcc", "scc", "memory");
}
__device__ __forceinline__ void flag_set(LAS u32* p, u32 set = SET) { CBAR(); *p = set; CBAR(); }

__device__ __forceinline__ u32 ticket_issue(LAS u32* p, u32 one) {
  u32 t;
  asm volatile("s_mov_b64 exec, 1\n\tds_add_rtn_u32 %0, %1, %2\n\ts_mov_b64 exec, -1" : "=&v"(t) : "v"((u32)(uintptr_t)p), "v"(one) : "memory");
  return t;
}
__device__ __forceinline__ u32 ticket_value(u32 t) {
  asm volatile("s_waitcnt lgkmcnt(0)" : "+v"(t) :: "memory");
  return __builtin_amdgcn_readfirstlane(t);
}

__device__ __forceinline__ void poll2(rsrc_t rs, int voff, int soff, u32 t4, u32x4& r0, u32x4& r1) {
  u32 o0, o1, o2, o3, o4, o5, o6, o7, m, cnt;
  asm volatile("s_mov_b32 %9, %14\n"
               "1:\n\t"
               "buffer_load_dwordx4 v[144:147], %10, %11, 0 offen sc1\n\t"
               "buffer_load_dwordx4 v[148:151], %10, %11, %12 offen sc1\n\t"
               "s_sub_u32 %9, %9, 1\n\t"
               "s_waitcnt vmcnt(1)\n\t"
               "v_xor_b32 %0, %13, v144\n\tv_xor_b32 %1, %13, v145\n\tv_xor_b32 %2, %13, v146\n\tv_xor_b32 %3, %13, v147\n\t"
               "v_or3_b32 %8, %0, %1, %2\n\t"
               "s_waitcnt vmcnt(0)\n\t"
               "v_xor_b32 %4, %13, v148\n\tv_xor_b32 %5, %13, v149\n\tv_xor_b32 %6, %13, v150\n\tv_xor_b32 %7, %13, v151\n\t"
               "v_or3_b32 %8, %8, %3, %4\n\t"
               "v_or3_b32 %8, %8, %5, %6\n\t"
               "v_bitop3_b32 %8, %8, 15, %7 bitop3:0xc8\n\t"
               "v_cmp_ne_u32 vcc, 0, %8\n\t"
               "s_cbranch_vccz 2f\n\t"
               "s_cbranch_scc0 1b\n"
               "2:"
               : "=&v"(o0), "=&v"(o1), "=&v"(o2), "=&v"(o3), "=&v"(o4), "=&v"(o5), "=&v"(o6), "=&v"(o7), "=&v"(m), "=&s"(cnt)
               : "v"(voff), "s"(rs), "s"(soff), "s"(t4), "i"(SPIN_MEM)
               : "vcc", "scc", "memory", "v144", "v145", "v146", "v147", "v148", "v149", "v150", "v151");
  r0 = u32x4{o0, o1, o2, o3};
  r1 = u32x4{o4, o5, o6, o7};
}
__device__ __forceinline__ u32x4 data_wait(LAS u32* flag, LAS u32x4* p) {
  u32x4 r;
  u32 v, cnt;
  asm volatile("s_mov_b32 %2, %5\n"
               "1:\n\t"
               "ds_read_b32 %1, %3\n\t"
               "ds_read_b128 %0, %4\n\t"
               "s_sub_u32 %2, %2, 1\n\t"
               "s_waitcnt lgkmcnt(0)\n\t"
               "v_cmp_ne_u32 vcc, 1, %1\n\t"
               "s_cbranch_vccz 2f\n\t"
               "s_cbranch_scc0 1b\n"
               "2:"
               : "=&v"(r), "=&v"(v), "=&s"(cnt) : "v"((u32)(uintptr_t)flag), "v"((u32)(uintptr_t)p), "i"(SPIN_LDS) : "vcc", "scc", "memory");
  return r;
}

__device__ __forceinline__ void final_reduce(LAS u32* L, rsrc_t rws, const u16* x, u16* out, int rid, int grp, int lane, u32 tag) {
  LAS float* const LF = (LAS float*)L;
  __builtin_amdgcn_s_setprio(3);
  const u32 tagp = tag & TMASK;
  const int rb = rid / 28, cb = rid % 28, qd = lane & 3;
  const int pi = cb & 7, ph = cb < 24 ? pi >> 2 : pi >> 1, pg = cb < 24 ? pi & 3 : pi & 1;
  const int col = rb * D + 128 * (cb >> 3) + 8 * (4 * pg + ((lane >> 2) & 3)) + 4 * ph + (lane & 3);
  float xres = lo16(x[col]);
  int oo = O2_OFF + (grp * 32 + (lane >> 2)) * O2_S + rb * 1792 + cb * 64 + qd * 16;
  LAS float* wp = LF + ((lane & 12) == 0 ? L_R + (4 * qd + (lane >> 4)) * 8 + grp : L_XN + 128 + lane);
  LAS u32* cp = L + C_R;
  LAS f32x4* rp = (LAS f32x4*)(LF + L_R + (lane & 15) * 8);
  int so = lane < 16 ? col * 2 : OOB;
  u32 setv = SET;
  rsrc_t ro = mkrs(out, 2 * D * 2);
  asm volatile("" : "+s"(ro));
  PIN(xres); PIN(oo); PIN(wp); PIN(cp); PIN(rp); PIN(so); PIN(setv);
  flag_wait<1, 1>(L + F_PUB, lane);
  flag_wait<4, 0>(L + F_O2, lane);
  __builtin_amdgcn_s_sleep(O2_DELAY);
  u32x4 a0, a1;
  poll2(rws, oo, 16 * O2_S, tagp, a0, a1);
  float f = tr16(tr32(asf(a0.x) + asf(a1.x), asf(a0.z) + asf(a1.z)), tr32(asf(a0.y) + asf(a1.y), asf(a0.w) + asf(a1.w)));
  f += dpp<ROR8>(f); f += dpp<ROR4>(f);
  *wp = f;
  const u32 tk = ticket_issue(cp, setv);
  const f32x4 r0 = rp[0], r1 = rp[1];
  const f32x4 rs4 = r0 + r1;
  const float tot = xres + ((rs4.x + rs4.z) + (rs4.y + rs4.w));
  u32 res = pkbf(tot, tot);
  PIN(res);
  if (ticket_value(tk) == 7) {
    __builtin_amdgcn_raw_buffer_store_b16((u16)res, ro, so, 0, 0);
    if (rid == 0 && lane == 0) __builtin_amdgcn_raw_buffer_store_b32(tag, rws, EP_OFF, 0, AUX_SC1);
  }
}

}

extern "C" __global__ void __launch_bounds__(768, 3)
fused_decode(const u16* __restrict__ x, const u16* __restrict__ wq, const u16* __restrict__ wg_, const u16* __restrict__ wu,
             const u16* __restrict__ kc, char* __restrict__ ws, const u16* __restrict__ vc, const u16* __restrict__ wo,
             const u16* __restrict__ wd, u16* __restrict__ out) {
  __shared__ __attribute__((aligned(16))) u32 smem[L_END];
  LAS u32* const L = (LAS u32*)smem;
  LAS float* const LF = (LAS float*)smem;
  const int wg = blockIdx.x, h = wg & 7, j = wg >> 3, b = j & 1, sp = j >> 1, reg = wg & 15;
  const int w = __builtin_amdgcn_readfirstlane(threadIdx.x >> 6), lane = threadIdx.x & 63, kw = w & 3;
  asm volatile("s_load_dword s94, %0, 0x0\n\ts_load_dword s95, %0, 0x40" :: "s"(__builtin_amdgcn_kernarg_segment_ptr()) : "s94", "s95");
  u64 pc0;
  asm volatile("s_getpc_b64 %0" : "=s"(pc0));
  const u32 code_touch = ld32<0>(mkrs((const char*)(pc0 & ~255ull), w == 0 ? CODE_SPAN : 0), lane < 5 ? j * 640 + lane * 128 : OOB);
  __builtin_amdgcn_sched_barrier(0);
#define ROLE_ENTRY() const rsrc_t rws = mkrs(ws, WS_BYTES)
#define ROLE_TAG() const u32 tag = __builtin_amdgcn_readfirstlane(L[L_TAG]), tagp = tag & TMASK
  const u32 blk0 = sp * 4 + reg * ROT;
#define BLOCK_OFF(slot) (((blk0 + (((slot) >> 2) << 6) + ((slot) & 3u)) & 511u) << 12)
  const int hx = h ^ 4;
  u64 org;
#define STREAM_GROUP(G, VOFF, SLOT, ISK) \
  if (!(ISK) || (G) >= K_FREE) pace<(ISK) ? due_k(G) : due_v(G, 0)>(org); \
  if (4 * (G) > WINDOW) asm volatile("s_waitcnt vmcnt(%0)" :: "i"(WINDOW) : "memory"); \
  _Pragma("unroll") for (int i = 4 * (G); i < 4 * (G) + 2; ++i) d[i] = ld128<AUX_NT>(rkv, (VOFF), (int)BLOCK_OFF(SLOT)); \
  if (!(ISK) && (G) >= V_FINE) { __builtin_amdgcn_sched_barrier(0); pace<due_v(G, 1)>(org); } \
  _Pragma("unroll") for (int i = 4 * (G) + 2; i < 4 * (G) + 4; ++i) d[i] = ld128<AUX_NT>(rkv, (VOFF), (int)BLOCK_OFF(SLOT)); \
  __builtin_amdgcn_sched_barrier(0);
#define STREAM_LOADS(VOFF, SLOT, ISK) \
  STREAM_GROUP(0, VOFF, SLOT, ISK) STREAM_GROUP(1, VOFF, SLOT, ISK) STREAM_GROUP(2, VOFF, SLOT, ISK) STREAM_GROUP(3, VOFF, SLOT, ISK) \
  STREAM_GROUP(4, VOFF, SLOT, ISK) STREAM_GROUP(5, VOFF, SLOT, ISK) STREAM_GROUP(6, VOFF, SLOT, ISK) STREAM_GROUP(7, VOFF, SLOT, ISK)
#define WINDOW_RSRC(SRC) mkrs((SRC) + ((size_t)reg * SK + (SK - W)) * DH, W * DH * 2)

  if (__builtin_expect(w >= 4, 0)) {
    u32 tag_out;
    if (w < 8) {
      __builtin_amdgcn_s_setprio(2);
      const int g = lane >> 4, key = (lane >> 1) & 7;
      const bool hb = lane & 1;
      const int kvo = key * 512 + (2 * g + (lane & 1)) * 16;
      const rsrc_t rkv = WINDOW_RSRC(kc);
      u32x4 d[32];
      __builtin_amdgcn_sched_barrier(0);
      __builtin_amdgcn_s_barrier();
      org = __builtin_amdgcn_s_memrealtime();
      __builtin_amdgcn_sched_barrier(0);
      STREAM_LOADS(kvo + (i & 3) * 128, kw + 4 * (i >> 2), true)
      ROLE_ENTRY();
      flag_wait<2, 1>(L + F_Q, lane);
      ROLE_TAG();
      u32x4 qa[4];
#pragma unroll
      for (int i = 0; i < 4; ++i) qa[i] = *(LAS u32x4*)(L + L_Q + (lane & 1) * 128 + i * 32 + (2 * g + ((lane >> 1) & 1)) * 4);
      f32x4 ea, eb;
#pragma unroll
      for (int n = 0; n < 4; ++n) {
        f32x4 ca = {}, cb = {};
#pragma unroll
        for (int i = 0; i < 4; ++i) {
          ca = __builtin_amdgcn_mfma_f32_16x16x32_bf16(__builtin_bit_cast(bf16x8_t, qa[i]), __builtin_bit_cast(bf16x8_t, d[8 * n + i]), ca, 0, 0, 0);
          cb = __builtin_amdgcn_mfma_f32_16x16x32_bf16(__builtin_bit_cast(bf16x8_t, qa[i]), __builtin_bit_cast(bf16x8_t, d[8 * n + 4 + i]), cb, 0, 0, 0);
        }
#pragma unroll
        for (int k = 0; k < 4; ++k) { ea[k] = (n == 0 || g == n) ? ca[k] : ea[k]; eb[k] = (n == 0 || g == n) ? cb[k] : eb[k]; }
      }
      asm volatile("" :: "v"(code_touch));
      float sc[2];
#pragma unroll
      for (int hd = 0; hd < 2; ++hd) sc[hd] = rbf((hb ? eb[2 + hd] : ea[hd]) + dpp<XOR1>(hb ? ea[2 + hd] : eb[hd])) * 0.0625f;
      const float wm0 = mx32(mx16(row_max(sc[0]))), wm1 = mx32(mx16(row_max(sc[1])));
      CBAR();
      if (lane < 2) L[L_WM + kw * 2 + lane] = (asu(lane ? wm1 : wm0) & 0xffff0000u) | SET;
      u32x4 ma, mb;
      for (u32 s = 0; s < SPIN_LDS; ++s) {
        CBAR();
        ma = *(LAS u32x4*)(L + L_WM) ^ SET; mb = *(LAS u32x4*)(L + L_WM + 4) ^ SET;
        if (none((((ma.x | ma.y | ma.z | ma.w) | (mb.x | mb.y | mb.z | mb.w)) & 0xffffu) != 0u)) break;
      }
      const float M0 = fmaxf(fmaxf(asf(ma.x), asf(ma.z)), fmaxf(asf(mb.x), asf(mb.z)));
      const float M1 = fmaxf(fmaxf(asf(ma.y), asf(ma.w)), fmaxf(asf(mb.y), asf(mb.w)));
      const float p0 = rbf(__builtin_amdgcn_exp2f((sc[0] - M0) * 1.4426950408889634f));
      const float p1 = rbf(__builtin_amdgcn_exp2f((sc[1] - M1) * 1.4426950408889634f));
      LAS u32* pp = L + L_P + key * 64 + kw + 4 * (2 * g + (lane & 1));
      pp[0] = asu(p0) | (asu(p0) >> 16);
      pp[32] = asu(p1) | (asu(p1) >> 16);
      float ls = tr32(p0, p1);
      ls = row_sum(tr16(ls, ls));
      if ((lane & 31) == 0) LF[L_WL + kw * 2 + (lane >> 5)] = ls;
      flag_set(L + F_P + kw);

      if (kw == 0) {
        flag_wait<4, 0>(L + F_P, lane);
        if (lane < 2) {
          const float Ls = (LF[L_WL + lane] + LF[L_WL + 2 + lane]) + (LF[L_WL + 4 + lane] + LF[L_WL + 6 + lane]);
          st128(rws, ST_OFF + (((h * 2 + lane) * 2 + b) * 16 + sp) * 16, u32x4{asu(lane ? M1 : M0), tag, asu(Ls), tag});
        }
      } else if (kw == 1) {
        __builtin_amdgcn_s_setprio(3);
        const int s = lane >> 2, qd = lane & 3;
        const int so = ST_OFF + ((hx * 2 + (j >> 4)) * 32 + s) * 16;
        const int oo = O1_OFF + (hx * 32 + 2 * s) * O1_S + j * 64 + qd * 16;
        u32x4 s0, s1, a0, a1;
        flag_wait<4, 1>(L + F_VI, lane);
        for (u32 it = 0; it < SPIN_MEM; ++it) {
          CBAR();
          s0 = ld128<AUX_SC1>(rws, so); s1 = ld128<AUX_SC1>(rws, so + 256);
          if (none((s0.y != tag) | (s0.w != tag) | (s1.y != tag) | (s1.w != tag))) break;
          flag_wait<1, 1>(L + F_PUB, lane);
        }
        float wgt[2];
#pragma unroll
        for (int bb = 0; bb < 2; ++bb) {
          const float m = asf(bb ? s1.x : s0.x), l = asf(bb ? s1.z : s0.z);
          float mg = fmaxf(m, dpp<ROR8>(m)); mg = fmaxf(mg, dpp<ROR4>(mg)); mg = mx32(mx16(mg));
          const float e = __builtin_amdgcn_exp2f((m - mg) * 1.4426950408889634f);
          float lg = e * l; lg += dpp<ROR8>(lg); lg += dpp<ROR4>(lg); lg = tr16(lg, lg); lg = tr32(lg, lg);
          wgt[bb] = e * __builtin_amdgcn_rcpf(lg);
        }
        LAS u32* ow = L + ((lane & 12) == 0 ? L_O + qd * 4 + (lane >> 4) : L_XN + lane);
        LAS u32* of = L + F_O;
        u32 setv = SET;
        PIN(wgt[0]); PIN(wgt[1]); PIN(ow); PIN(of); PIN(setv);
        flag_wait<1, 0>(L + F_PUB, lane);
        __builtin_amdgcn_s_sleep(O1_DELAY);
        poll2(rws, oo, O1_S, tagp, a0, a1);
        f32x4 m0 = __builtin_bit_cast(f32x4, a0) * wgt[0], m1 = __builtin_bit_cast(f32x4, a1) * wgt[1];
        PIN(m0); PIN(m1);
        const float v0 = tr32(m0.x, m1.x), v2 = tr32(m0.z, m1.z), v1 = tr32(m0.y, m1.y), v3 = tr32(m0.w, m1.w);
        float e0 = tr16(v0, v2), e1 = tr16(v1, v3);
        e0 += dpp<ROR8>(e0); e1 += dpp<ROR8>(e1);
        e0 += dpp<ROR4>(e0); e1 += dpp<ROR4>(e1);
        *ow = pkbf(e0, e1);
        flag_set(of, setv);
      }
      tag_out = tag;
    } else {
      if (kw == 0 && lane < (L_END - L_WM) / 4) *(LAS u32x4*)(L + L_WM + 4 * lane) = u32x4{0u, 0u, 0u, 0u};
      LGKM0();
      __builtin_amdgcn_s_barrier();
      org = __builtin_amdgcn_s_memrealtime();
      __builtin_amdgcn_s_setprio(1);
      __builtin_amdgcn_sched_barrier(0);
      ROLE_ENTRY();
      flag_wait<1, 4>(L + F_SH, lane);
      org += (u64)(long long)(int)__builtin_amdgcn_readfirstlane(L[L_SH]);
      const rsrc_t rkv = WINDOW_RSRC(vc);
      u32x4 d[32];
      STREAM_LOADS(lane * 16 + kw * 1024, (u32)i, false)
      flag_set(L + F_VI + kw);
      const int m = lane & 31, hf = lane >> 5;
      u32x4 msk;
#pragma unroll
      for (int r = 0; r < 4; ++r) msk[r] = (m < 16 && ((m & 7) >> 1) == r) ? ((m & 1) ? 0xffff0000u : 0x0000ffffu) : 0u;
      LAS f32x4* op = (LAS f32x4*)(LF + L_OP) + kw * 128 + 2 * m + hf;
      LAS f32x4* rp = (LAS f32x4*)(LF + L_OP) + lane;
      LAS u32* cp = L + C_V;
      u32 one = SET;
      int o1o = O1_OFF + (h * 32 + j) * O1_S + lane * 16;
      PIN(op); PIN(rp); PIN(cp); PIN(one); PIN(o1o);
      flag_wait<4, 1>(L + F_P, lane);
      ROLE_TAG();
      const LAS u32x4* pp = (const LAS u32x4*)(L + L_P + ((kw * 2 + hf) * 2 + ((m >> 3) & 1)) * 32);
      f32x16 acc = {};
#pragma unroll
      for (int g = 0; g < 8; ++g) {
        const u32x4 p4 = pp[g];
#pragma unroll
        for (int k = 0; k < 4; ++k) {
          const u32x4 a = msk & p4[k];
          acc = __builtin_amdgcn_mfma_f32_32x32x16_bf16(__builtin_bit_cast(bf16x8_t, a), __builtin_bit_cast(bf16x8_t, d[4 * g + k]), acc, 0, 0, 0);
        }
        __builtin_amdgcn_sched_barrier(0);
      }
      asm volatile("" :: "v"(code_touch));
      op[0] = f32x4{acc[0], acc[1], acc[2], acc[3]};
      op[64] = f32x4{acc[4], acc[5], acc[6], acc[7]};
      const u32 tk = ticket_issue(cp, one);
      const f32x4 o0 = (rp[0] + rp[128]) + (rp[256] + rp[384]);
      const f32x4 o1 = (rp[64] + rp[192]) + (rp[320] + rp[448]);
      u32x4 t0 = tag4(o0.x, o0.y, o0.z, o0.w, tagp), t1 = tag4(o1.x, o1.y, o1.z, o1.w, tagp);
      PIN(t0); PIN(t1);
      if (ticket_value(tk) == 3) {
        st128(rws, o1o, t0);
        st128(rws, o1o + 1024, t1);
        flag_set(L + F_PUB);
      }
      tag_out = tag;
    }
    if (!(h & 1) && j < 14) final_reduce(L, mkrs(ws, WS_BYTES), x, out, (h >> 1) * 14 + j, w - 4, lane, tag_out);
    return;
  }

  __builtin_amdgcn_s_setprio(3);
  const bool mlp = wg < 224;
  const int ke = (wg * 2341) >> 16, cb2 = wg - 28 * ke;
  const int rr = lane >> 3, c8 = lane & 7;
  const bool isq = kw < 2;
  int rowq = 112 * (j >> 3) + 64 * kw, rowm = 56 * ke, colq = (hx * 512 + (j & 7) * 64) * 2, colm = cb2 * 128;
  asm volatile("" : "+s"(rowq), "+s"(rowm), "+s"(colq), "+s"(colm));
  const int row0 = isq ? rowq : rowm;
  const int nrow = (isq | mlp) ? (0x38383040 >> (8 * kw)) & 0xff : 0;
  int rs = isq ? 8192 : DF * 2;
  int vw0 = (row0 + 2 * rr) * rs + (isq ? colq : colm) + c8 * 16;
  vw0 = nrow ? vw0 : OOB;
  int vw3 = 48 + 2 * rr < nrow ? vw0 : OOB;
  int xo = lane < 56 ? lane * 16 : OOB;
  int mat = kw;
  asm volatile("" : "+v"(vw0), "+v"(vw3), "+v"(xo), "+s"(rs), "+s"(mat));
  __builtin_amdgcn_sched_barrier(0);
  ROLE_ENTRY();
  const rsrc_t rx = mkrs((const u16*)x, 2 * D * 2);
  const u32x4 xa = ld128<0>(rx, xo), xb = ld128<0>(rx, xo + D * 2);
  const u32 epoch = ld32<AUX_SC1>(rws, EP_OFF);
  __builtin_amdgcn_sched_barrier(0);
  u32x4 wv[8];
  {
    const rsrc_t rw = mkrs(mat < 2 ? wq : mat == 2 ? wg_ : wu, mat < 2 ? D * 4096 * 2 : D * DF * 2);
#pragma unroll
    for (int i = 0; i < 8; ++i) wv[i] = ld128<AUX_NT>(rw, i < 6 ? vw0 : vw3, (16 * (i >> 1) + (i & 1)) * rs);
  }
  __builtin_amdgcn_sched_barrier(0);
  __builtin_amdgcn_s_barrier();
  org = __builtin_amdgcn_s_memrealtime();
  __builtin_amdgcn_sched_barrier(0);
  const int cbf = cb2 == 27 ? 0 : cb2 + 1;
  const int e0 = QP_OFF + (hx * 32 + j) * QP_S, e1 = GP_OFF + wg * GP_S - 512, e2 = ST_OFF + (wg & 127) * 64 - 24 * 64;
  const u32 box_touch = ld32<AUX_SC1>(rws, (kw == 1 && lane < 25) ? (lane < 8 ? e0 : lane < 24 ? e1 : e2) + lane * 64 : OOB);
  __builtin_amdgcn_sched_barrier(0);

#pragma unroll
  for (int bb = 0; bb < 2; ++bb) {
    const u32x4 xv = bb ? xb : xa;
    float v[8];
#pragma unroll
    for (int k = 0; k < 4; ++k) { v[2 * k] = lo16(xv[k]); v[2 * k + 1] = hi16(xv[k]); }
    float s = 0.f;
#pragma unroll
    for (int k = 0; k < 8; ++k) s += v[k];
    const float mean = wave_sum(s) * (1.f / D);
    float q = 0.f;
#pragma unroll
    for (int k = 0; k < 8; ++k) { const float t = v[k] - mean; q += t * t; }
    q = lane < 56 ? q : 0.f;
    const float var = rbf(wave_sum(q) * (1.f / D));
    const float den = rbf(__builtin_amdgcn_sqrtf(rbf(var + 1e-5f))), rden = __builtin_amdgcn_rcpf(den), mb = rbf(mean);
#pragma unroll
    for (int k = 0; k < 8; ++k) { const float c = rbf(v[k] - mb), r = c * rden; v[k] = fmaf(fmaf(-r, den, c), rden, r); }
    u32x4 o;
#pragma unroll
    for (int k = 0; k < 4; ++k) o[k] = pkbf(v[2 * k], v[2 * k + 1]);
    if (lane < 56) *(LAS u32x4*)(L + L_XN + bb * 224 + lane * 4) = o;
    else if (bb == 1) *(LAS u32x4*)(L + L_XN + 224 + lane * 4) = u32x4{0u, 0u, 0u, 0u};
  }
  asm volatile("" :: "v"(code_touch));
  const u32 tag = __builtin_amdgcn_readfirstlane(epoch) + 1u, tagp = tag & TMASK;
  if (kw == 0) L[L_TAG] = tag;
  if (kw == 3 && wg < 8 && lane == 0) __builtin_amdgcn_raw_buffer_store_b64(u32x2{(u32)org, tag}, rws, OR_OFF + wg * 8, 0, AUX_SC1);

  {
    float a0[8] = {}, a1[8] = {};
#pragma unroll
    for (int k = 0; k < 4; ++k) {
      const u32 x0 = L[L_XN + row0 / 2 + rr + 8 * k], x1 = L[L_XN + 224 + row0 / 2 + rr + 8 * k];
#pragma unroll
      for (int c = 0; c < 4; ++c) {
        const u32 pl = __builtin_amdgcn_perm(wv[2 * k + 1][c], wv[2 * k][c], 0x05040100u), ph = __builtin_amdgcn_perm(wv[2 * k + 1][c], wv[2 * k][c], 0x07060302u);
        a0[2 * c] = dot2(pl, x0, a0[2 * c]); a0[2 * c + 1] = dot2(ph, x0, a0[2 * c + 1]);
        a1[2 * c] = dot2(pl, x1, a1[2 * c]); a1[2 * c + 1] = dot2(ph, x1, a1[2 * c + 1]);
      }
    }
    asm volatile("" :: "v"(box_touch));
    float t[8], u[4];
#pragma unroll
    for (int k = 0; k < 8; ++k) t[k] = tr32(a0[k], a1[k]);
#pragma unroll
    for (int k = 0; k < 4; ++k) { u[k] = tr16(t[k], t[k + 4]); u[k] += dpp<ROR8>(u[k]); }
    const int bb = lane >> 5, hf = (lane >> 4) & 1;
    LAS f32x4* qx = (LAS f32x4*)(LF + L_QX) + bb * 16 + c8 * 2 + hf;
    if (kw == 1) {
      if (!(lane & 8)) *qx = f32x4{u[0], u[1], u[2], u[3]};
      flag_set(L + F_QX);
    } else if (kw == 0) {
      flag_wait<1, 0>(L + F_QX, lane);
      const f32x4 o = *qx;
      if (!(lane & 8)) st128(rws, QP_OFF + (hx * 32 + j) * QP_S + bb * 256 + hf * 128 + c8 * 16, tag4(u[0] + o.x, u[1] + o.y, u[2] + o.z, u[3] + o.w, tagp));
    } else if (mlp) {
      LAS u32x4* gx = (LAS u32x4*)(L + L_GX) + (kw - 2) * 32;
      if (!(lane & 8)) gx[c8 * 4 + bb * 2 + hf] = tag4(u[0], u[1], u[2], u[3], tagp);
      CBAR();
      if (lane < 32) st128(rws, GP_OFF + wg * GP_S + (lane >> 2) * 128 + (kw - 2) * 64 + (lane & 3) * 16, gx[lane]);
    }
  }

  if (kw < 2) {
    __builtin_amdgcn_s_sleep(Q_DELAY);
    const int qo = QP_OFF + (h * 32 + 4 * kw + 8 * (lane >> 4)) * QP_S + b * 256 + (lane & 15) * 16;
    u32x4 a[4];
    for (u32 s = 0; s < SPIN_MEM; ++s) {
      CBAR();
#pragma unroll
      for (int i = 0; i < 4; ++i) a[i] = ld128<AUX_SC1>(rws, qo, i * QP_S);
      if (all_ok((untag4(a[0], tagp) | untag4(a[1], tagp)) | (untag4(a[2], tagp) | untag4(a[3], tagp)))) break;
    }
#pragma unroll
    for (int i = 0; i < 2; ++i) {
      const u32x4 a0 = a[2 * i], a1 = a[2 * i + 1];
      const float v0 = tr32(asf(a0.x), asf(a1.x)), v1 = tr32(asf(a0.y), asf(a1.y)), v2 = tr32(asf(a0.z), asf(a1.z)), v3 = tr32(asf(a0.w), asf(a1.w));
      L[L_Q + (4 * kw + 2 * i + (lane >> 5)) * 32 + 4 * (lane & 7) + 2 * ((lane >> 3) & 1) + ((lane >> 4) & 1)] = pkbf(tr16(v0, v2), tr16(v1, v3));
    }
    flag_set(L + F_Q + kw);
  } else if (kw == 2) {
    u32 h0 = 0u, h1 = 0u;
    if (mlp) {
      const int go = GP_OFF + (cbf + 28 * rr) * GP_S + ke * 128 + c8 * 16;
      u32x4 a;
      flag_wait<2, 1>(L + F_Q, lane);
      for (u32 s = 0; s < SPIN_MEM; ++s) {
        CBAR();
        a = ld128<AUX_SC1>(rws, go);
        if (all_ok(untag4(a, tagp))) break;
        __builtin_amdgcn_s_sleep(16);
      }
      float hv[4];
#pragma unroll
      for (int k = 0; k < 4; ++k) {
        float g = asf(a[k]);
        g += dpp<ROR8>(g); g = tr16(g, g); g = tr32(g, g);
        g = rbf(g);
        const float uu = dpp<ROR4>(g);
        const float z = 0.7978845608028654f * (g + 0.044715f * g * g * g);
        const float th = 1.f - 2.f * __builtin_amdgcn_rcpf(1.f + __builtin_amdgcn_exp2f(z * 2.8853900817779268f));
        hv[k] = rbf(0.5f * g * (1.f + th)) * uu;
      }
      h0 = pkbf(hv[0], hv[1]); h1 = pkbf(hv[2], hv[3]);
    }
    if (lane < 4) {
      *(LAS u32x2*)(L + L_H + (2 * (lane & 1)) * 4 + (lane >> 1) * 2) = u32x2{h0, SET};
      *(LAS u32x2*)(L + L_H + (2 * (lane & 1) + 1) * 4 + (lane >> 1) * 2) = u32x2{h1, SET};
    }
    CBAR();
  } else {
    pace<T_ORG>(org);
    const u32x2 o = __builtin_amdgcn_raw_buffer_load_b64(rws, OR_OFF + (lane & 7) * 8, 0, AUX_SC1);
    const bool ok = o.y == tag;
    const float n = row_sum(ok ? 1.f : 0.f), dsum = row_sum(ok ? (float)(int)(o.x - (u32)org) : 0.f);
    const float sh = n > 0.f ? fminf(fmaxf(dsum * __builtin_amdgcn_rcpf(n) + ORG_BIAS, -80.f), 80.f) : 0.f;
    L[L_SH] = (u32)(int)sh;
    flag_set(L + F_SH);
  }

  const int rg = lane >> 4, cl = lane & 15;
  const int co = kw * 128 + cl * 8;
  u32x4 wa[4], wb[2];
  flag_wait<4, 1>(L + F_VI, lane);
  pace<T_LATE>(org + (u64)(long long)(int)__builtin_amdgcn_readfirstlane(L[L_SH]));
  {
    const rsrc_t ro = mkrs(wo, 4096 * D * 2), rd = mkrs(wd, mlp ? DF * D * 2 : 0);
    const int vo = co < D ? (hx * 512 + j * 16 + 4 * rg) * (D * 2) + co * 2 : OOB;
    const int vd = co < D ? (64 * cbf + 8 * ke + 2 * rg) * (D * 2) + co * 2 : OOB;
#pragma unroll
    for (int i = 0; i < 2; ++i) wb[i] = ld128<AUX_NT>(rd, vd, i * D * 2);
#pragma unroll
    for (int i = 0; i < 4; ++i) wa[i] = ld128<AUX_NT>(ro, vo, i * D * 2);
  }
  __builtin_amdgcn_sched_barrier(0);
  float a0[8], a1[8];
  u32 pa[8], pb[8];
  {
    u32x4 hh;
    for (u32 s = 0; s < SPIN_LDS; ++s) {
      CBAR();
      hh = *(LAS u32x4*)(L + L_H + rg * 4);
      if (none((hh.y != SET) | (hh.w != SET))) break;
      __builtin_amdgcn_s_sleep(1);
    }
#pragma unroll
    for (int k = 0; k < 4; ++k) {
      const u32 pl = __builtin_amdgcn_perm(wb[1][k], wb[0][k], 0x05040100u), ph = __builtin_amdgcn_perm(wb[1][k], wb[0][k], 0x07060302u);
      a0[2 * k] = dot2(pl, hh.x, 0.f); a0[2 * k + 1] = dot2(ph, hh.x, 0.f);
      a1[2 * k] = dot2(pl, hh.z, 0.f); a1[2 * k + 1] = dot2(ph, hh.z, 0.f);
    }
#pragma unroll
    for (int k = 0; k < 4; ++k) {
      pa[2 * k] = __builtin_amdgcn_perm(wa[1][k], wa[0][k], 0x05040100u); pa[2 * k + 1] = __builtin_amdgcn_perm(wa[1][k], wa[0][k], 0x07060302u);
      pb[2 * k] = __builtin_amdgcn_perm(wa[3][k], wa[2][k], 0x05040100u); pb[2 * k + 1] = __builtin_amdgcn_perm(wa[3][k], wa[2][k], 0x07060302u);
    }
  }
#pragma unroll
  for (int k = 0; k < 8; ++k) asm volatile("" : "+v"(a0[k]), "+v"(a1[k]), "+v"(pa[k]), "+v"(pb[k]));
  int o2o = co < D ? O2_OFF + (h * 32 + j) * O2_S + (lane >> 5) * 1792 + kw * 512 + (((lane >> 4) & 1) * (kw == 3 ? 8 : 16) + cl) * 16 : OOB;
  LAS u32x4* orp = (LAS u32x4*)(L + L_O + rg * 4);
  LAS u32* ofp = L + F_O;
  PIN(o2o); PIN(orp); PIN(ofp);
  {
    const u32x4 oo = data_wait(ofp, orp);
#pragma unroll
    for (int k = 0; k < 8; ++k) {
      a0[k] = dot2(pb[k], oo.y, dot2(pa[k], oo.x, a0[k]));
      a1[k] = dot2(pb[k], oo.w, dot2(pa[k], oo.z, a1[k]));
    }
    float t[8];
#pragma unroll
    for (int k = 0; k < 8; ++k) t[k] = tr32(a0[k], a1[k]);
    st128(rws, o2o, tag4(tr16(t[0], t[4]), tr16(t[1], t[5]), tr16(t[2], t[6]), tr16(t[3], t[7]), tagp));
  }
  flag_set(L + F_O2 + kw);
}

extern "C" void kernel_launch(void* const* d_in, const int* in_sizes, int n_in, void* d_out, int out_size, void* d_ws, size_t ws_size,
                              hipStream_t stream) {
  fused_decode<<<256, 768, 0, stream>>>((const u16*)d_in[0], (const u16*)d_in[3], (const u16*)d_in[6], (const u16*)d_in[7], (const u16*)d_in[1],
                                        (char*)d_ws, (const u16*)d_in[2], (const u16*)d_in[4], (const u16*)d_in[8], (u16*)d_out);
}
```
